```python
import torch

def _softmax(x, axis=-1):
    return torch.softmax(x, dim=axis)
def _gelu(x):
    return torch.nn.functional.gelu(x, approximate="tanh")
def _silu(x):
    return torch.nn.functional.silu(x)
def _sigmoid(x):
    return torch.sigmoid(x)
def _layernorm(x, axis=-1, eps=1e-5):
    m = x.mean(dim=axis, keepdim=True)
    v = x.var(dim=axis, keepdim=True, unbiased=False)
    return (x - m) / torch.sqrt(v + eps)
def _scatter_add(tgt, idx, src, axis=0):
    return tgt.clone().index_add_(axis, idx, src)
def _segment_sum(src, seg, n):
    out = torch.zeros((n,) + src.shape[1:], dtype=src.dtype)
    return out.index_add_(0, seg, src)
def _pad(x, pw):
    flat = tuple(v for pair in reversed(pw) for v in pair)
    return torch.nn.functional.pad(x, flat)

kernel_name = 'parallel_sum__spectral_fnet__moe_arch_mod__prompt'

def setup_inputs():

    _w = (lambda a: (lambda t: t.to(torch.bfloat16) if t.is_floating_point() else t)(torch.as_tensor(a)))
    def _A_setup():
        B, S, D = 1, 2048, 1024
        ns = torch.arange(S)[:, None] * torch.arange(S)[None, :]
        nd = torch.arange(D)[:, None] * torch.arange(D)[None, :]
        Ws_re = torch.cos(-2.0 * torch.pi * ns / S).to(float)
        Ws_im = torch.sin(-2.0 * torch.pi * ns / S).to(float)
        Wd_re = torch.cos(-2.0 * torch.pi * nd / D).to(float)
        Wd_im = torch.sin(-2.0 * torch.pi * nd / D).to(float)
        return {
            "x": _w((lambda *s: torch.randn(*s, dtype=torch.bfloat16) * 0.05)(B, S, D)),
            "dft_seq_re": _w(Ws_re),
            "dft_seq_im": _w(Ws_im),
            "dft_hid_re": _w(Wd_re),
            "dft_hid_im": _w(Wd_im),
        }
    _A_in = _A_setup()

    def _B_setup():
        B, S, D, F, C = 1, 2048, 1024, 3584, 2
        return {
            "x": _w((lambda *s: torch.randn(*s, dtype=torch.bfloat16) * 0.05)(B, S, D)),
            "w_r": _w((lambda *s: torch.randn(*s, dtype=torch.bfloat16) * 1.6)(D)),
            "w_gate": _w((lambda *s: torch.randn(*s, dtype=torch.bfloat16) * 0.4)(D, F)),
            "w_up": _w((lambda *s: torch.randn(*s, dtype=torch.bfloat16) * 0.4)(D, F)),
            "w_down": _w((lambda *s: torch.randn(*s, dtype=torch.bfloat16) * 0.4)(F, D)),
        }
    _B_in = _B_setup()

    return {
        "A_x": _A_in["x"],
        "A_dft_seq_re": _A_in["dft_seq_re"],
        "A_dft_seq_im": _A_in["dft_seq_im"],
        "A_dft_hid_re": _A_in["dft_hid_re"],
        "A_dft_hid_im": _A_in["dft_hid_im"],
        "B_x": _B_in["x"],
        "B_w_r": _B_in["w_r"],
        "B_w_gate": _B_in["w_gate"],
        "B_w_up": _B_in["w_up"],
        "B_w_down": _B_in["w_down"],
    }

def reference(A_x, A_dft_seq_re, A_dft_seq_im, A_dft_hid_re, A_dft_hid_im, B_x, B_w_r, B_w_gate, B_w_up, B_w_down):
    def _A_ref(x, dft_seq_re, dft_seq_im, dft_hid_re, dft_hid_im):
        a = torch.einsum("bsd,ed->bse", x, dft_hid_re)
        b = torch.einsum("bsd,ed->bse", x, dft_hid_im)
        y = torch.einsum("ts,bse->bte", dft_seq_re, a) - torch.einsum("ts,bse->bte", dft_seq_im, b)
        return y

    def _B_ref(x, w_r, w_gate, w_up, w_down):
        B, S, D = x.shape
        C = 2
        r = x @ w_r
        idx = torch.argsort(-r, axis=-1)[:, :C]
        idx_e = (lambda x, n, axis: torch.repeat_interleave(x, n, dim=axis))(idx.reshape(B, C, 1), D, axis=2)
        x_sel = (lambda x, i, axis: torch.take_along_dim(x, i, dim=axis))(x, idx_e, axis=1)
        g = _silu(x_sel @ w_gate)
        u = x_sel @ w_up
        y = (g * u) @ w_down
        r_sel = _sigmoid((lambda x, i, axis: torch.take_along_dim(x, i, dim=axis))(r, idx, axis=1))
        y = y * r_sel.reshape(B, C, 1)
        b_off = (torch.arange(B) * S).reshape(B, 1)
        flat_idx = (idx + b_off).reshape(B * C)
        zero = (x * 0.0).reshape(B * S, D)
        upd = _scatter_add(zero, flat_idx, y.reshape(B * C, D), axis=0)
        return x + upd.reshape(B, S, D)

    x = A_x
    xn = _layernorm(x, axis=-1)
    return x + _A_ref(xn, A_dft_seq_re, A_dft_seq_im, A_dft_hid_re, A_dft_hid_im) + _B_ref(xn, B_w_r, B_w_gate, B_w_up, B_w_down)
```

```cpp
#include <hip/hip_runtime.h>
#include <hip/hip_bf16.h>
#include <stdint.h>
#include <type_traits>

extern "C" __device__ unsigned long long llvm_dispatch_id(void) __asm("llvm.amdgcn.dispatch.id");
extern "C" __device__ float llvm_fmul_legacy(float, float) __asm("llvm.amdgcn.fmul.legacy");
typedef unsigned short u16;
typedef unsigned long long u64;
typedef __attribute__((ext_vector_type(8))) short bf16x8;
typedef __attribute__((ext_vector_type(4))) unsigned u32x4;
typedef __attribute__((ext_vector_type(2))) unsigned u32x2;
typedef __attribute__((ext_vector_type(4))) float f32x4;
typedef __attribute__((ext_vector_type(2))) float f32x2;
typedef __attribute__((ext_vector_type(2))) __bf16 bf16x2;
typedef __attribute__((address_space(3))) void* lds_vp;
typedef volatile __attribute__((address_space(3))) int* lds_vi;

constexpr int S_ = 2048, D_ = 1024, F_ = 3584;
template <int N> using IC = std::integral_constant<int, N>;

static __device__ __forceinline__ float bf2f(u16 u) { return __uint_as_float((unsigned)u << 16); }
static __device__ __forceinline__ u16 f2bf(float f) {
  __hip_bfloat16 b = __float2bfloat16(f);
  return *reinterpret_cast<u16*>(&b);
}
static __device__ __forceinline__ float rbf(float f) { return bf2f(f2bf(f)); }
static __device__ __forceinline__ float lo_bf(unsigned u) { return __uint_as_float(u << 16); }
static __device__ __forceinline__ float hi_bf(unsigned u) { return __uint_as_float(u & 0xffff0000u); }
static __device__ __forceinline__ unsigned pk2(float a, float b) {
  const bf16x2 v = __builtin_convertvector((f32x2){a, b}, bf16x2);
  return __builtin_bit_cast(unsigned, v);
}
static __device__ __forceinline__ float add_bf(unsigned u, unsigned e, float c) {
  return __builtin_amdgcn_fdot2_f32_bf16(__builtin_bit_cast(bf16x2, u), __builtin_bit_cast(bf16x2, e), c, false);
}
static __device__ __forceinline__ float ln_elem(float x, float mb, float sd, float ri) {
  const float d = rbf(x - mb);
  const float q0 = d * ri;
  return rbf(__builtin_fmaf(__builtin_fmaf(-q0, sd, d), ri, q0));
}
template <int CTRL> static __device__ __forceinline__ float dpp_f(float v) {
  return __int_as_float(__builtin_amdgcn_update_dpp(0, __float_as_int(v), CTRL, 0xf, 0xf, true));
}
template <int CTRL> static __device__ __forceinline__ unsigned dpp_u(unsigned v) {
  return (unsigned)__builtin_amdgcn_update_dpp(0, (int)v, CTRL, 0xf, 0xf, true);
}
static __device__ __forceinline__ float row_sum(float v) {
  v += dpp_f<0xB1>(v);
  v += dpp_f<0x4E>(v);
  v += dpp_f<0x141>(v);
  v += dpp_f<0x140>(v);
  return v;
}
static __device__ __forceinline__ float fold32(float a, float b) {
  const auto r = __builtin_amdgcn_permlane32_swap(__float_as_uint(a), __float_as_uint(b), false, false);
  return __uint_as_float(r[0]) + __uint_as_float(r[1]);
}
static __device__ __forceinline__ float fold16(float a, float b) {
  const auto r = __builtin_amdgcn_permlane16_swap(__float_as_uint(a), __float_as_uint(b), false, false);
  return __uint_as_float(r[0]) + __uint_as_float(r[1]);
}
static __device__ __forceinline__ float rdl(float v, int l) { return __int_as_float(__builtin_amdgcn_readlane(__float_as_int(v), l)); }

#define XMAP(g) (((~g & 1) << 2) | (g & 2) | ((g >> 2) & 1))
#define ST_JB 9
#define WSN 10
#define FSM 0xa2
#define GV_JB 7
#define DG 11
#define AB_STRIDE 32
#define SPIN_MAX (1 << 17)
#define FL0 168
#define FL1 192
#define FL2 192
#define VA0 5
#define VAN 4
#define VAP 116
#define VB0 12
#define VBN 0
#define VBP 6
#define SP0 3
#define SP1 7
#define SP1E 11
#define VC0 14
#define VCN 0
#define VCP 9
#define SPS 0x000cccccdddd00
#define SPE 0
#define PRIO2 3
#define N2A 8
#define N3A 8
#define WSL 2
#define LAGS 0x00000000000000
#define DVS 0x00000400300000
#define GVS 0x00000022000000
constexpr int dig(unsigned long long t, int q) { return q < 0 ? 0 : (int)((t >> (4 * q)) & 15); }
constexpr int acc_dig(unsigned long long t, int q) { int a = 0; for (int i = 0; i < q; ++i) a += dig(t, i); return a; }

static __device__ __forceinline__ unsigned lds_addr(const void* p) { return (unsigned)(size_t)(lds_vp)p; }

constexpr int MSG = 4096;
constexpr size_t WS_ABT   = 0;
constexpr size_t WS_SLAB  = WS_ABT + (size_t)D_ * 4096 * 2;
constexpr size_t WS_GU    = WS_SLAB + (size_t)64 * 64 * MSG;
constexpr size_t WS_DN    = WS_GU + (size_t)8 * F_ * 16;
constexpr size_t WS_FLAGS = WS_DN + (size_t)2 * 8 * 32 * 64 * 16;
constexpr int FL_X = 0;
constexpr int FL_AB = 64 * 64 * 32;
constexpr int FL_ST = FL_AB + 2048 * AB_STRIDE;
constexpr int FL_CE = FL_ST + 4096;
constexpr int FL_KEY = FL_CE + 4096;
constexpr int FL_EPOCH = FL_KEY + 4096;
constexpr int WS_MB = (int)((WS_FLAGS + (FL_EPOCH + 1) * 4 + 1048575) >> 20);

#define WAIT_VM(n) asm volatile("s_waitcnt vmcnt(" #n ")" ::: "memory")
template <int N> static __device__ __forceinline__ void wait_vm() { asm volatile("s_waitcnt vmcnt(%0)" :: "i"(N) : "memory"); }
#define BAR_LGKM() asm volatile("s_waitcnt lgkmcnt(0)\n\ts_barrier" ::: "memory")
#define BAR() asm volatile("s_barrier" ::: "memory")
#define SB() __builtin_amdgcn_sched_barrier(0)
#define PAGE(n) asm volatile("s_branch 2f\n\t.p2align 12\nk_pg" #n ":\n\ts_setpc_b64 s[42:43]\n2:")
#define CALL_PG(n) asm volatile("s_getpc_b64 s[40:41]\n3:\n\ts_mov_b32 s42, k_pg" #n "-3b\n\ts_ashr_i32 s43, s42, 31\n\ts_add_u32 s40, s40, s42\n\ts_addc_u32 s41, s41, s43\n\t" \
                                "s_swappc_b64 s[42:43], s[40:41]" ::: "s40", "s41", "s42", "s43", "scc", "memory")

static __device__ __forceinline__ __amdgpu_buffer_rsrc_t mk_rsrc(const void* p) {
  return __builtin_amdgcn_make_buffer_rsrc((void*)p, 0, 0x7fffffff, 0x00020000);
}
#define DMA_(rsrc, voff, mbase, MI, sbase, SI, pol) do { unsigned _t; \
    asm volatile("s_add_i32 m0, %2, %3\n\ts_add_i32 %0, %5, %6\n\tbuffer_load_dwordx4 %1, %4, %0 offen" pol " lds" \
                 : "=&s"(_t) : "v"(voff), "s"(mbase), "i"(MI), "s"(rsrc), "s"(sbase), "i"(SI) : "memory", "scc"); } while (0)
#define DMA_I(rsrc, voff, mbase, MI, SI) \
    asm volatile("s_add_i32 m0, %1, %2\n\ts_nop 0\n\tbuffer_load_dwordx4 %0, %3, 0 offen offset:%4 lds" \
                 :: "v"(voff), "s"(mbase), "i"((MI) - (SI)), "s"(rsrc), "i"(SI) : "memory", "scc")
#define DMA_S(rsrc, voff, mbase, MI, soff) \
    asm volatile("s_add_i32 m0, %1, %2\n\ts_nop 0\n\tbuffer_load_dwordx4 %0, %3, %4 offen lds" \
                 :: "v"(voff), "s"(mbase), "i"(MI), "s"(rsrc), "s"(soff) : "memory", "scc")
#define TOUCH(rsrc, voff, mbase, MI, soff) \
    asm volatile("s_add_i32 m0, %1, %2\n\ts_nop 0\n\tbuffer_load_dword %0, %3, %4 offen lds" \
                 :: "v"(voff), "s"(mbase), "i"(MI), "s"(rsrc), "s"(soff) : "memory", "scc")
template <int OFF> static __device__ __forceinline__ u32x4 ld16_s(const void* p, int voff) {
  u32x4 v;
  asm volatile("global_load_dwordx4 %0, %1, %2 offset:%3" : "=v"(v) : "v"(voff), "s"(p), "i"(OFF) : "memory");
  return v;
}
template <int OFF> static __device__ __forceinline__ void sld(unsigned& sink, const void* p, int soff) {
  asm volatile("s_load_dword %0, %1, %2 offset:%3" : "+s"(sink) : "s"(p), "s"(soff), "i"(OFF) : "memory");
}
static __device__ __forceinline__ unsigned gld4_s(const void* p, unsigned voff) {
  unsigned v;
  asm volatile("global_load_dword %0, %1, %2" : "=v"(v) : "v"(voff), "s"(p) : "memory");
  return v;
}
static __device__ __forceinline__ u32x2 bld8_sc1(__amdgpu_buffer_rsrc_t r, unsigned voff) {
  u32x2 v;
  asm volatile("buffer_load_dwordx2 %0, %1, %2, 0 offen sc1" : "=v"(v) : "v"(voff), "s"(r) : "memory");
  return v;
}
template <int N> static __device__ __forceinline__ void wait_xs(unsigned& a, u32x2& b) { asm volatile("s_waitcnt vmcnt(%2)" : "+v"(a), "+v"(b) : "i"(N) : "memory"); }
template <int N> static __device__ __forceinline__ void wait_xq(u32x2 (&a)[4], u32x2 (&b)[4]) {
  asm volatile("s_waitcnt vmcnt(%8)" : "+v"(a[0]), "+v"(a[1]), "+v"(a[2]), "+v"(a[3]), "+v"(b[0]), "+v"(b[1]), "+v"(b[2]), "+v"(b[3]) : "i"(N) : "memory");
}
static __device__ __forceinline__ u32x2 ld8_s(const void* p, int voff) {
  u32x2 v;
  asm volatile("global_load_dwordx2 %0, %1, %2" : "=v"(v) : "v"(voff), "s"(p) : "memory");
  return v;
}
static __device__ __forceinline__ u32x2 ld8_s_sc1(const void* p, int voff) {
  u32x2 v;
  asm volatile("global_load_dwordx2 %0, %1, %2 sc1" : "=v"(v) : "v"(voff), "s"(p) : "memory");
  return v;
}
static __device__ __forceinline__ void lgkm0(unsigned& sink) { asm volatile("s_waitcnt lgkmcnt(0)" : "+s"(sink) :: "memory"); }
template <typename T> static __device__ __forceinline__ void pin4(T& a, T& b, T& c, T& d) { asm volatile("" : "+s"(a), "+s"(b), "+s"(c), "+s"(d)); }
template <typename T, typename U> static __device__ __forceinline__ void pins(T& a, U& b) { asm volatile("" : "+s"(a), "+s"(b)); }
static __device__ __forceinline__ void launder(int& v, int& a, int& b) { asm volatile("" : "+v"(v)); asm volatile("" : "+s"(a), "+s"(b)); }
static __device__ __forceinline__ unsigned bld4_sc1(__amdgpu_buffer_rsrc_t r, int voff) {
  unsigned v;
  asm volatile("buffer_load_dword %0, %1, %2, 0 offen sc1" : "=v"(v) : "v"(voff), "s"(r) : "memory");
  return v;
}
static __device__ __forceinline__ u32x4 bld16_nt(__amdgpu_buffer_rsrc_t r, int voff, int soff) {
  u32x4 v;
  asm volatile("buffer_load_dwordx4 %0, %1, %2, %3 offen nt" : "=v"(v) : "v"(voff), "s"(r), "s"(soff) : "memory");
  return v;
}
static __device__ __forceinline__ void bld16_nt_io(u32x4& v, __amdgpu_buffer_rsrc_t r, int voff, int soff) {
  asm volatile("buffer_load_dwordx4 %0, %1, %2, %3 offen nt" : "+v"(v) : "v"(voff), "s"(r), "s"(soff) : "memory");
}
static __device__ __forceinline__ u32x4 bld16_sc1(__amdgpu_buffer_rsrc_t r, int voff, int soff) {
  u32x4 v;
  asm volatile("buffer_load_dwordx4 %0, %1, %2, %3 offen sc1" : "=v"(v) : "v"(voff), "s"(r), "s"(soff) : "memory");
  return v;
}
static __device__ __forceinline__ u32x2 granule_wait(const u64* p, unsigned tag) {
  u64 v = 0;
#pragma unroll 1
  for (int it = 0; it < SPIN_MAX; ++it) {
    v = __hip_atomic_load(p, __ATOMIC_RELAXED, __HIP_MEMORY_SCOPE_AGENT);
    if (__all((unsigned)(v >> 32) == tag)) break;
    __builtin_amdgcn_s_sleep(2);
  }
  return (u32x2){(unsigned)v, (unsigned)(v >> 32)};
}
static __device__ __forceinline__ float key_score(unsigned key) {
  const unsigned u = key >> 16;
  return bf2f((u16)((u & 0x8000u) ? (u & 0x7fffu) : (~u & 0xffffu)));
}
static __device__ __forceinline__ void merge2(unsigned& a1, unsigned& a2, unsigned b1, unsigned b2) {
  const unsigned lo = min(a1, b1);
  a1 = max(a1, b1);
  a2 = max(lo, max(a2, b2));
}
static __device__ __forceinline__ float sqdev16(const u32x4 (&v)[2], float mb) {
  f32x2 q2 = {0.f, 0.f};
#pragma unroll
  for (int i = 0; i < 8; ++i) {
    const f32x2 e = (f32x2){lo_bf(v[i >> 2][i & 3]), hi_bf(v[i >> 2][i & 3])} - mb;
    q2 += e * e;
  }
  return q2.x + q2.y;
}

constexpr int swz(int r) { return ((r >> 1) & 4) | (r & 2); }
static __device__ __forceinline__ int sym_dn(int p) { return (p ^ (p >> 1)) & 1; }
static __device__ __forceinline__ int sym_hi(int n, int w, int st, int b, int z, int p) {
  return ((p >> 1) * (n / 2 * st) + b * (w * st)) ^ (-sym_dn(p) & (((n - 1) ^ (z * (n / 2))) * st));
}
static __device__ __forceinline__ int sym_lo(int w, int st, int p, int c) { return ((c - sym_dn(p)) * st) & ((w - 1) * st); }
static __device__ __forceinline__ int sym_row(int n, int w, int st, int b, int z, int p, int c) { return sym_lo(w, st, p, c) ^ sym_hi(n, w, st, b, z, p); }
constexpr int G1_NST = 4, G1_NKT = 16, G1_STAGE = 32768;
constexpr int G2_NKT = 16, G2_MRING = 98304;
constexpr int n_off(int k) { return (k % 3) * 32768; }
constexpr int m_off(int k) { return ((k + 2) % 3) * 16384; }
constexpr int k_ord(int k) { return k < 2 ? k : (k < 9 ? 2 * (k - 1) : 2 * (k - 8) + 1); }
constexpr int LDS_DUMMY = G2_MRING + 49152;
constexpr int LDS_GU = LDS_DUMMY + 4096;
constexpr int LDS_FIN = LDS_GU + 4096;
constexpr int LDS_MB = LDS_FIN + 4096;
constexpr int LDS_SEL = LDS_MB + 1536;
constexpr int LDS_SIG = LDS_SEL + 64;
constexpr int LDS_KEY = LDS_SIG + 32;
constexpr int LDS_BYTES = LDS_KEY + 128;
constexpr int LDS_ACC = n_off(G2_NKT - 3);

constexpr int sp_tile(int q) {
  for (int k = 0; k < 16; ++k) {
    if ((k >= VA0 && k < VA0 + VAN) || (k >= VB0 && k < VB0 + VBN) || (k >= VC0 && k < VC0 + VCN)) continue;
    if (q-- == 0) return k;
  }
  return -1;
}
constexpr int tcnt(int q) { return (VAN > 0 && q == VAP) + (VBN > 0 && q == VBP) + (VCN > 0 && q == VCP); }
constexpr int dn_cnt(int q) { return dig(DVS, q); }
constexpr int dn_first(int q) { return acc_dig(DVS, q); }

static __device__ __forceinline__ void code_touch(unsigned& sink, unsigned off) {
  asm volatile("s_getpc_b64 s[40:41]\n\tk_main_rt:\n\ts_min_u32 %1, %1, k_main_end-k_main_rt-196\n\t"
               "s_load_dword %0, s[40:41], %1 offset:0\n\ts_load_dword %0, s[40:41], %1 offset:64\n\ts_load_dword %0, s[40:41], %1 offset:128\n\ts_load_dword %0, s[40:41], %1 offset:192"
               : "+s"(sink), "+s"(off) :: "s40", "s41", "scc", "memory");
}
static __device__ __forceinline__ void touch1(__amdgpu_buffer_rsrc_t r, int voff, unsigned mb, int soff) { TOUCH(r, voff, mb, LDS_DUMMY, soff); }
typedef const __attribute__((address_space(1))) u16* gptr;
typedef __attribute__((ext_vector_type(8))) u64 u64x8;
typedef __attribute__((ext_vector_type(2))) u64 u64x2;
extern "C" __global__ __launch_bounds__(512, 2) void k_main(const u16* x, const u16* wd_re, const u16* wd_im, const u16* w_r,
                                                 char* wsp, const u16* ws_re, const u16* ws_im,
                                                 const u16* w_gate, const u16* w_up, const u16* w_down, u16* out) {
  __shared__ __attribute__((aligned(16))) char smem[LDS_BYTES];
  u64x8 ka;
  u64x2 kb_;
  u64 kc;
  asm volatile("s_load_dwordx16 %0, %3, 0\n\ts_load_dwordx4 %1, %3, 64\n\ts_load_dwordx2 %2, %3, 80"
               : "=&s"(ka), "=&s"(kb_), "=&s"(kc) : "s"(__builtin_amdgcn_kernarg_segment_ptr()) : "memory");
  const int tid = threadIdx.x, lane = tid & 63;
  const int wid = __builtin_amdgcn_readfirstlane(tid >> 6);
  const int g = blockIdx.x, xcd = XMAP(g), j = g >> 3;
  if (wid < 4) {
    unsigned cw, co = (j * 4 + wid) * 448;
    asm volatile(".subsection 1\nk_main_end:\n.subsection 0\n\ts_min_u32 %1, %1, k_main_end-k_main-480\n\ts_getpc_b64 s[40:41]\n\ts_add_u32 s40, s40, %1\n\ts_addc_u32 s41, s41, 0\n\t"
                 "s_load_dword %0, s[40:41], 0\n\ts_load_dword %0, s[40:41], 64\n\ts_load_dword %0, s[40:41], 128\n\t"
                 "s_load_dword %0, s[40:41], 192\n\ts_load_dword %0, s[40:41], 256\n\ts_load_dword %0, s[40:41], 320\n\ts_load_dword %0, s[40:41], 384\n\ts_waitcnt lgkmcnt(0)"
                 : "=&s"(cw), "+s"(co) :: "s40", "s41", "scc", "memory");
  }
  const int pw = wid - 4;
  const int row0 = (wid & 3) * 8 + (lane >> 3);
  const int cm = ((lane & 7) ^ swz(row0)) * 16;
  int vm = ((row0 & 15) * 2 + (row0 >> 4)) * (D_ * 2) + cm;
  asm volatile("" : "+v"(vm));
  const int kq = xcd >> 1;
  const int tt = (xcd & 1) * 4 + (j >> 3);
  const int et = j & 7;
  const int z = kq >> 1, st = (kq & 1) * 8 + tt;
  const int lrow = (kq & 1) * 1024 + (xcd & 1) * 512 + z * 256 + j * 8 + pw * 2;
  const int drow = ((kq & 1) * 2 + (xcd & 1)) * 256 + j * 8 + pw * 2;
  unsigned o_m = st * 128 * D_ * 2, o_l = lrow * D_ * 2, o_d = drow * D_ * 2, mb0 = lds_addr(smem) + pw * 1024;
  int l16 = lane * 16;
  int vn[4];
#pragma unroll
  for (int q = 0; q < 4; ++q) vn[q] = sym_lo(32, D_ * 2, (row0 >> 2) & 3, q * 8 + (row0 >> 4) * 4 + (row0 & 3)) ^ (sym_hi(D_, 32, D_ * 2, et, z, (row0 >> 2) & 3) | cm);
  asm volatile("" : "+s"(o_m), "+s"(o_l), "+s"(o_d), "+s"(mb0));
  asm volatile("" : "+v"(l16), "+v"(vn[0]), "+v"(vn[1]), "+v"(vn[2]), "+v"(vn[3]));
  asm volatile("s_waitcnt lgkmcnt(0)" : "+s"(ka), "+s"(kb_), "+s"(kc) :: "memory");
  x = (const u16*)(gptr)ka[0]; wd_re = (const u16*)(gptr)ka[1]; wd_im = (const u16*)(gptr)ka[2]; w_r = (const u16*)(gptr)ka[3];
  wsp = (char*)(gptr)ka[4]; ws_re = (const u16*)(gptr)ka[5]; ws_im = (const u16*)(gptr)ka[6]; w_gate = (const u16*)(gptr)ka[7];
  w_up = (const u16*)(gptr)kb_[0]; w_down = (const u16*)(gptr)kb_[1]; out = (u16*)(gptr)kc;
  int* flags = (int*)(wsp + WS_FLAGS);
  u64* stat = (u64*)(flags + FL_ST);
  const u16* wdz = z ? wd_im : wd_re;
  const __amdgpu_buffer_rsrc_t rm = mk_rsrc(x);
  const __amdgpu_buffer_rsrc_t rn = mk_rsrc(wdz);
#define G1_X(kt, so, q) DMA_(rm, vm, mb0, (so) + (q) * 4096, o_m, (kt) * 128 + (((q) & 1) * 64 + ((q) >> 1) * 32) * D_ * 2, "")
#define G1_W(kt, so, q) DMA_I(rn, vn[q], mb0, (so) + 16384 + (q) * 4096, (kt) * 128)
#define G1_STAGE_TILE(kt, so, W) do { SB(); G1_X(kt, so, 0); W(0); G1_X(kt, so, 1); W(1); G1_X(kt, so, 2); W(2); G1_X(kt, so, 3); W(3); \
    G1_W(kt, so, 0); W(4); G1_W(kt, so, 1); W(5); G1_W(kt, so, 2); W(6); G1_W(kt, so, 3); W(7); SB(); } while (0)
#define G1_STAGE_A(kt, so, W) do { SB(); G1_X(kt, so, 0); W(0); G1_X(kt, so, 2); W(1); \
    G1_W(kt, so, 0); W(2); G1_W(kt, so, 1); W(3); G1_W(kt, so, 2); W(4); G1_W(kt, so, 3); W(5); W(6); W(7); SB(); } while (0)
#define G1_STAGE_B(kt, so) do { SB(); G1_X(kt, so, 1); G1_X(kt, so, 3); SB(); } while (0)
#define NOW(i)
  const int fr = lane & 15, fq = lane >> 4;
  const int ha = wid >> 2;
  int trow, xo[4], po, ml;
  const char *prs, *prc;
  auto col4 = [](int f) __attribute__((always_inline)) { return ((f & 2) << 3 | (f & 4) << 1 | (f & 1) << 2) ^ (-((f >> 3) & 1) & 28); };
  auto seg0 = [&](int hf, int dn) __attribute__((always_inline)) { return (hf * 512 + et * 32) ^ (-dn & (D_ - 32)); };
  auto epi_setup = [&](int part) __attribute__((always_inline)) {
    int t_ = tt, l_ = lane;
    asm volatile("" : "+s"(t_));
    asm volatile("" : "+v"(l_));
    const int ts = ((wid >> 1) & 1) * 8 + kq * 2 + ha;
    if (part == 0) { xo[3] = (seg0(wid & 1, (l_ >> 3) & 1) + col4(l_)) * 2; asm volatile("" : "+v"(xo[3])); }
    if (part == 1) {
      xo[3] |= sym_hi(S_, 64, D_ * 2, t_, z, l_ >> 4);
      ml = (l_ * 16) ^ (-sym_dn(l_ >> 4) & (z * 768));
      asm volatile("" : "+v"(xo[3]), "+v"(ml));
    }
    if (part == 2) {
#pragma unroll
      for (int r = 0; r < 4; ++r) xo[r] = sym_lo(64, D_ * 2, l_ >> 4, ts * 4 + r) ^ xo[3];
      asm volatile("" : "+v"(xo[0]), "+v"(xo[1]), "+v"(xo[2]), "+v"(xo[3]));
    }
    if (part == 3) {
      trow = t_ * 16 + ts;
      prs = wsp + WS_SLAB + (size_t)((t_ * 8 + et) * 64 + (wid & 3)) * MSG;
      prc = (const char*)(flags + FL_X + ((t_ * 8 + et) * 64 + (wid & 3)) * 32);
      asm volatile("" : "+s"(trow), "+s"(prs), "+s"(prc));
      po = l_ < 3 ? (kq * 4 + ((kq + 1 + l_) & 3)) * 512 : (int)0x80000000;
      asm volatile("" : "+v"(po));
    }
  };
  auto tok_tile = [](unsigned key) __attribute__((always_inline)) {
    const unsigned h = (0xffff - key) & 1023;
    return h < 512 ? h >> 2 : ((1023 - h) >> 6 << 4) | (((1024 - h) & 63) >> 2);
  };
#define rs mk_rsrc(prs)
#define rc mk_rsrc(prc)
  lds_vi sig = (lds_vi)(smem + LDS_SIG + (wid & 3) * 4);
  unsigned tag;
  u32x2 xq[4], sq[4];
  f32x4 ya[4];
  unsigned key[2];
  float cv[16];
  unsigned far0, far1;
  auto xq_load = [&](int n) __attribute__((always_inline)) {
    if (n < 4) xq[n] = ld8_s(x, xo[n]); else sq[n - 4] = ld8_s_sc1(stat, xo[n - 4] >> 11 << 3);
  };
#define PINV(a) asm volatile("" : "+v"(a))
  auto cv_calc = [&]() __attribute__((always_inline)) {
    if (__builtin_expect(!__all(((sq[0].y ^ tag) | (sq[1].y ^ tag) | (sq[2].y ^ tag) | (sq[3].y ^ tag)) == 0), 0)) {
#pragma unroll
      for (int r = 0; r < 4; ++r) sq[r] = granule_wait(stat + (xo[r] >> 11), tag);
    }
    far0 = tok_tile(key[0]) ^ trow; far1 = tok_tile(key[1]) ^ trow;
    asm volatile("" : "+s"(far0), "+s"(far1));
#pragma unroll
    for (int r = 0; r < 4; ++r) {
      const float mb = lo_bf(sq[r].x), ri = __builtin_amdgcn_rcpf(hi_bf(sq[r].x));
#pragma unroll
      for (int i = 0; i < 4; ++i) {
        const float xv = (i & 1) ? hi_bf(xq[r][i >> 1]) : lo_bf(xq[r][i >> 1]);
        cv[r * 4 + i] = __builtin_fmaf(xv - mb, ri, xv);
        PINV(cv[r * 4 + i]);
      }
    }
  };
#define MSG_IDX(m) (kq * 4 + ((kq + 1 + (m)) & 3))
  unsigned seen = 0;

  if (__builtin_expect(wid >= 4, 1)) {
    __builtin_amdgcn_s_setprio(3);
    G1_STAGE_TILE(0, 0, NOW);
    constexpr int O_ST = (FL_ST - FL_AB) * 4, O_CE = (FL_CE - FL_AB) * 4, O_KEY = (FL_KEY - FL_AB) * 4, O_EP = (FL_EPOCH - FL_AB) * 4;
    const __amdgpu_buffer_rsrc_t rf = mk_rsrc(wsp + WS_FLAGS + FL_AB * 4);
    u32x4 lv[2], lv2[2], lw[2], ld[2], ld2[2];
    unsigned epv;
    {
      const char* xrow = (const char*)x + o_l;
      const char* wdrow = (const char*)wdz + o_d;
      lv[0] = ld16_s<0>(xrow, l16); lv[1] = ld16_s<1024>(xrow, l16);
      lv2[0] = ld16_s<2048>(xrow, l16); lv2[1] = ld16_s<3072>(xrow, l16);
      ld[0] = ld16_s<0>(wdrow, l16); ld[1] = ld16_s<1024>(wdrow, l16);
      ld2[0] = ld16_s<2048>(wdrow, l16); ld2[1] = ld16_s<3072>(wdrow, l16);
      lw[0] = ld16_s<0>(w_r, l16); lw[1] = ld16_s<1024>(w_r, l16);
      asm volatile("buffer_load_dword %0, off, %1, %2 sc1" : "=v"(epv) : "s"(rf), "s"(O_EP) : "memory");
    }
    SB();
    const unsigned long long did = llvm_dispatch_id();
    const unsigned dmix = (unsigned)did * 0x85EBCA6Bu + (unsigned)(did >> 32) * 0xC2B2AE35u + 0x27D4EB2Fu;
    const bool moe = et != 7;
    unsigned epoch;
    const int ci = lane & 15, rq = lane >> 4, tk = (lane >> 3) & 1;
    int mat, cg, dq, d0;
    u32x4 wv[16];
    const u16 *pgu, *pn2;
    const char *pm2, *spb;
#define rw __builtin_amdgcn_make_buffer_rsrc((void*)pgu, 0, moe ? 0x7fffffff : 0, 0x00020000)
#define rn2 mk_rsrc(pn2)
#define rm2 mk_rsrc(pm2)
    int vw, vn2[8], vm2;
    auto k_blk = [&](int k) __attribute__((always_inline)) { return tt ^ (k_ord(k) >> 1); };
#define KROT(kk_) (k_blk(kk_) * 256 + (k_ord(kk_) & 1) * 128)
    int tv2[3];
    unsigned sd = 0;
    auto spf = [&](auto N) __attribute__((always_inline)) {
      constexpr int n = decltype(N)::value;
#define SPM 0
      if constexpr (n >= 0 && sp_tile(n >> 4) >= 0) {
        if constexpr (SPM == 0 || n < 80) sld<((n >> 1) & 7) * 4096 + (n & 1) * 64>(sd, spb, KROT(sp_tile(n >> 4)));
        else if constexpr (SPM == 1) sld<(n & 7) * 4096 + ((n >> 3) & 1) * 64>(sd, spb, KROT(sp_tile(n >> 4)));
        else if constexpr (SPM == 2) { constexpr int m = n - 80, q = 5 + (m >> 5) * 2 + ((m >> 3) & 1); if constexpr (sp_tile(q) >= 0) sld<(n & 7) * 4096 + ((m >> 4) & 1) * 64>(sd, spb, KROT(sp_tile(q))); }
        else if constexpr (SPM == 3) sld<(n & 7) * 4096>(sd, spb, KROT(5 + (((n - 80) >> 3) % 11)));
        else if constexpr (SPM == 4) sld<((n >> 1) & 7) * 4096 + (n & 1) * 64>(sd, spb, KROT((n >> 4) & 3));
      }
    };
    auto touches = [&](auto P) __attribute__((always_inline)) {
      constexpr int p = decltype(P)::value;
      if constexpr (VAN > 0 && p == VAP) touch1(mk_rsrc(spb), tv2[0], mb0, 0);
      if constexpr (VBN > 0 && p == VBP) touch1(mk_rsrc(spb), tv2[1], mb0, 0);
      if constexpr (VCN > 0 && p == VCP) touch1(mk_rsrc(spb), tv2[2], mb0, 0);
    };
    unsigned mq0, mq1, mq2, mq3;
    const int cg8 = g & 7, rgb = g >> 3;
    const __amdgpu_buffer_rsrc_t rdn = mk_rsrc(w_down);
    const __amdgpu_buffer_rsrc_t rgu = mk_rsrc(wsp + WS_GU);
    int vdn, vgu;
    u32x4 dv[7], gv[4];
    int abo, abq[3];
    unsigned lma;
    unsigned sel[2][4];
    float g0, g1;
    u32x4 kk[4];
    unsigned k1 = 0, k2 = 0;
    const int ko_ = O_KEY + pw * 4096;
#define MM(a, b, c) __builtin_amdgcn_mfma_f32_16x16x32_bf16(__builtin_bit_cast(bf16x8, a), __builtin_bit_cast(bf16x8, b), c, 0, 0, 0)
#define PIN1(a) asm volatile("" : "+v"(a))
#define PIN2(a, b) asm volatile("" : "+v"(a), "+v"(b))
#define PIN3(a, b, c) asm volatile("" : "+v"(a), "+v"(b), "+v"(c))
    {
      int gro, go;
      u32x2 gr;
      u32x4 oh[5];
      f32x4 sa4, gw[2], gx[2];
      float su, gs, ta, tb, sx, mbv;
      unsigned dk[4], kl;
      const int hb = lane >> 5;
#pragma unroll
      for (int l = 0; l < WSN; ++l) asm volatile("" : "=v"(wv[l]));
      auto diag = [&](const f32x4& m) __attribute__((always_inline)) {
        return __uint_as_float((__float_as_uint(m[0]) & dk[0]) | (__float_as_uint(m[1]) & dk[1]) | (__float_as_uint(m[2]) & dk[2]) | (__float_as_uint(m[3]) & dk[3]));
      };
      auto setup = [&](auto JB, auto GP) __attribute__((always_inline)) {
        constexpr int jb = decltype(JB)::value, gp = decltype(GP)::value;
        SB();
        {
          int ln = lane, jj = j, p_ = pw;
          launder(ln, jj, p_);
          const int c_ = ln & 15, r_ = ln >> 4, e_ = jj & 7, t_ = (xcd & 1) * 4 + (jj >> 3);
          if constexpr (jb == 1 && gp == 0) {
            pn2 = ((kq >> 1) ? ws_im : ws_re) + (kq & 1) * 1024;
            spb = (const char*)(pn2 + (sym_row(S_, 64, 1, t_, 0, e_ >> 1, 0) + (e_ & 1) * 32 + p_ * 8) * S_);
            pins(pn2, spb);
          }
          if constexpr (jb == 1 && gp >= 1 && gp < 6) {
            const unsigned o = c_ == gp - 1 ? 0x3f803f80u : 0u;
            oh[gp - 1] = (u32x4){o, o, o, o};
            PIN1(oh[gp - 1]);
          }
          if constexpr (jb == 1 && gp == 6) {
            const int rk = moe ? e_ * 8 + (jj >> 3) * 2 + (xcd >> 2) : 0, r3 = ((rk - 14) * 43) >> 7, rr = rk - 14 - r3 * 3;
            const int c56 = rk < 14 ? rk * 4 + 1 : r3 * 4 + rr + ((rr + 1) >> 1);
            mat = (c56 + 36) >> 6; cg = c56 - mat * 28; dq = xcd & 3; d0 = dq * 256 + p_ * 64;
            pin4(mat, cg, dq, d0);
          }
          if constexpr (jb == 2 && gp == 0) {
            pgu = mat ? w_up : w_gate;
            pm2 = wsp + WS_ABT + (kq * 8 + e_) * 262144;
            pins(pgu, pm2);
            vw = ((d0 + r_ * 16) * F_ + cg * 128 + c_ * 8) * 2;
            PIN1(vw);
          }
          if constexpr (jb == 2 && (gp == 1 || gp == 2)) {
            dk[gp * 2 - 2] = c_ == r_ * 4 + gp * 2 - 2 ? ~0u : 0u; dk[gp * 2 - 1] = c_ == r_ * 4 + gp * 2 - 1 ? ~0u : 0u;
            PIN2(dk[gp * 2 - 2], dk[gp * 2 - 1]);
          }
          if constexpr (jb == 2 && gp == 3) { kl = lrow + (ln >> 5); go = (ln & 30) ? (int)0x80000000 : ((ln & 1) ? O_KEY : O_ST) + (int)kl * 8; PIN2(kl, go); }
          if constexpr (jb == 2 && gp == 4) { kl = 0xffff - kl; go = (ln & ~1) == 2 ? O_CE + (z * D_ + drow + (ln & 1)) * 8 : go; PIN2(kl, go); }
          if constexpr (jb == 2 && gp == 5) {
            gro = p_ < 2 ? O_ST + (st * 128 + p_ * 64 + ln) * 8 : O_CE + z * D_ * 8 + sym_row(D_, 32, 8, e_, z, (ln >> 2) & 3, (p_ - 2) * 16 + r_ * 4 + (ln & 3));
            lma = lds_addr(smem + LDS_MB) + (p_ < 2 ? p_ * 256 : 512 + p_ * 256) + ln * 4;
            PIN2(gro, lma);
          }
          if constexpr (jb == 1 && gp == 7) {
#pragma unroll
            for (int u = 0; u < 3; ++u) {
              const int vn_ = u == 0 ? VAN : u == 1 ? VBN : VCN;
              if (vn_ == 0) continue;
              const int k = (u == 0 ? VA0 : u == 1 ? VB0 : VC0) + (ln >> 3);
              tv2[u] = (ln >> 3) < vn_ ? (ln & 7) * 4096 + ((t_ ^ (k < 9 ? k - 1 : k - 8)) << 8) + (k < 9 ? 0 : 128) : (int)0x80000000;
              PIN1(tv2[u]);
            }
          }
          if constexpr (jb == 7 && gp == 0) {
            vm2 = p_ * 1024 + ln * 16;
            PIN1(vm2);
          }
          if constexpr (jb == 2 && gp == 6) {
            const int r0 = p_ * 8 + (ln >> 3);
            vn2[6] = sym_lo(64, S_ * 2, (r0 >> 2) & 3, (r0 >> 4) * 4 + (r0 & 3)); vn2[7] = sym_hi(S_, 64, S_ * 2, t_, z, (r0 >> 2) & 3) | (vm & 127);
            PIN2(vn2[6], vn2[7]);
          }
          if constexpr ((jb == 2 && gp == 7) || (jb == 7 && gp >= 6)) {
            constexpr int q0 = jb == 2 ? 0 : gp == 6 ? 3 : 6;
            const int lo = vn2[6], hi = vn2[7];
#pragma unroll
            for (int q = q0; q < q0 + 3 && q < 8; ++q) { vn2[q] = ((lo + q * 8 * S_ * 2) & (63 * S_ * 2)) ^ hi; PIN1(vn2[q]); }
          }
          if constexpr (jb == 7 && gp == 1) { vdn = jj * 112 + p_ * 28 + r_ * 7; vgu = (((ln >> 3) & 1) * 4 * F_ + vdn + min(ln & 7, 6)) * 16; PIN2(vdn, vgu); }
          if constexpr (jb == 7 && gp == 2) { vdn = (vdn * D_ + cg8 * 128 + c_ * 8) * 2; PIN1(vdn); }
          if constexpr (jb == 7 && gp == 3) {
            abo = (((kq * 8 + e_) * 4 + p_) * 16 + c_) * AB_STRIDE * 4;
            abq[2] = ln >= 16 || (ln & 7) == t_ ? (int)0x80000000 : abo;
            PIN2(abo, abq[2]);
          }
          if constexpr (jb == 7 && gp == 4) {
            abq[1] = ln >= 8 ? (int)0x80000000 : abq[2];
            abq[0] = ((ln ^ t_) & 4) ? (int)0x80000000 : abq[1];
            PIN2(abq[0], abq[1]);
          }
          if constexpr (jb == 7 && gp == 5) {
            const unsigned mb = lds_addr(smem) + p_ * 1024;
            mq0 = mb + ((3 - kq) & 3) * 4096; mq1 = mb + ((4 - kq) & 3) * 4096; mq2 = mb + ((5 - kq) & 3) * 4096; mq3 = mb + ((6 - kq) & 3) * 4096;
            pin4(mq0, mq1, mq2, mq3);
          }
          if constexpr (jb == 8 && gp < 4) {
#pragma unroll
            for (int h = 0; h < 2; ++h) {
              const unsigned hv = h ? 0x0302u : 0x0100u;
              sel[h][gp] = ((ln & 7) >> 1) == gp ? ((ln & 1) ? (hv << 16) | 0x0c0cu : 0x0c0c0000u | hv) : 0x0c0c0c0cu;
            }
            PIN2(sel[0][gp], sel[1][gp]);
          }
        }
        SB();
      };
#define WS1(i) setup(IC<1>{}, IC<i>{})
#define WS2(i) setup(IC<2>{}, IC<i>{})
#define WS3(i) do { setup(IC<7>{}, IC<i>{}); if (i >= 4) setup(IC<8>{}, IC<(i) & 3>{}); spf(IC<(i < SP0 ? i : -1)>{}); } while (0)
      G1_STAGE_TILE(1, G1_STAGE, WS1);
      WAIT_VM(19); BAR();
      G1_STAGE_TILE(2, 2 * G1_STAGE, WS2);
      G1_STAGE_TILE(3, 3 * G1_STAGE, WS3);
      auto work1 = [&](auto JB, auto GP) __attribute__((always_inline)) {
        constexpr int jb = decltype(JB)::value, gp = decltype(GP)::value;
        SB();
        if constexpr (jb == 1) {
          const f32x4 z4 = {0.f, 0.f, 0.f, 0.f};
          if constexpr (gp == 0) { sa4 = MM(lv[0], oh[0], z4); gw[0] = MM(lv[0], lw[0], z4); gx[0] = MM(lv[0], lv[0], z4); PIN3(sa4, gw[0], gx[0]); }
          if constexpr (gp == 1) { sa4 = MM(lv2[0], oh[1], sa4); gw[1] = MM(lv2[0], lw[0], z4); gx[1] = MM(lv2[0], lv2[0], z4); PIN3(sa4, gw[1], gx[1]); }
          if constexpr (gp == 2) { sa4 = MM(lv[1], oh[0], sa4); gw[0] = MM(lv[1], lw[1], gw[0]); gx[0] = MM(lv[1], lv[1], gx[0]); PIN3(sa4, gw[0], gx[0]); }
          if constexpr (gp == 3) { sa4 = MM(lv2[1], oh[1], sa4); gw[1] = MM(lv2[1], lw[1], gw[1]); gx[1] = MM(lv2[1], lv2[1], gx[1]); PIN3(sa4, gw[1], gx[1]); }
          if constexpr (gp == 4) { sa4 = MM(ld[0], oh[2], sa4); sa4 = MM(ld[1], oh[2], sa4); PIN1(sa4); }
          if constexpr (gp == 5) { sa4 = MM(ld2[0], oh[3], sa4); sa4 = MM(ld2[1], oh[3], sa4); PIN1(sa4); }
          if constexpr (gp == 6) { sa4 = MM(lw[0], oh[4], sa4); sa4 = MM(lw[1], oh[4], sa4); PIN1(sa4);
            epoch = __builtin_amdgcn_readfirstlane(epv); tag = ((epoch + 1u) * 0x9E3779B1u ^ dmix) | 1u; }
        }
        if constexpr (jb == 2) {
          if constexpr (gp == 0) { su = (sa4[0] + sa4[1]) + (sa4[2] + sa4[3]); ta = diag(gw[0]); PIN2(su, ta); }
          if constexpr (gp == 1) { su = fold32(su, su); tb = diag(gw[1]); PIN2(su, tb); }
          if constexpr (gp == 2) { su = fold16(su, su); gs = fold32(ta, tb); PIN2(su, gs); }
          if constexpr (gp == 3) { ta = diag(gx[0]); tb = diag(gx[1]); PIN2(ta, tb); }
          if constexpr (gp == 4) { gs = fold16(gs, fold32(ta, tb)); PIN1(gs); }
          if constexpr (gp == 5) { gs += dpp_f<0xB1>(gs); gs += dpp_f<0x4E>(gs); PIN1(gs); }
          if constexpr (gp == 6) { gs += dpp_f<0x141>(gs); gs += dpp_f<0x140>(gs); PIN1(gs); }
        }
        if constexpr (jb == 3) {
          if constexpr (gp == 0) {
            const float s0 = rdl(su, 0), s1 = rdl(su, 1), q0 = rdl(gs, 16), q1 = rdl(gs, 48);
            sx = hb ? s1 : s0; ta = hb ? q1 : q0; PIN2(sx, ta);
          }
          if constexpr (gp == 1) {
            const float mean = sx * (1.0f / D_);
            mbv = rbf(mean);
            ta -= sx * mean;
            if (__builtin_expect(__any(!(sx * mean <= 16.f * ta)), 0)) {
              float q = fold32(sqdev16(lv, rdl(mbv, 0)), sqdev16(lv2, rdl(mbv, 32)));
              q = row_sum(fold16(q, q));
              const float dm = mean - mbv;
              ta = fmaxf(q - (float)D_ * dm * dm, 0.f);
            }
            PIN2(mbv, ta);
          }
          if constexpr (gp == 2) { ta = rbf(rbf(ta * (1.0f / D_)) + 1e-5f); gs -= mbv * rdl(su, 4); PIN2(ta, gs); }
          if constexpr (gp == 3) { ta = rbf(__builtin_amdgcn_sqrtf(ta)); tb = __builtin_amdgcn_rcpf(ta); PIN2(ta, tb); }
          if constexpr (gp == 4) {
            const unsigned rb = __float_as_uint(rbf(gs * tb));
            kl |= (rb ^ ((unsigned)((int)rb >> 31) | 0x80000000u)) & 0xffff0000u; PIN1(kl);
          }
          if constexpr (gp == 5) { kl = (lane & 2) ? __float_as_uint(su) : (lane & 1) ? kl : (__float_as_uint(mbv) >> 16) | __float_as_uint(ta); PIN1(kl); }
        }
        if constexpr (jb == ST_JB + 3) {
          if constexpr (gp == 0) { if (!__all(gr.y == tag)) gr = granule_wait((const u64*)((const char*)(flags + FL_AB) + gro), tag); PIN1(gr); }
          if constexpr (gp == 1) { g1 = (z ? -1.0f : 1.0f) / hi_bf(gr.x); PIN1(g1); }
          if constexpr (gp == 2) { g0 = pw < 2 ? -(lo_bf(gr.x) * g1) : __uint_as_float(gr.x); PIN1(g0); }
        }
        if constexpr (gp < SP1 && jb <= SP1E) spf(IC<SP0 + (jb - 1) * SP1 + gp>{});
        SB();
      };
      auto step1 = [&](auto JB) __attribute__((always_inline)) {
        constexpr int jb = decltype(JB)::value;
        if constexpr (jb == 1) PAGE(1);
        if constexpr (jb == 7) PAGE(2);
        constexpr int n16 = jb == 12 ? 12 : jb == 11 ? 14 : 16;
        constexpr auto wl = [](int q) { return (q == 3 ? 1 : 0) + (q == ST_JB ? 1 : 0); };
        if constexpr (jb == 1) {
          asm volatile("s_waitcnt vmcnt(%11)" : "+v"(lv[0]), "+v"(lv[1]), "+v"(lw[0]), "+v"(lw[1]), "+v"(ld[0]), "+v"(ld[1]),
                       "+v"(lv2[0]), "+v"(lv2[1]), "+v"(ld2[0]), "+v"(ld2[1]), "+v"(epv) : "i"(16) : "memory");
        } else if constexpr (jb == ST_JB + 3) {
          asm volatile("s_waitcnt vmcnt(%1)" : "+v"(gr) : "i"(n16 + wl(jb - 2) + wl(jb - 1)) : "memory");
        } else wait_vm<n16 + wl(jb - 3) + wl(jb - 2) + wl(jb - 1)>();
        BAR();
        if constexpr (jb == 2) { if (pw == 0 && lane < 16) ((int*)(smem + LDS_SEL))[lane < 8 ? lane : lane + 8] = 0; }
        if constexpr (jb == 4) { if (pw == 0 && lane == 0) ((unsigned*)(smem + LDS_SEL))[5] = tag; }
#define W1(i) work1(JB, IC<i>{})
        if constexpr (jb < 10) G1_STAGE_TILE(jb - 1 + G1_NST, ((jb - 1) % G1_NST) * G1_STAGE, W1);
        else G1_STAGE_A(jb - 1 + G1_NST, ((jb - 1) % G1_NST) * G1_STAGE, W1);
        if constexpr (jb == 12) { G1_STAGE_B(13, G1_STAGE); G1_STAGE_B(14, 2 * G1_STAGE); }
        if constexpr (jb == 3) __builtin_amdgcn_raw_buffer_store_b64((u32x2){kl, tag}, rf, go, 0, 16);
        if constexpr (jb == ST_JB) {
          asm volatile("buffer_load_dwordx2 %0, %1, %2, 0 offen sc1" : "=v"(gr) : "v"(gro), "s"(rf) : "memory");
        }
        touches(IC<100 + jb>{});
        if constexpr (jb <= 8) {
          if (et == jb - 1) {
#pragma unroll
            for (int l = 0; l < WSN; ++l) bld16_nt_io(wv[l], rw, vw, l * F_ * 2);
          }
        }
        SB();
      };
      static_assert(ST_JB + 3 <= 12 && ST_JB >= 6, "schedule");
      step1(IC<1>{}); step1(IC<2>{}); step1(IC<3>{}); step1(IC<4>{}); step1(IC<5>{}); step1(IC<6>{}); step1(IC<7>{}); step1(IC<8>{}); step1(IC<9>{}); step1(IC<10>{}); step1(IC<11>{}); step1(IC<12>{});
    }
#define ALIAS 0
#define G2_STAGE_N(kk_, W) do { const int kb = KROT(((ALIAS >> (kk_)) & 1) ? (kk_) & 3 : (kk_)); constexpr int so = n_off(kk_); SB(); \
    DMA_S(rn2, vn2[0], mq0, so, kb); W(0); DMA_S(rn2, vn2[1], mq1, so, kb); W(1); \
    DMA_S(rn2, vn2[2], mq2, so, kb); W(2); DMA_S(rn2, vn2[3], mq3, so, kb); W(3); \
    DMA_S(rn2, vn2[4], mq0, so + 16384, kb); W(4); DMA_S(rn2, vn2[5], mq1, so + 16384, kb); W(5); \
    DMA_S(rn2, vn2[6], mq2, so + 16384, kb); W(6); DMA_S(rn2, vn2[7], mq3, so + 16384, kb); W(7); SB(); } while (0)
#define G2_STAGE_NH(kk_, h) do { const int kb = KROT(kk_); constexpr int so = n_off(kk_) + (h) * 16384; SB(); \
    DMA_S(rn2, vn2[(h) * 4], mq0, so, kb); DMA_S(rn2, vn2[(h) * 4 + 1], mq1, so, kb); \
    DMA_S(rn2, vn2[(h) * 4 + 2], mq2, so, kb); DMA_S(rn2, vn2[(h) * 4 + 3], mq3, so, kb); SB(); } while (0)
#define G2_STAGE_M(kk_, W) do { const int kb = KROT(kk_) * 128; constexpr int so = G2_MRING + m_off(kk_); SB(); \
    DMA_(rm2, vm2, mb0, so, kb, 0, " sc1"); W(8); DMA_(rm2, vm2, mb0, so + 4096, kb, 4096, " sc1"); W(9); \
    DMA_(rm2, vm2, mb0, so + 8192, kb, 8192, " sc1"); W(10); DMA_(rm2, vm2, mb0, so + 12288, kb, 12288, " sc1"); W(11); SB(); } while (0)
    WAIT_VM(16); BAR();
    asm volatile("ds_write_b32 %0, %1" :: "v"(lma), "v"(g0) : "memory");
    if (pw < 2) asm volatile("ds_write_b32 %0, %1 offset:512" :: "v"(lma), "v"(g1) : "memory");
    u32x4 tv[4];
    unsigned ready = 0;
    auto need = [&](int k) __attribute__((always_inline)) {
      const int b = k_blk(k) + (k_ord(k) & 1) * 8;
#pragma unroll 1
      for (int it = 0; !((ready >> b) & 1) && it < SPIN_MAX; ++it) {
        ready |= (unsigned)__ballot(__builtin_amdgcn_raw_buffer_load_b32(rf, abq[k < 5 ? 0 : k < 9 ? 1 : 2], 0, 16) == tag);
        asm volatile("" ::: "memory");
      }
    };
    unsigned ksel, bad = 0;
    int isel;
    unsigned xw;
    auto bvec = [&](unsigned src, const unsigned (&sl)[4]) __attribute__((always_inline)) {
      return (u32x4){__builtin_amdgcn_perm(0, src, sl[0]), __builtin_amdgcn_perm(0, src, sl[1]),
                     __builtin_amdgcn_perm(0, src, sl[2]), __builtin_amdgcn_perm(0, src, sl[3])};
    };
#define BC8(v, p) (unsigned)__builtin_amdgcn_update_dpp(__builtin_amdgcn_update_dpp(0, (int)(v), 0x150 + (p), 0xf, 0x3, false), (int)(v), 0x158 + (p), 0xf, 0xc, false)
    float* lg = (float*)(smem + LDS_GU);
    f32x4 macc = {0.f, 0.f, 0.f, 0.f};
    u32x2 sv;
    unsigned hp, gbad;
    float s0, s1;
    auto work2 = [&](auto JB, auto GP) __attribute__((always_inline)) {
      constexpr int jb = decltype(JB)::value, gp = decltype(GP)::value;
      SB();
      if constexpr (jb == -1 && gp < 4) {
        constexpr int c = gp == 0 ? 0xB1 : gp == 1 ? 0x4E : gp == 2 ? 0x141 : 0x140;
        merge2(k1, k2, dpp_u<c>(k1), dpp_u<c>(k2));
        PIN2(k1, k2);
      }
      if constexpr (jb == -1 && gp == 4) {
        ksel = tk ? k2 : k1;
        isel = (0xffff - (int)(ksel & 0xffffu)) & (S_ - 1);
        k1 = (isel * D_ + d0 + rq * 16 + (lane & 7) * 2) * 2; k2 = O_ST + isel * 8;
        PIN1(ksel);
        xw = gld4_s(x, k1);
        sv = bld8_sc1(rf, k2);
      }
      if constexpr (jb == 0 && gp == 1) wait_xs<3 + 1 + (N2A < 2 ? N2A : 2) + tcnt(116) + tcnt(117)>(xw, sv);
      if constexpr (jb == 0) {
        if constexpr (gp == 1) { if (!__all(sv.y == tag)) sv = granule_wait(stat + isel, tag); s0 = __builtin_amdgcn_rcpf(hi_bf(sv.x)); PIN1(s0); }
        if constexpr (gp == 2) { s1 = ln_elem(lo_bf(xw), lo_bf(sv.x), hi_bf(sv.x), s0); PIN1(s1); }
        if constexpr (gp == 3) { xw = pk2(s1, ln_elem(hi_bf(xw), lo_bf(sv.x), hi_bf(sv.x), s0)); PIN1(xw); }
      }
      if constexpr (jb == DG && gp < 7) { PIN1(dv[gp]); macc = MM(dv[gp], bvec(BC8(hp, gp), sel[0]), macc); PIN1(macc); }
      if constexpr ((jb == 0 && gp >= 4) || jb == 1) {
        constexpr int l = jb == 0 ? gp - 4 : gp + 4;
        if constexpr (!(l & 1)) hp = BC8(xw, l >> 1);
        PIN1(wv[l]);
        macc = MM(wv[l], bvec(hp, sel[l & 1]), macc);
        PIN2(macc, hp);
      }
      if constexpr (jb == GV_JB + 2) {
        if constexpr (gp == 0) { gbad = (gv[0].y ^ tag) | (gv[0].w ^ tag) | (gv[1].y ^ tag) | (gv[1].w ^ tag); PIN1(gbad); }
        if constexpr (gp == 1) { gbad |= (gv[2].y ^ tag) | (gv[2].w ^ tag) | (gv[3].y ^ tag) | (gv[3].w ^ tag); PIN1(gbad); }
        if constexpr (gp == 2) {
          s0 = ((__uint_as_float(gv[0].x) + __uint_as_float(gv[1].x)) + __uint_as_float(gv[2].x)) + __uint_as_float(gv[3].x);
          s1 = ((__uint_as_float(gv[0].z) + __uint_as_float(gv[1].z)) + __uint_as_float(gv[2].z)) + __uint_as_float(gv[3].z);
          PIN2(s0, s1);
        }
        if constexpr (gp == 3) {
#pragma unroll 1
          for (int it = 0; !__all(gbad == 0) && it < SPIN_MAX; ++it) {
            u32x4 q[4];
            gbad = 0;
#pragma unroll
            for (int i = 0; i < 4; ++i) { q[i] = __builtin_amdgcn_raw_buffer_load_b128(rgu, vgu, i * F_ * 16, 16); gbad |= (q[i].y ^ tag) | (q[i].w ^ tag); }
            s0 = ((__uint_as_float(q[0].x) + __uint_as_float(q[1].x)) + __uint_as_float(q[2].x)) + __uint_as_float(q[3].x);
            s1 = ((__uint_as_float(q[0].z) + __uint_as_float(q[1].z)) + __uint_as_float(q[2].z)) + __uint_as_float(q[3].z);
            asm volatile("" ::: "memory");
          }
          const unsigned gp2 = pk2(s0, s1);
          const unsigned up2 = dpp_u<0x128>(gp2);
          s0 = tk ? hi_bf(up2) : lo_bf(gp2); s1 = tk ? hi_bf(gp2) : lo_bf(up2);
          PIN2(s0, s1);
        }
        if constexpr (gp == 4) { s0 = rbf(s0 * __builtin_amdgcn_rcpf(1.f + __expf(-s0))); PIN1(s0); }
        if constexpr (gp == 5) { hp = pk2(s0 * s1, 0.f); PIN1(hp); }
      }
      if constexpr (jb == 10 && gp == 4) code_touch(sd, (j * 4 + pw) * 256);
      SB();
    };
#define W2M(i) work2(IC<-1>{}, IC<i>{})
#define W20(i) work2(IC<0>{}, IC<i>{})
#define W21(i) work2(IC<1>{}, IC<i>{})
    auto piece = [&](auto K, auto Q) __attribute__((always_inline)) {
      constexpr int k = decltype(K)::value, q = decltype(Q)::value;
      if constexpr (q < 8) {
        const int kb = KROT(((ALIAS >> k) & 1) ? k & 3 : k);
        DMA_S(rn2, vn2[q], (q & 3) == 0 ? mq0 : (q & 3) == 1 ? mq1 : (q & 3) == 2 ? mq2 : mq3, n_off(k) + (q >> 2) * 16384, kb);
      } else {
        if constexpr (q == 8) need(k);
        const int kb = KROT(k) * 128;
        DMA_(rm2, vm2, mb0, G2_MRING + m_off(k) + (q - 8) * 4096, kb, (q - 8) * 4096, " sc1");
      }
    };
    unsigned fs;
    {
      G1_STAGE_B(15, 3 * G1_STAGE);
      G2_STAGE_NH(0, 0);
      lgkm0(sd);
      WAIT_VM(16); BAR();
      G2_STAGE_NH(0, 1);
      WAIT_VM(10); BAR();
#pragma unroll
      for (int i = 0; i < 4; ++i) kk[i] = bld16_sc1(rf, l16, ko_ + i * 1024);
#pragma unroll
      for (int l = WSN; l < 16; ++l) wv[l] = bld16_nt(rw, vw, l * F_ * 2);
      asm volatile("s_waitcnt vmcnt(%4)" : "+v"(kk[0]), "+v"(kk[1]), "+v"(kk[2]), "+v"(kk[3]) : "i"(16 - WSN) : "memory");
      touches(IC<115>{});
#pragma unroll
      for (int i = 0; i < 4; ++i) {
        k2 = max(k2, min(k1, kk[i].x)); k1 = max(k1, kk[i].x);
        k2 = max(k2, min(k1, kk[i].z)); k1 = max(k1, kk[i].z);
        bad |= (kk[i].y ^ tag) | (kk[i].w ^ tag);
      }
#pragma unroll 1
      for (int it = 0; !__all(bad == 0) && it < SPIN_MAX; ++it) {
        k1 = 0; k2 = 0; bad = 0;
#pragma unroll
        for (int i = 0; i < 4; ++i) {
          const u32x4 q = __builtin_amdgcn_raw_buffer_load_b128(rf, l16, ko_ + i * 1024, 16);
          k2 = max(k2, min(k1, q.x)); k1 = max(k1, q.x);
          k2 = max(k2, min(k1, q.z)); k1 = max(k1, q.z);
          bad |= (q.y ^ tag) | (q.w ^ tag);
        }
        asm volatile("" ::: "memory");
      }
      merge2(k1, k2, dpp_u<0xB1>(k1), dpp_u<0xB1>(k2));
      merge2(k1, k2, dpp_u<0x4E>(k1), dpp_u<0x4E>(k2));
      merge2(k1, k2, dpp_u<0x141>(k1), dpp_u<0x141>(k2));
      merge2(k1, k2, dpp_u<0x140>(k1), dpp_u<0x140>(k2));
      if ((lane & 15) == 0) *(u32x2*)(smem + LDS_KEY + (pw * 4 + rq) * 8) = (u32x2){k1, k2};
      PAGE(3);
      BAR_LGKM();
#pragma unroll
      for (int jj = 0; jj < 4; ++jj) tv[jj] = *(const u32x4*)(smem + G2_MRING + m_off(0) + (jj * 4 + pw) * 1024 + lane * 16);
      { const u32x2 kp = *(const u32x2*)(smem + LDS_KEY + ci * 8); k1 = kp.x; k2 = kp.y; }
#pragma unroll
      for (int jj = 0; jj < 4; ++jj) __builtin_amdgcn_raw_buffer_store_b128(tv[jj], rm2, vm2 + jj * 4096 + 2 * tt * 16384, 0, 16);
      G2_STAGE_N(1, W2M);
      touches(IC<116>{});
      wait_vm<10 + tcnt(116)>();
      if (lane == tt) __builtin_amdgcn_raw_buffer_store_b32(tag, rf, abo, 0, 16);
      touches(IC<117>{});
#define P2A(i) if constexpr (i < N2A) piece(IC<2>{}, IC<i>{}); W20(i);
      SB(); P2A(0) P2A(1) P2A(2) P2A(3) P2A(4) P2A(5) P2A(6) P2A(7)
      __builtin_amdgcn_s_sleep(WSL);
      wait_vm<1 + N2A + tcnt(116) + tcnt(117)>();
      fs = bld4_sc1(rf, abq[0]);
    }
    BAR();
#pragma unroll
    for (int jj = 0; jj < 4; ++jj) tv[jj] = *(const u32x4*)(smem + G2_MRING + m_off(1) + (jj * 4 + pw) * 1024 + lane * 16);
    constexpr int SPB = SP0 + SP1E * SP1;
#define P2B(i) if constexpr (i >= N2A) piece(IC<2>{}, IC<i>{});
    SB(); P2B(0) P2B(1) P2B(2) P2B(3) P2B(4) P2B(5) P2B(6) P2B(7)
#define P3A(i) if constexpr (i < N3A) piece(IC<3>{}, IC<i>{});
    P3A(0) P3A(1) P3A(2) P3A(3) P3A(4) P3A(5) P3A(6) P3A(7)
    asm volatile("s_waitcnt vmcnt(%1)" : "+v"(fs) : "i"(8 - N2A + N3A) : "memory");
    ready = (unsigned)__ballot(fs == tag);
    need(2);
    G2_STAGE_M(2, NOW);
#define P3B(i) if constexpr (i >= N3A) piece(IC<3>{}, IC<i>{});
    P3B(0) P3B(1) P3B(2) P3B(3) P3B(4) P3B(5) P3B(6) P3B(7)
    need(3);
    G2_STAGE_M(3, NOW);
    fs = bld4_sc1(rf, abq[1]);
    touches(IC<1>{});
#pragma unroll
    for (int i = 0; i < dn_cnt(1); ++i) dv[i] = bld16_nt(rdn, vdn, i * D_ * 2);
    W21(0); W21(1); W21(2); W21(3); W21(4); W21(5); W21(6); W21(7); W21(8); W21(9); W21(10); W21(11);
    asm volatile("" : "+v"(tv[0]), "+v"(tv[1]), "+v"(tv[2]), "+v"(tv[3]));
    auto step2 = [&](auto JB) __attribute__((always_inline)) {
      constexpr int jb = decltype(JB)::value;
      if constexpr (jb == 3) PAGE(4);
      if constexpr (jb == 2 && PRIO2 != 3) __builtin_amdgcn_s_setprio(PRIO2);
      if constexpr (jb == 9) PAGE(5);
      constexpr auto must = [](int q) { return (q == 2 ? 4 : 0) + ((FSM >> q) & 1) + dig(GVS, q); };
      constexpr auto late = [](int q) { return dn_cnt(q) + (VAN > 0 && q == VAP) + (VBN > 0 && q == VBP) + (VCN > 0 && q == VCP); };
      if constexpr (jb == 2) wait_vm<8 - N3A + 4 + must(1) + late(1)>();
      else wait_vm<(dig(LAGS, jb - 2) ? 0 : late(jb - 2) + (jb == 5 ? 1 : 0)) + 12 - dig(LAGS, jb - 1) + must(jb - 1) + late(jb - 1)>();
      if constexpr (jb == 3) { if (pw == 0 && (lane & ~8) == 0) ((unsigned*)(smem + LDS_SEL))[tk] = ksel; }
      if constexpr ((jb == 2 || jb == DG + 1)) {
        *(f32x4*)(lg + pw * 256 + lane * 4) = macc;
        macc = (f32x4){0.f, 0.f, 0.f, 0.f};
        lgkm0(sd);
      }
      BAR();
      float p0[4], p1[4];
      const int col = (pw & 1) * 64 + lane;
      if constexpr (jb == 3) {
        if (g == 0 && pw == 0 && lane == 0) __hip_atomic_store((unsigned*)flags + FL_EPOCH, epoch + 1u, __ATOMIC_RELAXED, __HIP_MEMORY_SCOPE_AGENT);
      }
      if constexpr (jb == DG + 1) {
        const float* p = lg + (pw & 1) * 32 + (lane >> 4) * 64 + ((2 * lane) & 7) * 4 + ((lane >> 2) & 3);
#pragma unroll
        for (int i = 0; i < 4; ++i) { p0[i] = p[i * 256]; p1[i] = p[i * 256 + 4]; }
      }
      if constexpr (jb == 2) {
        const float* p = lg + (col >> 5) * 64 + (col & 7) * 4 + ((col >> 3) & 3);
#pragma unroll
        for (int i = 0; i < 4; ++i) { p0[i] = p[i * 256]; p1[i] = p[i * 256 + 32]; }
      }
      if constexpr (jb == 4) {
        if (lane == 8 + tt) __builtin_amdgcn_raw_buffer_store_b32(tag, rf, abo, 0, 16);
      }
      if constexpr ((FSM >> (jb - 2)) & 1) {
        asm volatile("" : "+v"(fs));
        ready |= (unsigned)__ballot(fs == tag);
      }
      if constexpr (jb == GV_JB + 2) asm volatile("" : "+v"(gv[0]), "+v"(gv[1]), "+v"(gv[2]), "+v"(gv[3]));
      auto w2 = [&](auto GP) __attribute__((always_inline)) {
        constexpr int gp = decltype(GP)::value;
        if constexpr (jb == 2 && gp == 0) {
          p0[0] = (p0[0] + p0[1]) + (p0[2] + p0[3]); p1[0] = (p1[0] + p1[1]) + (p1[2] + p1[3]);
          PIN2(p0[0], p1[0]);
        }
        if constexpr (gp < 12) work2(JB, GP);
        if constexpr (gp < dig(SPS, jb) && !((SPE >> jb) & 1)) spf(IC<SPB + acc_dig(SPS, jb) + gp>{});
      };
      constexpr int l0 = dig(LAGS, jb - 1), np = l0 + 12 - dig(LAGS, jb);
      auto slot = [&](auto I) __attribute__((always_inline)) {
        constexpr int i = decltype(I)::value;
        if constexpr (i < l0) piece(IC<jb + 1>{}, IC<12 - l0 + i>{});
        else if constexpr (i < np) piece(IC<jb + 2>{}, IC<i - l0>{});
        if constexpr (i < 12 || i < np) w2(I);
      };
#define SL4(i) slot(IC<i>{}); slot(IC<i + 1>{}); slot(IC<i + 2>{}); slot(IC<i + 3>{});
      SB();
      SL4(0) SL4(4) SL4(8) SL4(12) SL4(16) SL4(20)
      SB();
      if constexpr (jb == 2) {
#pragma unroll
        for (int jj = 0; jj < 4; ++jj) __builtin_amdgcn_raw_buffer_store_b128(tv[jj], rm2, vm2 + jj * 4096 + (2 * tt + 1) * 16384, 0, 16);
      }
      if constexpr ((FSM >> jb) & 1) fs = bld4_sc1(rf, abq[jb < 5 ? 1 : 2]);
#pragma unroll
      for (int i = acc_dig(GVS, jb); i < acc_dig(GVS, jb + 1); ++i) gv[i] = bld16_sc1(rgu, vgu, i * F_ * 16);
      touches(JB);
#pragma unroll
      for (int i = dn_first(jb); i < dn_first(jb) + dn_cnt(jb); ++i) dv[i] = bld16_nt(rdn, vdn, i * D_ * 2);
      if constexpr (jb == DG + 1) {
        if (pw < 2)
          __builtin_amdgcn_raw_buffer_store_b128((u32x4){__float_as_uint((p0[0] + p0[1]) + (p0[2] + p0[3])), tag, __float_as_uint((p1[0] + p1[1]) + (p1[2] + p1[3])), tag},
                                                 mk_rsrc(wsp + WS_DN), (((pw * 8 + cg8) * 32 + rgb) * 64 + lane) * 16, 0, 16);
      }
      if constexpr (jb == 2) {
        if (moe && pw < 2)
          __builtin_amdgcn_raw_buffer_store_b128((u32x4){__float_as_uint(p0[0]), tag, __float_as_uint(p1[0]), tag},
                                                 rgu, ((mat * 4 + dq) * F_ + cg * 128 + col) * 16, 0, 16);
      }
      SB();
#define SPX(i) spf(IC<(((i >= np && i >= 12) || ((SPE >> jb) & 1)) && i < dig(SPS, jb) ? SPB + acc_dig(SPS, jb) + i : -1)>{})
      SPX(0); SPX(1); SPX(2); SPX(3); SPX(4); SPX(5); SPX(6); SPX(7); SPX(8); SPX(9); SPX(10); SPX(11); SPX(12); SPX(13); SPX(14);
      SB();
    };
    static_assert(DG + 1 <= 12 && DG >= GV_JB + 3 && dn_first(DG - 2) == 7 && acc_dig(GVS, GV_JB + 1) == 4 && dig(LAGS, 1) == 0 && dig(SPS, 1) == 0 && dig(LAGS, 0) == 0, "schedule");
    static_assert(ALIAS || sp_tile((SPB + acc_dig(SPS, 14)) >> 4) < 0, "all tiles are prefetched");
    step2(IC<2>{}); step2(IC<3>{}); step2(IC<4>{}); step2(IC<5>{}); step2(IC<6>{}); step2(IC<7>{}); step2(IC<8>{}); step2(IC<9>{}); step2(IC<10>{}); step2(IC<11>{}); step2(IC<12>{}); step2(IC<13>{});
    lgkm0(sd);
    wait_vm<12 - dig(LAGS, 13)>(); BAR();
    {
      constexpr int l0 = dig(LAGS, 13);
#define PL(i) if constexpr (i >= 12 - l0) piece(IC<15>{}, IC<i>{});
      PL(0) PL(1) PL(2) PL(3) PL(4) PL(5) PL(6) PL(7) PL(8) PL(9) PL(10) PL(11)
    }
    __builtin_amdgcn_s_setprio(0);
    epi_setup(0); epi_setup(1); epi_setup(2);
#pragma unroll
    for (int n = 0; n < 8; ++n) xq_load(n);
    WAIT_VM(8); BAR();
    epi_setup(3);
    wait_xq<0>(xq, sq);
    key[0] = __builtin_amdgcn_readlane(ksel, 0); key[1] = __builtin_amdgcn_readlane(ksel, 8);
    cv_calc();
#pragma unroll 1
    for (int c = 0; c < 2; ++c) {
      if (__builtin_expect(((c ? far1 : far0) & ~9) != 0, 1)) continue;
      const __amdgpu_buffer_rsrc_t rd = mk_rsrc(wsp + WS_DN + (size_t)(c * 8 * 32 + pw * 8) * 1024);
      const int ec = seg0(lane >> 5, (lane >> 4) & 1), dno = (ec >> 7) * 32768 + ((ec & 127) / 2 + (lane & 15)) * 16;
#pragma unroll 1
      for (int it = 0; it < SPIN_MAX; ++it) {
        if (__all(__builtin_amdgcn_raw_buffer_load_b32(rd, (ec >> 7) * 32768 + (lane & 7) * 1024 + 1020, 0, 16) == tag)) break;
        __builtin_amdgcn_s_sleep(8);
        asm volatile("" ::: "memory");
      }
      float s0, s1;
      unsigned bad = 1;
#pragma unroll 1
      for (int it = 0; !__all(bad == 0) && it < SPIN_MAX; ++it) {
        bad = 0; s0 = 0.f; s1 = 0.f;
#pragma unroll
        for (int i = 0; i < 8; ++i) {
          const u32x4 q = __builtin_amdgcn_raw_buffer_load_b128(rd, dno, i * 1024, 16);
          bad |= (q.y ^ tag) | (q.w ^ tag); s0 += __uint_as_float(q.x); s1 += __uint_as_float(q.z);
        }
        asm volatile("" ::: "memory");
      }
      *(f32x2*)(smem + LDS_FIN + (c * 4 + pw) * 512 + lane * 8) = (f32x2){s0, s1};
      asm volatile("s_waitcnt lgkmcnt(0)" ::: "memory");
      if (lane == 0) asm volatile("ds_add_u32 %0, %1" :: "v"(lds_addr(smem + LDS_SEL + 24 + c * 4)), "v"(1) : "memory");
    }
  } else {
  {
    const int q = j * 4 + wid;
    constexpr int Q1 = WS_MB, Q2 = Q1 + 8, Q3 = Q2 + 8, Q4 = Q3 + 7, Q5 = Q4 + 7, Q6 = Q5 + 7, Q7 = Q6 + 4, Q8 = Q7 + 4, QC = Q8 + 1;
    static_assert(QC <= 128, "warm-up slots");
    unsigned dmy = 0;
    if (q < QC) {
      const char* base; int b0;
      if (q < Q1) { base = wsp; b0 = 0; }
      else if (q < Q2) { base = (const char*)ws_re; b0 = Q1; }
      else if (q < Q3) { base = (const char*)ws_im; b0 = Q2; }
      else if (q < Q4) { base = (const char*)w_gate; b0 = Q3; }
      else if (q < Q5) { base = (const char*)w_up; b0 = Q4; }
      else if (q < Q6) { base = (const char*)w_down; b0 = Q5; }
      else if (q < Q7) { base = (const char*)out; b0 = Q6; }
      else if (q < Q8) { base = (const char*)x; b0 = Q7; }
      else { base = (const char*)w_r; b0 = Q8; }
      base += (size_t)(q - b0) << 20;
      asm volatile("s_load_dword %0, %1, 0" : "+s"(dmy) : "s"(base) : "memory");
    }
    if (wid == 0) { CALL_PG(1); CALL_PG(8); CALL_PG(5); }
    else if (wid == 1) { CALL_PG(2); CALL_PG(9); CALL_PG(6); }
    else if (wid == 2) { CALL_PG(3); CALL_PG(10); }
    else { CALL_PG(4); CALL_PG(11); }
    asm volatile("s_barrier\n\ts_waitcnt lgkmcnt(0)" : "+s"(dmy) :: "memory");
  }
  PAGE(8);
  epi_setup(0); epi_setup(1); epi_setup(2); epi_setup(3);
  const int wr = wid >> 1, wc = wid & 1;
  const int sw = swz(fr);
  const int ko0 = ((0 + fq) ^ sw) * 16, ko1 = ((4 + fq) ^ sw) * 16;
  bf16x8 fm[2][4], fn[2][4];
  {
    const char* pm = smem + (wr * 64 + fr) * 128;
    const char* pn = smem + 16384 + (wc * 64 + fr) * 128;
    f32x4 acc[4][4];
#pragma unroll
    for (int i = 0; i < 4; ++i)
#pragma unroll
      for (int jn = 0; jn < 4; ++jn)
#pragma unroll
        for (int q = 0; q < 4; ++q) acc[i][jn][q] = 0.f;
#define TO(i) ((i) * 2048)
#define LDSP(T, a) ((__attribute__((address_space(3))) T*)(size_t)(a))
#define SLOTS(n) _Pragma("unroll") for (int q = 0; q < (n); ++q)
#define MFA(c, m, n) asm volatile("v_mfma_f32_16x16x32_bf16 %0, %2, %1, %0" : "+v"(c) : "v"(m), "v"(n))
#define MFA2(c, m, n, m2, n2) asm volatile("v_mfma_f32_16x16x32_bf16 %0, %2, %1, %0\n\tv_mfma_f32_16x16x32_bf16 %0, %4, %3, %0" : "+v"(c) : "v"(m), "v"(n), "v"(m2), "v"(n2))
    unsigned lm[2][2], ln[2][2];
#pragma unroll
    for (int u = 0; u < 2; ++u)
#pragma unroll
      for (int kk = 0; kk < 2; ++kk) {
        lm[u][kk] = lds_addr(pm) + u * 2 * G1_STAGE + (kk ? ko1 : ko0);
        ln[u][kk] = lds_addr(pn) + u * 2 * G1_STAGE + (kk ? ko1 : ko0);
        asm volatile("" : "+v"(lm[u][kk]), "+v"(ln[u][kk]));
      }
    auto ldm = [&](int stg, int i, int kk) __attribute__((always_inline)) { fm[kk][i] = *LDSP(const bf16x8, lm[stg >> 1][kk] + (stg & 1) * G1_STAGE + TO(i)); };
    auto ldn = [&](int stg, int jn, int kk) __attribute__((always_inline)) { fn[kk][jn] = *LDSP(const bf16x8, ln[stg >> 1][kk] + (stg & 1) * G1_STAGE + TO(jn)); };
    auto quad = [&](int a, int c, int d, int s) __attribute__((always_inline)) {
      const int i = (s < 2 || s >= 6) ? a : a + 1, jn = s < 4 ? c : d, kk = (s == 1 || s == 2 || s == 5 || s == 6);
      MFA(acc[i][jn], fm[kk][i], fn[kk][jn]);
    };
    auto quad2 = [&](int a, int c, int d, int s) __attribute__((always_inline)) {
      const int i = (s < 2 || s >= 6) ? a : a + 1, jn = s < 4 ? c : d, kk = (s == 2 || s == 6);
      MFA2(acc[i][jn], fm[kk][i], fn[kk][jn], fm[1 - kk][i], fn[1 - kk][jn]);
    };
    auto half1 = [&](int stg) __attribute__((always_inline)) {
      const int ja = (stg & 1) * 2, jb = 2 - ja;
      SLOTS(8) {
        quad(0, ja, ja + 1, q);
        if (q < 4) ldn(stg, jb + (q >> 1), q & 1); else ldm(stg, 2 + ((q - 4) >> 1), q == 5 || q == 6);
        SB();
      }
      SLOTS(4) { quad2(0, jb, jb + 1, 2 * q); SB(); }
    };
    auto half2 = [&](int stg) __attribute__((always_inline)) {
      const int ja = (stg & 1) * 2, jb = 2 - ja, nx = (stg + 1) & 3;
      SLOTS(8) {
        quad(2, jb + 1, jb, q);
        if (!(q & 1)) ldm(nx, q >> 2, q == 2 || q == 4);
        SB();
      }
      SLOTS(8) {
        quad(2, ja, ja + 1, q);
        if (!(q & 1)) ldn(nx, jb + (q >> 2), (q >> 1) & 1);
        SB();
      }
    };
    unsigned ae[7];
    {
      int l_ = lane;
      asm volatile("" : "+v"(l_));
      const int p = l_ >> 4, dn = sym_dn(p), hf = (p >> 1) ^ (dn & z);
#pragma unroll
      for (int u = 0; u < 7; ++u) {
        const int T = u < 3 ? wc * 4 : (wc * 4 + u - 3 - dn) & 7, i = u < 3 ? (u + 1) ^ ((u & 1) ? 0 : dn * 2) : 0;
        const int f = dn * 8 + ((T & 2) << 1) + ((T & 4) >> 1) + (T & 1);
        ae[u] = lds_addr(smem + G2_MRING) + (hf * 64 + i * 16 + f) * 128 + (((wr * 4 + ((l_ >> 2) & 3)) ^ swz(f)) * 16) + (l_ & 3) * 4;
        asm volatile("" : "+v"(ae[u]));
      }
    }
    SLOTS(16) asm volatile("" : "+v"(acc[q >> 2][q & 3]));
    SB();
    SLOTS(4) { ldm(0, q >> 1, q & 1); ldn(0, q >> 1, q & 1); }
    SB();
#pragma unroll 1
    for (int k4 = 0; k4 < G1_NKT - 4; k4 += 4) {
      half1(0); BAR_LGKM();
      SB(); half2(0);
      half1(1); BAR_LGKM();
      SB(); half2(1);
      half1(2); BAR_LGKM();
      SB(); half2(2);
      half1(3); BAR_LGKM();
      SB(); half2(3);
    }
    bf16x8 gm[2][4], gn[2][8];
    unsigned am[2][2], an[2][2];
#pragma unroll
    for (int u = 0; u < 2; ++u)
#pragma unroll
      for (int kk = 0; kk < 2; ++kk) {
        am[u][kk] = lds_addr(pm) + (u ? 3 : 1) * G1_STAGE + (kk ? ko1 : ko0);
        an[u][kk] = lds_addr(pn) + (u ? 3 : 1) * G1_STAGE + (kk ? ko1 : ko0);
        asm volatile("" : "+v"(am[u][kk]), "+v"(an[u][kk]));
      }
    unsigned al = lds_addr(smem + LDS_MB) + (wr * 32 + fr * 2) * 4, ac = lds_addr(smem + LDS_MB) + (256 + wc * 64 + fq * 4) * 4;
    asm volatile("" : "+v"(al), "+v"(ac));
    auto rdg = [&](int set, int t, int h, int r) __attribute__((always_inline)) {
      const int u = t == 15, o = t == 14 ? G1_STAGE : 0;
      if (r == 0 || r == 2 || r == 4 || r == 5) {
        const int i = r >= 4, kk = r == 2 || r == 4;
        gm[set][kk * 2 + i] = *LDSP(const bf16x8, am[u][kk] + o + h * 4096 + i * 2048);
      } else {
        const int jn = r < 4 ? 0 : (r - 4) >> 1, kk = r < 4 ? r >> 1 : r & 1;
        gn[set][kk * 4 + jn] = *LDSP(const bf16x8, an[u][kk] + o + TO(jn));
      }
    };
    auto mmg = [&](int set, int h, int q) __attribute__((always_inline)) {
      const int jn = q >> 2, r = q & 3, i = (r >> 1) ^ (jn & 1), kk = r == 1 || r == 2;
      MFA(acc[h * 2 + i][jn], gm[set][kk * 2 + i], gn[set][kk * 4 + jn]);
    };
    f32x2 rsv, mrv;
    f32x4 cev[4];
    float tc[32];
    auto rds = [&](int h, int r) __attribute__((always_inline)) {
      if (r == 0) mrv = *LDSP(const f32x2, al + h * 256); else if (r == 1) rsv = *LDSP(const f32x2, al + 512 + h * 256);
    };
    auto prep = [&](int n) __attribute__((always_inline)) {
      tc[n] = llvm_fmul_legacy(mrv[n & 1], cev[n >> 3][(n >> 1) & 3]);
      asm volatile("" : "+v"(tc[n]));
    };
    auto epi = [&](int h, int q) __attribute__((always_inline)) {
      const int jn = q >> 1, r = (q & 1) * 2, o = m_off(h) + (jn & 1) * 128 + (jn >> 1) * 512;
      asm volatile("v_fmac_f32 %0, %4, %8\n\tv_fmac_f32 %1, %5, %9\n\tv_fmac_f32 %2, %6, %8\n\tv_fmac_f32 %3, %7, %9"
                   : "+v"(tc[q * 4]), "+v"(tc[q * 4 + 1]), "+v"(tc[q * 4 + 2]), "+v"(tc[q * 4 + 3])
                   : "v"(acc[h * 2][jn][r]), "v"(acc[h * 2 + 1][jn][r]), "v"(acc[h * 2][jn][r + 1]), "v"(acc[h * 2 + 1][jn][r + 1]),
                     "v"(rsv[0]), "v"(rsv[1]));
      *LDSP(unsigned, r ? ae[1] + o : ae[3 + jn] + m_off(h)) = pk2(tc[q * 4], tc[q * 4 + 1]);
      *LDSP(unsigned, ae[r] + o) = pk2(tc[q * 4 + 2], tc[q * 4 + 3]);
    };
    SB();
    half1(0);
    BAR_LGKM();
    SB();
    SLOTS(16) {
      if (q < 8) quad(2, 3, 2, q); else quad(2, 0, 1, q - 8);
      if (q < 12) rdg(0, 13, 0, q);
      SB();
    }
    BAR();
    SB();
    SLOTS(16) {
      mmg(0, 0, q);
      if (q < 12) rdg(1, 14, 0, q);
      else { rds(0, q - 12); cev[q - 12] = *LDSP(const f32x4, ac + (q - 12) * 64); }
      SB();
    }
    PAGE(9);
    BAR();
    SB();
    SLOTS(16) { mmg(1, 0, q); if ((q & 3) < 3) rdg(0, 15, 0, (q >> 2) * 3 + (q & 3)); if (q >= 8) { prep(2 * q - 16); prep(2 * q - 15); } SB(); }
    SLOTS(16) { mmg(0, 0, q); if ((q & 3) < 3) rdg(1, 13, 1, (q >> 2) * 3 + (q & 3)); prep(16 + q); SB(); if (q >= 9 && (q & 1)) { epi(0, (q - 9) >> 1); SB(); } }
    SLOTS(8) { mmg(1, 1, q); if (q < 6) { rdg(0, 14, 1, 2 * q); rdg(0, 14, 1, 2 * q + 1); } SB(); if (q & 1) { epi(0, 4 + (q >> 1)); SB(); } }
    BAR_LGKM();
    SB();
    SLOTS(8) { mmg(1, 1, 8 + q); if (q < 4) rdg(1, 15, 1, q == 0 ? 1 : q == 1 ? 3 : q + 4); else rds(1, q - 4); SB(); }
    SLOTS(16) { mmg(0, 1, q); if (q < 8) rdg(1, 15, 1, q == 0 ? 0 : q == 1 ? 2 : q < 4 ? q + 2 : q + 4); prep(q); SB(); }
    asm volatile("s_waitcnt lgkmcnt(0)" ::: "memory");
    if (lane == 0) asm volatile("ds_add_u32 %0, %1" :: "v"(lds_addr(smem + LDS_SEL + 12)), "v"(1) : "memory");
    SB();
    SLOTS(16) {
      mmg(1, 1, q);
      if (q < 4) fn[0][q] = *(const bf16x8*)(smem + (wr * 128 + fr) * 128 + (q >> 1) * 2048 + ((q & 1) ? ko1 : ko0));
      else if (q < 8) fm[q & 1][(q - 4) >> 1] = *(const bf16x8*)(smem + G2_MRING + m_off(0) + (wc * 64 + fr) * 128 + TO((q - 4) >> 1) + ((q & 1) ? ko1 : ko0));
      prep(16 + q);
      SB();
    }
    {
      lds_vi rel = (lds_vi)(smem + LDS_SEL + 12);
      int it = 0;
      while (rel[0] != 4 && it < SPIN_MAX * 8) ++it;
      asm volatile("" ::: "memory");
    }
    asm volatile("s_nop 7");
    SB();
    SLOTS(8) { epi(1, q); SB(); }
  }

  const char* pn = smem + (wr * 128 + fr) * 128;
  const char* pm = smem + G2_MRING + (wc * 64 + fr) * 128;
  f32x4 acc[8][4];
  auto mm = [&](bool imaj, int set, int q2, int s, int mode) __attribute__((always_inline)) {
    int i, jj, kk;
    if (imaj) { i = s >> 2; const int r = s & 3; jj = (r >> 1) ^ (i & 1); kk = (r == 1 || r == 2); }
    else { jj = s >> 3; const int r = s & 7; i = jj ? 3 - (r >> 1) : (r >> 1); kk = ((r + 1) >> 1) & 1; }
    if (mode == 1 && !(s & 1)) asm volatile("v_mfma_f32_16x16x32_bf16 %0, %2, %1, 0" : "=&v"(acc[q2 * 2 + jj][i]) : "v"(fm[kk][i]), "v"(fn[set][jj * 2 + kk]));
    else if (mode) asm volatile("v_mfma_f32_16x16x32_bf16 %0, %2, %1, %0" : "+v"(acc[q2 * 2 + jj][i]) : "v"(fm[kk][i]), "v"(fn[set][jj * 2 + kk]));
    else acc[q2 * 2 + jj][i] = __builtin_amdgcn_mfma_f32_16x16x32_bf16(fn[set][jj * 2 + kk], fm[kk][i], acc[q2 * 2 + jj][i], 0, 0, 0);
  };
  unsigned bn[2], bm[2], an[2], am[2];
#pragma unroll
  for (int kk = 0; kk < 2; ++kk) {
    bn[kk] = lds_addr(pn) + (kk ? ko1 : ko0); bm[kk] = lds_addr(pm) + (kk ? ko1 : ko0);
    asm volatile("" : "+v"(bn[kk]), "+v"(bm[kk]));
    an[kk] = bn[kk]; am[kk] = bm[kk] + m_off(0);
  }
#define RDN(jt, kk, o) (*LDSP(const bf16x8, an[kk] + (o) + (jt) * 2048))
#define RDM(i, kk, o) (*LDSP(const bf16x8, am[kk] + (o) + TO(i)))
  PAGE(10);
  tag = __builtin_amdgcn_readfirstlane(((const unsigned*)(smem + LDS_SEL))[5]);
  {
    int son = 0;
    auto tile = [&](int kt, int zero) __attribute__((always_inline)) {
      SLOTS(16) {
        mm(true, 0, 0, q, zero);
        if (!(q & 1)) {
          if (q < 8) fm[(q >> 1) & 1][2 + (q >> 2)] = RDM(2 + (q >> 2), (q >> 1) & 1, 0);
          else { const int r = (q - 8) >> 1; fn[1][r] = RDN(2 + (r >> 1), r & 1, 0); }
        }
        SB();
      }
      SLOTS(16) {
        mm(false, 1, 1, q, zero);
        if (!(q & 1) && q < 8) fn[0][q >> 1] = RDN(4 + (q >> 2), (q >> 1) & 1, 0);
        SB();
      }
      const int som = son >> 1;
      son = son == 65536 ? 0 : son + 32768;
      SLOTS(16) {
        mm(false, 0, 2, q, zero);
        if (!(q & 1) && q < 8) fn[1][q >> 1] = RDN(6 + (q >> 2), (q >> 1) & 1, 0);
        if (q == 9 || q == 11) { an[q == 11] = bn[q == 11] + son; asm volatile("" : "+v"(an[q == 11])); }
        if (q == 13 || q == 15) { am[q == 15] = bm[q == 15] + som; asm volatile("" : "+v"(am[q == 15])); }
        SB();
      }
      BAR_LGKM();
      SB();
      SLOTS(16) {
        mm(true, 1, 3, q, zero);
        if (!(q & 1)) {
          if (q < 8) fn[0][q >> 1] = RDN(q >> 2, (q >> 1) & 1, 0);
          else fm[(q >> 1) & 1][(q - 8) >> 2] = RDM((q - 8) >> 2, (q >> 1) & 1, 0);
        }
        SB();
      }
    };
    SB();
    tile(0, 1);
#pragma unroll 1
    for (int kt = 1; kt < G2_NKT - 3; ++kt) tile(kt, 0);
  }
  unsigned fx;
  int fo = lane == 0 ? kq * 512 : (int)0x80000000;
  {
    constexpr int NG = 12;
    static constexpr int g_t[NG] = {0, 0, 1, 1, 2, 2, 2, 1, 0, 0, 1, 2};
    static constexpr int g_q[NG] = {0, 1, 0, 1, 0, 1, 2, 2, 2, 3, 3, 3};
    constexpr int G14 = 1, G15 = 3, GP0 = 5, GP1 = 6;
    constexpr auto chp = [](int g) { return g == 0 || g_t[g] != g_t[g - 1]; };
    constexpr auto chn = [](int g) { return g + 1 < NG && g_t[g + 1] != g_t[g]; };
    static_assert(n_off(13) == 32768 && n_off(14) == 65536 && n_off(15) == 0, "");
    unsigned an2[2];
#pragma unroll
    for (int kk = 0; kk < 2; ++kk) {
      an[kk] = bn[kk]; an2[kk] = bn[kk] + 65536; am[kk] = bm[kk];
      asm volatile("" : "+v"(an[kk]), "+v"(an2[kk]), "+v"(am[kk]));
    }
    int vo;
    u32x4 pk;
    u32x2 kv;
    auto publish = [&](int d, int h) __attribute__((always_inline)) {
      if (h == 0) { vo = ml; PINV(vo); vo += (((d + kq + 1) & 3) * 4 + kq) * 4 * MSG; PINV(vo); }
      const f32x4 v = acc[2 * d + (h >> 2)][h & 3];
      pk[(h & 1) * 2] = pk2(v[0], v[1]); pk[(h & 1) * 2 + 1] = pk2(v[2], v[3]);
      if (h & 1) __builtin_amdgcn_raw_buffer_store_b128(pk, rs, vo + (h >> 1) * 1024, 0, 16);
    };
#define MSG_FLAG(d) __builtin_amdgcn_raw_buffer_store_b32(tag, rc, fo + (((d) + kq + 1) & 3) * 2048, 0, 16)
    auto rdn = [&](int g, int r) __attribute__((always_inline)) {
      const int t = g_t[g], jt = g_q[g] * 2 + (r >> 1);
      fn[g & 1][r] = *LDSP(const bf16x8, (t == 1 ? an2[r & 1] : an[r & 1]) + (t == 0 ? 32768 : 0) + jt * 2048);
    };
    auto rdm = [&](int t, int i, int kk) __attribute__((always_inline)) { fm[kk][i] = *LDSP(const bf16x8, am[kk] + m_off(13 + t) + TO(i)); };
    auto group = [&](auto GI) __attribute__((always_inline)) {
      constexpr int gi = decltype(GI)::value;
      if (gi == G14) { BAR();
        SB(); }
      if (gi == G15) {
        PAGE(11);
        BAR();
        SB();
        kv = *(const u32x2*)(smem + LDS_SEL);
        SB();
      }
#pragma unroll
      for (int q = 0; q < 16; ++q) {
        const int n = gi * 16 + q;
        if (n == 112) {
        }
        if (n == 152) {
        }
        if (n == FL0) { WAIT_VM(8); MSG_FLAG(0); SB(); }
        if (n == FL1) { WAIT_VM(5); MSG_FLAG(1); SB(); }
        if (n == FL2) { WAIT_VM(2); MSG_FLAG(2); fx = bld4_sc1(rc, po); SB(); }
        mm(chp(gi) || chn(gi), gi & 1, g_q[gi], q, 2);
        constexpr bool ni = gi + 1 < NG && (chp(gi + 1 < NG ? gi + 1 : gi) || chn(gi + 1 < NG ? gi + 1 : gi));
        auto nr = [](int r) { return ni && r >= 2 ? 5 - r : r; };
        if constexpr (chp(gi) && chn(gi)) {
          if (q < 4) rdm(g_t[gi], 2 + (q >> 1), q & 1); else if (q < 8) rdn(gi + 1, nr(q - 4)); else if (q < 12) rdm(g_t[gi + 1], (q - 8) >> 1, q & 1);
        } else if constexpr (chp(gi)) {
          if (q < 4) rdm(g_t[gi], 2 + (q >> 1), q & 1);
          else if (gi + 1 < NG && q < 12 && !(q & 1)) rdn(gi + 1, nr((q - 4) >> 1));
        } else if constexpr (chn(gi)) {
          if (!(q & 1)) { if (q < 8) rdn(gi + 1, nr(q >> 1)); else rdm(g_t[gi + 1], (q - 8) >> 2, (q >> 1) & 1); }
        } else {
          if (!(q & 1) && q < 8) rdn(gi + 1, nr(q >> 1));
        }
        if (gi == GP0 && q < 8) publish(0, q);
        if (gi == GP1 && q < 8) publish(1, q);
        if (gi == 9 && q < 8) publish(2, q);
        if (gi == G15 && (q & 1)) xq_load(q >> 1);
        if (n == 72) { key[0] = __builtin_amdgcn_readfirstlane(kv.x); key[1] = __builtin_amdgcn_readfirstlane(kv.y); }
        SB();
      }
    };
#define GR4(i) group(IC<i>{}); group(IC<i + 1>{}); group(IC<i + 2>{}); group(IC<i + 3>{});
    GR4(0) GR4(4) GR4(8)
    asm volatile("s_nop 7");
    SB();
  }
#pragma unroll
  for (int i = 0; i < 4; ++i) *(f32x4*)(smem + LDS_ACC + wr * 16384 + wc * 4096 + i * 1024 + lane * 16) = acc[7][i];
#pragma unroll
  for (int i = 0; i < 4; ++i) ya[i] = acc[6][i];
  asm volatile("s_waitcnt lgkmcnt(0)" ::: "memory");
  if (lane == 0) sig[0] = 2;
  SB();
  if (FL0 >= 192) { WAIT_VM(8); MSG_FLAG(0); SB(); }
  if (FL1 >= 192) { WAIT_VM(5); MSG_FLAG(1); SB(); }
  if (FL2 >= 192) { WAIT_VM(2); MSG_FLAG(2); fx = bld4_sc1(rc, po); SB(); }
  wait_xq<4>(xq, sq);
  cv_calc();
  asm volatile("s_waitcnt vmcnt(0)" : "+v"(fx) :: "memory");
  seen = (unsigned)__ballot(fx == tag);
  }

  PAGE(6);
  if (__builtin_expect(far0 == 0 || far1 == 0, 0)) {
#pragma unroll 1
    for (int c = 0; c < 2; ++c) {
      if (c ? far1 : far0) continue;
      const int ic = (0xffff - (c ? key[1] : key[0])) & (S_ - 1);
      const float sg = rbf(1.f / (1.f + __expf(-key_score(c ? key[1] : key[0]))));
      lds_vi cnt = (lds_vi)(smem + LDS_SEL + 24 + c * 4);
      int it = 0;
      while (cnt[0] != 4 && it < SPIN_MAX * 8) { __builtin_amdgcn_s_sleep(1); ++it; }
      asm volatile("" ::: "memory");
      const float* q = (const float*)(smem + LDS_FIN + c * 2048) + (wid & 1) * 64 + (fr >> 3) * 32 + col4(fr);
      const f32x4 v = (*(const f32x4*)q + *(const f32x4*)(q + 128)) + (*(const f32x4*)(q + 256) + *(const f32x4*)(q + 384));
#pragma unroll
      for (int r = 0; r < 4; ++r)
#pragma unroll
        for (int k = 0; k < 4; ++k) {
          cv[r * 4 + k] += (xo[r] >> 11) == ic ? v[k] * sg : 0.f;
        }
    }
  }
  if (ha) {
    int it = 0;
    while (sig[0] != 2 && it < SPIN_MAX * 8) { __builtin_amdgcn_s_sleep(1); ++it; }
    asm volatile("" ::: "memory");
#pragma unroll
    for (int i = 0; i < 4; ++i) ya[i] = *(const f32x4*)(smem + LDS_ACC + (wid & 2) * 8192 + (wid & 1) * 4096 + i * 1024 + lane * 16);
  }
  u32x4 pp[3][2];
  unsigned e_lo = 0x00003f80u, e_hi = 0x3f800000u;
  asm volatile("" : "+v"(e_lo), "+v"(e_hi));
  __amdgpu_buffer_rsrc_t rs_ = rs, rc_ = rc;
  int mo[3] = {MSG_IDX(0) * 4 * MSG, MSG_IDX(1) * 4 * MSG, MSG_IDX(2) * 4 * MSG}, mv = ml + ha * 2048, mv2 = mv + 1024;
  asm volatile("" : "+s"(rs_), "+s"(rc_), "+s"(mo[0]), "+s"(mo[1]), "+s"(mo[2]));
  asm volatile("" : "+v"(mv), "+v"(mv2));
#define MSG_POLL(m) do { \
    if (!((seen >> (m)) & 1)) { \
      unsigned pv_, it_ = SPIN_MAX; \
      asm volatile("1:\n\tbuffer_load_dword %0, %3, %4, 0 offen sc1\n\ts_waitcnt vmcnt(0)\n\tv_cmp_eq_u32 vcc, %5, %0\n\ts_mov_b32 %1, vcc_lo\n\t" \
                   "s_bitcmp1_b32 %1, %6\n\ts_cbranch_scc1 2f\n\ts_sub_u32 %2, %2, 1\n\ts_cbranch_scc1 2f\n\ts_sleep 1\n\ts_branch 1b\n2:" \
                   : "=&v"(pv_), "+s"(seen), "+s"(it_) : "v"(po), "s"(rc_), "s"(tag), "i"(m) : "vcc", "scc", "memory"); \
    } \
    pp[m][0] = __builtin_amdgcn_raw_buffer_load_b128(rs_, mv, mo[m], 16); \
    pp[m][1] = __builtin_amdgcn_raw_buffer_load_b128(rs_, mv2, mo[m], 16); } while (0)
#define MSG_ADD(m) do { \
    _Pragma("unroll") for (int u = 0; u < 8; ++u) { \
      ya[u >> 1][(u & 1) * 2] = add_bf(pp[m][u >> 2][u & 3], e_lo, ya[u >> 1][(u & 1) * 2]); ya[u >> 1][(u & 1) * 2 + 1] = add_bf(pp[m][u >> 2][u & 3], e_hi, ya[u >> 1][(u & 1) * 2 + 1]); } } while (0)
  MSG_POLL(2); MSG_POLL(1); MSG_POLL(0);
  SB();
#pragma unroll
  for (int e = 0; e < 16; ++e) ya[e & 3][e >> 2] += cv[e];
  asm volatile("" : "+v"(ya[0]), "+v"(ya[1]), "+v"(ya[2]), "+v"(ya[3]));
  SB();
  MSG_ADD(2); MSG_ADD(1);
  asm volatile("" : "+v"(ya[0]), "+v"(ya[1]), "+v"(ya[2]), "+v"(ya[3]));
  SB();
  MSG_ADD(0);
  {
#pragma unroll
    for (int r = 0; r < 4; ++r) *(u32x2*)((char*)out + (unsigned)xo[r]) = (u32x2){pk2(ya[0][r], ya[1][r]), pk2(ya[2][r], ya[3][r])};
  }
}

extern "C" void kernel_launch(void* const* d_in, const int* in_sizes, int n_in, void* d_out, int out_size,
                              void* d_ws, size_t ws_size, hipStream_t stream) {
  const u16* x = (const u16*)d_in[0];
  const u16* ws_re = (const u16*)d_in[1];
  const u16* ws_im = (const u16*)d_in[2];
  const u16* wd_re = (const u16*)d_in[3];
  const u16* wd_im = (const u16*)d_in[4];
  const u16* w_r = (const u16*)d_in[6];
  const u16* w_gate = (const u16*)d_in[7];
  const u16* w_up = (const u16*)d_in[8];
  const u16* w_down = (const u16*)d_in[9];
  k_main<<<256, 512, 0, stream>>>(x, wd_re, wd_im, w_r, (char*)d_ws, ws_re, ws_im, w_gate, w_up, w_down, (u16*)d_out);
}
```

```cpp
#include <hip/hip_runtime.h>
#include <hip/hip_bf16.h>
#include <stdint.h>
#include <type_traits>

extern "C" __device__ unsigned long long llvm_dispatch_id(void) __asm("llvm.amdgcn.dispatch.id");
extern "C" __device__ float llvm_fmul_legacy(float, float) __asm("llvm.amdgcn.fmul.legacy");
typedef unsigned short u16;
typedef unsigned long long u64;
typedef __attribute__((ext_vector_type(8))) short bf16x8;
typedef __attribute__((ext_vector_type(4))) unsigned u32x4;
typedef __attribute__((ext_vector_type(2))) unsigned u32x2;
typedef __attribute__((ext_vector_type(4))) float f32x4;
typedef __attribute__((ext_vector_type(2))) float f32x2;
typedef __attribute__((ext_vector_type(2))) __bf16 bf16x2;
typedef __attribute__((address_space(3))) void* lds_vp;
typedef volatile __attribute__((address_space(3))) int* lds_vi;

constexpr int S_ = 2048, D_ = 1024, F_ = 3584;
template <int N> using IC = std::integral_constant<int, N>;

static __device__ __forceinline__ float bf2f(u16 u) { return __uint_as_float((unsigned)u << 16); }
static __device__ __forceinline__ u16 f2bf(float f) {
  __hip_bfloat16 b = __float2bfloat16(f);
  return *reinterpret_cast<u16*>(&b);
}
static __device__ __forceinline__ float rbf(float f) { return bf2f(f2bf(f)); }
static __device__ __forceinline__ float lo_bf(unsigned u) { return __uint_as_float(u << 16); }
static __device__ __forceinline__ float hi_bf(unsigned u) { return __uint_as_float(u & 0xffff0000u); }
static __device__ __forceinline__ unsigned pk2(float a, float b) {
  const bf16x2 v = __builtin_convertvector((f32x2){a, b}, bf16x2);
  return __builtin_bit_cast(unsigned, v);
}
static __device__ __forceinline__ float add_bf(unsigned u, unsigned e, float c) {
  return __builtin_amdgcn_fdot2_f32_bf16(__builtin_bit_cast(bf16x2, u), __builtin_bit_cast(bf16x2, e), c, false);
}
static __device__ __forceinline__ float ln_elem(float x, float mb, float sd, float ri) {
  const float d = rbf(x - mb);
  const float q0 = d * ri;
  return rbf(__builtin_fmaf(__builtin_fmaf(-q0, sd, d), ri, q0));
}
template <int CTRL> static __device__ __forceinline__ float dpp_f(float v) {
  return __int_as_float(__builtin_amdgcn_update_dpp(0, __float_as_int(v), CTRL, 0xf, 0xf, true));
}
template <int CTRL> static __device__ __forceinline__ unsigned dpp_u(unsigned v) {
  return (unsigned)__builtin_amdgcn_update_dpp(0, (int)v, CTRL, 0xf, 0xf, true);
}
static __device__ __forceinline__ float row_sum(float v) {
  v += dpp_f<0xB1>(v);
  v += dpp_f<0x4E>(v);
  v += dpp_f<0x141>(v);
  v += dpp_f<0x140>(v);
  return v;
}
static __device__ __forceinline__ float fold32(float a, float b) {
  const auto r = __builtin_amdgcn_permlane32_swap(__float_as_uint(a), __float_as_uint(b), false, false);
  return __uint_as_float(r[0]) + __uint_as_float(r[1]);
}
static __device__ __forceinline__ float fold16(float a, float b) {
  const auto r = __builtin_amdgcn_permlane16_swap(__float_as_uint(a), __float_as_uint(b), false, false);
  return __uint_as_float(r[0]) + __uint_as_float(r[1]);
}
static __device__ __forceinline__ float rdl(float v, int l) { return __int_as_float(__builtin_amdgcn_readlane(__float_as_int(v), l)); }

#define XMAP(g) (((~g & 1) << 2) | (g & 2) | ((g >> 2) & 1))
#define ST_JB 9
#define WSN 10
#define FSM 0xa2
#define GV_JB 7
#define DG 11
#define AB_STRIDE 32
#define SPIN_MAX (1 << 17)
#define FL0 168
#define FL1 192
#define FL2 192
#define VA0 5
#define VAN 4
#define VAP 116
#define VB0 12
#define VBN 0
#define VBP 6
#define SP0 3
#define SP1 7
#define SP1E 11
#define VC0 14
#define VCN 0
#define VCP 9
#define SPS 0x000cccccdddd00
#define SPE 0
#define PRIO2 3
#define N2A 8
#define N3A 8
#define WSL 2
#define LNP 0
#define LNS 2
#define EPP 0
#define DGS 0
#define DGP 0
#define LAGS 0x00000000000000
#define DVS 0x00000400300000
#define GVS 0x00000022000000
constexpr int dig(unsigned long long t, int q) { return q < 0 ? 0 : (int)((t >> (4 * q)) & 15); }
constexpr int acc_dig(unsigned long long t, int q) { int a = 0; for (int i = 0; i < q; ++i) a += dig(t, i); return a; }

static __device__ __forceinline__ unsigned lds_addr(const void* p) { return (unsigned)(size_t)(lds_vp)p; }

constexpr int MSG = 4096;
constexpr size_t WS_ABT   = 0;
constexpr size_t WS_SLAB  = WS_ABT + (size_t)D_ * 4096 * 2;
constexpr size_t WS_GU    = WS_SLAB + (size_t)64 * 64 * MSG;
constexpr size_t WS_DN    = WS_GU + (size_t)8 * F_ * 16;
constexpr size_t WS_FLAGS = WS_DN + (size_t)2 * 8 * 32 * 64 * 16;
constexpr int FL_X = 0;
constexpr int FL_AB = 64 * 64 * 32;
constexpr int FL_ST = FL_AB + 2048 * AB_STRIDE;
constexpr int FL_CE = FL_ST + 4096;
constexpr int FL_KEY = FL_CE + 4096;
constexpr int FL_EPOCH = FL_KEY + 4096;
constexpr int WS_MB = (int)((WS_FLAGS + (FL_EPOCH + 1) * 4 + 1048575) >> 20);

#define WAIT_VM(n) asm volatile("s_waitcnt vmcnt(" #n ")" ::: "memory")
template <int N> static __device__ __forceinline__ void wait_vm() { asm volatile("s_waitcnt vmcnt(%0)" :: "i"(N) : "memory"); }
#define BAR_LGKM() asm volatile("s_waitcnt lgkmcnt(0)\n\ts_barrier" ::: "memory")
#define BAR() asm volatile("s_barrier" ::: "memory")
#define SB() __builtin_amdgcn_sched_barrier(0)
#define PAGE(n) asm volatile("s_branch 2f\n\t.p2align 12\nk_pg" #n ":\n\ts_setpc_b64 s[42:43]\n2:")
#define CALL_PG(n) asm volatile("s_getpc_b64 s[40:41]\n3:\n\ts_mov_b32 s42, k_pg" #n "-3b\n\ts_ashr_i32 s43, s42, 31\n\ts_add_u32 s40, s40, s42\n\ts_addc_u32 s41, s41, s43\n\t" \
                                "s_swappc_b64 s[42:43], s[40:41]" ::: "s40", "s41", "s42", "s43", "scc", "memory")

static __device__ __forceinline__ __amdgpu_buffer_rsrc_t mk_rsrc(const void* p) {
  return __builtin_amdgcn_make_buffer_rsrc((void*)p, 0, 0x7fffffff, 0x00020000);
}
#define DMA_(rsrc, voff, mbase, MI, sbase, SI, pol) do { unsigned _t; \
    asm volatile("s_add_i32 m0, %2, %3\n\ts_add_i32 %0, %5, %6\n\tbuffer_load_dwordx4 %1, %4, %0 offen" pol " lds" \
                 : "=&s"(_t) : "v"(voff), "s"(mbase), "i"(MI), "s"(rsrc), "s"(sbase), "i"(SI) : "memory", "scc"); } while (0)
#define DMA_I(rsrc, voff, mbase, MI, SI) \
    asm volatile("s_add_i32 m0, %1, %2\n\ts_nop 0\n\tbuffer_load_dwordx4 %0, %3, 0 offen offset:%4 lds" \
                 :: "v"(voff), "s"(mbase), "i"((MI) - (SI)), "s"(rsrc), "i"(SI) : "memory", "scc")
#define DMA_S(rsrc, voff, mbase, MI, soff) \
    asm volatile("s_add_i32 m0, %1, %2\n\ts_nop 0\n\tbuffer_load_dwordx4 %0, %3, %4 offen lds" \
                 :: "v"(voff), "s"(mbase), "i"(MI), "s"(rsrc), "s"(soff) : "memory", "scc")
#define TPOL ""
#define TOUCH(rsrc, voff, mbase, MI, soff) \
    asm volatile("s_add_i32 m0, %1, %2\n\ts_nop 0\n\tbuffer_load_dword %0, %3, %4 offen" TPOL " lds" \
                 :: "v"(voff), "s"(mbase), "i"(MI), "s"(rsrc), "s"(soff) : "memory", "scc")
template <int OFF> static __device__ __forceinline__ u32x4 ld16_s(const void* p, int voff) {
  u32x4 v;
  asm volatile("global_load_dwordx4 %0, %1, %2 offset:%3" : "=v"(v) : "v"(voff), "s"(p), "i"(OFF) : "memory");
  return v;
}
template <int OFF> static __device__ __forceinline__ void sld(unsigned& sink, const void* p, int soff) {
  asm volatile("s_load_dword %0, %1, %2 offset:%3" : "+s"(sink) : "s"(p), "s"(soff), "i"(OFF) : "memory");
}
static __device__ __forceinline__ unsigned gld4_s(const void* p, unsigned voff) {
  unsigned v;
  asm volatile("global_load_dword %0, %1, %2" : "=v"(v) : "v"(voff), "s"(p) : "memory");
  return v;
}
static __device__ __forceinline__ u32x2 bld8_sc1(__amdgpu_buffer_rsrc_t r, unsigned voff) {
  u32x2 v;
  asm volatile("buffer_load_dwordx2 %0, %1, %2, 0 offen sc1" : "=v"(v) : "v"(voff), "s"(r) : "memory");
  return v;
}
template <int N> static __device__ __forceinline__ void wait_xs(unsigned& a, u32x2& b) { asm volatile("s_waitcnt vmcnt(%2)" : "+v"(a), "+v"(b) : "i"(N) : "memory"); }
template <int N> static __device__ __forceinline__ void wait_xq(u32x2 (&a)[4], u32x2 (&b)[4]) {
  asm volatile("s_waitcnt vmcnt(%8)" : "+v"(a[0]), "+v"(a[1]), "+v"(a[2]), "+v"(a[3]), "+v"(b[0]), "+v"(b[1]), "+v"(b[2]), "+v"(b[3]) : "i"(N) : "memory");
}
static __device__ __forceinline__ u32x2 ld8_s(const void* p, int voff) {
  u32x2 v;
  asm volatile("global_load_dwordx2 %0, %1, %2" : "=v"(v) : "v"(voff), "s"(p) : "memory");
  return v;
}
static __device__ __forceinline__ u32x2 ld8_s_sc1(const void* p, int voff) {
  u32x2 v;
  asm volatile("global_load_dwordx2 %0, %1, %2 sc1" : "=v"(v) : "v"(voff), "s"(p) : "memory");
  return v;
}
static __device__ __forceinline__ void lgkm0(unsigned& sink) { asm volatile("s_waitcnt lgkmcnt(0)" : "+s"(sink) :: "memory"); }
template <typename T> static __device__ __forceinline__ void pin4(T& a, T& b, T& c, T& d) { asm volatile("" : "+s"(a), "+s"(b), "+s"(c), "+s"(d)); }
template <typename T, typename U> static __device__ __forceinline__ void pins(T& a, U& b) { asm volatile("" : "+s"(a), "+s"(b)); }
static __device__ __forceinline__ void launder(int& v, int& a, int& b) { asm volatile("" : "+v"(v)); asm volatile("" : "+s"(a), "+s"(b)); }
static __device__ __forceinline__ unsigned bld4_sc1(__amdgpu_buffer_rsrc_t r, int voff) {
  unsigned v;
  asm volatile("buffer_load_dword %0, %1, %2, 0 offen sc1" : "=v"(v) : "v"(voff), "s"(r) : "memory");
  return v;
}
static __device__ __forceinline__ u32x4 bld16_nt(__amdgpu_buffer_rsrc_t r, int voff, int soff) {
  u32x4 v;
  asm volatile("buffer_load_dwordx4 %0, %1, %2, %3 offen nt" : "=v"(v) : "v"(voff), "s"(r), "s"(soff) : "memory");
  return v;
}
static __device__ __forceinline__ void bld16_nt_io(u32x4& v, __amdgpu_buffer_rsrc_t r, int voff, int soff) {
  asm volatile("buffer_load_dwordx4 %0, %1, %2, %3 offen nt" : "+v"(v) : "v"(voff), "s"(r), "s"(soff) : "memory");
}
static __device__ __forceinline__ u32x4 bld16_sc1(__amdgpu_buffer_rsrc_t r, int voff, int soff) {
  u32x4 v;
  asm volatile("buffer_load_dwordx4 %0, %1, %2, %3 offen sc1" : "=v"(v) : "v"(voff), "s"(r), "s"(soff) : "memory");
  return v;
}
static __device__ __forceinline__ u32x2 granule_wait(const u64* p, unsigned tag) {
  u64 v = 0;
#pragma unroll 1
  for (int it = 0; it < SPIN_MAX; ++it) {
    v = __hip_atomic_load(p, __ATOMIC_RELAXED, __HIP_MEMORY_SCOPE_AGENT);
    if (__all((unsigned)(v >> 32) == tag)) break;
    __builtin_amdgcn_s_sleep(2);
  }
  return (u32x2){(unsigned)v, (unsigned)(v >> 32)};
}
static __device__ __forceinline__ float key_score(unsigned key) {
  const unsigned u = key >> 16;
  return bf2f((u16)((u & 0x8000u) ? (u & 0x7fffu) : (~u & 0xffffu)));
}
static __device__ __forceinline__ unsigned med3u(unsigned a, unsigned b, unsigned c) { return max(min(a, b), min(max(a, b), c)); }
static __device__ __forceinline__ void merge2(unsigned& a1, unsigned& a2, unsigned b1, unsigned b2) {
  const unsigned lo = min(a1, b1);
  a1 = max(a1, b1);
  a2 = max(lo, max(a2, b2));
}
static __device__ __forceinline__ float sqdev16(const u32x4 (&v)[2], float mb) {
  f32x2 q2 = {0.f, 0.f};
#pragma unroll
  for (int i = 0; i < 8; ++i) {
    const f32x2 e = (f32x2){lo_bf(v[i >> 2][i & 3]), hi_bf(v[i >> 2][i & 3])} - mb;
    q2 += e * e;
  }
  return q2.x + q2.y;
}

constexpr int lrw(int r) { return (r & ~6) | ((r & 2) << 1) | ((r & 4) >> 1); }
constexpr int swz(int r) { return ((r >> 1) & 4) | (r & 2); }
static __device__ __forceinline__ int sym_dn(int p) { return (p ^ (p >> 1)) & 1; }
static __device__ __forceinline__ int sym_hi(int n, int w, int st, int b, int z, int p) {
  return ((p >> 1) * (n / 2 * st) + b * (w * st)) ^ (-sym_dn(p) & (((n - 1) ^ (z * (n / 2))) * st));
}
static __device__ __forceinline__ int sym_lo(int w, int st, int p, int c) { return ((c - sym_dn(p)) * st) & ((w - 1) * st); }
static __device__ __forceinline__ int sym_row(int n, int w, int st, int b, int z, int p, int c) { return sym_lo(w, st, p, c) ^ sym_hi(n, w, st, b, z, p); }
constexpr int G1_NST = 4, G1_NKT = 16, G1_STAGE = 32768;
constexpr int G2_NKT = 16, G2_MRING = 98304;
constexpr int n_off(int k) { return (k % 3) * 32768; }
constexpr int m_off(int k) { return ((k + 2) % 3) * 16384; }
constexpr int k_ord(int k) { return k < 2 ? k : (k < 9 ? 2 * (k - 1) : 2 * (k - 8) + 1); }
constexpr int LDS_DUMMY = G2_MRING + 49152;
constexpr int LDS_GU = LDS_DUMMY + 4096;
constexpr int LDS_FIN = LDS_GU + 4096;
constexpr int LDS_MB = LDS_FIN + 4096;
constexpr int LDS_SEL = LDS_MB + 1536;
constexpr int LDS_SIG = LDS_SEL + 64;
constexpr int LDS_KEY = LDS_SIG + 32;
constexpr int LDS_BYTES = LDS_KEY + 128;
constexpr int LDS_ACC = n_off(G2_NKT - 3);

constexpr int sp_tile(int q) {
  for (int k = 0; k < 16; ++k) {
    if ((k >= VA0 && k < VA0 + VAN) || (k >= VB0 && k < VB0 + VBN) || (k >= VC0 && k < VC0 + VCN)) continue;
    if (q-- == 0) return k;
  }
  return -1;
}
constexpr int tcnt(int q) { return (VAN > 0 && q == VAP) + (VBN > 0 && q == VBP) + (VCN > 0 && q == VCP); }
constexpr int dn_cnt(int q) { return dig(DVS, q); }
constexpr int dn_first(int q) { return acc_dig(DVS, q); }

static __device__ __forceinline__ void code_touch(unsigned& sink, unsigned off) {
  asm volatile("s_getpc_b64 s[40:41]\n\tk_main_rt:\n\ts_min_u32 %1, %1, k_main_end-k_main_rt-196\n\t"
               "s_load_dword %0, s[40:41], %1 offset:0\n\ts_load_dword %0, s[40:41], %1 offset:64\n\ts_load_dword %0, s[40:41], %1 offset:128\n\ts_load_dword %0, s[40:41], %1 offset:192"
               : "+s"(sink), "+s"(off) :: "s40", "s41", "scc", "memory");
}
static __device__ __forceinline__ void touch1(__amdgpu_buffer_rsrc_t r, int voff, unsigned mb, int soff) { TOUCH(r, voff, mb, LDS_DUMMY, soff); }
typedef const __attribute__((address_space(1))) u16* gptr;
typedef __attribute__((ext_vector_type(8))) u64 u64x8;
typedef __attribute__((ext_vector_type(2))) u64 u64x2;
extern "C" __global__ __launch_bounds__(512, 2) void k_main(const u16* x, const u16* wd_re, const u16* wd_im, const u16* w_r,
                                                 char* wsp, const u16* ws_re, const u16* ws_im,
                                                 const u16* w_gate, const u16* w_up, const u16* w_down, u16* out) {
  __shared__ __attribute__((aligned(16))) char smem[LDS_BYTES];
  u64x8 ka;
  u64x2 kb_;
  u64 kc;
  asm volatile("s_load_dwordx16 %0, %3, 0\n\ts_load_dwordx4 %1, %3, 64\n\ts_load_dwordx2 %2, %3, 80"
               : "=&s"(ka), "=&s"(kb_), "=&s"(kc) : "s"(__builtin_amdgcn_kernarg_segment_ptr()) : "memory");
  const int tid = threadIdx.x, lane = tid & 63;
  const int wid = __builtin_amdgcn_readfirstlane(tid >> 6);
  const int g = blockIdx.x, xcd = XMAP(g), j = g >> 3;
  if (wid < 4) {
    unsigned cw, co = (j * 4 + wid) * 448;
    asm volatile(".subsection 1\nk_main_end:\n.subsection 0\n\ts_min_u32 %1, %1, k_main_end-k_main-480\n\ts_getpc_b64 s[40:41]\n\ts_add_u32 s40, s40, %1\n\ts_addc_u32 s41, s41, 0\n\t"
                 "s_load_dword %0, s[40:41], 0\n\ts_load_dword %0, s[40:41], 64\n\ts_load_dword %0, s[40:41], 128\n\t"
                 "s_load_dword %0, s[40:41], 192\n\ts_load_dword %0, s[40:41], 256\n\ts_load_dword %0, s[40:41], 320\n\ts_load_dword %0, s[40:41], 384\n\ts_waitcnt lgkmcnt(0)"
                 : "=&s"(cw), "+s"(co) :: "s40", "s41", "scc", "memory");
  }
  const int pw = wid - 4;
  const int row0 = (wid & 3) * 8 + lrw(lane >> 3);
  const int cm = ((lane & 7) ^ swz(row0)) * 16;
  int vm = ((row0 & 15) * 2 + (row0 >> 4)) * (D_ * 2) + cm;
  asm volatile("" : "+v"(vm));
  const int kq = xcd >> 1;
  const int tt = (xcd & 1) * 4 + (j >> 3);
  const int et = j & 7;
  const int z = kq >> 1, st = (kq & 1) * 8 + tt;
  const int lrow = (kq & 1) * 1024 + (xcd & 1) * 512 + z * 256 + j * 8 + pw * 2;
  const int drow = ((kq & 1) * 2 + (xcd & 1)) * 256 + j * 8 + pw * 2;
  unsigned o_m = st * 128 * D_ * 2, o_l = lrow * D_ * 2, o_d = drow * D_ * 2, mb0 = lds_addr(smem) + pw * 1024;
  int l16 = lane * 16;
  int vn[4];
#pragma unroll
  for (int q = 0; q < 4; ++q) vn[q] = sym_lo(32, D_ * 2, (row0 >> 2) & 3, q * 8 + (row0 >> 4) * 4 + (row0 & 3)) ^ (sym_hi(D_, 32, D_ * 2, et, z, (row0 >> 2) & 3) | cm);
  asm volatile("" : "+s"(o_m), "+s"(o_l), "+s"(o_d), "+s"(mb0));
  asm volatile("" : "+v"(l16), "+v"(vn[0]), "+v"(vn[1]), "+v"(vn[2]), "+v"(vn[3]));
  asm volatile("s_waitcnt lgkmcnt(0)" : "+s"(ka), "+s"(kb_), "+s"(kc) :: "memory");
  x = (const u16*)(gptr)ka[0]; wd_re = (const u16*)(gptr)ka[1]; wd_im = (const u16*)(gptr)ka[2]; w_r = (const u16*)(gptr)ka[3];
  wsp = (char*)(gptr)ka[4]; ws_re = (const u16*)(gptr)ka[5]; ws_im = (const u16*)(gptr)ka[6]; w_gate = (const u16*)(gptr)ka[7];
  w_up = (const u16*)(gptr)kb_[0]; w_down = (const u16*)(gptr)kb_[1]; out = (u16*)(gptr)kc;
  int* flags = (int*)(wsp + WS_FLAGS);
  u64* stat = (u64*)(flags + FL_ST);
  const u16* wdz = z ? wd_im : wd_re;
  const __amdgpu_buffer_rsrc_t rm = mk_rsrc(x);
  const __amdgpu_buffer_rsrc_t rn = mk_rsrc(wdz);
#define G1_X(kt, so, q) DMA_(rm, vm, mb0, (so) + (q) * 4096, o_m, (kt) * 128 + (((q) & 1) * 64 + ((q) >> 1) * 32) * D_ * 2, "")
#define G1_W(kt, so, q) DMA_I(rn, vn[q], mb0, (so) + 16384 + (q) * 4096, (kt) * 128)
#define G1_STAGE_TILE(kt, so, W) do { SB(); G1_X(kt, so, 0); W(0); G1_X(kt, so, 1); W(1); G1_X(kt, so, 2); W(2); G1_X(kt, so, 3); W(3); \
    G1_W(kt, so, 0); W(4); G1_W(kt, so, 1); W(5); G1_W(kt, so, 2); W(6); G1_W(kt, so, 3); W(7); SB(); } while (0)
#define G1_STAGE_A(kt, so, W) do { SB(); G1_X(kt, so, 0); W(0); G1_X(kt, so, 2); W(1); \
    G1_W(kt, so, 0); W(2); G1_W(kt, so, 1); W(3); G1_W(kt, so, 2); W(4); G1_W(kt, so, 3); W(5); W(6); W(7); SB(); } while (0)
#define G1_STAGE_B(kt, so) do { SB(); G1_X(kt, so, 1); G1_X(kt, so, 3); SB(); } while (0)
#define NOW(i)
  const int fr = lane & 15, fq = lane >> 4;
  const int ha = wid >> 2;
  int trow, xo[4], po, ml;
  const char *prs, *prc;
  auto col4 = [](int f) __attribute__((always_inline)) { return ((f & 2) << 3 | (f & 4) << 1 | (f & 1) << 2) ^ (-((f >> 3) & 1) & 28); };
  auto seg0 = [&](int hf, int dn) __attribute__((always_inline)) { return (hf * 512 + et * 32) ^ (-dn & (D_ - 32)); };
  auto epi_setup = [&](int part) __attribute__((always_inline)) {
    int t_ = tt, l_ = lane;
    asm volatile("" : "+s"(t_));
    asm volatile("" : "+v"(l_));
    const int ts = ((wid >> 1) & 1) * 8 + kq * 2 + ha;
    if (part == 0) { xo[3] = (seg0(wid & 1, (l_ >> 3) & 1) + col4(l_)) * 2; asm volatile("" : "+v"(xo[3])); }
    if (part == 1) {
      xo[3] |= sym_hi(S_, 64, D_ * 2, t_, z, l_ >> 4);
      ml = (l_ * 16) ^ (-sym_dn(l_ >> 4) & (z * 768));
      asm volatile("" : "+v"(xo[3]), "+v"(ml));
    }
    if (part == 2) {
#pragma unroll
      for (int r = 0; r < 4; ++r) xo[r] = sym_lo(64, D_ * 2, l_ >> 4, ts * 4 + r) ^ xo[3];
      asm volatile("" : "+v"(xo[0]), "+v"(xo[1]), "+v"(xo[2]), "+v"(xo[3]));
    }
    if (part == 3) {
      trow = t_ * 16 + ts;
      prs = wsp + WS_SLAB + (size_t)((t_ * 8 + et) * 64 + (wid & 3)) * MSG;
      prc = (const char*)(flags + FL_X + ((t_ * 8 + et) * 64 + (wid & 3)) * 32);
      asm volatile("" : "+s"(trow), "+s"(prs), "+s"(prc));
      po = l_ < 3 ? (kq * 4 + ((kq + 1 + l_) & 3)) * 512 : (int)0x80000000;
      asm volatile("" : "+v"(po));
    }
  };
  auto tok_tile = [](unsigned key) __attribute__((always_inline)) {
    const unsigned h = (0xffff - key) & 1023;
    return h < 512 ? h >> 2 : ((1023 - h) >> 6 << 4) | (((1024 - h) & 63) >> 2);
  };
#define rs mk_rsrc(prs)
#define rc mk_rsrc(prc)
  lds_vi sig = (lds_vi)(smem + LDS_SIG + (wid & 3) * 4);
  unsigned tag;
  u32x2 xq[4], sq[4];
  f32x4 ya[4];
  unsigned key[2];
  float cv[16];
  unsigned far0, far1;
  auto xq_load = [&](int n) __attribute__((always_inline)) {
    if (n < 4) xq[n] = ld8_s(x, xo[n]); else sq[n - 4] = ld8_s_sc1(stat, xo[n - 4] >> 11 << 3);
  };
#define PINV(a) asm volatile("" : "+v"(a))
  auto cv_calc = [&]() __attribute__((always_inline)) {
    if (__builtin_expect(!__all(((sq[0].y ^ tag) | (sq[1].y ^ tag) | (sq[2].y ^ tag) | (sq[3].y ^ tag)) == 0), 0)) {
#pragma unroll
      for (int r = 0; r < 4; ++r) sq[r] = granule_wait(stat + (xo[r] >> 11), tag);
    }
    far0 = tok_tile(key[0]) ^ trow; far1 = tok_tile(key[1]) ^ trow;
    asm volatile("" : "+s"(far0), "+s"(far1));
#pragma unroll
    for (int r = 0; r < 4; ++r) {
      const float mb = lo_bf(sq[r].x), ri = __builtin_amdgcn_rcpf(hi_bf(sq[r].x));
#pragma unroll
      for (int i = 0; i < 4; ++i) {
        const float xv = (i & 1) ? hi_bf(xq[r][i >> 1]) : lo_bf(xq[r][i >> 1]);
        cv[r * 4 + i] = __builtin_fmaf(xv - mb, ri, xv);
        PINV(cv[r * 4 + i]);
      }
    }
  };
#define MSG_IDX(m) (kq * 4 + ((kq + 1 + (m)) & 3))
  unsigned seen = 0;

  if (__builtin_expect(wid >= 4, 1)) {
    __builtin_amdgcn_s_setprio(3);
    G1_STAGE_TILE(0, 0, NOW);
    constexpr int O_ST = (FL_ST - FL_AB) * 4, O_CE = (FL_CE - FL_AB) * 4, O_KEY = (FL_KEY - FL_AB) * 4, O_EP = (FL_EPOCH - FL_AB) * 4;
    const __amdgpu_buffer_rsrc_t rf = mk_rsrc(wsp + WS_FLAGS + FL_AB * 4);
    u32x4 lv[2], lv2[2], lw[2], ld[2], ld2[2];
    unsigned epv;
    {
      const char* xrow = (const char*)x + o_l;
      const char* wdrow = (const char*)wdz + o_d;
      lv[0] = ld16_s<0>(xrow, l16); lv[1] = ld16_s<1024>(xrow, l16);
      lv2[0] = ld16_s<2048>(xrow, l16); lv2[1] = ld16_s<3072>(xrow, l16);
      ld[0] = ld16_s<0>(wdrow, l16); ld[1] = ld16_s<1024>(wdrow, l16);
      ld2[0] = ld16_s<2048>(wdrow, l16); ld2[1] = ld16_s<3072>(wdrow, l16);
      lw[0] = ld16_s<0>(w_r, l16); lw[1] = ld16_s<1024>(w_r, l16);
      asm volatile("buffer_load_dword %0, off, %1, %2 sc1" : "=v"(epv) : "s"(rf), "s"(O_EP) : "memory");
    }
    SB();
    const unsigned long long did = llvm_dispatch_id();
    const unsigned dmix = (unsigned)did * 0x85EBCA6Bu + (unsigned)(did >> 32) * 0xC2B2AE35u + 0x27D4EB2Fu;
    const bool moe = et != 7;
    unsigned epoch;
    const int ci = lane & 15, rq = lane >> 4, tk = (lane >> 3) & 1;
    int mat, cg, dq, d0;
    u32x4 wv[16];
    const u16 *pgu, *pn2;
    const char *pm2, *spb;
#define rw __builtin_amdgcn_make_buffer_rsrc((void*)pgu, 0, moe ? 0x7fffffff : 0, 0x00020000)
#define rn2 mk_rsrc(pn2)
#define rm2 mk_rsrc(pm2)
    int vw, vn2[8], vm2;
    auto k_blk = [&](int k) __attribute__((always_inline)) { return tt ^ (k_ord(k) >> 1); };
#define KROT(kk_) (k_blk(kk_) * 256 + (k_ord(kk_) & 1) * 128)
    int tv2[3];
    unsigned sd = 0;
    auto spf = [&](auto N) __attribute__((always_inline)) {
      constexpr int n = decltype(N)::value;
      if constexpr (n >= 0 && sp_tile(n >> 4) >= 0) sld<((n >> 1) & 7) * 4096 + (n & 1) * 64>(sd, spb, KROT(sp_tile(n >> 4)));
    };
    auto touches = [&](auto P) __attribute__((always_inline)) {
      constexpr int p = decltype(P)::value;
      if constexpr (VAN > 0 && p == VAP) touch1(mk_rsrc(spb), tv2[0], mb0, 0);
      if constexpr (VBN > 0 && p == VBP) touch1(mk_rsrc(spb), tv2[1], mb0, 0);
      if constexpr (VCN > 0 && p == VCP) touch1(mk_rsrc(spb), tv2[2], mb0, 0);
    };
    unsigned mq0, mq1, mq2, mq3;
    const int cg8 = et ^ ((et & 1) << 1), rgb = (g & 7) * 4 + (j >> 3);
    const __amdgpu_buffer_rsrc_t rdn = mk_rsrc(w_down);
    const __amdgpu_buffer_rsrc_t rgu = mk_rsrc(wsp + WS_GU);
    int vdn, vgu;
    u32x4 dv[7], gv[4];
    int abo, abq[3];
    unsigned lma;
    unsigned sel[2][4];
    float g0, g1;
    u32x4 kk[4];
    unsigned k1 = 0, k2 = 0;
    const int ko_ = O_KEY + pw * 4096;
#define MM(a, b, c) __builtin_amdgcn_mfma_f32_16x16x32_bf16(__builtin_bit_cast(bf16x8, a), __builtin_bit_cast(bf16x8, b), c, 0, 0, 0)
#define PIN1(a) asm volatile("" : "+v"(a))
#define PIN2(a, b) asm volatile("" : "+v"(a), "+v"(b))
#define PIN3(a, b, c) asm volatile("" : "+v"(a), "+v"(b), "+v"(c))
    {
      int gro, go;
      u32x2 gr;
      u32x4 oh[5];
      f32x4 sa4, gw[2], gx[2];
      float su, gs, ta, tb, sx, mbv;
      unsigned dk[4], kl;
      const int hb = lane >> 5;
#pragma unroll
      for (int l = 0; l < WSN; ++l) asm volatile("" : "=v"(wv[l]));
      auto diag = [&](const f32x4& m) __attribute__((always_inline)) {
        return __uint_as_float((__float_as_uint(m[0]) & dk[0]) | (__float_as_uint(m[1]) & dk[1]) | (__float_as_uint(m[2]) & dk[2]) | (__float_as_uint(m[3]) & dk[3]));
      };
      auto setup = [&](auto JB, auto GP) __attribute__((always_inline)) {
        constexpr int jb = decltype(JB)::value, gp = decltype(GP)::value;
        SB();
        {
          int ln = lane, jj = j, p_ = pw;
          launder(ln, jj, p_);
          const int c_ = ln & 15, r_ = ln >> 4, e_ = jj & 7, t_ = (xcd & 1) * 4 + (jj >> 3);
          if constexpr (jb == 1 && gp == 0) {
            pn2 = ((kq >> 1) ? ws_im : ws_re) + (kq & 1) * 1024;
            spb = (const char*)(pn2 + (sym_row(S_, 64, 1, t_, 0, e_ >> 1, 0) + (e_ & 1) * 32 + p_ * 8) * S_);
            pins(pn2, spb);
          }
          if constexpr (jb == 1 && gp >= 1 && gp < 6) {
            const unsigned o = c_ == gp - 1 ? 0x3f803f80u : 0u;
            oh[gp - 1] = (u32x4){o, o, o, o};
            PIN1(oh[gp - 1]);
          }
          if constexpr (jb == 1 && gp == 6) {
            const int rk = moe ? e_ * 8 + (jj >> 3) * 2 + (xcd >> 2) : 0, r3 = ((rk - 14) * 43) >> 7, rr = rk - 14 - r3 * 3;
            const int c56 = rk < 14 ? rk * 4 + 1 : r3 * 4 + rr + ((rr + 1) >> 1);
            mat = (c56 + 36) >> 6; cg = c56 - mat * 28; dq = xcd & 3; d0 = dq * 256 + p_ * 64;
            pin4(mat, cg, dq, d0);
          }
          if constexpr (jb == 2 && gp == 0) {
            pgu = mat ? w_up : w_gate;
            pm2 = wsp + WS_ABT + (kq * 8 + e_) * 262144;
            pins(pgu, pm2);
            vw = ((d0 + r_ * 16) * F_ + cg * 128 + c_ * 8) * 2;
            PIN1(vw);
          }
          if constexpr (jb == 2 && (gp == 1 || gp == 2)) {
            dk[gp * 2 - 2] = c_ == r_ * 4 + gp * 2 - 2 ? ~0u : 0u; dk[gp * 2 - 1] = c_ == r_ * 4 + gp * 2 - 1 ? ~0u : 0u;
            PIN2(dk[gp * 2 - 2], dk[gp * 2 - 1]);
          }
          if constexpr (jb == 2 && gp == 3) { kl = lrow + (ln >> 5); go = (ln & 30) ? (int)0x80000000 : ((ln & 1) ? O_KEY : O_ST) + (int)kl * 8; PIN2(kl, go); }
          if constexpr (jb == 2 && gp == 4) { kl = 0xffff - kl; go = (ln & ~1) == 2 ? O_CE + (z * D_ + drow + (ln & 1)) * 8 : go; PIN2(kl, go); }
          if constexpr (jb == 2 && gp == 5) {
            gro = p_ < 2 ? O_ST + (st * 128 + p_ * 64 + ln) * 8 : O_CE + z * D_ * 8 + sym_row(D_, 32, 8, e_, z, (ln >> 2) & 3, (p_ - 2) * 16 + r_ * 4 + (ln & 3));
            lma = lds_addr(smem + LDS_MB) + (p_ < 2 ? p_ * 256 : 512 + p_ * 256) + ln * 4;
            PIN2(gro, lma);
          }
          if constexpr (jb == 1 && gp == 7) {
#pragma unroll
            for (int u = 0; u < 3; ++u) {
              const int vn_ = u == 0 ? VAN : u == 1 ? VBN : VCN;
              if (vn_ == 0) continue;
              const int k = (u == 0 ? VA0 : u == 1 ? VB0 : VC0) + (ln >> 3);
              tv2[u] = (ln >> 3) < vn_ ? (ln & 7) * 4096 + ((t_ ^ (k < 9 ? k - 1 : k - 8)) << 8) + (k < 9 ? 0 : 128) : (int)0x80000000;
              PIN1(tv2[u]);
            }
          }
          if constexpr (jb == 7 && gp == 0) {
            vm2 = p_ * 1024 + ln * 16;
            PIN1(vm2);
          }
          if constexpr (jb == 2 && gp == 6) {
            const int r0 = p_ * 8 + lrw(ln >> 3);
            vn2[6] = sym_lo(64, S_ * 2, (r0 >> 2) & 3, (r0 >> 4) * 4 + (r0 & 3)); vn2[7] = sym_hi(S_, 64, S_ * 2, t_, z, (r0 >> 2) & 3) | (vm & 127);
            PIN2(vn2[6], vn2[7]);
          }
          if constexpr ((jb == 2 && gp == 7) || (jb == 7 && gp >= 6)) {
            constexpr int q0 = jb == 2 ? 0 : gp == 6 ? 3 : 6;
            const int lo = vn2[6], hi = vn2[7];
#pragma unroll
            for (int q = q0; q < q0 + 3 && q < 8; ++q) { vn2[q] = ((lo + q * 8 * S_ * 2) & (63 * S_ * 2)) ^ hi; PIN1(vn2[q]); }
          }
          if constexpr (jb == 7 && gp == 1) { vdn = rgb * 112 + p_ * 28 + r_ * 7; vgu = (((ln >> 3) & 1) * 4 * F_ + vdn + min(ln & 7, 6)) * 16; PIN2(vdn, vgu); }
          if constexpr (jb == 7 && gp == 2) { vdn = (vdn * D_ + cg8 * 128 + c_ * 8) * 2; PIN1(vdn); }
          if constexpr (jb == 7 && gp == 3) {
            abo = (((kq * 8 + e_) * 4 + p_) * 16 + c_) * AB_STRIDE * 4;
            abq[2] = ln >= 16 || (ln & 7) == t_ ? (int)0x80000000 : abo;
            PIN2(abo, abq[2]);
          }
          if constexpr (jb == 7 && gp == 4) {
            abq[1] = ln >= 8 ? (int)0x80000000 : abq[2];
            abq[0] = ((ln ^ t_) & 4) ? (int)0x80000000 : abq[1];
            PIN2(abq[0], abq[1]);
          }
          if constexpr (jb == 7 && gp == 5) {
            const unsigned mb = lds_addr(smem) + p_ * 1024;
            mq0 = mb + ((3 - kq) & 3) * 4096; mq1 = mb + ((4 - kq) & 3) * 4096; mq2 = mb + ((5 - kq) & 3) * 4096; mq3 = mb + ((6 - kq) & 3) * 4096;
            pin4(mq0, mq1, mq2, mq3);
          }
          if constexpr (jb == 8 && gp < 4) {
#pragma unroll
            for (int h = 0; h < 2; ++h) {
              const unsigned hv = h ? 0x0302u : 0x0100u;
              sel[h][gp] = ((ln & 7) >> 1) == gp ? ((ln & 1) ? (hv << 16) | 0x0c0cu : 0x0c0c0000u | hv) : 0x0c0c0c0cu;
            }
            PIN2(sel[0][gp], sel[1][gp]);
          }
        }
        SB();
      };
#define WS1(i) setup(IC<1>{}, IC<i>{})
#define WS2(i) setup(IC<2>{}, IC<i>{})
#define WS3(i) do { setup(IC<7>{}, IC<i>{}); if (i >= 4) setup(IC<8>{}, IC<(i) & 3>{}); spf(IC<(i < SP0 ? i : -1)>{}); } while (0)
      G1_STAGE_TILE(1, G1_STAGE, WS1);
      WAIT_VM(19); BAR();
      G1_STAGE_TILE(2, 2 * G1_STAGE, WS2);
      G1_STAGE_TILE(3, 3 * G1_STAGE, WS3);
      auto lnu = [&](auto U) __attribute__((always_inline)) {
        constexpr int jb = 1 + decltype(U)::value / 7, gp = decltype(U)::value % 7;
        SB();
        if constexpr (jb == 1) {
          const f32x4 z4 = {0.f, 0.f, 0.f, 0.f};
          if constexpr (gp == 0) { sa4 = MM(lv[0], oh[0], z4); gw[0] = MM(lv[0], lw[0], z4); gx[0] = MM(lv[0], lv[0], z4); PIN3(sa4, gw[0], gx[0]); }
          if constexpr (gp == 1) { sa4 = MM(lv2[0], oh[1], sa4); gw[1] = MM(lv2[0], lw[0], z4); gx[1] = MM(lv2[0], lv2[0], z4); PIN3(sa4, gw[1], gx[1]); }
          if constexpr (gp == 2) { sa4 = MM(lv[1], oh[0], sa4); gw[0] = MM(lv[1], lw[1], gw[0]); gx[0] = MM(lv[1], lv[1], gx[0]); PIN3(sa4, gw[0], gx[0]); }
          if constexpr (gp == 3) { sa4 = MM(lv2[1], oh[1], sa4); gw[1] = MM(lv2[1], lw[1], gw[1]); gx[1] = MM(lv2[1], lv2[1], gx[1]); PIN3(sa4, gw[1], gx[1]); }
          if constexpr (gp == 4) { sa4 = MM(ld[0], oh[2], sa4); sa4 = MM(ld[1], oh[2], sa4); PIN1(sa4); }
          if constexpr (gp == 5) { sa4 = MM(ld2[0], oh[3], sa4); sa4 = MM(ld2[1], oh[3], sa4); PIN1(sa4); }
          if constexpr (gp == 6) { sa4 = MM(lw[0], oh[4], sa4); sa4 = MM(lw[1], oh[4], sa4); PIN1(sa4);
            epoch = __builtin_amdgcn_readfirstlane(epv); tag = ((epoch + 1u) * 0x9E3779B1u ^ dmix) | 1u; }
        }
        if constexpr (jb == 2) {
          if constexpr (gp == 0) { su = (sa4[0] + sa4[1]) + (sa4[2] + sa4[3]); ta = diag(gw[0]); PIN2(su, ta); }
          if constexpr (gp == 1) { su = fold32(su, su); tb = diag(gw[1]); PIN2(su, tb); }
          if constexpr (gp == 2) { su = fold16(su, su); gs = fold32(ta, tb); PIN2(su, gs); }
          if constexpr (gp == 3) { ta = diag(gx[0]); tb = diag(gx[1]); PIN2(ta, tb); }
          if constexpr (gp == 4) { gs = fold16(gs, fold32(ta, tb)); PIN1(gs); }
          if constexpr (gp == 5) { gs += dpp_f<0xB1>(gs); gs += dpp_f<0x4E>(gs); PIN1(gs); }
          if constexpr (gp == 6) { gs += dpp_f<0x141>(gs); gs += dpp_f<0x140>(gs); PIN1(gs); }
        }
        if constexpr (jb == 3) {
          if constexpr (gp == 0) {
            const float s0 = rdl(su, 0), s1 = rdl(su, 1), q0 = rdl(gs, 16), q1 = rdl(gs, 48);
            sx = hb ? s1 : s0; ta = hb ? q1 : q0; PIN2(sx, ta);
          }
          if constexpr (gp == 1) {
            const float mean = sx * (1.0f / D_);
            mbv = rbf(mean);
            ta -= sx * mean;
            if (__builtin_expect(__any(!(sx * mean <= 16.f * ta)), 0)) {
              float q = fold32(sqdev16(lv, rdl(mbv, 0)), sqdev16(lv2, rdl(mbv, 32)));
              q = row_sum(fold16(q, q));
              const float dm = mean - mbv;
              ta = fmaxf(q - (float)D_ * dm * dm, 0.f);
            }
            PIN2(mbv, ta);
          }
          if constexpr (gp == 2) { ta = rbf(rbf(ta * (1.0f / D_)) + 1e-5f); gs -= mbv * rdl(su, 4); PIN2(ta, gs); }
          if constexpr (gp == 3) { ta = rbf(__builtin_amdgcn_sqrtf(ta)); tb = __builtin_amdgcn_rcpf(ta); PIN2(ta, tb); }
          if constexpr (gp == 4) {
            const unsigned rb = __float_as_uint(rbf(gs * tb));
            kl |= (rb ^ ((unsigned)((int)rb >> 31) | 0x80000000u)) & 0xffff0000u; PIN1(kl);
          }
          if constexpr (gp == 5) { kl = (lane & 2) ? __float_as_uint(su) : (lane & 1) ? kl : (__float_as_uint(mbv) >> 16) | __float_as_uint(ta); PIN1(kl); }
        }
        SB();
      };
      constexpr int LN_JB = 1 + ((19 - LNP) * LNS) / 8;
      auto work1 = [&](auto JB, auto GP) __attribute__((always_inline)) {
        constexpr int jb = decltype(JB)::value, gp = decltype(GP)::value, pos = (jb - 1) * 8 + gp;
        SB();
        if constexpr (pos % LNS == 0 && LNP + pos / LNS < 20) lnu(IC<LNP + pos / LNS>{});
        if constexpr (jb == ST_JB + 3) {
          if constexpr (gp == 0) { if (!__all(gr.y == tag)) gr = granule_wait((const u64*)((const char*)(flags + FL_AB) + gro), tag); PIN1(gr); }
          if constexpr (gp == 1) { g1 = __builtin_amdgcn_rcpf(__uint_as_float((gr.x & 0xffff0000u) ^ ((unsigned)z << 31))); PIN1(g1); }
          if constexpr (gp == 2) { g0 = pw < 2 ? -(lo_bf(gr.x) * g1) : __uint_as_float(gr.x); PIN1(g0); }
        }
        if constexpr (gp < SP1 && jb <= SP1E) spf(IC<SP0 + (jb - 1) * SP1 + gp>{});
        SB();
      };
      auto step1 = [&](auto JB) __attribute__((always_inline)) {
        constexpr int jb = decltype(JB)::value;
        if constexpr (jb == 1) PAGE(1);
        if constexpr (jb == 7) PAGE(2);
        constexpr int n16 = jb == 12 ? 12 : jb == 11 ? 14 : 16;
        constexpr auto wl = [](int q) { return (q == LN_JB ? 1 : 0) + (q == ST_JB ? 1 : 0); };
        if constexpr (jb == 1) {
          asm volatile("s_waitcnt vmcnt(%11)" : "+v"(lv[0]), "+v"(lv[1]), "+v"(lw[0]), "+v"(lw[1]), "+v"(ld[0]), "+v"(ld[1]),
                       "+v"(lv2[0]), "+v"(lv2[1]), "+v"(ld2[0]), "+v"(ld2[1]), "+v"(epv) : "i"(LNP ? 24 : 16) : "memory");
#define LU(u) if constexpr (u < LNP) lnu(IC<u>{});
          LU(0) LU(1) LU(2) LU(3) LU(4) LU(5) LU(6) LU(7) LU(8) LU(9) LU(10) LU(11) LU(12) LU(13) LU(14) LU(15) LU(16) LU(17) LU(18)
          if (LNP) WAIT_VM(16);
        } else if constexpr (jb == ST_JB + 3) {
          asm volatile("s_waitcnt vmcnt(%1)" : "+v"(gr) : "i"(n16 + wl(jb - 2) + wl(jb - 1)) : "memory");
        } else wait_vm<n16 + wl(jb - 3) + wl(jb - 2) + wl(jb - 1)>();
        BAR();
        if constexpr (jb == 2) { if (pw == 0 && lane < 16) ((int*)(smem + LDS_SEL))[lane < 8 ? lane : lane + 8] = 0; }
        if constexpr (jb == 4) { if (pw == 0 && lane == 0) ((unsigned*)(smem + LDS_SEL))[5] = tag; }
#define W1(i) work1(JB, IC<i>{})
        if constexpr (jb < 10) G1_STAGE_TILE(jb - 1 + G1_NST, ((jb - 1) % G1_NST) * G1_STAGE, W1);
        else G1_STAGE_A(jb - 1 + G1_NST, ((jb - 1) % G1_NST) * G1_STAGE, W1);
        if constexpr (jb == 12) { G1_STAGE_B(13, G1_STAGE); G1_STAGE_B(14, 2 * G1_STAGE); }
        if constexpr (jb == LN_JB) __builtin_amdgcn_raw_buffer_store_b64((u32x2){kl, tag}, rf, go, 0, 16);
        if constexpr (jb == ST_JB) {
          asm volatile("buffer_load_dwordx2 %0, %1, %2, 0 offen sc1" : "=v"(gr) : "v"(gro), "s"(rf) : "memory");
        }
        touches(IC<100 + jb>{});
        if constexpr (jb <= 8) {
          if (et == jb - 1) {
#pragma unroll
            for (int l = 0; l < WSN; ++l) bld16_nt_io(wv[l], rw, vw, l * F_ * 2);
          }
        }
        SB();
      };
      static_assert(ST_JB + 3 <= 12 && ST_JB >= 6, "schedule");
      step1(IC<1>{}); step1(IC<2>{}); step1(IC<3>{}); step1(IC<4>{}); step1(IC<5>{}); step1(IC<6>{}); step1(IC<7>{}); step1(IC<8>{}); step1(IC<9>{}); step1(IC<10>{}); step1(IC<11>{}); step1(IC<12>{});
    }
#define ALIAS 0
#define G2_STAGE_N(kk_, W) do { const int kb = KROT(((ALIAS >> (kk_)) & 1) ? (kk_) & 3 : (kk_)); constexpr int so = n_off(kk_); SB(); \
    DMA_S(rn2, vn2[0], mq0, so, kb); W(0); DMA_S(rn2, vn2[1], mq1, so, kb); W(1); \
    DMA_S(rn2, vn2[2], mq2, so, kb); W(2); DMA_S(rn2, vn2[3], mq3, so, kb); W(3); \
    DMA_S(rn2, vn2[4], mq0, so + 16384, kb); W(4); DMA_S(rn2, vn2[5], mq1, so + 16384, kb); W(5); \
    DMA_S(rn2, vn2[6], mq2, so + 16384, kb); W(6); DMA_S(rn2, vn2[7], mq3, so + 16384, kb); W(7); SB(); } while (0)
#define G2_STAGE_NH(kk_, h) do { const int kb = KROT(kk_); constexpr int so = n_off(kk_) + (h) * 16384; SB(); \
    DMA_S(rn2, vn2[(h) * 4], mq0, so, kb); DMA_S(rn2, vn2[(h) * 4 + 1], mq1, so, kb); \
    DMA_S(rn2, vn2[(h) * 4 + 2], mq2, so, kb); DMA_S(rn2, vn2[(h) * 4 + 3], mq3, so, kb); SB(); } while (0)
#define G2_STAGE_M(kk_, W) do { const int kb = KROT(kk_) * 128; constexpr int so = G2_MRING + m_off(kk_); SB(); \
    DMA_(rm2, vm2, mb0, so, kb, 0, " sc1"); W(8); DMA_(rm2, vm2, mb0, so + 4096, kb, 4096, " sc1"); W(9); \
    DMA_(rm2, vm2, mb0, so + 8192, kb, 8192, " sc1"); W(10); DMA_(rm2, vm2, mb0, so + 12288, kb, 12288, " sc1"); W(11); SB(); } while (0)
    WAIT_VM(16); BAR();
    asm volatile("ds_write_b32 %0, %1" :: "v"(lma), "v"(g0) : "memory");
    if (pw < 2) asm volatile("ds_write_b32 %0, %1 offset:512" :: "v"(lma), "v"(g1) : "memory");
    u32x4 tv[4];
    unsigned ready = 0;
    auto need = [&](int k) __attribute__((always_inline)) {
      const int b = k_blk(k) + (k_ord(k) & 1) * 8;
#pragma unroll 1
      for (int it = 0; !((ready >> b) & 1) && it < SPIN_MAX; ++it) {
        ready |= (unsigned)__ballot(__builtin_amdgcn_raw_buffer_load_b32(rf, abq[k < 5 ? 0 : k < 9 ? 1 : 2], 0, 16) == tag);
        asm volatile("" ::: "memory");
      }
    };
    unsigned ksel, bad = 0;
    int isel;
    unsigned xw;
    auto bvec = [&](unsigned src, const unsigned (&sl)[4]) __attribute__((always_inline)) {
      return (u32x4){__builtin_amdgcn_perm(0, src, sl[0]), __builtin_amdgcn_perm(0, src, sl[1]),
                     __builtin_amdgcn_perm(0, src, sl[2]), __builtin_amdgcn_perm(0, src, sl[3])};
    };
#define BC8(v, p) (unsigned)__builtin_amdgcn_update_dpp(__builtin_amdgcn_update_dpp(0, (int)(v), 0x150 + (p), 0xf, 0x3, false), (int)(v), 0x158 + (p), 0xf, 0xc, false)
    float* lg = (float*)(smem + LDS_GU);
    f32x4 macc = {0.f, 0.f, 0.f, 0.f};
    u32x2 sv;
    unsigned hp, gbad;
    float s0, s1;
    auto work2 = [&](auto JB, auto GP) __attribute__((always_inline)) {
      constexpr int jb = decltype(JB)::value, gp = decltype(GP)::value;
      SB();
      if constexpr (jb == -1 && gp < 4) {
        constexpr int c = gp == 0 ? 0xB1 : gp == 1 ? 0x4E : gp == 2 ? 0x141 : 0x140;
        merge2(k1, k2, dpp_u<c>(k1), dpp_u<c>(k2));
        PIN2(k1, k2);
      }
      if constexpr (jb == -1 && gp == 4) {
        ksel = tk ? k2 : k1;
        isel = (0xffff - (int)(ksel & 0xffffu)) & (S_ - 1);
        k1 = (isel * D_ + d0 + rq * 16 + (lane & 7) * 2) * 2; k2 = O_ST + isel * 8;
        PIN1(ksel);
        xw = gld4_s(x, k1);
        sv = bld8_sc1(rf, k2);
      }
      if constexpr (jb == 0 && gp == 1) wait_xs<3 + 1 + (N2A < 2 ? N2A : 2) + tcnt(116) + tcnt(117)>(xw, sv);
      if constexpr (jb == 0) {
        if constexpr (gp == 1) { if (!__all(sv.y == tag)) sv = granule_wait(stat + isel, tag); s0 = __builtin_amdgcn_rcpf(hi_bf(sv.x)); PIN1(s0); }
        if constexpr (gp == 2) { s1 = ln_elem(lo_bf(xw), lo_bf(sv.x), hi_bf(sv.x), s0); PIN1(s1); }
        if constexpr (gp == 3) { xw = pk2(s1, ln_elem(hi_bf(xw), lo_bf(sv.x), hi_bf(sv.x), s0)); PIN1(xw); }
      }
      if constexpr (DGS == 0 && jb == DG && gp < 7) { PIN1(dv[gp]); macc = MM(dv[gp], bvec(BC8(hp, gp), sel[0]), macc); PIN1(macc); }
      if constexpr (jb >= 100 && gp < 7) { PIN1(dv[gp]); macc = MM(dv[gp], bvec(BC8(hp, gp), sel[0]), macc); PIN1(macc); }
      if constexpr ((jb == 0 && gp >= 4) || jb == 1) {
        constexpr int l = jb == 0 ? gp - 4 : gp + 4;
        if constexpr (!(l & 1)) hp = BC8(xw, l >> 1);
        PIN1(wv[l]);
        macc = MM(wv[l], bvec(hp, sel[l & 1]), macc);
        PIN2(macc, hp);
      }
      if constexpr (jb == GV_JB + 2) {
        if constexpr (gp == 0) { gbad = (gv[0].y ^ tag) | (gv[0].w ^ tag) | (gv[1].y ^ tag) | (gv[1].w ^ tag); PIN1(gbad); }
        if constexpr (gp == 1) { gbad |= (gv[2].y ^ tag) | (gv[2].w ^ tag) | (gv[3].y ^ tag) | (gv[3].w ^ tag); PIN1(gbad); }
        if constexpr (gp == 2) {
          s0 = ((__uint_as_float(gv[0].x) + __uint_as_float(gv[1].x)) + __uint_as_float(gv[2].x)) + __uint_as_float(gv[3].x);
          s1 = ((__uint_as_float(gv[0].z) + __uint_as_float(gv[1].z)) + __uint_as_float(gv[2].z)) + __uint_as_float(gv[3].z);
          PIN2(s0, s1);
        }
        if constexpr (gp == 3) {
#pragma unroll 1
          for (int it = 0; !__all(gbad == 0) && it < SPIN_MAX; ++it) {
            u32x4 q[4];
            gbad = 0;
#pragma unroll
            for (int i = 0; i < 4; ++i) { q[i] = __builtin_amdgcn_raw_buffer_load_b128(rgu, vgu, i * F_ * 16, 16); gbad |= (q[i].y ^ tag) | (q[i].w ^ tag); }
            s0 = ((__uint_as_float(q[0].x) + __uint_as_float(q[1].x)) + __uint_as_float(q[2].x)) + __uint_as_float(q[3].x);
            s1 = ((__uint_as_float(q[0].z) + __uint_as_float(q[1].z)) + __uint_as_float(q[2].z)) + __uint_as_float(q[3].z);
            asm volatile("" ::: "memory");
          }
          const unsigned gp2 = pk2(s0, s1);
          const unsigned up2 = dpp_u<0x128>(gp2);
          s0 = tk ? hi_bf(up2) : lo_bf(gp2); s1 = tk ? hi_bf(gp2) : lo_bf(up2);
          PIN2(s0, s1);
        }
        if constexpr (gp == 4) { s0 = rbf(s0 * __builtin_amdgcn_rcpf(1.f + __expf(-s0))); PIN1(s0); }
        if constexpr (gp == 5) { hp = pk2(s0 * s1, 0.f); PIN1(hp); }
      }
      if constexpr (jb == 10 && gp == 4) code_touch(sd, (j * 4 + pw) * 256);
      SB();
    };
#define W2M(i) work2(IC<-1>{}, IC<i>{})
#define W20(i) work2(IC<0>{}, IC<i>{})
#define W21(i) work2(IC<1>{}, IC<i>{})
    auto piece = [&](auto K, auto Q) __attribute__((always_inline)) {
      constexpr int k = decltype(K)::value, q = decltype(Q)::value;
      if constexpr (q < 8) {
        const int kb = KROT(((ALIAS >> k) & 1) ? k & 3 : k);
        DMA_S(rn2, vn2[q], (q & 3) == 0 ? mq0 : (q & 3) == 1 ? mq1 : (q & 3) == 2 ? mq2 : mq3, n_off(k) + (q >> 2) * 16384, kb);
      } else {
        if constexpr (q == 8) need(k);
        const int kb = KROT(k) * 128;
        DMA_(rm2, vm2, mb0, G2_MRING + m_off(k) + (q - 8) * 4096, kb, (q - 8) * 4096, " sc1");
      }
    };
    unsigned fs;
    {
      G1_STAGE_B(15, 3 * G1_STAGE);
      G2_STAGE_NH(0, 0);
      lgkm0(sd);
      WAIT_VM(16); BAR();
      G2_STAGE_NH(0, 1);
      WAIT_VM(10); BAR();
#pragma unroll
      for (int i = 0; i < 4; ++i) kk[i] = bld16_sc1(rf, l16, ko_ + i * 1024);
#pragma unroll
      for (int l = WSN; l < 16; ++l) wv[l] = bld16_nt(rw, vw, l * F_ * 2);
      asm volatile("s_waitcnt vmcnt(%4)" : "+v"(kk[0]), "+v"(kk[1]), "+v"(kk[2]), "+v"(kk[3]) : "i"(16 - WSN) : "memory");
      touches(IC<115>{});
#pragma unroll
      for (int i = 0; i < 4; ++i) {
        k2 = med3u(k1, k2, kk[i].x); k1 = max(k1, kk[i].x);
        k2 = med3u(k1, k2, kk[i].z); k1 = max(k1, kk[i].z);
        bad |= (kk[i].y ^ tag) | (kk[i].w ^ tag);
      }
#pragma unroll 1
      for (int it = 0; !__all(bad == 0) && it < SPIN_MAX; ++it) {
        k1 = 0; k2 = 0; bad = 0;
#pragma unroll
        for (int i = 0; i < 4; ++i) {
          const u32x4 q = __builtin_amdgcn_raw_buffer_load_b128(rf, l16, ko_ + i * 1024, 16);
          k2 = med3u(k1, k2, q.x); k1 = max(k1, q.x);
          k2 = med3u(k1, k2, q.z); k1 = max(k1, q.z);
          bad |= (q.y ^ tag) | (q.w ^ tag);
        }
        asm volatile("" ::: "memory");
      }
      merge2(k1, k2, dpp_u<0xB1>(k1), dpp_u<0xB1>(k2));
      merge2(k1, k2, dpp_u<0x4E>(k1), dpp_u<0x4E>(k2));
      merge2(k1, k2, dpp_u<0x141>(k1), dpp_u<0x141>(k2));
      merge2(k1, k2, dpp_u<0x140>(k1), dpp_u<0x140>(k2));
      if ((lane & 15) == 0) *(u32x2*)(smem + LDS_KEY + (pw * 4 + rq) * 8) = (u32x2){k1, k2};
      PAGE(3);
      BAR_LGKM();
#pragma unroll
      for (int jj = 0; jj < 4; ++jj) tv[jj] = *(const u32x4*)(smem + G2_MRING + m_off(0) + (jj * 4 + pw) * 1024 + lane * 16);
      { const u32x2 kp = *(const u32x2*)(smem + LDS_KEY + ci * 8); k1 = kp.x; k2 = kp.y; }
#pragma unroll
      for (int jj = 0; jj < 4; ++jj) __builtin_amdgcn_raw_buffer_store_b128(tv[jj], rm2, vm2 + jj * 4096 + 2 * tt * 16384, 0, 16);
      G2_STAGE_N(1, W2M);
      touches(IC<116>{});
      wait_vm<10 + tcnt(116)>();
      if (lane == tt) __builtin_amdgcn_raw_buffer_store_b32(tag, rf, abo, 0, 16);
      touches(IC<117>{});
#define P2A(i) if constexpr (i < N2A) piece(IC<2>{}, IC<i>{}); W20(i);
      SB(); P2A(0) P2A(1) P2A(2) P2A(3) P2A(4) P2A(5) P2A(6) P2A(7)
      __builtin_amdgcn_s_sleep(WSL);
      wait_vm<1 + N2A + tcnt(116) + tcnt(117)>();
      fs = bld4_sc1(rf, abq[0]);
    }
    BAR();
#pragma unroll
    for (int jj = 0; jj < 4; ++jj) tv[jj] = *(const u32x4*)(smem + G2_MRING + m_off(1) + (jj * 4 + pw) * 1024 + lane * 16);
    constexpr int SPB = SP0 + SP1E * SP1;
#define P2B(i) if constexpr (i >= N2A) piece(IC<2>{}, IC<i>{});
    SB(); P2B(0) P2B(1) P2B(2) P2B(3) P2B(4) P2B(5) P2B(6) P2B(7)
#define P3A(i) if constexpr (i < N3A) piece(IC<3>{}, IC<i>{});
    P3A(0) P3A(1) P3A(2) P3A(3) P3A(4) P3A(5) P3A(6) P3A(7)
    asm volatile("s_waitcnt vmcnt(%1)" : "+v"(fs) : "i"(8 - N2A + N3A) : "memory");
    ready = (unsigned)__ballot(fs == tag);
    need(2);
    G2_STAGE_M(2, NOW);
#define P3B(i) if constexpr (i >= N3A) piece(IC<3>{}, IC<i>{});
    P3B(0) P3B(1) P3B(2) P3B(3) P3B(4) P3B(5) P3B(6) P3B(7)
    need(3);
    G2_STAGE_M(3, NOW);
    fs = bld4_sc1(rf, abq[1]);
    touches(IC<1>{});
#pragma unroll
    for (int i = 0; i < dn_cnt(1); ++i) dv[i] = bld16_nt(rdn, vdn, i * D_ * 2);
    W21(0); W21(1); W21(2); W21(3); W21(4); W21(5); W21(6); W21(7); W21(8); W21(9); W21(10); W21(11);
    asm volatile("" : "+v"(tv[0]), "+v"(tv[1]), "+v"(tv[2]), "+v"(tv[3]));
    auto step2 = [&](auto JB) __attribute__((always_inline)) {
      constexpr int jb = decltype(JB)::value;
      if constexpr (jb == 3) PAGE(4);
      if constexpr (jb == 2 && PRIO2 != 3) __builtin_amdgcn_s_setprio(PRIO2);
      if constexpr (jb == 9) PAGE(5);
      constexpr auto must = [](int q) { return (q == 2 ? 4 : 0) + ((FSM >> q) & 1) + dig(GVS, q); };
      constexpr auto late = [](int q) { return dn_cnt(q) + (VAN > 0 && q == VAP) + (VBN > 0 && q == VBP) + (VCN > 0 && q == VCP); };
      if constexpr (jb == 2) wait_vm<8 - N3A + 4 + must(1) + late(1)>();
      else wait_vm<(dig(LAGS, jb - 2) ? 0 : late(jb - 2) + (jb == 5 ? 1 : 0)) + 12 - dig(LAGS, jb - 1) + must(jb - 1) + late(jb - 1)>();
      if constexpr (jb == 3) { if (pw == 0 && (lane & ~8) == 0) ((unsigned*)(smem + LDS_SEL))[tk] = ksel; }
      if constexpr ((jb == 2 || jb == DG + 1)) {
        *(f32x4*)(lg + pw * 256 + lane * 4) = macc;
        macc = (f32x4){0.f, 0.f, 0.f, 0.f};
        lgkm0(sd);
      }
      BAR();
      float p0[4], p1[4];
      const int col = (pw & 1) * 64 + lane;
      if constexpr (jb == 3) {
        if (g == 0 && pw == 0 && lane == 0) __hip_atomic_store((unsigned*)flags + FL_EPOCH, epoch + 1u, __ATOMIC_RELAXED, __HIP_MEMORY_SCOPE_AGENT);
      }
      if constexpr (jb == DG + 1) {
        const float* p = lg + (pw & 1) * 32 + (lane >> 4) * 64 + ((2 * lane) & 7) * 4 + ((lane >> 2) & 3);
#pragma unroll
        for (int i = 0; i < 4; ++i) { p0[i] = p[i * 256]; p1[i] = p[i * 256 + 4]; }
      }
      if constexpr (jb == 2) {
        const float* p = lg + (col >> 5) * 64 + (col & 7) * 4 + ((col >> 3) & 3);
#pragma unroll
        for (int i = 0; i < 4; ++i) { p0[i] = p[i * 256]; p1[i] = p[i * 256 + 32]; }
      }
      if constexpr (jb == 4) {
        if (lane == 8 + tt) __builtin_amdgcn_raw_buffer_store_b32(tag, rf, abo, 0, 16);
      }
      if constexpr ((FSM >> (jb - 2)) & 1) {
        asm volatile("" : "+v"(fs));
        ready |= (unsigned)__ballot(fs == tag);
      }
      if constexpr (jb == GV_JB + 2) asm volatile("" : "+v"(gv[0]), "+v"(gv[1]), "+v"(gv[2]), "+v"(gv[3]));
      auto w2 = [&](auto GP) __attribute__((always_inline)) {
        constexpr int gp = decltype(GP)::value;
        if constexpr (jb == 2 && gp == 0) {
          p0[0] = (p0[0] + p0[1]) + (p0[2] + p0[3]); p1[0] = (p1[0] + p1[1]) + (p1[2] + p1[3]);
          PIN2(p0[0], p1[0]);
        }
        if constexpr (gp < 12) work2(JB, GP);
        if constexpr (gp < dig(SPS, jb) && !((SPE >> jb) & 1)) spf(IC<SPB + acc_dig(SPS, jb) + gp>{});
      };
      constexpr int l0 = dig(LAGS, jb - 1), np = l0 + 12 - dig(LAGS, jb);
      auto slot = [&](auto I) __attribute__((always_inline)) {
        constexpr int i = decltype(I)::value;
        if constexpr (i < l0) piece(IC<jb + 1>{}, IC<12 - l0 + i>{});
        else if constexpr (i < np) piece(IC<jb + 2>{}, IC<i - l0>{});
        if constexpr (i < 12 || i < np) w2(I);
      };
#define SL4(i) slot(IC<i>{}); slot(IC<i + 1>{}); slot(IC<i + 2>{}); slot(IC<i + 3>{});
      SB();
      SL4(0) SL4(4) SL4(8) SL4(12) SL4(16) SL4(20)
      SB();
      if constexpr (jb == 2) {
#pragma unroll
        for (int jj = 0; jj < 4; ++jj) __builtin_amdgcn_raw_buffer_store_b128(tv[jj], rm2, vm2 + jj * 4096 + (2 * tt + 1) * 16384, 0, 16);
      }
      if constexpr ((FSM >> jb) & 1) fs = bld4_sc1(rf, abq[jb < 5 ? 1 : 2]);
#pragma unroll
      for (int i = acc_dig(GVS, jb); i < acc_dig(GVS, jb + 1); ++i) gv[i] = bld16_sc1(rgu, vgu, i * F_ * 16);
      touches(JB);
#pragma unroll
      for (int i = dn_first(jb); i < dn_first(jb) + dn_cnt(jb); ++i) dv[i] = bld16_nt(rdn, vdn, i * D_ * 2);
      if constexpr (jb == DG + 1) {
        if (pw < 2)
          __builtin_amdgcn_raw_buffer_store_b128((u32x4){__float_as_uint((p0[0] + p0[1]) + (p0[2] + p0[3])), tag, __float_as_uint((p1[0] + p1[1]) + (p1[2] + p1[3])), tag},
                                                 mk_rsrc(wsp + WS_DN), (((pw * 8 + cg8) * 32 + rgb) * 64 + lane) * 16, 0, 16);
      }
      if constexpr (jb == 2) {
        if (moe && pw < 2)
          __builtin_amdgcn_raw_buffer_store_b128((u32x4){__float_as_uint(p0[0]), tag, __float_as_uint(p1[0]), tag},
                                                 rgu, ((mat * 4 + dq) * F_ + cg * 128 + col) * 16, 0, 16);
      }
      SB();
#define SPX(i) spf(IC<(((i >= np && i >= 12) || ((SPE >> jb) & 1)) && i < dig(SPS, jb) ? SPB + acc_dig(SPS, jb) + i : -1)>{})
      if constexpr (dig(DGS, jb) > 0) {
        if (DGP != 3) __builtin_amdgcn_s_setprio(DGP);
#define DGU(i) if constexpr (i >= acc_dig(DGS, jb) && i < acc_dig(DGS, jb + 1)) work2(IC<100>{}, IC<i>{});
        DGU(0) DGU(1) DGU(2) DGU(3) DGU(4) DGU(5) DGU(6)
        if (DGP != 3) __builtin_amdgcn_s_setprio(3);
      }
      SPX(0); SPX(1); SPX(2); SPX(3); SPX(4); SPX(5); SPX(6); SPX(7); SPX(8); SPX(9); SPX(10); SPX(11); SPX(12); SPX(13); SPX(14);
      SB();
    };
    static_assert(DG + 1 <= 12 && DG >= GV_JB + 3 && dn_first(DG - 2) == 7 && acc_dig(GVS, GV_JB + 1) == 4 && dig(LAGS, 1) == 0 && dig(SPS, 1) == 0 && dig(LAGS, 0) == 0, "schedule");
    static_assert(DGS == 0 || (acc_dig(DGS, DG + 1) == 7 && acc_dig(DGS, GV_JB + 2) == 0 && acc_dig(DGS, 10) <= dn_first(7) && acc_dig(DGS, 11) <= dn_first(8) && acc_dig(DGS, 9) <= dn_first(6)), "units need their loads");
    static_assert(ALIAS || sp_tile((SPB + acc_dig(SPS, 14)) >> 4) < 0, "all tiles are prefetched");
    step2(IC<2>{}); step2(IC<3>{}); step2(IC<4>{}); step2(IC<5>{}); step2(IC<6>{}); step2(IC<7>{}); step2(IC<8>{}); step2(IC<9>{}); step2(IC<10>{}); step2(IC<11>{}); step2(IC<12>{}); step2(IC<13>{});
    lgkm0(sd);
    wait_vm<12 - dig(LAGS, 13)>(); BAR();
    {
      constexpr int l0 = dig(LAGS, 13);
#define PL(i) if constexpr (i >= 12 - l0) piece(IC<15>{}, IC<i>{});
      PL(0) PL(1) PL(2) PL(3) PL(4) PL(5) PL(6) PL(7) PL(8) PL(9) PL(10) PL(11)
    }
    __builtin_amdgcn_s_setprio(0);
    if (EPP) { WAIT_VM(0); BAR(); }
    epi_setup(0); epi_setup(1); epi_setup(2);
#pragma unroll
    for (int n = 0; n < 8; ++n) xq_load(n);
    if (!EPP) { WAIT_VM(8); BAR(); }
    epi_setup(3);
    wait_xq<0>(xq, sq);
    key[0] = __builtin_amdgcn_readlane(ksel, 0); key[1] = __builtin_amdgcn_readlane(ksel, 8);
    cv_calc();
#pragma unroll 1
    for (int c = 0; c < 2; ++c) {
      if (__builtin_expect(((c ? far1 : far0) & ~9) != 0, 1)) continue;
      const __amdgpu_buffer_rsrc_t rd = mk_rsrc(wsp + WS_DN + (size_t)(c * 8 * 32 + pw * 8) * 1024);
      const int ec = seg0(lane >> 5, (lane >> 4) & 1), dno = (ec >> 7) * 32768 + ((ec & 127) / 2 + (lane & 15)) * 16;
#pragma unroll 1
      for (int it = 0; it < SPIN_MAX; ++it) {
        if (__all(__builtin_amdgcn_raw_buffer_load_b32(rd, (ec >> 7) * 32768 + (lane & 7) * 1024 + 1020, 0, 16) == tag)) break;
        __builtin_amdgcn_s_sleep(8);
        asm volatile("" ::: "memory");
      }
      float s0, s1;
      unsigned bad = 1;
#pragma unroll 1
      for (int it = 0; !__all(bad == 0) && it < SPIN_MAX; ++it) {
        bad = 0; s0 = 0.f; s1 = 0.f;
#pragma unroll
        for (int i = 0; i < 8; ++i) {
          const u32x4 q = __builtin_amdgcn_raw_buffer_load_b128(rd, dno, i * 1024, 16);
          bad |= (q.y ^ tag) | (q.w ^ tag); s0 += __uint_as_float(q.x); s1 += __uint_as_float(q.z);
        }
        asm volatile("" ::: "memory");
      }
      *(f32x2*)(smem + LDS_FIN + (c * 4 + pw) * 512 + lane * 8) = (f32x2){s0, s1};
      asm volatile("s_waitcnt lgkmcnt(0)" ::: "memory");
      if (lane == 0) asm volatile("ds_add_u32 %0, %1" :: "v"(lds_addr(smem + LDS_SEL + 24 + c * 4)), "v"(1) : "memory");
    }
  } else {
  {
    const int q = j * 4 + wid;
    constexpr int Q1 = WS_MB, Q2 = Q1 + 8, Q3 = Q2 + 8, Q4 = Q3 + 7, Q5 = Q4 + 7, Q6 = Q5 + 7, Q7 = Q6 + 4, Q8 = Q7 + 4, QC = Q8 + 1;
    static_assert(QC <= 128, "warm-up slots");
    unsigned dmy = 0;
    if (q < QC) {
      const char* base; int b0;
      if (q < Q1) { base = wsp; b0 = 0; }
      else if (q < Q2) { base = (const char*)ws_re; b0 = Q1; }
      else if (q < Q3) { base = (const char*)ws_im; b0 = Q2; }
      else if (q < Q4) { base = (const char*)w_gate; b0 = Q3; }
      else if (q < Q5) { base = (const char*)w_up; b0 = Q4; }
      else if (q < Q6) { base = (const char*)w_down; b0 = Q5; }
      else if (q < Q7) { base = (const char*)out; b0 = Q6; }
      else if (q < Q8) { base = (const char*)x; b0 = Q7; }
      else { base = (const char*)w_r; b0 = Q8; }
      base += (size_t)(q - b0) << 20;
      asm volatile("s_load_dword %0, %1, 0" : "+s"(dmy) : "s"(base) : "memory");
    }
    if (wid == 0) { CALL_PG(1); CALL_PG(8); CALL_PG(5); }
    else if (wid == 1) { CALL_PG(2); CALL_PG(9); CALL_PG(6); }
    else if (wid == 2) { CALL_PG(3); CALL_PG(10); }
    else { CALL_PG(4); CALL_PG(11); }
    BAR();
    lgkm0(dmy);
  }
  PAGE(8);
  epi_setup(0); epi_setup(1); epi_setup(2); epi_setup(3);
  const int wr = wid >> 1, wc = wid & 1;
  const int sw = swz(fr), fl = lrw(fr);
  const int ko0 = ((0 + fq) ^ sw) * 16, ko1 = ((4 + fq) ^ sw) * 16;
  bf16x8 fm[2][4], fn[2][4];
  {
    const char* pm = smem + (wr * 64 + fl) * 128;
    const char* pn = smem + 16384 + (wc * 64 + fl) * 128;
    f32x4 acc[4][4];
#pragma unroll
    for (int i = 0; i < 4; ++i)
#pragma unroll
      for (int jn = 0; jn < 4; ++jn)
#pragma unroll
        for (int q = 0; q < 4; ++q) acc[i][jn][q] = 0.f;
#define TO(i) ((i) * 2048)
#define LDSP(T, a) ((__attribute__((address_space(3))) T*)(size_t)(a))
#define SLOTS(n) _Pragma("unroll") for (int q = 0; q < (n); ++q)
#define MFA(c, m, n) asm volatile("v_mfma_f32_16x16x32_bf16 %0, %2, %1, %0" : "+v"(c) : "v"(m), "v"(n))
#define MFA2(c, m, n, m2, n2) asm volatile("v_mfma_f32_16x16x32_bf16 %0, %2, %1, %0\n\tv_mfma_f32_16x16x32_bf16 %0, %4, %3, %0" : "+v"(c) : "v"(m), "v"(n), "v"(m2), "v"(n2))
    unsigned lm[2][2], ln[2][2];
#pragma unroll
    for (int u = 0; u < 2; ++u)
#pragma unroll
      for (int kk = 0; kk < 2; ++kk) {
        lm[u][kk] = lds_addr(pm) + u * 2 * G1_STAGE + (kk ? ko1 : ko0);
        ln[u][kk] = lds_addr(pn) + u * 2 * G1_STAGE + (kk ? ko1 : ko0);
        asm volatile("" : "+v"(lm[u][kk]), "+v"(ln[u][kk]));
      }
    auto ldm = [&](int stg, int i, int kk) __attribute__((always_inline)) { fm[kk][i] = *LDSP(const bf16x8, lm[stg >> 1][kk] + (stg & 1) * G1_STAGE + TO(i)); };
    auto ldn = [&](int stg, int jn, int kk) __attribute__((always_inline)) { fn[kk][jn] = *LDSP(const bf16x8, ln[stg >> 1][kk] + (stg & 1) * G1_STAGE + TO(jn)); };
    auto quad = [&](int a, int c, int d, int s) __attribute__((always_inline)) {
      const int i = (s < 2 || s >= 6) ? a : a + 1, jn = s < 4 ? c : d, kk = (s == 1 || s == 2 || s == 5 || s == 6);
      MFA(acc[i][jn], fm[kk][i], fn[kk][jn]);
    };
    auto quad2 = [&](int a, int c, int d, int s) __attribute__((always_inline)) {
      const int i = (s < 2 || s >= 6) ? a : a + 1, jn = s < 4 ? c : d, kk = (s == 2 || s == 6);
      MFA2(acc[i][jn], fm[kk][i], fn[kk][jn], fm[1 - kk][i], fn[1 - kk][jn]);
    };
    auto half1 = [&](int stg) __attribute__((always_inline)) {
      const int ja = (stg & 1) * 2, jb = 2 - ja;
      SLOTS(8) {
        quad(0, ja, ja + 1, q);
        if (q < 4) ldn(stg, jb + (q >> 1), q & 1); else ldm(stg, 2 + ((q - 4) >> 1), q == 5 || q == 6);
        SB();
      }
      SLOTS(4) { quad2(0, jb, jb + 1, 2 * q); SB(); }
    };
    auto half2 = [&](int stg) __attribute__((always_inline)) {
      const int ja = (stg & 1) * 2, jb = 2 - ja, nx = (stg + 1) & 3;
      SLOTS(8) {
        quad(2, jb + 1, jb, q);
        if (!(q & 1)) ldm(nx, q >> 2, q == 2 || q == 4);
        SB();
      }
      SLOTS(8) {
        quad(2, ja, ja + 1, q);
        if (!(q & 1)) ldn(nx, jb + (q >> 2), (q >> 1) & 1);
        SB();
      }
    };
    unsigned ae[7];
    {
      int l_ = lane;
      asm volatile("" : "+v"(l_));
      const int p = l_ >> 4, dn = sym_dn(p), hf = (p >> 1) ^ (dn & z);
#pragma unroll
      for (int u = 0; u < 7; ++u) {
        const int T = u < 3 ? wc * 4 : (wc * 4 + u - 3 - dn) & 7, i = u < 3 ? (u + 1) ^ ((u & 1) ? 0 : dn * 2) : 0;
        const int f = dn * 8 + ((T & 2) << 1) + ((T & 4) >> 1) + (T & 1);
        ae[u] = lds_addr(smem + G2_MRING) + (hf * 64 + i * 16 + lrw(f)) * 128 + (((wr * 4 + ((l_ >> 2) & 3)) ^ swz(f)) * 16) + (l_ & 3) * 4;
        asm volatile("" : "+v"(ae[u]));
      }
    }
    unsigned al = lds_addr(smem + LDS_MB) + (wr * 32 + fr * 2) * 4, ac = lds_addr(smem + LDS_MB) + (256 + wc * 64 + fq * 4) * 4;
    asm volatile("" : "+v"(al), "+v"(ac));
    SLOTS(16) asm volatile("" : "+v"(acc[q >> 2][q & 3]));
    SB();
    SLOTS(4) { ldm(0, q >> 1, q & 1); ldn(0, q >> 1, q & 1); }
    SB();
#pragma unroll 1
    for (int k4 = 0; k4 < G1_NKT - 4; k4 += 4) {
      half1(0); BAR_LGKM();
      SB(); half2(0);
      half1(1); BAR_LGKM();
      SB(); half2(1);
      half1(2); BAR_LGKM();
      SB(); half2(2);
      half1(3); BAR_LGKM();
      SB(); half2(3);
    }
    bf16x8 gm[2][4], gn[2][8];
    auto rdg = [&](int set, int t, int h, int r) __attribute__((always_inline)) {
      const int u = (t >> 1) & 1, o = (t & 1) * G1_STAGE;
      if (r == 0 || r == 2 || r == 4 || r == 5) {
        const int i = r >= 4, kk = r == 2 || r == 4;
        gm[set][kk * 2 + i] = *LDSP(const bf16x8, lm[u][kk] + o + h * 4096 + i * 2048);
      } else {
        const int jn = r < 4 ? 0 : (r - 4) >> 1, kk = r < 4 ? r >> 1 : r & 1;
        gn[set][kk * 4 + jn] = *LDSP(const bf16x8, ln[u][kk] + o + TO(jn));
      }
    };
    auto mmg = [&](int set, int h, int q) __attribute__((always_inline)) {
      const int jn = q >> 2, r = q & 3, i = (r >> 1) ^ (jn & 1), kk = r == 1 || r == 2;
      MFA(acc[h * 2 + i][jn], gm[set][kk * 2 + i], gn[set][kk * 4 + jn]);
    };
    f32x2 rsv, mrv;
    f32x4 cev[4];
    float tc[32];
    auto rds = [&](int h, int r) __attribute__((always_inline)) {
      if (r == 0) mrv = *LDSP(const f32x2, al + h * 256); else if (r == 1) rsv = *LDSP(const f32x2, al + 512 + h * 256);
    };
    auto prep = [&](int n) __attribute__((always_inline)) {
      tc[n] = llvm_fmul_legacy(mrv[n & 1], cev[n >> 3][(n >> 1) & 3]);
      asm volatile("" : "+v"(tc[n]));
    };
    auto epi = [&](int h, int q) __attribute__((always_inline)) {
      const int jn = q >> 1, r = (q & 1) * 2, o = m_off(h) + (jn & 1) * 128 + (jn >> 1) * lrw(4) * 128;
      asm volatile("v_fmac_f32 %0, %4, %8\n\tv_fmac_f32 %1, %5, %9\n\tv_fmac_f32 %2, %6, %8\n\tv_fmac_f32 %3, %7, %9"
                   : "+v"(tc[q * 4]), "+v"(tc[q * 4 + 1]), "+v"(tc[q * 4 + 2]), "+v"(tc[q * 4 + 3])
                   : "v"(acc[h * 2][jn][r]), "v"(acc[h * 2 + 1][jn][r]), "v"(acc[h * 2][jn][r + 1]), "v"(acc[h * 2 + 1][jn][r + 1]),
                     "v"(rsv[0]), "v"(rsv[1]));
      *LDSP(unsigned, r ? ae[1] + o : ae[3 + jn] + m_off(h)) = pk2(tc[q * 4], tc[q * 4 + 1]);
      *LDSP(unsigned, ae[r] + o) = pk2(tc[q * 4 + 2], tc[q * 4 + 3]);
    };
    SB();
    half1(0);
    BAR_LGKM();
    SB();
    SLOTS(16) {
      if (q < 8) quad(2, 3, 2, q); else quad(2, 0, 1, q - 8);
      if (q < 12) rdg(0, 13, 0, q);
      SB();
    }
    BAR();
    SB();
    SLOTS(16) {
      mmg(0, 0, q);
      if (q < 12) rdg(1, 14, 0, q);
      else { rds(0, q - 12); cev[q - 12] = *LDSP(const f32x4, ac + (q - 12) * 64); }
      SB();
    }
    PAGE(9);
    BAR();
    SB();
    SLOTS(16) { mmg(1, 0, q); if ((q & 3) < 3) rdg(0, 15, 0, (q >> 2) * 3 + (q & 3)); if (q >= 8) { prep(2 * q - 16); prep(2 * q - 15); } SB(); }
    SLOTS(16) { mmg(0, 0, q); if ((q & 3) < 3) rdg(1, 13, 1, (q >> 2) * 3 + (q & 3)); prep(16 + q); SB(); if (q >= 9 && (q & 1)) { epi(0, (q - 9) >> 1); SB(); } }
    SLOTS(8) { mmg(1, 1, q); if (q < 6) { rdg(0, 14, 1, 2 * q); rdg(0, 14, 1, 2 * q + 1); } SB(); if (q & 1) { epi(0, 4 + (q >> 1)); SB(); } }
    BAR_LGKM();
    SB();
    SLOTS(8) { mmg(1, 1, 8 + q); if (q < 4) rdg(1, 15, 1, q == 0 ? 1 : q == 1 ? 3 : q + 4); else rds(1, q - 4); SB(); }
    SLOTS(16) { mmg(0, 1, q); if (q < 8) rdg(1, 15, 1, q == 0 ? 0 : q == 1 ? 2 : q < 4 ? q + 2 : q + 4); prep(q); SB(); }
    asm volatile("s_waitcnt lgkmcnt(0)" ::: "memory");
    if (lane == 0) asm volatile("ds_add_u32 %0, %1" :: "v"(lds_addr(smem + LDS_SEL + 12)), "v"(1) : "memory");
    SB();
    SLOTS(16) {
      mmg(1, 1, q);
      if (q < 4) fn[0][q] = *(const bf16x8*)(smem + (wr * 128 + fl) * 128 + (q >> 1) * 2048 + ((q & 1) ? ko1 : ko0));
      else if (q < 8) fm[q & 1][(q - 4) >> 1] = *(const bf16x8*)(smem + G2_MRING + m_off(0) + (wc * 64 + fl) * 128 + TO((q - 4) >> 1) + ((q & 1) ? ko1 : ko0));
      prep(16 + q);
      SB();
    }
    {
      lds_vi rel = (lds_vi)(smem + LDS_SEL + 12);
      int it = 0;
      while (rel[0] != 4 && it < SPIN_MAX * 8) ++it;
      asm volatile("" ::: "memory");
    }
    asm volatile("s_nop 7");
    SB();
    SLOTS(8) { epi(1, q); SB(); }
  }

  const char* pn = smem + (wr * 128 + fl) * 128;
  const char* pm = smem + G2_MRING + (wc * 64 + fl) * 128;
  f32x4 acc[8][4];
  auto mm = [&](bool imaj, int set, int q2, int s, int mode) __attribute__((always_inline)) {
    int i, jj, kk;
    if (imaj) { i = s >> 2; const int r = s & 3; jj = (r >> 1) ^ (i & 1); kk = (r == 1 || r == 2); }
    else { jj = s >> 3; const int r = s & 7; i = jj ? 3 - (r >> 1) : (r >> 1); kk = ((r + 1) >> 1) & 1; }
    if (mode == 1 && !(s & 1)) asm volatile("v_mfma_f32_16x16x32_bf16 %0, %2, %1, 0" : "=&v"(acc[q2 * 2 + jj][i]) : "v"(fm[kk][i]), "v"(fn[set][jj * 2 + kk]));
    else if (mode) asm volatile("v_mfma_f32_16x16x32_bf16 %0, %2, %1, %0" : "+v"(acc[q2 * 2 + jj][i]) : "v"(fm[kk][i]), "v"(fn[set][jj * 2 + kk]));
    else acc[q2 * 2 + jj][i] = __builtin_amdgcn_mfma_f32_16x16x32_bf16(fn[set][jj * 2 + kk], fm[kk][i], acc[q2 * 2 + jj][i], 0, 0, 0);
  };
  unsigned bn[2], bm[2], an[2], am[2];
#pragma unroll
  for (int kk = 0; kk < 2; ++kk) {
    bn[kk] = lds_addr(pn) + (kk ? ko1 : ko0); bm[kk] = lds_addr(pm) + (kk ? ko1 : ko0);
    asm volatile("" : "+v"(bn[kk]), "+v"(bm[kk]));
    an[kk] = bn[kk]; am[kk] = bm[kk] + m_off(0);
  }
#define RDN(jt, kk, o) (*LDSP(const bf16x8, an[kk] + (o) + (jt) * 2048))
#define RDM(i, kk, o) (*LDSP(const bf16x8, am[kk] + (o) + TO(i)))
  PAGE(10);
  tag = __builtin_amdgcn_readfirstlane(((const unsigned*)(smem + LDS_SEL))[5]);
  {
    int son = 0;
    auto tile = [&](int kt, int zero) __attribute__((always_inline)) {
      SLOTS(16) {
        mm(true, 0, 0, q, zero);
        if (!(q & 1)) {
          if (q < 8) fm[(q >> 1) & 1][2 + (q >> 2)] = RDM(2 + (q >> 2), (q >> 1) & 1, 0);
          else { const int r = (q - 8) >> 1; fn[1][r] = RDN(2 + (r >> 1), r & 1, 0); }
        }
        SB();
      }
      SLOTS(16) {
        mm(false, 1, 1, q, zero);
        if (!(q & 1) && q < 8) fn[0][q >> 1] = RDN(4 + (q >> 2), (q >> 1) & 1, 0);
        SB();
      }
      const int som = son >> 1;
      son = son == 65536 ? 0 : son + 32768;
      SLOTS(16) {
        mm(false, 0, 2, q, zero);
        if (!(q & 1) && q < 8) fn[1][q >> 1] = RDN(6 + (q >> 2), (q >> 1) & 1, 0);
        if (q == 9 || q == 11) { an[q == 11] = bn[q == 11] + son; asm volatile("" : "+v"(an[q == 11])); }
        if (q == 13 || q == 15) { am[q == 15] = bm[q == 15] + som; asm volatile("" : "+v"(am[q == 15])); }
        SB();
      }
      BAR_LGKM();
      SB();
      SLOTS(16) {
        mm(true, 1, 3, q, zero);
        if (!(q & 1)) {
          if (q < 8) fn[0][q >> 1] = RDN(q >> 2, (q >> 1) & 1, 0);
          else fm[(q >> 1) & 1][(q - 8) >> 2] = RDM((q - 8) >> 2, (q >> 1) & 1, 0);
        }
        SB();
      }
    };
    SB();
    tile(0, 1);
#pragma unroll 1
    for (int kt = 1; kt < G2_NKT - 3; ++kt) tile(kt, 0);
  }
  unsigned fx;
  int fo = lane == 0 ? kq * 512 : (int)0x80000000;
  {
    constexpr int NG = 12;
    static constexpr int g_t[NG] = {0, 0, 1, 1, 2, 2, 2, 1, 0, 0, 1, 2};
    static constexpr int g_q[NG] = {0, 1, 0, 1, 0, 1, 2, 2, 2, 3, 3, 3};
    constexpr int G14 = 1, G15 = 3, GP0 = 5, GP1 = 6;
    constexpr auto chp = [](int g) { return g == 0 || g_t[g] != g_t[g - 1]; };
    constexpr auto chn = [](int g) { return g + 1 < NG && g_t[g + 1] != g_t[g]; };
    static_assert(n_off(13) == 32768 && n_off(14) == 65536 && n_off(15) == 0, "");
    unsigned an2[2];
#pragma unroll
    for (int kk = 0; kk < 2; ++kk) {
      an[kk] = bn[kk]; an2[kk] = bn[kk] + 65536; am[kk] = bm[kk];
      asm volatile("" : "+v"(an[kk]), "+v"(an2[kk]), "+v"(am[kk]));
    }
    int vo;
    u32x4 pk;
    u32x2 kv;
    auto publish = [&](int d, int h) __attribute__((always_inline)) {
      if (h == 0) { vo = ml; PINV(vo); vo += (((d + kq + 1) & 3) * 4 + kq) * 4 * MSG; PINV(vo); }
      const f32x4 v = acc[2 * d + (h >> 2)][h & 3];
      pk[(h & 1) * 2] = pk2(v[0], v[1]); pk[(h & 1) * 2 + 1] = pk2(v[2], v[3]);
      if (h & 1) __builtin_amdgcn_raw_buffer_store_b128(pk, rs, vo + (h >> 1) * 1024, 0, 16);
    };
#define MSG_FLAG(d) __builtin_amdgcn_raw_buffer_store_b32(tag, rc, fo + (((d) + kq + 1) & 3) * 2048, 0, 16)
    auto rdn = [&](int g, int r) __attribute__((always_inline)) {
      const int t = g_t[g], jt = g_q[g] * 2 + (r >> 1);
      fn[g & 1][r] = *LDSP(const bf16x8, (t == 1 ? an2[r & 1] : an[r & 1]) + (t == 0 ? 32768 : 0) + jt * 2048);
    };
    auto rdm = [&](int t, int i, int kk) __attribute__((always_inline)) { fm[kk][i] = *LDSP(const bf16x8, am[kk] + m_off(13 + t) + TO(i)); };
    auto group = [&](auto GI) __attribute__((always_inline)) {
      constexpr int gi = decltype(GI)::value;
      if (gi == G14) { BAR();
        SB(); }
      if (gi == G15) {
        PAGE(11);
        BAR();
        SB();
        kv = *(const u32x2*)(smem + LDS_SEL);
        SB();
      }
#pragma unroll
      for (int q = 0; q < 16; ++q) {
        const int n = gi * 16 + q;
        if (n == 112) {
        }
        if (n == 152) {
        }
        if (n == FL0) { WAIT_VM(8); MSG_FLAG(0); SB(); }
        if (n == FL1) { WAIT_VM(5); MSG_FLAG(1); SB(); }
        if (n == FL2) { WAIT_VM(2); MSG_FLAG(2); fx = bld4_sc1(rc, po); SB(); }
        mm(chp(gi) || chn(gi), gi & 1, g_q[gi], q, 2);
        constexpr bool ni = gi + 1 < NG && (chp(gi + 1 < NG ? gi + 1 : gi) || chn(gi + 1 < NG ? gi + 1 : gi));
        auto nr = [](int r) { return ni && r >= 2 ? 5 - r : r; };
        if constexpr (chp(gi) && chn(gi)) {
          if (q < 4) rdm(g_t[gi], 2 + (q >> 1), q & 1); else if (q < 8) rdn(gi + 1, nr(q - 4)); else if (q < 12) rdm(g_t[gi + 1], (q - 8) >> 1, q & 1);
        } else if constexpr (chp(gi)) {
          if (q < 4) rdm(g_t[gi], 2 + (q >> 1), q & 1);
          else if (gi + 1 < NG && q < 12 && !(q & 1)) rdn(gi + 1, nr((q - 4) >> 1));
        } else if constexpr (chn(gi)) {
          if (!(q & 1)) { if (q < 8) rdn(gi + 1, nr(q >> 1)); else rdm(g_t[gi + 1], (q - 8) >> 2, (q >> 1) & 1); }
        } else {
          if (!(q & 1) && q < 8) rdn(gi + 1, nr(q >> 1));
        }
        if (gi == GP0 && q < 8) publish(0, q);
        if (gi == GP1 && q < 8) publish(1, q);
        if (gi == 9 && q < 8) publish(2, q);
        if (gi == G15 && (q & 1)) xq_load(q >> 1);
        if (n == 72) { key[0] = __builtin_amdgcn_readfirstlane(kv.x); key[1] = __builtin_amdgcn_readfirstlane(kv.y); }
        SB();
      }
    };
#define GR4(i) group(IC<i>{}); group(IC<i + 1>{}); group(IC<i + 2>{}); group(IC<i + 3>{});
    GR4(0) GR4(4) GR4(8)
    asm volatile("s_nop 7");
    SB();
  }
#pragma unroll
  for (int i = 0; i < 4; ++i) *(f32x4*)(smem + LDS_ACC + wr * 16384 + wc * 4096 + i * 1024 + lane * 16) = acc[7][i];
#pragma unroll
  for (int i = 0; i < 4; ++i) ya[i] = acc[6][i];
  asm volatile("s_waitcnt lgkmcnt(0)" ::: "memory");
  if (lane == 0) sig[0] = 2;
  SB();
  if (FL0 >= 192) { WAIT_VM(8); MSG_FLAG(0); SB(); }
  if (FL1 >= 192) { WAIT_VM(5); MSG_FLAG(1); SB(); }
  if (FL2 >= 192) { WAIT_VM(2); MSG_FLAG(2); fx = bld4_sc1(rc, po); SB(); }
  wait_xq<4>(xq, sq);
  cv_calc();
  asm volatile("s_waitcnt vmcnt(0)" : "+v"(fx) :: "memory");
  seen = (unsigned)__ballot(fx == tag);
  }

  PAGE(6);
  if (__builtin_expect(far0 == 0 || far1 == 0, 0)) {
#pragma unroll 1
    for (int c = 0; c < 2; ++c) {
      if (c ? far1 : far0) continue;
      const int ic = (0xffff - (c ? key[1] : key[0])) & (S_ - 1);
      const float sg = rbf(1.f / (1.f + __expf(-key_score(c ? key[1] : key[0]))));
      lds_vi cnt = (lds_vi)(smem + LDS_SEL + 24 + c * 4);
      int it = 0;
      while (cnt[0] != 4 && it < SPIN_MAX * 8) { __builtin_amdgcn_s_sleep(1); ++it; }
      asm volatile("" ::: "memory");
      const float* q = (const float*)(smem + LDS_FIN + c * 2048) + (wid & 1) * 64 + (fr >> 3) * 32 + col4(fr);
      const f32x4 v = (*(const f32x4*)q + *(const f32x4*)(q + 128)) + (*(const f32x4*)(q + 256) + *(const f32x4*)(q + 384));
#pragma unroll
      for (int r = 0; r < 4; ++r)
#pragma unroll
        for (int k = 0; k < 4; ++k) {
          cv[r * 4 + k] += (xo[r] >> 11) == ic ? v[k] * sg : 0.f;
        }
    }
  }
  if (ha) {
    int it = 0;
    while (sig[0] != 2 && it < SPIN_MAX * 8) { __builtin_amdgcn_s_sleep(1); ++it; }
    asm volatile("" ::: "memory");
#pragma unroll
    for (int i = 0; i < 4; ++i) ya[i] = *(const f32x4*)(smem + LDS_ACC + (wid & 2) * 8192 + (wid & 1) * 4096 + i * 1024 + lane * 16);
  }
  u32x4 pp[3][2];
  unsigned e_lo = 0x00003f80u, e_hi = 0x3f800000u;
  asm volatile("" : "+v"(e_lo), "+v"(e_hi));
  __amdgpu_buffer_rsrc_t rs_ = rs, rc_ = rc;
  int mo[3] = {MSG_IDX(0) * 4 * MSG, MSG_IDX(1) * 4 * MSG, MSG_IDX(2) * 4 * MSG}, mv = ml + ha * 2048, mv2 = mv + 1024;
  asm volatile("" : "+s"(rs_), "+s"(rc_), "+s"(mo[0]), "+s"(mo[1]), "+s"(mo[2]));
  asm volatile("" : "+v"(mv), "+v"(mv2));
#define MSG_POLL(m) do { \
    if (!((seen >> (m)) & 1)) { \
      unsigned pv_, it_ = SPIN_MAX; \
      asm volatile("1:\n\tbuffer_load_dword %0, %3, %4, 0 offen sc1\n\ts_waitcnt vmcnt(0)\n\tv_cmp_eq_u32 vcc, %5, %0\n\ts_mov_b32 %1, vcc_lo\n\t" \
                   "s_bitcmp1_b32 %1, %6\n\ts_cbranch_scc1 2f\n\ts_sub_u32 %2, %2, 1\n\ts_cbranch_scc1 2f\n\ts_sleep 1\n\ts_branch 1b\n2:" \
                   : "=&v"(pv_), "+s"(seen), "+s"(it_) : "v"(po), "s"(rc_), "s"(tag), "i"(m) : "vcc", "scc", "memory"); \
    } \
    pp[m][0] = __builtin_amdgcn_raw_buffer_load_b128(rs_, mv, mo[m], 16); \
    pp[m][1] = __builtin_amdgcn_raw_buffer_load_b128(rs_, mv2, mo[m], 16); } while (0)
#define MSG_ADD(m) do { \
    _Pragma("unroll") for (int u = 0; u < 8; ++u) { \
      ya[u >> 1][(u & 1) * 2] = add_bf(pp[m][u >> 2][u & 3], e_lo, ya[u >> 1][(u & 1) * 2]); ya[u >> 1][(u & 1) * 2 + 1] = add_bf(pp[m][u >> 2][u & 3], e_hi, ya[u >> 1][(u & 1) * 2 + 1]); } } while (0)
  MSG_POLL(2); MSG_POLL(1); MSG_POLL(0);
  SB();
#pragma unroll
  for (int e = 0; e < 16; ++e) ya[e & 3][e >> 2] += cv[e];
  asm volatile("" : "+v"(ya[0]), "+v"(ya[1]), "+v"(ya[2]), "+v"(ya[3]));
  SB();
  MSG_ADD(2); MSG_ADD(1);
  asm volatile("" : "+v"(ya[0]), "+v"(ya[1]), "+v"(ya[2]), "+v"(ya[3]));
  SB();
  MSG_ADD(0);
  {
#pragma unroll
    for (int r = 0; r < 4; ++r) *(u32x2*)((char*)out + (unsigned)xo[r]) = (u32x2){pk2(ya[0][r], ya[1][r]), pk2(ya[2][r], ya[3][r])};
  }
}

extern "C" void kernel_launch(void* const* d_in, const int* in_sizes, int n_in, void* d_out, int out_size,
                              void* d_ws, size_t ws_size, hipStream_t stream) {
  const u16* x = (const u16*)d_in[0];
  const u16* ws_re = (const u16*)d_in[1];
  const u16* ws_im = (const u16*)d_in[2];
  const u16* wd_re = (const u16*)d_in[3];
  const u16* wd_im = (const u16*)d_in[4];
  const u16* w_r = (const u16*)d_in[6];
  const u16* w_gate = (const u16*)d_in[7];
  const u16* w_up = (const u16*)d_in[8];
  const u16* w_down = (const u16*)d_in[9];
  k_main<<<256, 512, 0, stream>>>(x, wd_re, wd_im, w_r, (char*)d_ws, ws_re, ws_im, w_gate, w_up, w_down, (u16*)d_out);
}
```

```cpp
#include <hip/hip_runtime.h>
#include <hip/hip_bf16.h>
#include <stdint.h>
#include <type_traits>

extern "C" __device__ unsigned long long llvm_dispatch_id(void) __asm("llvm.amdgcn.dispatch.id");
extern "C" __device__ float llvm_fmul_legacy(float, float) __asm("llvm.amdgcn.fmul.legacy");
typedef unsigned short u16;
typedef unsigned long long u64;
typedef __attribute__((ext_vector_type(8))) short bf16x8;
typedef __attribute__((ext_vector_type(4))) unsigned u32x4;
typedef __attribute__((ext_vector_type(2))) unsigned u32x2;
typedef __attribute__((ext_vector_type(4))) float f32x4;
typedef __attribute__((ext_vector_type(2))) float f32x2;
typedef __attribute__((ext_vector_type(2))) __bf16 bf16x2;
typedef __attribute__((address_space(3))) void* lds_vp;
typedef volatile __attribute__((address_space(3))) int* lds_vi;

constexpr int S_ = 2048, D_ = 1024, F_ = 3584;
template <int N> using IC = std::integral_constant<int, N>;

static __device__ __forceinline__ float bf2f(u16 u) { return __uint_as_float((unsigned)u << 16); }
static __device__ __forceinline__ u16 f2bf(float f) {
  __hip_bfloat16 b = __float2bfloat16(f);
  return *reinterpret_cast<u16*>(&b);
}
static __device__ __forceinline__ float rbf(float f) { return bf2f(f2bf(f)); }
static __device__ __forceinline__ float lo_bf(unsigned u) { return __uint_as_float(u << 16); }
static __device__ __forceinline__ float hi_bf(unsigned u) { return __uint_as_float(u & 0xffff0000u); }
static __device__ __forceinline__ unsigned pk2(float a, float b) {
  const bf16x2 v = __builtin_convertvector((f32x2){a, b}, bf16x2);
  return __builtin_bit_cast(unsigned, v);
}
static __device__ __forceinline__ float add_bf(unsigned u, unsigned e, float c) {
  return __builtin_amdgcn_fdot2_f32_bf16(__builtin_bit_cast(bf16x2, u), __builtin_bit_cast(bf16x2, e), c, false);
}
static __device__ __forceinline__ float ln_elem(float x, float mb, float sd, float ri) {
  const float d = rbf(x - mb);
  const float q0 = d * ri;
  return rbf(__builtin_fmaf(__builtin_fmaf(-q0, sd, d), ri, q0));
}
template <int CTRL> static __device__ __forceinline__ float dpp_f(float v) {
  return __int_as_float(__builtin_amdgcn_update_dpp(0, __float_as_int(v), CTRL, 0xf, 0xf, true));
}
template <int CTRL> static __device__ __forceinline__ unsigned dpp_u(unsigned v) {
  return (unsigned)__builtin_amdgcn_update_dpp(0, (int)v, CTRL, 0xf, 0xf, true);
}
static __device__ __forceinline__ float row_sum(float v) {
  v += dpp_f<0xB1>(v);
  v += dpp_f<0x4E>(v);
  v += dpp_f<0x141>(v);
  v += dpp_f<0x140>(v);
  return v;
}
static __device__ __forceinline__ float fold32(float a, float b) {
  const auto r = __builtin_amdgcn_permlane32_swap(__float_as_uint(a), __float_as_uint(b), false, false);
  return __uint_as_float(r[0]) + __uint_as_float(r[1]);
}
static __device__ __forceinline__ float fold16(float a, float b) {
  const auto r = __builtin_amdgcn_permlane16_swap(__float_as_uint(a), __float_as_uint(b), false, false);
  return __uint_as_float(r[0]) + __uint_as_float(r[1]);
}
static __device__ __forceinline__ float rdl(float v, int l) { return __int_as_float(__builtin_amdgcn_readlane(__float_as_int(v), l)); }

#define XMAP(g) (((~g & 1) << 2) | (g & 2) | ((g >> 2) & 1))
#define ST_JB 9
#define WSN 10
#define FSM 0xa2
#define GV_JB 7
#define DG 11
#define AB_STRIDE 32
#define SPIN_MAX (1 << 17)
#define FL0 168
#define FL1 192
#define FL2 192
#define VA0 5
#define VAN 4
#define VAP 116
#define VB0 12
#define VBN 0
#define VBP 6
#define SP0 3
#define SP1 7
#define SP1E 11
#define VC0 14
#define VCN 0
#define VCP 9
#define SPS 0x000cccccdddd00
#define SPE 0
#define PRIO2 3
#define N2A 8
#define N3A 8
#define WSL 2
#define LNP 0
#define LNS 2
#define EPP 0
#define DGS 0
#define DGP 0
#define TS1 112
#define TS2 152
#define XL0 33
#define XLS 4
#define CPRI 0
#define LAGS 0x00000000000000
#define DVS 0x00000400300000
#define GVS 0x00000022000000
constexpr int dig(unsigned long long t, int q) { return q < 0 ? 0 : (int)((t >> (4 * q)) & 15); }
constexpr int acc_dig(unsigned long long t, int q) { int a = 0; for (int i = 0; i < q; ++i) a += dig(t, i); return a; }

static __device__ __forceinline__ unsigned lds_addr(const void* p) { return (unsigned)(size_t)(lds_vp)p; }

constexpr int MSG = 4096;
constexpr size_t WS_ABT   = 0;
constexpr size_t WS_SLAB  = WS_ABT + (size_t)D_ * 4096 * 2;
constexpr size_t WS_GU    = WS_SLAB + (size_t)64 * 64 * MSG;
constexpr size_t WS_DN    = WS_GU + (size_t)8 * F_ * 16;
constexpr size_t WS_FLAGS = WS_DN + (size_t)2 * 8 * 32 * 64 * 16;
constexpr int FL_X = 0;
constexpr int FL_AB = 64 * 64 * 32;
constexpr int FL_ST = FL_AB + 2048 * AB_STRIDE;
constexpr int FL_CE = FL_ST + 4096;
constexpr int FL_KEY = FL_CE + 4096;
constexpr int FL_EPOCH = FL_KEY + 4096;
constexpr int WS_MB = (int)((WS_FLAGS + (FL_EPOCH + 1) * 4 + 1048575) >> 20);

#define WAIT_VM(n) asm volatile("s_waitcnt vmcnt(" #n ")" ::: "memory")
template <int N> static __device__ __forceinline__ void wait_vm() { asm volatile("s_waitcnt vmcnt(%0)" :: "i"(N) : "memory"); }
#define BAR_LGKM() asm volatile("s_waitcnt lgkmcnt(0)\n\ts_barrier" ::: "memory")
#define BAR() asm volatile("s_barrier" ::: "memory")
#define SB() __builtin_amdgcn_sched_barrier(0)
#define PAGE(n) asm volatile("s_branch 2f\n\t.p2align 12\nk_pg" #n ":\n\ts_setpc_b64 s[42:43]\n2:")
#define CALL_PG(n) asm volatile("s_getpc_b64 s[40:41]\n3:\n\ts_mov_b32 s42, k_pg" #n "-3b\n\ts_ashr_i32 s43, s42, 31\n\ts_add_u32 s40, s40, s42\n\ts_addc_u32 s41, s41, s43\n\t" \
                                "s_swappc_b64 s[42:43], s[40:41]" ::: "s40", "s41", "s42", "s43", "scc", "memory")

static __device__ __forceinline__ __amdgpu_buffer_rsrc_t mk_rsrc(const void* p) {
  return __builtin_amdgcn_make_buffer_rsrc((void*)p, 0, 0x7fffffff, 0x00020000);
}
#define DMA_(rsrc, voff, mbase, MI, sbase, SI, pol) do { unsigned _t; \
    asm volatile("s_add_i32 m0, %2, %3\n\ts_add_i32 %0, %5, %6\n\tbuffer_load_dwordx4 %1, %4, %0 offen" pol " lds" \
                 : "=&s"(_t) : "v"(voff), "s"(mbase), "i"(MI), "s"(rsrc), "s"(sbase), "i"(SI) : "memory", "scc"); } while (0)
#define DMA_I(rsrc, voff, mbase, MI, SI) \
    asm volatile("s_add_i32 m0, %1, %2\n\ts_nop 0\n\tbuffer_load_dwordx4 %0, %3, 0 offen offset:%4 lds" \
                 :: "v"(voff), "s"(mbase), "i"((MI) - (SI)), "s"(rsrc), "i"(SI) : "memory", "scc")
#define DMA_S(rsrc, voff, mbase, MI, soff) \
    asm volatile("s_add_i32 m0, %1, %2\n\ts_nop 0\n\tbuffer_load_dwordx4 %0, %3, %4 offen lds" \
                 :: "v"(voff), "s"(mbase), "i"(MI), "s"(rsrc), "s"(soff) : "memory", "scc")
#define TPOL ""
#define TOUCH(rsrc, voff, mbase, MI, soff) \
    asm volatile("s_add_i32 m0, %1, %2\n\ts_nop 0\n\tbuffer_load_dword %0, %3, %4 offen" TPOL " lds" \
                 :: "v"(voff), "s"(mbase), "i"(MI), "s"(rsrc), "s"(soff) : "memory", "scc")
template <int OFF> static __device__ __forceinline__ u32x4 ld16_s(const void* p, int voff) {
  u32x4 v;
  asm volatile("global_load_dwordx4 %0, %1, %2 offset:%3" : "=v"(v) : "v"(voff), "s"(p), "i"(OFF) : "memory");
  return v;
}
template <int OFF> static __device__ __forceinline__ void sld(unsigned& sink, const void* p, int soff) {
  asm volatile("s_load_dword %0, %1, %2 offset:%3" : "+s"(sink) : "s"(p), "s"(soff), "i"(OFF) : "memory");
}
static __device__ __forceinline__ unsigned gld4_s(const void* p, unsigned voff) {
  unsigned v;
  asm volatile("global_load_dword %0, %1, %2" : "=v"(v) : "v"(voff), "s"(p) : "memory");
  return v;
}
static __device__ __forceinline__ u32x2 bld8_sc1(__amdgpu_buffer_rsrc_t r, unsigned voff) {
  u32x2 v;
  asm volatile("buffer_load_dwordx2 %0, %1, %2, 0 offen sc1" : "=v"(v) : "v"(voff), "s"(r) : "memory");
  return v;
}
template <int N> static __device__ __forceinline__ void wait_xs(unsigned& a, u32x2& b) { asm volatile("s_waitcnt vmcnt(%2)" : "+v"(a), "+v"(b) : "i"(N) : "memory"); }
template <int N> static __device__ __forceinline__ void wait_xq(u32x4 (&a)[2], u32x2& b) { asm volatile("s_waitcnt vmcnt(%3)" : "+v"(a[0]), "+v"(a[1]), "+v"(b) : "i"(N) : "memory"); }
static __device__ __forceinline__ u32x2 ld8_s_sc1(const void* p, int voff) {
  u32x2 v;
  asm volatile("global_load_dwordx2 %0, %1, %2 sc1" : "=v"(v) : "v"(voff), "s"(p) : "memory");
  return v;
}
static __device__ __forceinline__ void lgkm0(unsigned& sink) { asm volatile("s_waitcnt lgkmcnt(0)" : "+s"(sink) :: "memory"); }
template <typename T> static __device__ __forceinline__ void pin4(T& a, T& b, T& c, T& d) { asm volatile("" : "+s"(a), "+s"(b), "+s"(c), "+s"(d)); }
template <typename T, typename U> static __device__ __forceinline__ void pins(T& a, U& b) { asm volatile("" : "+s"(a), "+s"(b)); }
static __device__ __forceinline__ void launder(int& v, int& a, int& b) { asm volatile("" : "+v"(v)); asm volatile("" : "+s"(a), "+s"(b)); }
static __device__ __forceinline__ unsigned bld4_sc1(__amdgpu_buffer_rsrc_t r, int voff) {
  unsigned v;
  asm volatile("buffer_load_dword %0, %1, %2, 0 offen sc1" : "=v"(v) : "v"(voff), "s"(r) : "memory");
  return v;
}
static __device__ __forceinline__ u32x4 bld16_nt(__amdgpu_buffer_rsrc_t r, int voff, int soff) {
  u32x4 v;
  asm volatile("buffer_load_dwordx4 %0, %1, %2, %3 offen nt" : "=v"(v) : "v"(voff), "s"(r), "s"(soff) : "memory");
  return v;
}
static __device__ __forceinline__ void bld16_nt_io(u32x4& v, __amdgpu_buffer_rsrc_t r, int voff, int soff) {
  asm volatile("buffer_load_dwordx4 %0, %1, %2, %3 offen nt" : "+v"(v) : "v"(voff), "s"(r), "s"(soff) : "memory");
}
static __device__ __forceinline__ u32x4 bld16_sc1(__amdgpu_buffer_rsrc_t r, int voff, int soff) {
  u32x4 v;
  asm volatile("buffer_load_dwordx4 %0, %1, %2, %3 offen sc1" : "=v"(v) : "v"(voff), "s"(r), "s"(soff) : "memory");
  return v;
}
static __device__ __forceinline__ u32x2 granule_wait(const u64* p, unsigned tag) {
  u64 v = 0;
#pragma unroll 1
  for (int it = 0; it < SPIN_MAX; ++it) {
    v = __hip_atomic_load(p, __ATOMIC_RELAXED, __HIP_MEMORY_SCOPE_AGENT);
    if (__all((unsigned)(v >> 32) == tag)) break;
    __builtin_amdgcn_s_sleep(2);
  }
  return (u32x2){(unsigned)v, (unsigned)(v >> 32)};
}
static __device__ __forceinline__ float key_score(unsigned key) {
  const unsigned u = key >> 16;
  return bf2f((u16)((u & 0x8000u) ? (u & 0x7fffu) : (~u & 0xffffu)));
}
static __device__ __forceinline__ unsigned med3u(unsigned a, unsigned b, unsigned c) { return max(min(a, b), min(max(a, b), c)); }
static __device__ __forceinline__ void merge2(unsigned& a1, unsigned& a2, unsigned b1, unsigned b2) {
  const unsigned lo = min(a1, b1);
  a1 = max(a1, b1);
  a2 = max(lo, max(a2, b2));
}
static __device__ __forceinline__ float sqdev16(const u32x4 (&v)[2], float mb) {
  f32x2 q2 = {0.f, 0.f};
#pragma unroll
  for (int i = 0; i < 8; ++i) {
    const f32x2 e = (f32x2){lo_bf(v[i >> 2][i & 3]), hi_bf(v[i >> 2][i & 3])} - mb;
    q2 += e * e;
  }
  return q2.x + q2.y;
}

constexpr int lrw(int r) { return (r & ~6) | ((r & 2) << 1) | ((r & 4) >> 1); }
constexpr int swz(int r) { return ((r >> 1) & 4) | (r & 2); }
static __device__ __forceinline__ int sym_dn(int p) { return (p ^ (p >> 1)) & 1; }
static __device__ __forceinline__ int sym_hi(int n, int w, int st, int b, int z, int p) {
  return ((p >> 1) * (n / 2 * st) + b * (w * st)) ^ (-sym_dn(p) & (((n - 1) ^ (z * (n / 2))) * st));
}
static __device__ __forceinline__ int sym_lo(int w, int st, int p, int c) { return ((c - sym_dn(p)) * st) & ((w - 1) * st); }
static __device__ __forceinline__ int sym_row(int n, int w, int st, int b, int z, int p, int c) { return sym_lo(w, st, p, c) ^ sym_hi(n, w, st, b, z, p); }
constexpr int G1_NST = 4, G1_NKT = 16, G1_STAGE = 32768;
constexpr int G2_NKT = 16, G2_MRING = 98304;
constexpr int n_off(int k) { return (k % 3) * 32768; }
constexpr int m_off(int k) { return ((k + 2) % 3) * 16384; }
constexpr int k_ord(int k) { return k < 2 ? k : (k < 9 ? 2 * (k - 1) : 2 * (k - 8) + 1); }
constexpr int LDS_DUMMY = G2_MRING + 49152;
constexpr int LDS_GU = LDS_DUMMY + 4096;
constexpr int LDS_FIN = LDS_GU + 4096;
constexpr int LDS_MB = LDS_FIN + 4096;
constexpr int LDS_SEL = LDS_MB + 1536;
constexpr int LDS_SIG = LDS_SEL + 64;
constexpr int LDS_KEY = LDS_SIG + 32;
constexpr int LDS_BYTES = LDS_KEY + 128;
constexpr int LDS_ACC = n_off(G2_NKT - 3);

constexpr int sp_tile(int q) {
  for (int k = 0; k < 16; ++k) {
    if ((k >= VA0 && k < VA0 + VAN) || (k >= VB0 && k < VB0 + VBN) || (k >= VC0 && k < VC0 + VCN)) continue;
    if (q-- == 0) return k;
  }
  return -1;
}
constexpr int tcnt(int q) { return (VAN > 0 && q == VAP) + (VBN > 0 && q == VBP) + (VCN > 0 && q == VCP); }
constexpr int dn_cnt(int q) { return dig(DVS, q); }
constexpr int dn_first(int q) { return acc_dig(DVS, q); }

static __device__ __forceinline__ void code_touch(unsigned& sink, unsigned off) {
  asm volatile("s_getpc_b64 s[40:41]\n\tk_main_rt:\n\ts_min_u32 %1, %1, k_main_end-k_main_rt-196\n\t"
               "s_load_dword %0, s[40:41], %1 offset:0\n\ts_load_dword %0, s[40:41], %1 offset:64\n\ts_load_dword %0, s[40:41], %1 offset:128\n\ts_load_dword %0, s[40:41], %1 offset:192"
               : "+s"(sink), "+s"(off) :: "s40", "s41", "scc", "memory");
}
static __device__ __forceinline__ void touch1(__amdgpu_buffer_rsrc_t r, int voff, unsigned mb, int soff) { TOUCH(r, voff, mb, LDS_DUMMY, soff); }
typedef const __attribute__((address_space(1))) u16* gptr;
typedef __attribute__((ext_vector_type(8))) u64 u64x8;
typedef __attribute__((ext_vector_type(2))) u64 u64x2;
extern "C" __global__ __launch_bounds__(512, 2) void k_main(const u16* x, const u16* wd_re, const u16* wd_im, const u16* w_r,
                                                 char* wsp, const u16* ws_re, const u16* ws_im,
                                                 const u16* w_gate, const u16* w_up, const u16* w_down, u16* out) {
  __shared__ __attribute__((aligned(16))) char smem[LDS_BYTES];
  u64x8 ka;
  u64x2 kb_;
  u64 kc;
  asm volatile("s_load_dwordx16 %0, %3, 0\n\ts_load_dwordx4 %1, %3, 64\n\ts_load_dwordx2 %2, %3, 80"
               : "=&s"(ka), "=&s"(kb_), "=&s"(kc) : "s"(__builtin_amdgcn_kernarg_segment_ptr()) : "memory");
  const int tid = threadIdx.x, lane = tid & 63;
  const int wid = __builtin_amdgcn_readfirstlane(tid >> 6);
  const int g = blockIdx.x, xcd = XMAP(g), j = g >> 3;
  if (wid < 4) {
    unsigned cw, co = (j * 4 + wid) * 448;
    asm volatile(".subsection 1\nk_main_end:\n.subsection 0\n\ts_min_u32 %1, %1, k_main_end-k_main-480\n\ts_getpc_b64 s[40:41]\n\ts_add_u32 s40, s40, %1\n\ts_addc_u32 s41, s41, 0\n\t"
                 "s_load_dword %0, s[40:41], 0\n\ts_load_dword %0, s[40:41], 64\n\ts_load_dword %0, s[40:41], 128\n\t"
                 "s_load_dword %0, s[40:41], 192\n\ts_load_dword %0, s[40:41], 256\n\ts_load_dword %0, s[40:41], 320\n\ts_load_dword %0, s[40:41], 384\n\ts_waitcnt lgkmcnt(0)"
                 : "=&s"(cw), "+s"(co) :: "s40", "s41", "scc", "memory");
  }
  const int pw = wid - 4;
  const int row0 = (wid & 3) * 8 + lrw(lane >> 3);
  const int cm = ((lane & 7) ^ swz(row0)) * 16;
  int vm = ((row0 & 15) * 2 + (row0 >> 4)) * (D_ * 2) + cm;
  asm volatile("" : "+v"(vm));
  const int kq = xcd >> 1;
  const int tt = (xcd & 1) * 4 + (j >> 3);
  const int et = j & 7;
  const int z = kq >> 1, st = (kq & 1) * 8 + tt;
  const int lrow = (kq & 1) * 1024 + (xcd & 1) * 512 + z * 256 + j * 8 + pw * 2;
  const int drow = ((kq & 1) * 2 + (xcd & 1)) * 256 + j * 8 + pw * 2;
  unsigned o_m = st * 128 * D_ * 2, o_l = lrow * D_ * 2, o_d = drow * D_ * 2, mb0 = lds_addr(smem) + pw * 1024;
  int l16 = lane * 16;
  int vn[4];
#pragma unroll
  for (int q = 0; q < 4; ++q) vn[q] = sym_lo(32, D_ * 2, (row0 >> 2) & 3, q * 8 + (row0 >> 4) * 4 + (row0 & 3)) ^ (sym_hi(D_, 32, D_ * 2, et, z, (row0 >> 2) & 3) | cm);
  asm volatile("" : "+s"(o_m), "+s"(o_l), "+s"(o_d), "+s"(mb0));
  asm volatile("" : "+v"(l16), "+v"(vn[0]), "+v"(vn[1]), "+v"(vn[2]), "+v"(vn[3]));
  asm volatile("s_waitcnt lgkmcnt(0)" : "+s"(ka), "+s"(kb_), "+s"(kc) :: "memory");
  x = (const u16*)(gptr)ka[0]; wd_re = (const u16*)(gptr)ka[1]; wd_im = (const u16*)(gptr)ka[2]; w_r = (const u16*)(gptr)ka[3];
  wsp = (char*)(gptr)ka[4]; ws_re = (const u16*)(gptr)ka[5]; ws_im = (const u16*)(gptr)ka[6]; w_gate = (const u16*)(gptr)ka[7];
  w_up = (const u16*)(gptr)kb_[0]; w_down = (const u16*)(gptr)kb_[1]; out = (u16*)(gptr)kc;
  int* flags = (int*)(wsp + WS_FLAGS);
  u64* stat = (u64*)(flags + FL_ST);
  const u16* wdz = z ? wd_im : wd_re;
  const __amdgpu_buffer_rsrc_t rm = mk_rsrc(x);
  const __amdgpu_buffer_rsrc_t rn = mk_rsrc(wdz);
#define G1_X(kt, so, q) DMA_(rm, vm, mb0, (so) + (q) * 4096, o_m, (kt) * 128 + (((q) & 1) * 64 + ((q) >> 1) * 32) * D_ * 2, "")
#define G1_W(kt, so, q) DMA_I(rn, vn[q], mb0, (so) + 16384 + (q) * 4096, (kt) * 128)
#define G1_STAGE_TILE(kt, so, W) do { SB(); G1_X(kt, so, 0); W(0); G1_X(kt, so, 1); W(1); G1_X(kt, so, 2); W(2); G1_X(kt, so, 3); W(3); \
    G1_W(kt, so, 0); W(4); G1_W(kt, so, 1); W(5); G1_W(kt, so, 2); W(6); G1_W(kt, so, 3); W(7); SB(); } while (0)
#define G1_STAGE_A(kt, so, W) do { SB(); G1_X(kt, so, 0); W(0); G1_X(kt, so, 2); W(1); \
    G1_W(kt, so, 0); W(2); G1_W(kt, so, 1); W(3); G1_W(kt, so, 2); W(4); G1_W(kt, so, 3); W(5); W(6); W(7); SB(); } while (0)
#define G1_STAGE_B(kt, so) do { SB(); G1_X(kt, so, 1); G1_X(kt, so, 3); SB(); } while (0)
#define NOW(i)
  const int fr = lane & 15, fq = lane >> 4;
  const int ha = wid >> 2;
  int trow, xo[4], xa[3], po, ml;
  const char *prs, *prc;
  auto col4 = [](int f) __attribute__((always_inline)) { return ((f & 2) << 3 | (f & 4) << 1 | (f & 1) << 2) ^ (-((f >> 3) & 1) & 28); };
  auto seg0 = [&](int hf, int dn) __attribute__((always_inline)) { return (hf * 512 + et * 32) ^ (-dn & (D_ - 32)); };
  auto epi_setup = [&](int part) __attribute__((always_inline)) {
    int t_ = tt, l_ = lane;
    asm volatile("" : "+s"(t_));
    asm volatile("" : "+v"(l_));
    const int ts = ((wid >> 1) & 1) * 8 + kq * 2 + ha;
    if (part == 0) { xo[3] = (seg0(wid & 1, (l_ >> 3) & 1) + col4(l_)) * 2; asm volatile("" : "+v"(xo[3])); }
    if (part == 1) {
      xo[3] |= sym_hi(S_, 64, D_ * 2, t_, z, l_ >> 4);
      ml = (l_ * 16) ^ (-sym_dn(l_ >> 4) & (z * 768));
      asm volatile("" : "+v"(xo[3]), "+v"(ml));
    }
    if (part == 2) {
      const int xh = xo[3];
#pragma unroll
      for (int r = 0; r < 4; ++r) xo[r] = sym_lo(64, D_ * 2, l_ >> 4, ts * 4 + r) ^ xh;
      const bool hi = xo[0] & 8;
      xa[0] = (hi ? xo[2] : xo[0]) & ~15; xa[1] = (hi ? xo[3] : xo[1]) & ~15;
      xa[2] = (sym_lo(64, D_ * 2, l_ >> 4, ts * 4 + (l_ & 3)) ^ xh) >> 11 << 3;
      asm volatile("" : "+v"(xo[0]), "+v"(xo[1]), "+v"(xo[2]), "+v"(xo[3]), "+v"(xa[0]), "+v"(xa[1]), "+v"(xa[2]));
    }
    if (part == 3) {
      trow = t_ * 16 + ts;
      prs = wsp + WS_SLAB + (size_t)((t_ * 8 + et) * 64 + (wid & 3)) * MSG;
      prc = (const char*)(flags + FL_X + ((t_ * 8 + et) * 64 + (wid & 3)) * 32);
      asm volatile("" : "+s"(trow), "+s"(prs), "+s"(prc));
      po = l_ < 3 ? (kq * 4 + ((kq + 1 + l_) & 3)) * 512 : (int)0x80000000;
      asm volatile("" : "+v"(po));
    }
  };
  auto tok_tile = [](unsigned key) __attribute__((always_inline)) {
    const unsigned h = (0xffff - key) & 1023;
    return h < 512 ? h >> 2 : ((1023 - h) >> 6 << 4) | (((1024 - h) & 63) >> 2);
  };
#define rs mk_rsrc(prs)
#define rc mk_rsrc(prc)
  lds_vi sig = (lds_vi)(smem + LDS_SIG + (wid & 3) * 4);
  unsigned tag;
  u32x4 xq[2];
  u32x2 sq;
  f32x4 ya[4];
  unsigned key[2];
  float cv[16];
  unsigned far0, far1;
  auto xq_load = [&](int n) __attribute__((always_inline)) {
    if (n < 2) xq[n] = ld16_s<0>(x, xa[n]); else sq = ld8_s_sc1(stat, xa[2]);
  };
#define PINV(a) asm volatile("" : "+v"(a))
  auto cv_calc = [&]() __attribute__((always_inline)) {
    if (__builtin_expect(!__all(sq.y == tag), 0)) sq = granule_wait(stat + (xa[2] >> 3), tag);
    far0 = tok_tile(key[0]) ^ trow; far1 = tok_tile(key[1]) ^ trow;
    asm volatile("" : "+s"(far0), "+s"(far1));
    const bool hi = xo[0] & 8;
    const float mb_ = lo_bf(sq.x), ri_ = __builtin_amdgcn_rcpf(hi_bf(sq.x));
#pragma unroll
    for (int r = 0; r < 4; ++r) {
      const float mb = r == 0 ? dpp_f<0x00>(mb_) : r == 1 ? dpp_f<0x55>(mb_) : r == 2 ? dpp_f<0xAA>(mb_) : dpp_f<0xFF>(mb_);
      const float ri = r == 0 ? dpp_f<0x00>(ri_) : r == 1 ? dpp_f<0x55>(ri_) : r == 2 ? dpp_f<0xAA>(ri_) : dpp_f<0xFF>(ri_);
#pragma unroll
      for (int u = 0; u < 2; ++u) {
        const unsigned own = xq[r & 1][(r & 2) + u], oth = dpp_u<0xB1>(xq[r & 1][2 - (r & 2) + u]);
        const unsigned w = hi == (r >= 2) ? own : oth;
        cv[r * 4 + 2 * u] = __builtin_fmaf(lo_bf(w) - mb, ri, lo_bf(w));
        cv[r * 4 + 2 * u + 1] = __builtin_fmaf(hi_bf(w) - mb, ri, hi_bf(w));
        PINV(cv[r * 4 + 2 * u]); PINV(cv[r * 4 + 2 * u + 1]);
      }
    }
  };
#define MSG_IDX(m) (kq * 4 + ((kq + 1 + (m)) & 3))
  unsigned seen = 0;

  if (__builtin_expect(wid >= 4, 1)) {
    __builtin_amdgcn_s_setprio(3);
    G1_STAGE_TILE(0, 0, NOW);
    constexpr int O_ST = (FL_ST - FL_AB) * 4, O_CE = (FL_CE - FL_AB) * 4, O_KEY = (FL_KEY - FL_AB) * 4, O_EP = (FL_EPOCH - FL_AB) * 4;
    const __amdgpu_buffer_rsrc_t rf = mk_rsrc(wsp + WS_FLAGS + FL_AB * 4);
    u32x4 lv[2], lv2[2], lw[2], ld[2], ld2[2];
    unsigned epv;
    {
      const char* xrow = (const char*)x + o_l;
      const char* wdrow = (const char*)wdz + o_d;
      lv[0] = ld16_s<0>(xrow, l16); lv[1] = ld16_s<1024>(xrow, l16);
      lv2[0] = ld16_s<2048>(xrow, l16); lv2[1] = ld16_s<3072>(xrow, l16);
      ld[0] = ld16_s<0>(wdrow, l16); ld[1] = ld16_s<1024>(wdrow, l16);
      ld2[0] = ld16_s<2048>(wdrow, l16); ld2[1] = ld16_s<3072>(wdrow, l16);
      lw[0] = ld16_s<0>(w_r, l16); lw[1] = ld16_s<1024>(w_r, l16);
      asm volatile("buffer_load_dword %0, off, %1, %2 sc1" : "=v"(epv) : "s"(rf), "s"(O_EP) : "memory");
    }
    SB();
    const unsigned long long did = llvm_dispatch_id();
    const unsigned dmix = (unsigned)did * 0x85EBCA6Bu + (unsigned)(did >> 32) * 0xC2B2AE35u + 0x27D4EB2Fu;
    const bool moe = et != 7;
    unsigned epoch;
    const int ci = lane & 15, rq = lane >> 4, tk = (lane >> 3) & 1;
    int mat, cg, dq, d0;
    u32x4 wv[16];
    const u16 *pgu, *pn2;
    const char *pm2, *spb;
#define rw __builtin_amdgcn_make_buffer_rsrc((void*)pgu, 0, moe ? 0x7fffffff : 0, 0x00020000)
#define rn2 mk_rsrc(pn2)
#define rm2 mk_rsrc(pm2)
    int vw, vn2[8], vm2;
    auto k_blk = [&](int k) __attribute__((always_inline)) { return tt ^ (k_ord(k) >> 1); };
#define KROT(kk_) (k_blk(kk_) * 256 + (k_ord(kk_) & 1) * 128)
    int tv2[3];
    unsigned sd = 0;
    auto spf = [&](auto N) __attribute__((always_inline)) {
      constexpr int n = decltype(N)::value;
      if constexpr (n >= 0 && sp_tile(n >> 4) >= 0) sld<((n >> 1) & 7) * 4096 + (n & 1) * 64>(sd, spb, KROT(sp_tile(n >> 4)));
    };
    auto touches = [&](auto P) __attribute__((always_inline)) {
      constexpr int p = decltype(P)::value;
      if constexpr (VAN > 0 && p == VAP) touch1(mk_rsrc(spb), tv2[0], mb0, 0);
      if constexpr (VBN > 0 && p == VBP) touch1(mk_rsrc(spb), tv2[1], mb0, 0);
      if constexpr (VCN > 0 && p == VCP) touch1(mk_rsrc(spb), tv2[2], mb0, 0);
    };
    unsigned mq0, mq1, mq2, mq3;
    const int cg8 = et ^ ((et & 1) << 1), rgb = (g & 7) * 4 + (j >> 3);
    const __amdgpu_buffer_rsrc_t rdn = mk_rsrc(w_down);
    const __amdgpu_buffer_rsrc_t rgu = mk_rsrc(wsp + WS_GU);
    int vdn, vgu;
    u32x4 dv[7], gv[4];
    int abo, abq[3];
    unsigned lma;
    unsigned sel[2][4];
    float g0, g1;
    u32x4 kk[4];
    unsigned k1 = 0, k2 = 0;
    const int ko_ = O_KEY + pw * 4096;
#define MM(a, b, c) __builtin_amdgcn_mfma_f32_16x16x32_bf16(__builtin_bit_cast(bf16x8, a), __builtin_bit_cast(bf16x8, b), c, 0, 0, 0)
#define PIN1(a) asm volatile("" : "+v"(a))
#define PIN2(a, b) asm volatile("" : "+v"(a), "+v"(b))
#define PIN3(a, b, c) asm volatile("" : "+v"(a), "+v"(b), "+v"(c))
    {
      int gro, go;
      u32x2 gr;
      u32x4 oh[5];
      f32x4 sa4, gw[2], gx[2];
      float su, gs, ta, tb, sx, mbv;
      unsigned dk[4], kl;
      const int hb = lane >> 5;
#pragma unroll
      for (int l = 0; l < WSN; ++l) asm volatile("" : "=v"(wv[l]));
      auto diag = [&](const f32x4& m) __attribute__((always_inline)) {
        return __uint_as_float((__float_as_uint(m[0]) & dk[0]) | (__float_as_uint(m[1]) & dk[1]) | (__float_as_uint(m[2]) & dk[2]) | (__float_as_uint(m[3]) & dk[3]));
      };
      auto setup = [&](auto JB, auto GP) __attribute__((always_inline)) {
        constexpr int jb = decltype(JB)::value, gp = decltype(GP)::value;
        SB();
        {
          int ln = lane, jj = j, p_ = pw;
          launder(ln, jj, p_);
          const int c_ = ln & 15, r_ = ln >> 4, e_ = jj & 7, t_ = (xcd & 1) * 4 + (jj >> 3);
          if constexpr (jb == 1 && gp == 0) {
            pn2 = ((kq >> 1) ? ws_im : ws_re) + (kq & 1) * 1024;
            spb = (const char*)(pn2 + (sym_row(S_, 64, 1, t_, 0, e_ >> 1, 0) + (e_ & 1) * 32 + p_ * 8) * S_);
            pins(pn2, spb);
          }
          if constexpr (jb == 1 && gp >= 1 && gp < 6) {
            const unsigned o = c_ == gp - 1 ? 0x3f803f80u : 0u;
            oh[gp - 1] = (u32x4){o, o, o, o};
            PIN1(oh[gp - 1]);
          }
          if constexpr (jb == 1 && gp == 6) {
            const int rk = moe ? e_ * 8 + (jj >> 3) * 2 + (xcd >> 2) : 0, r3 = ((rk - 14) * 43) >> 7, rr = rk - 14 - r3 * 3;
            const int c56 = rk < 14 ? rk * 4 + 1 : r3 * 4 + rr + ((rr + 1) >> 1);
            mat = (c56 + 36) >> 6; cg = c56 - mat * 28; dq = xcd & 3; d0 = dq * 256 + p_ * 64;
            pin4(mat, cg, dq, d0);
          }
          if constexpr (jb == 2 && gp == 0) {
            pgu = mat ? w_up : w_gate;
            pm2 = wsp + WS_ABT + (kq * 8 + e_) * 262144;
            pins(pgu, pm2);
            vw = ((d0 + r_ * 16) * F_ + cg * 128 + c_ * 8) * 2;
            PIN1(vw);
          }
          if constexpr (jb == 2 && (gp == 1 || gp == 2)) {
            dk[gp * 2 - 2] = c_ == r_ * 4 + gp * 2 - 2 ? ~0u : 0u; dk[gp * 2 - 1] = c_ == r_ * 4 + gp * 2 - 1 ? ~0u : 0u;
            PIN2(dk[gp * 2 - 2], dk[gp * 2 - 1]);
          }
          if constexpr (jb == 2 && gp == 3) { kl = lrow + (ln >> 5); go = (ln & 30) ? (int)0x80000000 : ((ln & 1) ? O_KEY : O_ST) + (int)kl * 8; PIN2(kl, go); }
          if constexpr (jb == 2 && gp == 4) { kl = 0xffff - kl; go = (ln & ~1) == 2 ? O_CE + (z * D_ + drow + (ln & 1)) * 8 : go; PIN2(kl, go); }
          if constexpr (jb == 2 && gp == 5) {
            gro = p_ < 2 ? O_ST + (st * 128 + p_ * 64 + ln) * 8 : O_CE + z * D_ * 8 + sym_row(D_, 32, 8, e_, z, (ln >> 2) & 3, (p_ - 2) * 16 + r_ * 4 + (ln & 3));
            lma = lds_addr(smem + LDS_MB) + (p_ < 2 ? p_ * 256 : 512 + p_ * 256) + ln * 4;
            PIN2(gro, lma);
          }
          if constexpr (jb == 1 && gp == 7) {
#pragma unroll
            for (int u = 0; u < 3; ++u) {
              const int vn_ = u == 0 ? VAN : u == 1 ? VBN : VCN;
              if (vn_ == 0) continue;
              const int k = (u == 0 ? VA0 : u == 1 ? VB0 : VC0) + (ln >> 3);
              tv2[u] = (ln >> 3) < vn_ ? (ln & 7) * 4096 + ((t_ ^ (k < 9 ? k - 1 : k - 8)) << 8) + (k < 9 ? 0 : 128) : (int)0x80000000;
              PIN1(tv2[u]);
            }
          }
          if constexpr (jb == 7 && gp == 0) {
            vm2 = p_ * 1024 + ln * 16;
            PIN1(vm2);
          }
          if constexpr (jb == 2 && gp == 6) {
            const int r0 = p_ * 8 + lrw(ln >> 3);
            vn2[6] = sym_lo(64, S_ * 2, (r0 >> 2) & 3, (r0 >> 4) * 4 + (r0 & 3)); vn2[7] = sym_hi(S_, 64, S_ * 2, t_, z, (r0 >> 2) & 3) | (vm & 127);
            PIN2(vn2[6], vn2[7]);
          }
          if constexpr ((jb == 2 && gp == 7) || (jb == 7 && gp >= 6)) {
            constexpr int q0 = jb == 2 ? 0 : gp == 6 ? 3 : 6;
            const int lo = vn2[6], hi = vn2[7];
#pragma unroll
            for (int q = q0; q < q0 + 3 && q < 8; ++q) { vn2[q] = ((lo + q * 8 * S_ * 2) & (63 * S_ * 2)) ^ hi; PIN1(vn2[q]); }
          }
          if constexpr (jb == 7 && gp == 1) { vdn = rgb * 112 + p_ * 28 + r_ * 7; vgu = (((ln >> 3) & 1) * 4 * F_ + vdn + min(ln & 7, 6)) * 16; PIN2(vdn, vgu); }
          if constexpr (jb == 7 && gp == 2) { vdn = (vdn * D_ + cg8 * 128 + c_ * 8) * 2; PIN1(vdn); }
          if constexpr (jb == 7 && gp == 3) {
            abo = (((kq * 8 + e_) * 4 + p_) * 16 + c_) * AB_STRIDE * 4;
            abq[2] = ln >= 16 || (ln & 7) == t_ ? (int)0x80000000 : abo;
            PIN2(abo, abq[2]);
          }
          if constexpr (jb == 7 && gp == 4) {
            abq[1] = ln >= 8 ? (int)0x80000000 : abq[2];
            abq[0] = ((ln ^ t_) & 4) ? (int)0x80000000 : abq[1];
            PIN2(abq[0], abq[1]);
          }
          if constexpr (jb == 7 && gp == 5) {
            const unsigned mb = lds_addr(smem) + p_ * 1024;
            mq0 = mb + ((3 - kq) & 3) * 4096; mq1 = mb + ((4 - kq) & 3) * 4096; mq2 = mb + ((5 - kq) & 3) * 4096; mq3 = mb + ((6 - kq) & 3) * 4096;
            pin4(mq0, mq1, mq2, mq3);
          }
          if constexpr (jb == 8 && gp < 4) {
#pragma unroll
            for (int h = 0; h < 2; ++h) {
              const unsigned hv = h ? 0x0302u : 0x0100u;
              sel[h][gp] = ((ln & 7) >> 1) == gp ? ((ln & 1) ? (hv << 16) | 0x0c0cu : 0x0c0c0000u | hv) : 0x0c0c0c0cu;
            }
            PIN2(sel[0][gp], sel[1][gp]);
          }
        }
        SB();
      };
#define WS1(i) setup(IC<1>{}, IC<i>{})
#define WS2(i) setup(IC<2>{}, IC<i>{})
#define WS3(i) do { setup(IC<7>{}, IC<i>{}); if (i >= 4) setup(IC<8>{}, IC<(i) & 3>{}); spf(IC<(i < SP0 ? i : -1)>{}); } while (0)
      G1_STAGE_TILE(1, G1_STAGE, WS1);
      WAIT_VM(19); BAR();
      G1_STAGE_TILE(2, 2 * G1_STAGE, WS2);
      G1_STAGE_TILE(3, 3 * G1_STAGE, WS3);
      auto lnu = [&](auto U) __attribute__((always_inline)) {
        constexpr int jb = 1 + decltype(U)::value / 7, gp = decltype(U)::value % 7;
        SB();
        if constexpr (jb == 1) {
          const f32x4 z4 = {0.f, 0.f, 0.f, 0.f};
          if constexpr (gp == 0) { sa4 = MM(lv[0], oh[0], z4); gw[0] = MM(lv[0], lw[0], z4); gx[0] = MM(lv[0], lv[0], z4); PIN3(sa4, gw[0], gx[0]); }
          if constexpr (gp == 1) { sa4 = MM(lv2[0], oh[1], sa4); gw[1] = MM(lv2[0], lw[0], z4); gx[1] = MM(lv2[0], lv2[0], z4); PIN3(sa4, gw[1], gx[1]); }
          if constexpr (gp == 2) { sa4 = MM(lv[1], oh[0], sa4); gw[0] = MM(lv[1], lw[1], gw[0]); gx[0] = MM(lv[1], lv[1], gx[0]); PIN3(sa4, gw[0], gx[0]); }
          if constexpr (gp == 3) { sa4 = MM(lv2[1], oh[1], sa4); gw[1] = MM(lv2[1], lw[1], gw[1]); gx[1] = MM(lv2[1], lv2[1], gx[1]); PIN3(sa4, gw[1], gx[1]); }
          if constexpr (gp == 4) { sa4 = MM(ld[0], oh[2], sa4); sa4 = MM(ld[1], oh[2], sa4); PIN1(sa4); }
          if constexpr (gp == 5) { sa4 = MM(ld2[0], oh[3], sa4); sa4 = MM(ld2[1], oh[3], sa4); PIN1(sa4); }
          if constexpr (gp == 6) { sa4 = MM(lw[0], oh[4], sa4); sa4 = MM(lw[1], oh[4], sa4); PIN1(sa4);
            epoch = __builtin_amdgcn_readfirstlane(epv); tag = ((epoch + 1u) * 0x9E3779B1u ^ dmix) | 1u; }
        }
        if constexpr (jb == 2) {
          if constexpr (gp == 0) { su = (sa4[0] + sa4[1]) + (sa4[2] + sa4[3]); ta = diag(gw[0]); PIN2(su, ta); }
          if constexpr (gp == 1) { su = fold32(su, su); tb = diag(gw[1]); PIN2(su, tb); }
          if constexpr (gp == 2) { su = fold16(su, su); gs = fold32(ta, tb); PIN2(su, gs); }
          if constexpr (gp == 3) { ta = diag(gx[0]); tb = diag(gx[1]); PIN2(ta, tb); }
          if constexpr (gp == 4) { gs = fold16(gs, fold32(ta, tb)); PIN1(gs); }
          if constexpr (gp == 5) { gs += dpp_f<0xB1>(gs); gs += dpp_f<0x4E>(gs); PIN1(gs); }
          if constexpr (gp == 6) { gs += dpp_f<0x141>(gs); gs += dpp_f<0x140>(gs); PIN1(gs); }
        }
        if constexpr (jb == 3) {
          if constexpr (gp == 0) {
            const float s0 = rdl(su, 0), s1 = rdl(su, 1), q0 = rdl(gs, 16), q1 = rdl(gs, 48);
            sx = hb ? s1 : s0; ta = hb ? q1 : q0; PIN2(sx, ta);
          }
          if constexpr (gp == 1) {
            const float mean = sx * (1.0f / D_);
            mbv = rbf(mean);
            ta -= sx * mean;
            if (__builtin_expect(__any(!(sx * mean <= 16.f * ta)), 0)) {
              float q = fold32(sqdev16(lv, rdl(mbv, 0)), sqdev16(lv2, rdl(mbv, 32)));
              q = row_sum(fold16(q, q));
              const float dm = mean - mbv;
              ta = fmaxf(q - (float)D_ * dm * dm, 0.f);
            }
            PIN2(mbv, ta);
          }
          if constexpr (gp == 2) { ta = rbf(rbf(ta * (1.0f / D_)) + 1e-5f); gs -= mbv * rdl(su, 4); PIN2(ta, gs); }
          if constexpr (gp == 3) { ta = rbf(__builtin_amdgcn_sqrtf(ta)); tb = __builtin_amdgcn_rcpf(ta); PIN2(ta, tb); }
          if constexpr (gp == 4) {
            const unsigned rb = __float_as_uint(rbf(gs * tb));
            kl |= (rb ^ ((unsigned)((int)rb >> 31) | 0x80000000u)) & 0xffff0000u; PIN1(kl);
          }
          if constexpr (gp == 5) { kl = (lane & 2) ? __float_as_uint(su) : (lane & 1) ? kl : (__float_as_uint(mbv) >> 16) | __float_as_uint(ta); PIN1(kl); }
        }
        SB();
      };
      constexpr int LN_JB = 1 + ((19 - LNP) * LNS) / 8;
      auto work1 = [&](auto JB, auto GP) __attribute__((always_inline)) {
        constexpr int jb = decltype(JB)::value, gp = decltype(GP)::value, pos = (jb - 1) * 8 + gp;
        SB();
        if constexpr (pos % LNS == 0 && LNP + pos / LNS < 20) lnu(IC<LNP + pos / LNS>{});
        if constexpr (jb == ST_JB + 3) {
          if constexpr (gp == 0) { if (!__all(gr.y == tag)) gr = granule_wait((const u64*)((const char*)(flags + FL_AB) + gro), tag); PIN1(gr); }
          if constexpr (gp == 1) { g1 = __builtin_amdgcn_rcpf(__uint_as_float((gr.x & 0xffff0000u) ^ ((unsigned)z << 31))); PIN1(g1); }
          if constexpr (gp == 2) { g0 = pw < 2 ? -(lo_bf(gr.x) * g1) : __uint_as_float(gr.x); PIN1(g0); }
        }
        if constexpr (gp < SP1 && jb <= SP1E) spf(IC<SP0 + (jb - 1) * SP1 + gp>{});
        SB();
      };
      auto step1 = [&](auto JB) __attribute__((always_inline)) {
        constexpr int jb = decltype(JB)::value;
        if constexpr (jb == 1) PAGE(1);
        if constexpr (jb == 7) PAGE(2);
        constexpr int n16 = jb == 12 ? 12 : jb == 11 ? 14 : 16;
        constexpr auto wl = [](int q) { return (q == LN_JB ? 1 : 0) + (q == ST_JB ? 1 : 0); };
        if constexpr (jb == 1) {
          asm volatile("s_waitcnt vmcnt(%11)" : "+v"(lv[0]), "+v"(lv[1]), "+v"(lw[0]), "+v"(lw[1]), "+v"(ld[0]), "+v"(ld[1]),
                       "+v"(lv2[0]), "+v"(lv2[1]), "+v"(ld2[0]), "+v"(ld2[1]), "+v"(epv) : "i"(LNP ? 24 : 16) : "memory");
#define LU(u) if constexpr (u < LNP) lnu(IC<u>{});
          LU(0) LU(1) LU(2) LU(3) LU(4) LU(5) LU(6) LU(7) LU(8) LU(9) LU(10) LU(11) LU(12) LU(13) LU(14) LU(15) LU(16) LU(17) LU(18)
          if (LNP) WAIT_VM(16);
        } else if constexpr (jb == ST_JB + 3) {
          asm volatile("s_waitcnt vmcnt(%1)" : "+v"(gr) : "i"(n16 + wl(jb - 2) + wl(jb - 1)) : "memory");
        } else wait_vm<n16 + wl(jb - 3) + wl(jb - 2) + wl(jb - 1)>();
        BAR();
        if constexpr (jb == 2) { if (pw == 0 && lane < 16) ((int*)(smem + LDS_SEL))[lane < 8 ? lane : lane + 8] = 0; }
        if constexpr (jb == 4) { if (pw == 0 && lane == 0) ((unsigned*)(smem + LDS_SEL))[5] = tag; }
#define W1(i) work1(JB, IC<i>{})
        if constexpr (jb < 10) G1_STAGE_TILE(jb - 1 + G1_NST, ((jb - 1) % G1_NST) * G1_STAGE, W1);
        else G1_STAGE_A(jb - 1 + G1_NST, ((jb - 1) % G1_NST) * G1_STAGE, W1);
        if constexpr (jb == 12) { G1_STAGE_B(13, G1_STAGE); G1_STAGE_B(14, 2 * G1_STAGE); }
        if constexpr (jb == LN_JB) __builtin_amdgcn_raw_buffer_store_b64((u32x2){kl, tag}, rf, go, 0, 16);
        if constexpr (jb == ST_JB) {
          asm volatile("buffer_load_dwordx2 %0, %1, %2, 0 offen sc1" : "=v"(gr) : "v"(gro), "s"(rf) : "memory");
        }
        touches(IC<100 + jb>{});
        if constexpr (jb <= 8) {
          if (et == jb - 1) {
#pragma unroll
            for (int l = 0; l < WSN; ++l) bld16_nt_io(wv[l], rw, vw, l * F_ * 2);
          }
        }
        SB();
      };
      static_assert(ST_JB + 3 <= 12 && ST_JB >= 6, "schedule");
      step1(IC<1>{}); step1(IC<2>{}); step1(IC<3>{}); step1(IC<4>{}); step1(IC<5>{}); step1(IC<6>{}); step1(IC<7>{}); step1(IC<8>{}); step1(IC<9>{}); step1(IC<10>{}); step1(IC<11>{}); step1(IC<12>{});
    }
#define ALIAS 0
#define G2_STAGE_N(kk_, W) do { const int kb = KROT(((ALIAS >> (kk_)) & 1) ? (kk_) & 3 : (kk_)); constexpr int so = n_off(kk_); SB(); \
    DMA_S(rn2, vn2[0], mq0, so, kb); W(0); DMA_S(rn2, vn2[1], mq1, so, kb); W(1); \
    DMA_S(rn2, vn2[2], mq2, so, kb); W(2); DMA_S(rn2, vn2[3], mq3, so, kb); W(3); \
    DMA_S(rn2, vn2[4], mq0, so + 16384, kb); W(4); DMA_S(rn2, vn2[5], mq1, so + 16384, kb); W(5); \
    DMA_S(rn2, vn2[6], mq2, so + 16384, kb); W(6); DMA_S(rn2, vn2[7], mq3, so + 16384, kb); W(7); SB(); } while (0)
#define G2_STAGE_NH(kk_, h) do { const int kb = KROT(kk_); constexpr int so = n_off(kk_) + (h) * 16384; SB(); \
    DMA_S(rn2, vn2[(h) * 4], mq0, so, kb); DMA_S(rn2, vn2[(h) * 4 + 1], mq1, so, kb); \
    DMA_S(rn2, vn2[(h) * 4 + 2], mq2, so, kb); DMA_S(rn2, vn2[(h) * 4 + 3], mq3, so, kb); SB(); } while (0)
#define G2_STAGE_M(kk_, W) do { const int kb = KROT(kk_) * 128; constexpr int so = G2_MRING + m_off(kk_); SB(); \
    DMA_(rm2, vm2, mb0, so, kb, 0, " sc1"); W(8); DMA_(rm2, vm2, mb0, so + 4096, kb, 4096, " sc1"); W(9); \
    DMA_(rm2, vm2, mb0, so + 8192, kb, 8192, " sc1"); W(10); DMA_(rm2, vm2, mb0, so + 12288, kb, 12288, " sc1"); W(11); SB(); } while (0)
    WAIT_VM(16); BAR();
    asm volatile("ds_write_b32 %0, %1" :: "v"(lma), "v"(g0) : "memory");
    if (pw < 2) asm volatile("ds_write_b32 %0, %1 offset:512" :: "v"(lma), "v"(g1) : "memory");
    u32x4 tv[4];
    unsigned ready = 0;
    auto need = [&](int k) __attribute__((always_inline)) {
      const int b = k_blk(k) + (k_ord(k) & 1) * 8;
#pragma unroll 1
      for (int it = 0; !((ready >> b) & 1) && it < SPIN_MAX; ++it) {
        ready |= (unsigned)__ballot(__builtin_amdgcn_raw_buffer_load_b32(rf, abq[k < 5 ? 0 : k < 9 ? 1 : 2], 0, 16) == tag);
        asm volatile("" ::: "memory");
      }
    };
    unsigned ksel, bad = 0;
    int isel;
    unsigned xw;
    auto bvec = [&](unsigned src, const unsigned (&sl)[4]) __attribute__((always_inline)) {
      return (u32x4){__builtin_amdgcn_perm(0, src, sl[0]), __builtin_amdgcn_perm(0, src, sl[1]),
                     __builtin_amdgcn_perm(0, src, sl[2]), __builtin_amdgcn_perm(0, src, sl[3])};
    };
#define BC8(v, p) (unsigned)__builtin_amdgcn_update_dpp(__builtin_amdgcn_update_dpp(0, (int)(v), 0x150 + (p), 0xf, 0x3, false), (int)(v), 0x158 + (p), 0xf, 0xc, false)
    float* lg = (float*)(smem + LDS_GU);
    f32x4 macc = {0.f, 0.f, 0.f, 0.f};
    u32x2 sv;
    unsigned hp, gbad;
    float s0, s1;
    auto work2 = [&](auto JB, auto GP) __attribute__((always_inline)) {
      constexpr int jb = decltype(JB)::value, gp = decltype(GP)::value;
      SB();
      if constexpr (jb == -1 && gp < 4) {
        constexpr int c = gp == 0 ? 0xB1 : gp == 1 ? 0x4E : gp == 2 ? 0x141 : 0x140;
        merge2(k1, k2, dpp_u<c>(k1), dpp_u<c>(k2));
        PIN2(k1, k2);
      }
      if constexpr (jb == -1 && gp == 4) {
        ksel = tk ? k2 : k1;
        isel = (0xffff - (int)(ksel & 0xffffu)) & (S_ - 1);
        k1 = (isel * D_ + d0 + rq * 16 + (lane & 7) * 2) * 2; k2 = O_ST + isel * 8;
        PIN1(ksel);
        xw = gld4_s(x, k1);
        sv = bld8_sc1(rf, k2);
      }
      if constexpr (jb == 0 && gp == 1) wait_xs<3 + 1 + (N2A < 2 ? N2A : 2) + tcnt(116) + tcnt(117)>(xw, sv);
      if constexpr (jb == 0) {
        if constexpr (gp == 1) { if (!__all(sv.y == tag)) sv = granule_wait(stat + isel, tag); s0 = __builtin_amdgcn_rcpf(hi_bf(sv.x)); PIN1(s0); }
        if constexpr (gp == 2) { s1 = ln_elem(lo_bf(xw), lo_bf(sv.x), hi_bf(sv.x), s0); PIN1(s1); }
        if constexpr (gp == 3) { xw = pk2(s1, ln_elem(hi_bf(xw), lo_bf(sv.x), hi_bf(sv.x), s0)); PIN1(xw); }
      }
      if constexpr (DGS == 0 && jb == DG && gp < 7) { PIN1(dv[gp]); macc = MM(dv[gp], bvec(BC8(hp, gp), sel[0]), macc); PIN1(macc); }
      if constexpr (jb >= 100 && gp < 7) { PIN1(dv[gp]); macc = MM(dv[gp], bvec(BC8(hp, gp), sel[0]), macc); PIN1(macc); }
      if constexpr ((jb == 0 && gp >= 4) || jb == 1) {
        constexpr int l = jb == 0 ? gp - 4 : gp + 4;
        if constexpr (!(l & 1)) hp = BC8(xw, l >> 1);
        PIN1(wv[l]);
        macc = MM(wv[l], bvec(hp, sel[l & 1]), macc);
        PIN2(macc, hp);
      }
      if constexpr (jb == GV_JB + 2) {
        if constexpr (gp == 0) { gbad = (gv[0].y ^ tag) | (gv[0].w ^ tag) | (gv[1].y ^ tag) | (gv[1].w ^ tag); PIN1(gbad); }
        if constexpr (gp == 1) { gbad |= (gv[2].y ^ tag) | (gv[2].w ^ tag) | (gv[3].y ^ tag) | (gv[3].w ^ tag); PIN1(gbad); }
        if constexpr (gp == 2) {
          s0 = ((__uint_as_float(gv[0].x) + __uint_as_float(gv[1].x)) + __uint_as_float(gv[2].x)) + __uint_as_float(gv[3].x);
          s1 = ((__uint_as_float(gv[0].z) + __uint_as_float(gv[1].z)) + __uint_as_float(gv[2].z)) + __uint_as_float(gv[3].z);
          PIN2(s0, s1);
        }
        if constexpr (gp == 3) {
#pragma unroll 1
          for (int it = 0; !__all(gbad == 0) && it < SPIN_MAX; ++it) {
            u32x4 q[4];
            gbad = 0;
#pragma unroll
            for (int i = 0; i < 4; ++i) { q[i] = __builtin_amdgcn_raw_buffer_load_b128(rgu, vgu, i * F_ * 16, 16); gbad |= (q[i].y ^ tag) | (q[i].w ^ tag); }
            s0 = ((__uint_as_float(q[0].x) + __uint_as_float(q[1].x)) + __uint_as_float(q[2].x)) + __uint_as_float(q[3].x);
            s1 = ((__uint_as_float(q[0].z) + __uint_as_float(q[1].z)) + __uint_as_float(q[2].z)) + __uint_as_float(q[3].z);
            asm volatile("" ::: "memory");
          }
          const unsigned gp2 = pk2(s0, s1);
          const unsigned up2 = dpp_u<0x128>(gp2);
          s0 = tk ? hi_bf(up2) : lo_bf(gp2); s1 = tk ? hi_bf(gp2) : lo_bf(up2);
          PIN2(s0, s1);
        }
        if constexpr (gp == 4) { s0 = rbf(s0 * __builtin_amdgcn_rcpf(1.f + __expf(-s0))); PIN1(s0); }
        if constexpr (gp == 5) { hp = pk2(s0 * s1, 0.f); PIN1(hp); }
      }
      if constexpr (jb == 10 && gp == 4) code_touch(sd, (j * 4 + pw) * 256);
      SB();
    };
#define W2M(i) work2(IC<-1>{}, IC<i>{})
#define W20(i) work2(IC<0>{}, IC<i>{})
#define W21(i) work2(IC<1>{}, IC<i>{})
    auto piece = [&](auto K, auto Q) __attribute__((always_inline)) {
      constexpr int k = decltype(K)::value, q = decltype(Q)::value;
      if constexpr (q < 8) {
        const int kb = KROT(((ALIAS >> k) & 1) ? k & 3 : k);
        DMA_S(rn2, vn2[q], (q & 3) == 0 ? mq0 : (q & 3) == 1 ? mq1 : (q & 3) == 2 ? mq2 : mq3, n_off(k) + (q >> 2) * 16384, kb);
      } else {
        if constexpr (q == 8) need(k);
        const int kb = KROT(k) * 128;
        DMA_(rm2, vm2, mb0, G2_MRING + m_off(k) + (q - 8) * 4096, kb, (q - 8) * 4096, " sc1");
      }
    };
    unsigned fs;
    {
      G1_STAGE_B(15, 3 * G1_STAGE);
      G2_STAGE_NH(0, 0);
      lgkm0(sd);
      WAIT_VM(16); BAR();
      G2_STAGE_NH(0, 1);
      WAIT_VM(10); BAR();
#pragma unroll
      for (int i = 0; i < 4; ++i) kk[i] = bld16_sc1(rf, l16, ko_ + i * 1024);
#pragma unroll
      for (int l = WSN; l < 16; ++l) wv[l] = bld16_nt(rw, vw, l * F_ * 2);
      asm volatile("s_waitcnt vmcnt(%4)" : "+v"(kk[0]), "+v"(kk[1]), "+v"(kk[2]), "+v"(kk[3]) : "i"(16 - WSN) : "memory");
      touches(IC<115>{});
#pragma unroll
      for (int i = 0; i < 4; ++i) {
        k2 = med3u(k1, k2, kk[i].x); k1 = max(k1, kk[i].x);
        k2 = med3u(k1, k2, kk[i].z); k1 = max(k1, kk[i].z);
        bad |= (kk[i].y ^ tag) | (kk[i].w ^ tag);
      }
#pragma unroll 1
      for (int it = 0; !__all(bad == 0) && it < SPIN_MAX; ++it) {
        k1 = 0; k2 = 0; bad = 0;
#pragma unroll
        for (int i = 0; i < 4; ++i) {
          const u32x4 q = __builtin_amdgcn_raw_buffer_load_b128(rf, l16, ko_ + i * 1024, 16);
          k2 = med3u(k1, k2, q.x); k1 = max(k1, q.x);
          k2 = med3u(k1, k2, q.z); k1 = max(k1, q.z);
          bad |= (q.y ^ tag) | (q.w ^ tag);
        }
        asm volatile("" ::: "memory");
      }
      merge2(k1, k2, dpp_u<0xB1>(k1), dpp_u<0xB1>(k2));
      merge2(k1, k2, dpp_u<0x4E>(k1), dpp_u<0x4E>(k2));
      merge2(k1, k2, dpp_u<0x141>(k1), dpp_u<0x141>(k2));
      merge2(k1, k2, dpp_u<0x140>(k1), dpp_u<0x140>(k2));
      if ((lane & 15) == 0) *(u32x2*)(smem + LDS_KEY + (pw * 4 + rq) * 8) = (u32x2){k1, k2};
      PAGE(3);
      BAR_LGKM();
#pragma unroll
      for (int jj = 0; jj < 4; ++jj) tv[jj] = *(const u32x4*)(smem + G2_MRING + m_off(0) + (jj * 4 + pw) * 1024 + lane * 16);
      { const u32x2 kp = *(const u32x2*)(smem + LDS_KEY + ci * 8); k1 = kp.x; k2 = kp.y; }
#pragma unroll
      for (int jj = 0; jj < 4; ++jj) __builtin_amdgcn_raw_buffer_store_b128(tv[jj], rm2, vm2 + jj * 4096 + 2 * tt * 16384, 0, 16);
      G2_STAGE_N(1, W2M);
      touches(IC<116>{});
      wait_vm<10 + tcnt(116)>();
      if (lane == tt) __builtin_amdgcn_raw_buffer_store_b32(tag, rf, abo, 0, 16);
      touches(IC<117>{});
#define P2A(i) if constexpr (i < N2A) piece(IC<2>{}, IC<i>{}); W20(i);
      SB(); P2A(0) P2A(1) P2A(2) P2A(3) P2A(4) P2A(5) P2A(6) P2A(7)
      __builtin_amdgcn_s_sleep(WSL);
      wait_vm<1 + N2A + tcnt(116) + tcnt(117)>();
      fs = bld4_sc1(rf, abq[0]);
    }
    BAR();
#pragma unroll
    for (int jj = 0; jj < 4; ++jj) tv[jj] = *(const u32x4*)(smem + G2_MRING + m_off(1) + (jj * 4 + pw) * 1024 + lane * 16);
    constexpr int SPB = SP0 + SP1E * SP1;
#define P2B(i) if constexpr (i >= N2A) piece(IC<2>{}, IC<i>{});
    SB(); P2B(0) P2B(1) P2B(2) P2B(3) P2B(4) P2B(5) P2B(6) P2B(7)
#define P3A(i) if constexpr (i < N3A) piece(IC<3>{}, IC<i>{});
    P3A(0) P3A(1) P3A(2) P3A(3) P3A(4) P3A(5) P3A(6) P3A(7)
    asm volatile("s_waitcnt vmcnt(%1)" : "+v"(fs) : "i"(8 - N2A + N3A) : "memory");
    ready = (unsigned)__ballot(fs == tag);
    need(2);
    G2_STAGE_M(2, NOW);
#define P3B(i) if constexpr (i >= N3A) piece(IC<3>{}, IC<i>{});
    P3B(0) P3B(1) P3B(2) P3B(3) P3B(4) P3B(5) P3B(6) P3B(7)
    need(3);
    G2_STAGE_M(3, NOW);
    fs = bld4_sc1(rf, abq[1]);
    touches(IC<1>{});
#pragma unroll
    for (int i = 0; i < dn_cnt(1); ++i) dv[i] = bld16_nt(rdn, vdn, i * D_ * 2);
    W21(0); W21(1); W21(2); W21(3); W21(4); W21(5); W21(6); W21(7); W21(8); W21(9); W21(10); W21(11);
    asm volatile("" : "+v"(tv[0]), "+v"(tv[1]), "+v"(tv[2]), "+v"(tv[3]));
    auto step2 = [&](auto JB) __attribute__((always_inline)) {
      constexpr int jb = decltype(JB)::value;
      if constexpr (jb == 3) PAGE(4);
      if constexpr (jb == 2 && PRIO2 != 3) __builtin_amdgcn_s_setprio(PRIO2);
      if constexpr (jb == 9) PAGE(5);
      constexpr auto must = [](int q) { return (q == 2 ? 4 : 0) + ((FSM >> q) & 1) + dig(GVS, q); };
      constexpr auto late = [](int q) { return dn_cnt(q) + (VAN > 0 && q == VAP) + (VBN > 0 && q == VBP) + (VCN > 0 && q == VCP); };
      if constexpr (jb == 2) wait_vm<8 - N3A + 4 + must(1) + late(1)>();
      else wait_vm<(dig(LAGS, jb - 2) ? 0 : late(jb - 2) + (jb == 5 ? 1 : 0)) + 12 - dig(LAGS, jb - 1) + must(jb - 1) + late(jb - 1)>();
      if constexpr (jb == 3) { if (pw == 0 && (lane & ~8) == 0) ((unsigned*)(smem + LDS_SEL))[tk] = ksel; }
      if constexpr ((jb == 2 || jb == DG + 1)) {
        *(f32x4*)(lg + pw * 256 + lane * 4) = macc;
        macc = (f32x4){0.f, 0.f, 0.f, 0.f};
        lgkm0(sd);
      }
      BAR();
      float p0[4], p1[4];
      const int col = (pw & 1) * 64 + lane;
      if constexpr (jb == 3) {
        if (g == 0 && pw == 0 && lane == 0) __hip_atomic_store((unsigned*)flags + FL_EPOCH, epoch + 1u, __ATOMIC_RELAXED, __HIP_MEMORY_SCOPE_AGENT);
      }
      if constexpr (jb == DG + 1) {
        const float* p = lg + (pw & 1) * 32 + (lane >> 4) * 64 + ((2 * lane) & 7) * 4 + ((lane >> 2) & 3);
#pragma unroll
        for (int i = 0; i < 4; ++i) { p0[i] = p[i * 256]; p1[i] = p[i * 256 + 4]; }
      }
      if constexpr (jb == 2) {
        const float* p = lg + (col >> 5) * 64 + (col & 7) * 4 + ((col >> 3) & 3);
#pragma unroll
        for (int i = 0; i < 4; ++i) { p0[i] = p[i * 256]; p1[i] = p[i * 256 + 32]; }
      }
      if constexpr (jb == 4) {
        if (lane == 8 + tt) __builtin_amdgcn_raw_buffer_store_b32(tag, rf, abo, 0, 16);
      }
      if constexpr ((FSM >> (jb - 2)) & 1) {
        asm volatile("" : "+v"(fs));
        ready |= (unsigned)__ballot(fs == tag);
      }
      if constexpr (jb == GV_JB + 2) asm volatile("" : "+v"(gv[0]), "+v"(gv[1]), "+v"(gv[2]), "+v"(gv[3]));
      auto w2 = [&](auto GP) __attribute__((always_inline)) {
        constexpr int gp = decltype(GP)::value;
        if constexpr (jb == 2 && gp == 0) {
          p0[0] = (p0[0] + p0[1]) + (p0[2] + p0[3]); p1[0] = (p1[0] + p1[1]) + (p1[2] + p1[3]);
          PIN2(p0[0], p1[0]);
        }
        if constexpr (gp < 12) work2(JB, GP);
        if constexpr (gp < dig(SPS, jb) && !((SPE >> jb) & 1)) spf(IC<SPB + acc_dig(SPS, jb) + gp>{});
      };
      constexpr int l0 = dig(LAGS, jb - 1), np = l0 + 12 - dig(LAGS, jb);
      auto slot = [&](auto I) __attribute__((always_inline)) {
        constexpr int i = decltype(I)::value;
        if constexpr (i < l0) piece(IC<jb + 1>{}, IC<12 - l0 + i>{});
        else if constexpr (i < np) piece(IC<jb + 2>{}, IC<i - l0>{});
        if constexpr (i < 12 || i < np) w2(I);
      };
#define SL4(i) slot(IC<i>{}); slot(IC<i + 1>{}); slot(IC<i + 2>{}); slot(IC<i + 3>{});
      SB();
      SL4(0) SL4(4) SL4(8) SL4(12) SL4(16) SL4(20)
      SB();
      if constexpr (jb == 2) {
#pragma unroll
        for (int jj = 0; jj < 4; ++jj) __builtin_amdgcn_raw_buffer_store_b128(tv[jj], rm2, vm2 + jj * 4096 + (2 * tt + 1) * 16384, 0, 16);
      }
      if constexpr ((FSM >> jb) & 1) fs = bld4_sc1(rf, abq[jb < 5 ? 1 : 2]);
#pragma unroll
      for (int i = acc_dig(GVS, jb); i < acc_dig(GVS, jb + 1); ++i) gv[i] = bld16_sc1(rgu, vgu, i * F_ * 16);
      touches(JB);
#pragma unroll
      for (int i = dn_first(jb); i < dn_first(jb) + dn_cnt(jb); ++i) dv[i] = bld16_nt(rdn, vdn, i * D_ * 2);
      if constexpr (jb == DG + 1) {
        if (pw < 2)
          __builtin_amdgcn_raw_buffer_store_b128((u32x4){__float_as_uint((p0[0] + p0[1]) + (p0[2] + p0[3])), tag, __float_as_uint((p1[0] + p1[1]) + (p1[2] + p1[3])), tag},
                                                 mk_rsrc(wsp + WS_DN), (((pw * 8 + cg8) * 32 + rgb) * 64 + lane) * 16, 0, 16);
      }
      if constexpr (jb == 2) {
        if (moe && pw < 2)
          __builtin_amdgcn_raw_buffer_store_b128((u32x4){__float_as_uint(p0[0]), tag, __float_as_uint(p1[0]), tag},
                                                 rgu, ((mat * 4 + dq) * F_ + cg * 128 + col) * 16, 0, 16);
      }
      SB();
#define SPX(i) spf(IC<(((i >= np && i >= 12) || ((SPE >> jb) & 1)) && i < dig(SPS, jb) ? SPB + acc_dig(SPS, jb) + i : -1)>{})
      if constexpr (dig(DGS, jb) > 0) {
        if (DGP != 3) __builtin_amdgcn_s_setprio(DGP);
#define DGU(i) if constexpr (i >= acc_dig(DGS, jb) && i < acc_dig(DGS, jb + 1)) work2(IC<100>{}, IC<i>{});
        DGU(0) DGU(1) DGU(2) DGU(3) DGU(4) DGU(5) DGU(6)
        if (DGP != 3) __builtin_amdgcn_s_setprio(3);
      }
      SPX(0); SPX(1); SPX(2); SPX(3); SPX(4); SPX(5); SPX(6); SPX(7); SPX(8); SPX(9); SPX(10); SPX(11); SPX(12); SPX(13); SPX(14);
      SB();
    };
    static_assert(DG + 1 <= 12 && DG >= GV_JB + 3 && dn_first(DG - 2) == 7 && acc_dig(GVS, GV_JB + 1) == 4 && dig(LAGS, 1) == 0 && dig(SPS, 1) == 0 && dig(LAGS, 0) == 0, "schedule");
    static_assert(DGS == 0 || (acc_dig(DGS, DG + 1) == 7 && acc_dig(DGS, GV_JB + 2) == 0 && acc_dig(DGS, 10) <= dn_first(7) && acc_dig(DGS, 11) <= dn_first(8) && acc_dig(DGS, 9) <= dn_first(6)), "units need their loads");
    static_assert(ALIAS || sp_tile((SPB + acc_dig(SPS, 14)) >> 4) < 0, "all tiles are prefetched");
    step2(IC<2>{}); step2(IC<3>{}); step2(IC<4>{}); step2(IC<5>{}); step2(IC<6>{}); step2(IC<7>{}); step2(IC<8>{}); step2(IC<9>{}); step2(IC<10>{}); step2(IC<11>{}); step2(IC<12>{}); step2(IC<13>{});
    lgkm0(sd);
    wait_vm<12 - dig(LAGS, 13)>(); BAR();
    {
      constexpr int l0 = dig(LAGS, 13);
#define PL(i) if constexpr (i >= 12 - l0) piece(IC<15>{}, IC<i>{});
      PL(0) PL(1) PL(2) PL(3) PL(4) PL(5) PL(6) PL(7) PL(8) PL(9) PL(10) PL(11)
    }
    __builtin_amdgcn_s_setprio(0);
    if (EPP == 1) { WAIT_VM(0); BAR(); }
    epi_setup(0); epi_setup(1); epi_setup(2);
    if (EPP == 2) { WAIT_VM(0); BAR(); }
#pragma unroll
    for (int n = 0; n < 3; ++n) xq_load(n);
    if (!EPP) { WAIT_VM(3); BAR(); }
    epi_setup(3);
    wait_xq<0>(xq, sq);
    key[0] = __builtin_amdgcn_readlane(ksel, 0); key[1] = __builtin_amdgcn_readlane(ksel, 8);
    cv_calc();
#pragma unroll 1
    for (int c = 0; c < 2; ++c) {
      if (__builtin_expect(((c ? far1 : far0) & ~9) != 0, 1)) continue;
      const __amdgpu_buffer_rsrc_t rd = mk_rsrc(wsp + WS_DN + (size_t)(c * 8 * 32 + pw * 8) * 1024);
      const int ec = seg0(lane >> 5, (lane >> 4) & 1), dno = (ec >> 7) * 32768 + ((ec & 127) / 2 + (lane & 15)) * 16;
#pragma unroll 1
      for (int it = 0; it < SPIN_MAX; ++it) {
        if (__all(__builtin_amdgcn_raw_buffer_load_b32(rd, (ec >> 7) * 32768 + (lane & 7) * 1024 + 1020, 0, 16) == tag)) break;
        __builtin_amdgcn_s_sleep(8);
        asm volatile("" ::: "memory");
      }
      float s0, s1;
      unsigned bad = 1;
#pragma unroll 1
      for (int it = 0; !__all(bad == 0) && it < SPIN_MAX; ++it) {
        bad = 0; s0 = 0.f; s1 = 0.f;
#pragma unroll
        for (int i = 0; i < 8; ++i) {
          const u32x4 q = __builtin_amdgcn_raw_buffer_load_b128(rd, dno, i * 1024, 16);
          bad |= (q.y ^ tag) | (q.w ^ tag); s0 += __uint_as_float(q.x); s1 += __uint_as_float(q.z);
        }
        asm volatile("" ::: "memory");
      }
      *(f32x2*)(smem + LDS_FIN + (c * 4 + pw) * 512 + lane * 8) = (f32x2){s0, s1};
      asm volatile("s_waitcnt lgkmcnt(0)" ::: "memory");
      if (lane == 0) asm volatile("ds_add_u32 %0, %1" :: "v"(lds_addr(smem + LDS_SEL + 24 + c * 4)), "v"(1) : "memory");
    }
  } else {
  {
    const int q = j * 4 + wid;
    constexpr int Q1 = WS_MB, Q2 = Q1 + 8, Q3 = Q2 + 8, Q4 = Q3 + 7, Q5 = Q4 + 7, Q6 = Q5 + 7, Q7 = Q6 + 4, Q8 = Q7 + 4, QC = Q8 + 1;
    static_assert(QC <= 128, "warm-up slots");
    unsigned dmy = 0;
    if (q < QC) {
      const char* base; int b0;
      if (q < Q1) { base = wsp; b0 = 0; }
      else if (q < Q2) { base = (const char*)ws_re; b0 = Q1; }
      else if (q < Q3) { base = (const char*)ws_im; b0 = Q2; }
      else if (q < Q4) { base = (const char*)w_gate; b0 = Q3; }
      else if (q < Q5) { base = (const char*)w_up; b0 = Q4; }
      else if (q < Q6) { base = (const char*)w_down; b0 = Q5; }
      else if (q < Q7) { base = (const char*)out; b0 = Q6; }
      else if (q < Q8) { base = (const char*)x; b0 = Q7; }
      else { base = (const char*)w_r; b0 = Q8; }
      base += (size_t)(q - b0) << 20;
      asm volatile("s_load_dword %0, %1, 0" : "+s"(dmy) : "s"(base) : "memory");
    }
    if (wid == 0) { CALL_PG(1); CALL_PG(8); CALL_PG(5); }
    else if (wid == 1) { CALL_PG(2); CALL_PG(9); CALL_PG(6); }
    else if (wid == 2) { CALL_PG(3); CALL_PG(10); }
    else { CALL_PG(4); CALL_PG(11); }
    BAR();
    lgkm0(dmy);
  }
  PAGE(8);
  epi_setup(0); epi_setup(1); epi_setup(2); epi_setup(3);
  const int wr = wid >> 1, wc = wid & 1;
  const int sw = swz(fr), fl = lrw(fr);
  const int ko0 = ((0 + fq) ^ sw) * 16, ko1 = ((4 + fq) ^ sw) * 16;
  bf16x8 fm[2][4], fn[2][4];
  {
    const char* pm = smem + (wr * 64 + fl) * 128;
    const char* pn = smem + 16384 + (wc * 64 + fl) * 128;
    f32x4 acc[4][4];
#pragma unroll
    for (int i = 0; i < 4; ++i)
#pragma unroll
      for (int jn = 0; jn < 4; ++jn)
#pragma unroll
        for (int q = 0; q < 4; ++q) acc[i][jn][q] = 0.f;
#define TO(i) ((i) * 2048)
#define LDSP(T, a) ((__attribute__((address_space(3))) T*)(size_t)(a))
#define SLOTS(n) _Pragma("unroll") for (int q = 0; q < (n); ++q)
#define MFA(c, m, n) asm volatile("v_mfma_f32_16x16x32_bf16 %0, %2, %1, %0" : "+v"(c) : "v"(m), "v"(n))
#define MFA2(c, m, n, m2, n2) asm volatile("v_mfma_f32_16x16x32_bf16 %0, %2, %1, %0\n\tv_mfma_f32_16x16x32_bf16 %0, %4, %3, %0" : "+v"(c) : "v"(m), "v"(n), "v"(m2), "v"(n2))
    unsigned lm[2][2], ln[2][2];
#pragma unroll
    for (int u = 0; u < 2; ++u)
#pragma unroll
      for (int kk = 0; kk < 2; ++kk) {
        lm[u][kk] = lds_addr(pm) + u * 2 * G1_STAGE + (kk ? ko1 : ko0);
        ln[u][kk] = lds_addr(pn) + u * 2 * G1_STAGE + (kk ? ko1 : ko0);
        asm volatile("" : "+v"(lm[u][kk]), "+v"(ln[u][kk]));
      }
    auto ldm = [&](int stg, int i, int kk) __attribute__((always_inline)) { fm[kk][i] = *LDSP(const bf16x8, lm[stg >> 1][kk] + (stg & 1) * G1_STAGE + TO(i)); };
    auto ldn = [&](int stg, int jn, int kk) __attribute__((always_inline)) { fn[kk][jn] = *LDSP(const bf16x8, ln[stg >> 1][kk] + (stg & 1) * G1_STAGE + TO(jn)); };
    auto quad = [&](int a, int c, int d, int s) __attribute__((always_inline)) {
      const int i = (s < 2 || s >= 6) ? a : a + 1, jn = s < 4 ? c : d, kk = (s == 1 || s == 2 || s == 5 || s == 6);
      MFA(acc[i][jn], fm[kk][i], fn[kk][jn]);
    };
    auto quad2 = [&](int a, int c, int d, int s) __attribute__((always_inline)) {
      const int i = (s < 2 || s >= 6) ? a : a + 1, jn = s < 4 ? c : d, kk = (s == 2 || s == 6);
      MFA2(acc[i][jn], fm[kk][i], fn[kk][jn], fm[1 - kk][i], fn[1 - kk][jn]);
    };
    auto half1 = [&](int stg) __attribute__((always_inline)) {
      const int ja = (stg & 1) * 2, jb = 2 - ja;
      SLOTS(8) {
        quad(0, ja, ja + 1, q);
        if (q < 4) ldn(stg, jb + (q >> 1), q & 1); else ldm(stg, 2 + ((q - 4) >> 1), q == 5 || q == 6);
        SB();
      }
      SLOTS(4) { quad2(0, jb, jb + 1, 2 * q); SB(); }
    };
    auto half2 = [&](int stg) __attribute__((always_inline)) {
      const int ja = (stg & 1) * 2, jb = 2 - ja, nx = (stg + 1) & 3;
      SLOTS(8) {
        quad(2, jb + 1, jb, q);
        if (!(q & 1)) ldm(nx, q >> 2, q == 2 || q == 4);
        SB();
      }
      SLOTS(8) {
        quad(2, ja, ja + 1, q);
        if (!(q & 1)) ldn(nx, jb + (q >> 2), (q >> 1) & 1);
        SB();
      }
    };
    unsigned ae[7];
    {
      int l_ = lane;
      asm volatile("" : "+v"(l_));
      const int p = l_ >> 4, dn = sym_dn(p), hf = (p >> 1) ^ (dn & z);
#pragma unroll
      for (int u = 0; u < 7; ++u) {
        const int T = u < 3 ? wc * 4 : (wc * 4 + u - 3 - dn) & 7, i = u < 3 ? (u + 1) ^ ((u & 1) ? 0 : dn * 2) : 0;
        const int f = dn * 8 + ((T & 2) << 1) + ((T & 4) >> 1) + (T & 1);
        ae[u] = lds_addr(smem + G2_MRING) + (hf * 64 + i * 16 + lrw(f)) * 128 + (((wr * 4 + ((l_ >> 2) & 3)) ^ swz(f)) * 16) + (l_ & 3) * 4;
        asm volatile("" : "+v"(ae[u]));
      }
    }
    unsigned al = lds_addr(smem + LDS_MB) + (wr * 32 + fr * 2) * 4, ac = lds_addr(smem + LDS_MB) + (256 + wc * 64 + fq * 4) * 4;
    asm volatile("" : "+v"(al), "+v"(ac));
    SLOTS(16) asm volatile("" : "+v"(acc[q >> 2][q & 3]));
    SB();
    SLOTS(4) { ldm(0, q >> 1, q & 1); ldn(0, q >> 1, q & 1); }
    SB();
#pragma unroll 1
    for (int k4 = 0; k4 < G1_NKT - 4; k4 += 4) {
      half1(0); BAR_LGKM();
      SB(); half2(0);
      half1(1); BAR_LGKM();
      SB(); half2(1);
      half1(2); BAR_LGKM();
      SB(); half2(2);
      half1(3); BAR_LGKM();
      SB(); half2(3);
    }
    bf16x8 gm[2][4], gn[2][8];
    auto rdg = [&](int set, int t, int h, int r) __attribute__((always_inline)) {
      const int u = (t >> 1) & 1, o = (t & 1) * G1_STAGE;
      if (r == 0 || r == 2 || r == 4 || r == 5) {
        const int i = r >= 4, kk = r == 2 || r == 4;
        gm[set][kk * 2 + i] = *LDSP(const bf16x8, lm[u][kk] + o + h * 4096 + i * 2048);
      } else {
        const int jn = r < 4 ? 0 : (r - 4) >> 1, kk = r < 4 ? r >> 1 : r & 1;
        gn[set][kk * 4 + jn] = *LDSP(const bf16x8, ln[u][kk] + o + TO(jn));
      }
    };
    auto mmg = [&](int set, int h, int q) __attribute__((always_inline)) {
      const int jn = q >> 2, r = q & 3, i = (r >> 1) ^ (jn & 1), kk = r == 1 || r == 2;
      MFA(acc[h * 2 + i][jn], gm[set][kk * 2 + i], gn[set][kk * 4 + jn]);
    };
    f32x2 rsv, mrv;
    f32x4 cev[4];
    float tc[32];
    auto rds = [&](int h, int r) __attribute__((always_inline)) {
      if (r == 0) mrv = *LDSP(const f32x2, al + h * 256); else if (r == 1) rsv = *LDSP(const f32x2, al + 512 + h * 256);
    };
    auto prep = [&](int n) __attribute__((always_inline)) {
      tc[n] = llvm_fmul_legacy(mrv[n & 1], cev[n >> 3][(n >> 1) & 3]);
      asm volatile("" : "+v"(tc[n]));
    };
    auto epi = [&](int h, int q) __attribute__((always_inline)) {
      const int jn = q >> 1, r = (q & 1) * 2, o = m_off(h) + (jn & 1) * 128 + (jn >> 1) * lrw(4) * 128;
      asm volatile("v_fmac_f32 %0, %4, %8\n\tv_fmac_f32 %1, %5, %9\n\tv_fmac_f32 %2, %6, %8\n\tv_fmac_f32 %3, %7, %9"
                   : "+v"(tc[q * 4]), "+v"(tc[q * 4 + 1]), "+v"(tc[q * 4 + 2]), "+v"(tc[q * 4 + 3])
                   : "v"(acc[h * 2][jn][r]), "v"(acc[h * 2 + 1][jn][r]), "v"(acc[h * 2][jn][r + 1]), "v"(acc[h * 2 + 1][jn][r + 1]),
                     "v"(rsv[0]), "v"(rsv[1]));
      *LDSP(unsigned, r ? ae[1] + o : ae[3 + jn] + m_off(h)) = pk2(tc[q * 4], tc[q * 4 + 1]);
      *LDSP(unsigned, ae[r] + o) = pk2(tc[q * 4 + 2], tc[q * 4 + 3]);
    };
    SB();
    half1(0);
    BAR_LGKM();
    SB();
    SLOTS(16) {
      if (q < 8) quad(2, 3, 2, q); else quad(2, 0, 1, q - 8);
      if (q < 12) rdg(0, 13, 0, q);
      SB();
    }
    BAR();
    SB();
    SLOTS(16) {
      mmg(0, 0, q);
      if (q < 12) rdg(1, 14, 0, q);
      else { rds(0, q - 12); cev[q - 12] = *LDSP(const f32x4, ac + (q - 12) * 64); }
      SB();
    }
    PAGE(9);
    BAR();
    SB();
    SLOTS(16) { mmg(1, 0, q); if ((q & 3) < 3) rdg(0, 15, 0, (q >> 2) * 3 + (q & 3)); if (q >= 8) { prep(2 * q - 16); prep(2 * q - 15); } SB(); }
    SLOTS(16) { mmg(0, 0, q); if ((q & 3) < 3) rdg(1, 13, 1, (q >> 2) * 3 + (q & 3)); prep(16 + q); SB(); if (q >= 9 && (q & 1)) { epi(0, (q - 9) >> 1); SB(); } }
    SLOTS(8) { mmg(1, 1, q); if (q < 6) { rdg(0, 14, 1, 2 * q); rdg(0, 14, 1, 2 * q + 1); } SB(); if (q & 1) { epi(0, 4 + (q >> 1)); SB(); } }
    BAR_LGKM();
    SB();
    SLOTS(8) { mmg(1, 1, 8 + q); if (q < 4) rdg(1, 15, 1, q == 0 ? 1 : q == 1 ? 3 : q + 4); else rds(1, q - 4); SB(); }
    SLOTS(16) { mmg(0, 1, q); if (q < 8) rdg(1, 15, 1, q == 0 ? 0 : q == 1 ? 2 : q < 4 ? q + 2 : q + 4); prep(q); SB(); }
    asm volatile("s_waitcnt lgkmcnt(0)" ::: "memory");
    if (lane == 0) asm volatile("ds_add_u32 %0, %1" :: "v"(lds_addr(smem + LDS_SEL + 12)), "v"(1) : "memory");
    SB();
    SLOTS(16) {
      mmg(1, 1, q);
      if (q < 4) fn[0][q] = *(const bf16x8*)(smem + (wr * 128 + fl) * 128 + (q >> 1) * 2048 + ((q & 1) ? ko1 : ko0));
      else if (q < 8) fm[q & 1][(q - 4) >> 1] = *(const bf16x8*)(smem + G2_MRING + m_off(0) + (wc * 64 + fl) * 128 + TO((q - 4) >> 1) + ((q & 1) ? ko1 : ko0));
      prep(16 + q);
      SB();
    }
    {
      lds_vi rel = (lds_vi)(smem + LDS_SEL + 12);
      int it = 0;
      while (rel[0] != 4 && it < SPIN_MAX * 8) ++it;
      asm volatile("" ::: "memory");
    }
    asm volatile("s_nop 7");
    SB();
    SLOTS(8) { epi(1, q); SB(); }
  }

  const char* pn = smem + (wr * 128 + fl) * 128;
  const char* pm = smem + G2_MRING + (wc * 64 + fl) * 128;
  f32x4 acc[8][4];
  auto mm = [&](bool imaj, int set, int q2, int s, int mode) __attribute__((always_inline)) {
    int i, jj, kk;
    if (imaj) { i = s >> 2; const int r = s & 3; jj = (r >> 1) ^ (i & 1); kk = (r == 1 || r == 2); }
    else { jj = s >> 3; const int r = s & 7; i = jj ? 3 - (r >> 1) : (r >> 1); kk = ((r + 1) >> 1) & 1; }
    if (mode == 1 && !(s & 1)) asm volatile("v_mfma_f32_16x16x32_bf16 %0, %2, %1, 0" : "=&v"(acc[q2 * 2 + jj][i]) : "v"(fm[kk][i]), "v"(fn[set][jj * 2 + kk]));
    else if (mode) asm volatile("v_mfma_f32_16x16x32_bf16 %0, %2, %1, %0" : "+v"(acc[q2 * 2 + jj][i]) : "v"(fm[kk][i]), "v"(fn[set][jj * 2 + kk]));
    else acc[q2 * 2 + jj][i] = __builtin_amdgcn_mfma_f32_16x16x32_bf16(fn[set][jj * 2 + kk], fm[kk][i], acc[q2 * 2 + jj][i], 0, 0, 0);
  };
  unsigned bn[2], bm[2], an[2], am[2];
#pragma unroll
  for (int kk = 0; kk < 2; ++kk) {
    bn[kk] = lds_addr(pn) + (kk ? ko1 : ko0); bm[kk] = lds_addr(pm) + (kk ? ko1 : ko0);
    asm volatile("" : "+v"(bn[kk]), "+v"(bm[kk]));
    an[kk] = bn[kk]; am[kk] = bm[kk] + m_off(0);
  }
#define RDN(jt, kk, o) (*LDSP(const bf16x8, an[kk] + (o) + (jt) * 2048))
#define RDM(i, kk, o) (*LDSP(const bf16x8, am[kk] + (o) + TO(i)))
  PAGE(10);
  tag = __builtin_amdgcn_readfirstlane(((const unsigned*)(smem + LDS_SEL))[5]);
  {
    int son = 0;
    auto tile = [&](int kt, int zero) __attribute__((always_inline)) {
      SLOTS(16) {
        mm(true, 0, 0, q, zero);
        if (!(q & 1)) {
          if (q < 8) fm[(q >> 1) & 1][2 + (q >> 2)] = RDM(2 + (q >> 2), (q >> 1) & 1, 0);
          else { const int r = (q - 8) >> 1; fn[1][r] = RDN(2 + (r >> 1), r & 1, 0); }
        }
        SB();
      }
      SLOTS(16) {
        mm(false, 1, 1, q, zero);
        if (!(q & 1) && q < 8) fn[0][q >> 1] = RDN(4 + (q >> 2), (q >> 1) & 1, 0);
        SB();
      }
      const int som = son >> 1;
      son = son == 65536 ? 0 : son + 32768;
      SLOTS(16) {
        mm(false, 0, 2, q, zero);
        if (!(q & 1) && q < 8) fn[1][q >> 1] = RDN(6 + (q >> 2), (q >> 1) & 1, 0);
        if (q == 9 || q == 11) { an[q == 11] = bn[q == 11] + son; asm volatile("" : "+v"(an[q == 11])); }
        if (q == 13 || q == 15) { am[q == 15] = bm[q == 15] + som; asm volatile("" : "+v"(am[q == 15])); }
        SB();
      }
      BAR_LGKM();
      SB();
      SLOTS(16) {
        mm(true, 1, 3, q, zero);
        if (!(q & 1)) {
          if (q < 8) fn[0][q >> 1] = RDN(q >> 2, (q >> 1) & 1, 0);
          else fm[(q >> 1) & 1][(q - 8) >> 2] = RDM((q - 8) >> 2, (q >> 1) & 1, 0);
        }
        SB();
      }
    };
    SB();
    tile(0, 1);
#pragma unroll 1
    for (int kt = 1; kt < G2_NKT - 3; ++kt) tile(kt, 0);
  }
  unsigned fx;
  constexpr int XC1 = XL0 >= 104 ? 3 : 0, XC2 = XL0 >= 152 ? 3 : 0;
  static_assert(XL0 + 2 * XLS < FL0 && (XL0 + 2 * XLS < 80 || XL0 >= 152 || (XL0 >= 104 && XL0 + 2 * XLS < 144)), "");
  int fo = lane == 0 ? kq * 512 : (int)0x80000000;
  {
    constexpr int NG = 12;
    static constexpr int g_t[NG] = {0, 0, 1, 1, 2, 2, 2, 1, 0, 0, 1, 2};
    static constexpr int g_q[NG] = {0, 1, 0, 1, 0, 1, 2, 2, 2, 3, 3, 3};
    constexpr int G14 = 1, G15 = 3, GP0 = 5, GP1 = 6;
    constexpr auto chp = [](int g) { return g == 0 || g_t[g] != g_t[g - 1]; };
    constexpr auto chn = [](int g) { return g + 1 < NG && g_t[g + 1] != g_t[g]; };
    static_assert(n_off(13) == 32768 && n_off(14) == 65536 && n_off(15) == 0, "");
    unsigned an2[2];
#pragma unroll
    for (int kk = 0; kk < 2; ++kk) {
      an[kk] = bn[kk]; an2[kk] = bn[kk] + 65536; am[kk] = bm[kk];
      asm volatile("" : "+v"(an[kk]), "+v"(an2[kk]), "+v"(am[kk]));
    }
    int vo;
    u32x4 pk;
    u32x2 kv;
    auto publish = [&](int d, int h) __attribute__((always_inline)) {
      if (h == 0) { vo = ml; PINV(vo); vo += (((d + kq + 1) & 3) * 4 + kq) * 4 * MSG; PINV(vo); }
      const f32x4 v = acc[2 * d + (h >> 2)][h & 3];
      pk[(h & 1) * 2] = pk2(v[0], v[1]); pk[(h & 1) * 2 + 1] = pk2(v[2], v[3]);
      if (h & 1) __builtin_amdgcn_raw_buffer_store_b128(pk, rs, vo + (h >> 1) * 1024, 0, 16);
    };
#define MSG_FLAG(d) __builtin_amdgcn_raw_buffer_store_b32(tag, rc, fo + (((d) + kq + 1) & 3) * 2048, 0, 16)
    auto rdn = [&](int g, int r) __attribute__((always_inline)) {
      const int t = g_t[g], jt = g_q[g] * 2 + (r >> 1);
      fn[g & 1][r] = *LDSP(const bf16x8, (t == 1 ? an2[r & 1] : an[r & 1]) + (t == 0 ? 32768 : 0) + jt * 2048);
    };
    auto rdm = [&](int t, int i, int kk) __attribute__((always_inline)) { fm[kk][i] = *LDSP(const bf16x8, am[kk] + m_off(13 + t) + TO(i)); };
    auto group = [&](auto GI) __attribute__((always_inline)) {
      constexpr int gi = decltype(GI)::value;
      if (gi == G14) { BAR(); if (CPRI) __builtin_amdgcn_s_setprio(CPRI);
        SB(); }
      if (gi == G15) {
        PAGE(11);
        BAR();
        SB();
        kv = *(const u32x2*)(smem + LDS_SEL);
        SB();
      }
#pragma unroll
      for (int q = 0; q < 16; ++q) {
        const int n = gi * 16 + q;
        if (n == TS1) {
        }
        if (n == TS2) {
        }
        if (n == FL0) { wait_vm<8 + XC1>(); MSG_FLAG(0); SB(); }
        if (n == FL1) { wait_vm<5 + XC1>(); MSG_FLAG(1); SB(); }
        if (n == FL2) { wait_vm<2 + XC2>(); MSG_FLAG(2); fx = bld4_sc1(rc, po); SB(); }
        mm(chp(gi) || chn(gi), gi & 1, g_q[gi], q, 2);
        constexpr bool ni = gi + 1 < NG && (chp(gi + 1 < NG ? gi + 1 : gi) || chn(gi + 1 < NG ? gi + 1 : gi));
        auto nr = [](int r) { return ni && r >= 2 ? 5 - r : r; };
        if constexpr (chp(gi) && chn(gi)) {
          if (q < 4) rdm(g_t[gi], 2 + (q >> 1), q & 1); else if (q < 8) rdn(gi + 1, nr(q - 4)); else if (q < 12) rdm(g_t[gi + 1], (q - 8) >> 1, q & 1);
        } else if constexpr (chp(gi)) {
          if (q < 4) rdm(g_t[gi], 2 + (q >> 1), q & 1);
          else if (gi + 1 < NG && q < 12 && !(q & 1)) rdn(gi + 1, nr((q - 4) >> 1));
        } else if constexpr (chn(gi)) {
          if (!(q & 1)) { if (q < 8) rdn(gi + 1, nr(q >> 1)); else rdm(g_t[gi + 1], (q - 8) >> 2, (q >> 1) & 1); }
        } else {
          if (!(q & 1) && q < 8) rdn(gi + 1, nr(q >> 1));
        }
        if (gi == GP0 && q < 8) publish(0, q);
        if (gi == GP1 && q < 8) publish(1, q);
        if (gi == 9 && q < 8) publish(2, q);
        if (n >= XL0 && n < XL0 + 3 * XLS && (n - XL0) % XLS == 0) xq_load((n - XL0) / XLS);
        if (n == 72) { key[0] = __builtin_amdgcn_readfirstlane(kv.x); key[1] = __builtin_amdgcn_readfirstlane(kv.y); }
        SB();
      }
    };
#define GR4(i) group(IC<i>{}); group(IC<i + 1>{}); group(IC<i + 2>{}); group(IC<i + 3>{});
    GR4(0) GR4(4) GR4(8)
    asm volatile("s_nop 7");
    SB();
  }
#pragma unroll
  for (int i = 0; i < 4; ++i) *(f32x4*)(smem + LDS_ACC + wr * 16384 + wc * 4096 + i * 1024 + lane * 16) = acc[7][i];
#pragma unroll
  for (int i = 0; i < 4; ++i) ya[i] = acc[6][i];
  asm volatile("s_waitcnt lgkmcnt(0)" ::: "memory");
  if (lane == 0) sig[0] = 2;
  SB();
  if (FL0 >= 192) { wait_vm<8 + XC1>(); MSG_FLAG(0); SB(); }
  if (FL1 >= 192) { wait_vm<5 + XC1>(); MSG_FLAG(1); SB(); }
  if (FL2 >= 192) { wait_vm<2 + XC2>(); MSG_FLAG(2); fx = bld4_sc1(rc, po); SB(); }
  wait_xq<4>(xq, sq);
  cv_calc();
  asm volatile("s_waitcnt vmcnt(0)" : "+v"(fx) :: "memory");
  seen = (unsigned)__ballot(fx == tag);
  }

  PAGE(6);
  if (__builtin_expect(far0 == 0 || far1 == 0, 0)) {
#pragma unroll 1
    for (int c = 0; c < 2; ++c) {
      if (c ? far1 : far0) continue;
      const int ic = (0xffff - (c ? key[1] : key[0])) & (S_ - 1);
      const float sg = rbf(1.f / (1.f + __expf(-key_score(c ? key[1] : key[0]))));
      lds_vi cnt = (lds_vi)(smem + LDS_SEL + 24 + c * 4);
      int it = 0;
      while (cnt[0] != 4 && it < SPIN_MAX * 8) { __builtin_amdgcn_s_sleep(1); ++it; }
      asm volatile("" ::: "memory");
      const float* q = (const float*)(smem + LDS_FIN + c * 2048) + (wid & 1) * 64 + (fr >> 3) * 32 + col4(fr);
      const f32x4 v = (*(const f32x4*)q + *(const f32x4*)(q + 128)) + (*(const f32x4*)(q + 256) + *(const f32x4*)(q + 384));
#pragma unroll
      for (int r = 0; r < 4; ++r)
#pragma unroll
        for (int k = 0; k < 4; ++k) {
          cv[r * 4 + k] += (xo[r] >> 11) == ic ? v[k] * sg : 0.f;
        }
    }
  }
  if (ha) {
    int it = 0;
    while (sig[0] != 2 && it < SPIN_MAX * 8) { __builtin_amdgcn_s_sleep(1); ++it; }
    asm volatile("" ::: "memory");
#pragma unroll
    for (int i = 0; i < 4; ++i) ya[i] = *(const f32x4*)(smem + LDS_ACC + (wid & 2) * 8192 + (wid & 1) * 4096 + i * 1024 + lane * 16);
  }
  u32x4 pp[3][2];
  unsigned e_lo = 0x00003f80u, e_hi = 0x3f800000u;
  asm volatile("" : "+v"(e_lo), "+v"(e_hi));
  __amdgpu_buffer_rsrc_t rs_ = rs, rc_ = rc;
  int mo[3] = {MSG_IDX(0) * 4 * MSG, MSG_IDX(1) * 4 * MSG, MSG_IDX(2) * 4 * MSG}, mv = ml + ha * 2048, mv2 = mv + 1024;
  asm volatile("" : "+s"(rs_), "+s"(rc_), "+s"(mo[0]), "+s"(mo[1]), "+s"(mo[2]));
  asm volatile("" : "+v"(mv), "+v"(mv2));
#define MSG_POLL(m) do { \
    if (!((seen >> (m)) & 1)) { \
      unsigned pv_, it_ = SPIN_MAX; \
      asm volatile("1:\n\tbuffer_load_dword %0, %3, %4, 0 offen sc1\n\ts_waitcnt vmcnt(0)\n\tv_cmp_eq_u32 vcc, %5, %0\n\ts_mov_b32 %1, vcc_lo\n\t" \
                   "s_bitcmp1_b32 %1, %6\n\ts_cbranch_scc1 2f\n\ts_sub_u32 %2, %2, 1\n\ts_cbranch_scc1 2f\n\ts_sleep 1\n\ts_branch 1b\n2:" \
                   : "=&v"(pv_), "+s"(seen), "+s"(it_) : "v"(po), "s"(rc_), "s"(tag), "i"(m) : "vcc", "scc", "memory"); \
    } \
    pp[m][0] = __builtin_amdgcn_raw_buffer_load_b128(rs_, mv, mo[m], 16); \
    pp[m][1] = __builtin_amdgcn_raw_buffer_load_b128(rs_, mv2, mo[m], 16); } while (0)
#define MSG_ADD(m) do { \
    _Pragma("unroll") for (int u = 0; u < 8; ++u) { \
      ya[u >> 1][(u & 1) * 2] = add_bf(pp[m][u >> 2][u & 3], e_lo, ya[u >> 1][(u & 1) * 2]); ya[u >> 1][(u & 1) * 2 + 1] = add_bf(pp[m][u >> 2][u & 3], e_hi, ya[u >> 1][(u & 1) * 2 + 1]); } } while (0)
  MSG_POLL(2); MSG_POLL(1); MSG_POLL(0);
  SB();
#pragma unroll
  for (int e = 0; e < 16; ++e) ya[e & 3][e >> 2] += cv[e];
  asm volatile("" : "+v"(ya[0]), "+v"(ya[1]), "+v"(ya[2]), "+v"(ya[3]));
  SB();
  MSG_ADD(2); MSG_ADD(1);
  asm volatile("" : "+v"(ya[0]), "+v"(ya[1]), "+v"(ya[2]), "+v"(ya[3]));
  SB();
  MSG_ADD(0);
  {
#pragma unroll
    for (int r = 0; r < 4; ++r) *(u32x2*)((char*)out + (unsigned)xo[r]) = (u32x2){pk2(ya[0][r], ya[1][r]), pk2(ya[2][r], ya[3][r])};
  }
}

extern "C" void kernel_launch(void* const* d_in, const int* in_sizes, int n_in, void* d_out, int out_size,
                              void* d_ws, size_t ws_size, hipStream_t stream) {
  const u16* x = (const u16*)d_in[0];
  const u16* ws_re = (const u16*)d_in[1];
  const u16* ws_im = (const u16*)d_in[2];
  const u16* wd_re = (const u16*)d_in[3];
  const u16* wd_im = (const u16*)d_in[4];
  const u16* w_r = (const u16*)d_in[6];
  const u16* w_gate = (const u16*)d_in[7];
  const u16* w_up = (const u16*)d_in[8];
  const u16* w_down = (const u16*)d_in[9];
  k_main<<<256, 512, 0, stream>>>(x, wd_re, wd_im, w_r, (char*)d_ws, ws_re, ws_im, w_gate, w_up, w_down, (u16*)d_out);
}
```

```cpp
#include <hip/hip_runtime.h>
#include <hip/hip_bf16.h>
#include <stdint.h>
#include <type_traits>

extern "C" __device__ unsigned long long llvm_dispatch_id(void) __asm("llvm.amdgcn.dispatch.id");
extern "C" __device__ float llvm_fmul_legacy(float, float) __asm("llvm.amdgcn.fmul.legacy");
typedef unsigned short u16;
typedef unsigned long long u64;
typedef __attribute__((ext_vector_type(8))) short bf16x8;
typedef __attribute__((ext_vector_type(4))) unsigned u32x4;
typedef __attribute__((ext_vector_type(2))) unsigned u32x2;
typedef __attribute__((ext_vector_type(4))) float f32x4;
typedef __attribute__((ext_vector_type(2))) float f32x2;
typedef __attribute__((ext_vector_type(2))) __bf16 bf16x2;
typedef __attribute__((address_space(3))) void* lds_vp;
typedef volatile __attribute__((address_space(3))) int* lds_vi;

constexpr int S_ = 2048, D_ = 1024, F_ = 3584;
template <int N> using IC = std::integral_constant<int, N>;

static __device__ __forceinline__ float bf2f(u16 u) { return __uint_as_float((unsigned)u << 16); }
static __device__ __forceinline__ u16 f2bf(float f) {
  __hip_bfloat16 b = __float2bfloat16(f);
  return *reinterpret_cast<u16*>(&b);
}
static __device__ __forceinline__ float rbf(float f) { return bf2f(f2bf(f)); }
static __device__ __forceinline__ float lo_bf(unsigned u) { return __uint_as_float(u << 16); }
static __device__ __forceinline__ float hi_bf(unsigned u) { return __uint_as_float(u & 0xffff0000u); }
static __device__ __forceinline__ unsigned pk2(float a, float b) {
  const bf16x2 v = __builtin_convertvector((f32x2){a, b}, bf16x2);
  return __builtin_bit_cast(unsigned, v);
}
static __device__ __forceinline__ float add_bf(unsigned u, unsigned e, float c) {
  return __builtin_amdgcn_fdot2_f32_bf16(__builtin_bit_cast(bf16x2, u), __builtin_bit_cast(bf16x2, e), c, false);
}
static __device__ __forceinline__ float ln_elem(float x, float mb, float sd, float ri) {
  const float d = rbf(x - mb);
  const float q0 = d * ri;
  return rbf(__builtin_fmaf(__builtin_fmaf(-q0, sd, d), ri, q0));
}
template <int CTRL> static __device__ __forceinline__ float dpp_f(float v) {
  return __int_as_float(__builtin_amdgcn_update_dpp(0, __float_as_int(v), CTRL, 0xf, 0xf, true));
}
template <int CTRL> static __device__ __forceinline__ unsigned dpp_u(unsigned v) {
  return (unsigned)__builtin_amdgcn_update_dpp(0, (int)v, CTRL, 0xf, 0xf, true);
}
static __device__ __forceinline__ float row_sum(float v) {
  v += dpp_f<0xB1>(v);
  v += dpp_f<0x4E>(v);
  v += dpp_f<0x141>(v);
  v += dpp_f<0x140>(v);
  return v;
}
static __device__ __forceinline__ float fold32(float a, float b) {
  const auto r = __builtin_amdgcn_permlane32_swap(__float_as_uint(a), __float_as_uint(b), false, false);
  return __uint_as_float(r[0]) + __uint_as_float(r[1]);
}
static __device__ __forceinline__ float fold16(float a, float b) {
  const auto r = __builtin_amdgcn_permlane16_swap(__float_as_uint(a), __float_as_uint(b), false, false);
  return __uint_as_float(r[0]) + __uint_as_float(r[1]);
}
static __device__ __forceinline__ float rdl(float v, int l) { return __int_as_float(__builtin_amdgcn_readlane(__float_as_int(v), l)); }

#define TL(I) do { if ((I) == 14 || (I) == 1) { const unsigned _t = (unsigned)__builtin_amdgcn_s_memrealtime(); tlv = lane == (I) ? _t : tlv; } } while (0)
#define XMAP(g) (((~g & 1) << 2) | (g & 2) | ((g >> 2) & 1))
#define ST_JB 9
#define WSN 10
#define FSM 0xa2
#define GV_JB 7
#define DG 11
#define AB_STRIDE 32
#define SPIN_MAX (1 << 17)
#define FL0 168
#define FL1 192
#define FL2 192
#define VA0 5
#define VAN 4
#define VAP 116
#define VB0 12
#define VBN 0
#define VBP 6
#define SP0 3
#define SP1 7
#define SP1E 11
#define VC0 14
#define VCN 0
#define VCP 9
#define SPS 0x000cccccdddd00
#define SPE 0
#define PRIO2 3
#define N2A 8
#define N3A 8
#define WSL 2
#define LNP 0
#define LNS 2
#define EPP 0
#define DGS 0
#define DGP 0
#define TS1 112
#define TS2 152
#define XL0 33
#define XLS 4
#define CPRI 0
#define LAGS 0x00000000000000
#define DVS 0x00000400300000
#define GVS 0x00000022000000
constexpr int dig(unsigned long long t, int q) { return q < 0 ? 0 : (int)((t >> (4 * q)) & 15); }
constexpr int acc_dig(unsigned long long t, int q) { int a = 0; for (int i = 0; i < q; ++i) a += dig(t, i); return a; }

static __device__ __forceinline__ unsigned lds_addr(const void* p) { return (unsigned)(size_t)(lds_vp)p; }

constexpr int MSG = 4096;
constexpr size_t WS_ABT   = 0;
constexpr size_t WS_SLAB  = WS_ABT + (size_t)D_ * 4096 * 2;
constexpr size_t WS_GU    = WS_SLAB + (size_t)64 * 64 * MSG;
constexpr size_t WS_DN    = WS_GU + (size_t)8 * F_ * 16;
constexpr size_t WS_FLAGS = WS_DN + (size_t)2 * 8 * 32 * 64 * 16;
constexpr int FL_X = 0;
constexpr int FL_AB = 64 * 64 * 32;
constexpr int FL_ST = FL_AB + 2048 * AB_STRIDE;
constexpr int FL_CE = FL_ST + 4096;
constexpr int FL_KEY = FL_CE + 4096;
constexpr int FL_EPOCH = FL_KEY + 4096;
constexpr int WS_MB = (int)((WS_FLAGS + (FL_EPOCH + 1) * 4 + 1048575) >> 20);

#define WAIT_VM(n) asm volatile("s_waitcnt vmcnt(" #n ")" ::: "memory")
template <int N> static __device__ __forceinline__ void wait_vm() { asm volatile("s_waitcnt vmcnt(%0)" :: "i"(N) : "memory"); }
#define BAR_LGKM() asm volatile("s_waitcnt lgkmcnt(0)\n\ts_barrier" ::: "memory")
#define BAR() asm volatile("s_barrier" ::: "memory")
#define SB() __builtin_amdgcn_sched_barrier(0)
#define PAGE(n) asm volatile("s_branch 2f\n\t.p2align 12\nk_pg" #n ":\n\ts_setpc_b64 s[42:43]\n2:")
#define CALL_PG(n) asm volatile("s_getpc_b64 s[40:41]\n3:\n\ts_mov_b32 s42, k_pg" #n "-3b\n\ts_ashr_i32 s43, s42, 31\n\ts_add_u32 s40, s40, s42\n\ts_addc_u32 s41, s41, s43\n\t" \
                                "s_swappc_b64 s[42:43], s[40:41]" ::: "s40", "s41", "s42", "s43", "scc", "memory")

static __device__ __forceinline__ __amdgpu_buffer_rsrc_t mk_rsrc(const void* p) {
  return __builtin_amdgcn_make_buffer_rsrc((void*)p, 0, 0x7fffffff, 0x00020000);
}
#define DMA_(rsrc, voff, mbase, MI, sbase, SI, pol) do { unsigned _t; \
    asm volatile("s_add_i32 m0, %2, %3\n\ts_add_i32 %0, %5, %6\n\tbuffer_load_dwordx4 %1, %4, %0 offen" pol " lds" \
                 : "=&s"(_t) : "v"(voff), "s"(mbase), "i"(MI), "s"(rsrc), "s"(sbase), "i"(SI) : "memory", "scc"); } while (0)
#define DMA_I(rsrc, voff, mbase, MI, SI) \
    asm volatile("s_add_i32 m0, %1, %2\n\ts_nop 0\n\tbuffer_load_dwordx4 %0, %3, 0 offen offset:%4 lds" \
                 :: "v"(voff), "s"(mbase), "i"((MI) - (SI)), "s"(rsrc), "i"(SI) : "memory", "scc")
#define DMA_S(rsrc, voff, mbase, MI, soff) \
    asm volatile("s_add_i32 m0, %1, %2\n\ts_nop 0\n\tbuffer_load_dwordx4 %0, %3, %4 offen lds" \
                 :: "v"(voff), "s"(mbase), "i"(MI), "s"(rsrc), "s"(soff) : "memory", "scc")
#define TPOL ""
#define TOUCH(rsrc, voff, mbase, MI, soff) \
    asm volatile("s_add_i32 m0, %1, %2\n\ts_nop 0\n\tbuffer_load_dword %0, %3, %4 offen" TPOL " lds" \
                 :: "v"(voff), "s"(mbase), "i"(MI), "s"(rsrc), "s"(soff) : "memory", "scc")
template <int OFF> static __device__ __forceinline__ u32x4 ld16_s(const void* p, int voff) {
  u32x4 v;
  asm volatile("global_load_dwordx4 %0, %1, %2 offset:%3" : "=v"(v) : "v"(voff), "s"(p), "i"(OFF) : "memory");
  return v;
}
template <int OFF> static __device__ __forceinline__ void sld(unsigned& sink, const void* p, int soff) {
  asm volatile("s_load_dword %0, %1, %2 offset:%3" : "+s"(sink) : "s"(p), "s"(soff), "i"(OFF) : "memory");
}
static __device__ __forceinline__ unsigned gld4_s(const void* p, unsigned voff) {
  unsigned v;
  asm volatile("global_load_dword %0, %1, %2" : "=v"(v) : "v"(voff), "s"(p) : "memory");
  return v;
}
static __device__ __forceinline__ u32x2 bld8_sc1(__amdgpu_buffer_rsrc_t r, unsigned voff) {
  u32x2 v;
  asm volatile("buffer_load_dwordx2 %0, %1, %2, 0 offen sc1" : "=v"(v) : "v"(voff), "s"(r) : "memory");
  return v;
}
template <int N> static __device__ __forceinline__ void wait_xs(unsigned& a, u32x2& b) { asm volatile("s_waitcnt vmcnt(%2)" : "+v"(a), "+v"(b) : "i"(N) : "memory"); }
template <int N> static __device__ __forceinline__ void wait_xq(u32x4 (&a)[2], u32x2& b) { asm volatile("s_waitcnt vmcnt(%3)" : "+v"(a[0]), "+v"(a[1]), "+v"(b) : "i"(N) : "memory"); }
static __device__ __forceinline__ u32x2 ld8_s_sc1(const void* p, int voff) {
  u32x2 v;
  asm volatile("global_load_dwordx2 %0, %1, %2 sc1" : "=v"(v) : "v"(voff), "s"(p) : "memory");
  return v;
}
static __device__ __forceinline__ void lgkm0(unsigned& sink) { asm volatile("s_waitcnt lgkmcnt(0)" : "+s"(sink) :: "memory"); }
template <typename T> static __device__ __forceinline__ void pin4(T& a, T& b, T& c, T& d) { asm volatile("" : "+s"(a), "+s"(b), "+s"(c), "+s"(d)); }
template <typename T, typename U> static __device__ __forceinline__ void pins(T& a, U& b) { asm volatile("" : "+s"(a), "+s"(b)); }
static __device__ __forceinline__ void launder(int& v, int& a, int& b) { asm volatile("" : "+v"(v)); asm volatile("" : "+s"(a), "+s"(b)); }
static __device__ __forceinline__ unsigned bld4_sc1(__amdgpu_buffer_rsrc_t r, int voff) {
  unsigned v;
  asm volatile("buffer_load_dword %0, %1, %2, 0 offen sc1" : "=v"(v) : "v"(voff), "s"(r) : "memory");
  return v;
}
static __device__ __forceinline__ u32x4 bld16_nt(__amdgpu_buffer_rsrc_t r, int voff, int soff) {
  u32x4 v;
  asm volatile("buffer_load_dwordx4 %0, %1, %2, %3 offen nt" : "=v"(v) : "v"(voff), "s"(r), "s"(soff) : "memory");
  return v;
}
static __device__ __forceinline__ void bld16_nt_io(u32x4& v, __amdgpu_buffer_rsrc_t r, int voff, int soff) {
  asm volatile("buffer_load_dwordx4 %0, %1, %2, %3 offen nt" : "+v"(v) : "v"(voff), "s"(r), "s"(soff) : "memory");
}
static __device__ __forceinline__ u32x4 bld16_sc1(__amdgpu_buffer_rsrc_t r, int voff, int soff) {
  u32x4 v;
  asm volatile("buffer_load_dwordx4 %0, %1, %2, %3 offen sc1" : "=v"(v) : "v"(voff), "s"(r), "s"(soff) : "memory");
  return v;
}
static __device__ __forceinline__ u32x2 granule_wait(const u64* p, unsigned tag) {
  u64 v = 0;
#pragma unroll 1
  for (int it = 0; it < SPIN_MAX; ++it) {
    v = __hip_atomic_load(p, __ATOMIC_RELAXED, __HIP_MEMORY_SCOPE_AGENT);
    if (__all((unsigned)(v >> 32) == tag)) break;
    __builtin_amdgcn_s_sleep(2);
  }
  return (u32x2){(unsigned)v, (unsigned)(v >> 32)};
}
static __device__ __forceinline__ float key_score(unsigned key) {
  const unsigned u = key >> 16;
  return bf2f((u16)((u & 0x8000u) ? (u & 0x7fffu) : (~u & 0xffffu)));
}
static __device__ __forceinline__ unsigned med3u(unsigned a, unsigned b, unsigned c) { return max(min(a, b), min(max(a, b), c)); }
static __device__ __forceinline__ void merge2(unsigned& a1, unsigned& a2, unsigned b1, unsigned b2) {
  const unsigned lo = min(a1, b1);
  a1 = max(a1, b1);
  a2 = max(lo, max(a2, b2));
}
static __device__ __forceinline__ float sqdev16(const u32x4 (&v)[2], float mb) {
  f32x2 q2 = {0.f, 0.f};
#pragma unroll
  for (int i = 0; i < 8; ++i) {
    const f32x2 e = (f32x2){lo_bf(v[i >> 2][i & 3]), hi_bf(v[i >> 2][i & 3])} - mb;
    q2 += e * e;
  }
  return q2.x + q2.y;
}

constexpr int lrw(int r) { return (r & ~6) | ((r & 2) << 1) | ((r & 4) >> 1); }
constexpr int swz(int r) { return ((r >> 1) & 4) | (r & 2); }
static __device__ __forceinline__ int sym_dn(int p) { return (p ^ (p >> 1)) & 1; }
static __device__ __forceinline__ int sym_hi(int n, int w, int st, int b, int z, int p) {
  return ((p >> 1) * (n / 2 * st) + b * (w * st)) ^ (-sym_dn(p) & (((n - 1) ^ (z * (n / 2))) * st));
}
static __device__ __forceinline__ int sym_lo(int w, int st, int p, int c) { return ((c - sym_dn(p)) * st) & ((w - 1) * st); }
static __device__ __forceinline__ int sym_row(int n, int w, int st, int b, int z, int p, int c) { return sym_lo(w, st, p, c) ^ sym_hi(n, w, st, b, z, p); }
constexpr int G1_NST = 4, G1_NKT = 16, G1_STAGE = 32768;
constexpr int G2_NKT = 16, G2_MRING = 98304;
constexpr int n_off(int k) { return (k % 3) * 32768; }
constexpr int m_off(int k) { return ((k + 2) % 3) * 16384; }
constexpr int k_ord(int k) { return k < 2 ? k : (k < 9 ? 2 * (k - 1) : 2 * (k - 8) + 1); }
constexpr int LDS_DUMMY = G2_MRING + 49152;
constexpr int LDS_GU = LDS_DUMMY + 4096;
constexpr int LDS_FIN = LDS_GU + 4096;
constexpr int LDS_MB = LDS_FIN + 4096;
constexpr int LDS_SEL = LDS_MB + 1536;
constexpr int LDS_SIG = LDS_SEL + 64;
constexpr int LDS_KEY = LDS_SIG + 32;
constexpr int LDS_BYTES = LDS_KEY + 128;
constexpr int LDS_ACC = n_off(G2_NKT - 3);

constexpr int sp_tile(int q) {
  for (int k = 0; k < 16; ++k) {
    if ((k >= VA0 && k < VA0 + VAN) || (k >= VB0 && k < VB0 + VBN) || (k >= VC0 && k < VC0 + VCN)) continue;
    if (q-- == 0) return k;
  }
  return -1;
}
constexpr int tcnt(int q) { return (VAN > 0 && q == VAP) + (VBN > 0 && q == VBP) + (VCN > 0 && q == VCP); }
constexpr int dn_cnt(int q) { return dig(DVS, q); }
constexpr int dn_first(int q) { return acc_dig(DVS, q); }

static __device__ __forceinline__ void code_touch(unsigned& sink, unsigned off) {
  asm volatile("s_getpc_b64 s[40:41]\n\tk_main_rt:\n\ts_min_u32 %1, %1, k_main_end-k_main_rt-196\n\t"
               "s_load_dword %0, s[40:41], %1 offset:0\n\ts_load_dword %0, s[40:41], %1 offset:64\n\ts_load_dword %0, s[40:41], %1 offset:128\n\ts_load_dword %0, s[40:41], %1 offset:192"
               : "+s"(sink), "+s"(off) :: "s40", "s41", "scc", "memory");
}
static __device__ __forceinline__ void touch1(__amdgpu_buffer_rsrc_t r, int voff, unsigned mb, int soff) { TOUCH(r, voff, mb, LDS_DUMMY, soff); }
typedef const __attribute__((address_space(1))) u16* gptr;
typedef __attribute__((ext_vector_type(8))) u64 u64x8;
typedef __attribute__((ext_vector_type(2))) u64 u64x2;
extern "C" __global__ __launch_bounds__(512, 2) void k_main(const u16* x, const u16* wd_re, const u16* wd_im, const u16* w_r,
                                                 char* wsp, const u16* ws_re, const u16* ws_im,
                                                 const u16* w_gate, const u16* w_up, const u16* w_down, u16* out) {
  __shared__ __attribute__((aligned(16))) char smem[LDS_BYTES];
  u64x8 ka;
  u64x2 kb_;
  u64 kc;
  asm volatile("s_load_dwordx16 %0, %3, 0\n\ts_load_dwordx4 %1, %3, 64\n\ts_load_dwordx2 %2, %3, 80"
               : "=&s"(ka), "=&s"(kb_), "=&s"(kc) : "s"(__builtin_amdgcn_kernarg_segment_ptr()) : "memory");
  unsigned long long t00;
  asm volatile("s_memrealtime %0" : "=s"(t00) :: "memory");
  const int tid = threadIdx.x, lane = tid & 63;
  const int wid = __builtin_amdgcn_readfirstlane(tid >> 6);
  const int g = blockIdx.x, xcd = XMAP(g), j = g >> 3;
  if (wid < 4) {
    unsigned cw, co = (j * 4 + wid) * 448;
    asm volatile(".subsection 1\nk_main_end:\n.subsection 0\n\ts_min_u32 %1, %1, k_main_end-k_main-480\n\ts_getpc_b64 s[40:41]\n\ts_add_u32 s40, s40, %1\n\ts_addc_u32 s41, s41, 0\n\t"
                 "s_load_dword %0, s[40:41], 0\n\ts_load_dword %0, s[40:41], 64\n\ts_load_dword %0, s[40:41], 128\n\t"
                 "s_load_dword %0, s[40:41], 192\n\ts_load_dword %0, s[40:41], 256\n\ts_load_dword %0, s[40:41], 320\n\ts_load_dword %0, s[40:41], 384\n\ts_waitcnt lgkmcnt(0)"
                 : "=&s"(cw), "+s"(co) :: "s40", "s41", "scc", "memory");
  }
  const int pw = wid - 4;
  const int row0 = (wid & 3) * 8 + lrw(lane >> 3);
  const int cm = ((lane & 7) ^ swz(row0)) * 16;
  int vm = ((row0 & 15) * 2 + (row0 >> 4)) * (D_ * 2) + cm;
  asm volatile("" : "+v"(vm));
  const int kq = xcd >> 1;
  const int tt = (xcd & 1) * 4 + (j >> 3);
  const int et = j & 7;
  const int z = kq >> 1, st = (kq & 1) * 8 + tt;
  const int lrow = (kq & 1) * 1024 + (xcd & 1) * 512 + z * 256 + j * 8 + pw * 2;
  const int drow = ((kq & 1) * 2 + (xcd & 1)) * 256 + j * 8 + pw * 2;
  unsigned o_m = st * 128 * D_ * 2, o_l = lrow * D_ * 2, o_d = drow * D_ * 2, mb0 = lds_addr(smem) + pw * 1024;
  int l16 = lane * 16;
  int vn[4];
#pragma unroll
  for (int q = 0; q < 4; ++q) vn[q] = sym_lo(32, D_ * 2, (row0 >> 2) & 3, q * 8 + (row0 >> 4) * 4 + (row0 & 3)) ^ (sym_hi(D_, 32, D_ * 2, et, z, (row0 >> 2) & 3) | cm);
  asm volatile("" : "+s"(o_m), "+s"(o_l), "+s"(o_d), "+s"(mb0));
  asm volatile("" : "+v"(l16), "+v"(vn[0]), "+v"(vn[1]), "+v"(vn[2]), "+v"(vn[3]));
  asm volatile("s_waitcnt lgkmcnt(0)" : "+s"(ka), "+s"(kb_), "+s"(kc) :: "memory");
  x = (const u16*)(gptr)ka[0]; wd_re = (const u16*)(gptr)ka[1]; wd_im = (const u16*)(gptr)ka[2]; w_r = (const u16*)(gptr)ka[3];
  wsp = (char*)(gptr)ka[4]; ws_re = (const u16*)(gptr)ka[5]; ws_im = (const u16*)(gptr)ka[6]; w_gate = (const u16*)(gptr)ka[7];
  w_up = (const u16*)(gptr)kb_[0]; w_down = (const u16*)(gptr)kb_[1]; out = (u16*)(gptr)kc;
  int* flags = (int*)(wsp + WS_FLAGS);
  u64* stat = (u64*)(flags + FL_ST);
  unsigned tlv = 0;
  asm volatile("s_waitcnt lgkmcnt(0)" : "+s"(t00) :: "memory");
  tlv = lane == 52 ? (unsigned)t00 : tlv;
  const u16* wdz = z ? wd_im : wd_re;
  const __amdgpu_buffer_rsrc_t rm = mk_rsrc(x);
  const __amdgpu_buffer_rsrc_t rn = mk_rsrc(wdz);
#define G1_X(kt, so, q) DMA_(rm, vm, mb0, (so) + (q) * 4096, o_m, (kt) * 128 + (((q) & 1) * 64 + ((q) >> 1) * 32) * D_ * 2, "")
#define G1_W(kt, so, q) DMA_I(rn, vn[q], mb0, (so) + 16384 + (q) * 4096, (kt) * 128)
#define G1_STAGE_TILE(kt, so, W) do { SB(); G1_X(kt, so, 0); W(0); G1_X(kt, so, 1); W(1); G1_X(kt, so, 2); W(2); G1_X(kt, so, 3); W(3); \
    G1_W(kt, so, 0); W(4); G1_W(kt, so, 1); W(5); G1_W(kt, so, 2); W(6); G1_W(kt, so, 3); W(7); SB(); } while (0)
#define G1_STAGE_A(kt, so, W) do { SB(); G1_X(kt, so, 0); W(0); G1_X(kt, so, 2); W(1); \
    G1_W(kt, so, 0); W(2); G1_W(kt, so, 1); W(3); G1_W(kt, so, 2); W(4); G1_W(kt, so, 3); W(5); W(6); W(7); SB(); } while (0)
#define G1_STAGE_B(kt, so) do { SB(); G1_X(kt, so, 1); G1_X(kt, so, 3); SB(); } while (0)
#define NOW(i)
  const int fr = lane & 15, fq = lane >> 4;
  const int ha = wid >> 2;
  int trow, xo[4], xa[3], po, ml;
  const char *prs, *prc;
  auto col4 = [](int f) __attribute__((always_inline)) { return ((f & 2) << 3 | (f & 4) << 1 | (f & 1) << 2) ^ (-((f >> 3) & 1) & 28); };
  auto seg0 = [&](int hf, int dn) __attribute__((always_inline)) { return (hf * 512 + et * 32) ^ (-dn & (D_ - 32)); };
  auto epi_setup = [&](int part) __attribute__((always_inline)) {
    int t_ = tt, l_ = lane;
    asm volatile("" : "+s"(t_));
    asm volatile("" : "+v"(l_));
    const int ts = ((wid >> 1) & 1) * 8 + kq * 2 + ha;
    if (part == 0) { xo[3] = (seg0(wid & 1, (l_ >> 3) & 1) + col4(l_)) * 2; asm volatile("" : "+v"(xo[3])); }
    if (part == 1) {
      xo[3] |= sym_hi(S_, 64, D_ * 2, t_, z, l_ >> 4);
      ml = (l_ * 16) ^ (-sym_dn(l_ >> 4) & (z * 768));
      asm volatile("" : "+v"(xo[3]), "+v"(ml));
    }
    if (part == 2) {
      const int xh = xo[3];
#pragma unroll
      for (int r = 0; r < 4; ++r) xo[r] = sym_lo(64, D_ * 2, l_ >> 4, ts * 4 + r) ^ xh;
      const bool hi = xo[0] & 8;
      xa[0] = (hi ? xo[2] : xo[0]) & ~15; xa[1] = (hi ? xo[3] : xo[1]) & ~15;
      xa[2] = (sym_lo(64, D_ * 2, l_ >> 4, ts * 4 + (l_ & 3)) ^ xh) >> 11 << 3;
      asm volatile("" : "+v"(xo[0]), "+v"(xo[1]), "+v"(xo[2]), "+v"(xo[3]), "+v"(xa[0]), "+v"(xa[1]), "+v"(xa[2]));
    }
    if (part == 3) {
      trow = t_ * 16 + ts;
      prs = wsp + WS_SLAB + (size_t)((t_ * 8 + et) * 64 + (wid & 3)) * MSG;
      prc = (const char*)(flags + FL_X + ((t_ * 8 + et) * 64 + (wid & 3)) * 32);
      asm volatile("" : "+s"(trow), "+s"(prs), "+s"(prc));
      po = l_ < 3 ? (kq * 4 + ((kq + 1 + l_) & 3)) * 512 : (int)0x80000000;
      asm volatile("" : "+v"(po));
    }
  };
  auto tok_tile = [](unsigned key) __attribute__((always_inline)) {
    const unsigned h = (0xffff - key) & 1023;
    return h < 512 ? h >> 2 : ((1023 - h) >> 6 << 4) | (((1024 - h) & 63) >> 2);
  };
#define rs mk_rsrc(prs)
#define rc mk_rsrc(prc)
  lds_vi sig = (lds_vi)(smem + LDS_SIG + (wid & 3) * 4);
  unsigned tag;
  u32x4 xq[2];
  u32x2 sq;
  f32x4 ya[4];
  unsigned key[2];
  float cv[16];
  unsigned far0, far1;
  auto xq_load = [&](int n) __attribute__((always_inline)) {
    if (n < 2) xq[n] = ld16_s<0>(x, xa[n]); else sq = ld8_s_sc1(stat, xa[2]);
  };
#define PINV(a) asm volatile("" : "+v"(a))
  auto cv_calc = [&]() __attribute__((always_inline)) {
    if (__builtin_expect(!__all(sq.y == tag), 0)) sq = granule_wait(stat + (xa[2] >> 3), tag);
    far0 = tok_tile(key[0]) ^ trow; far1 = tok_tile(key[1]) ^ trow;
    asm volatile("" : "+s"(far0), "+s"(far1));
    const bool hi = xo[0] & 8;
    const float mb_ = lo_bf(sq.x), ri_ = __builtin_amdgcn_rcpf(hi_bf(sq.x));
#pragma unroll
    for (int r = 0; r < 4; ++r) {
      const float mb = r == 0 ? dpp_f<0x00>(mb_) : r == 1 ? dpp_f<0x55>(mb_) : r == 2 ? dpp_f<0xAA>(mb_) : dpp_f<0xFF>(mb_);
      const float ri = r == 0 ? dpp_f<0x00>(ri_) : r == 1 ? dpp_f<0x55>(ri_) : r == 2 ? dpp_f<0xAA>(ri_) : dpp_f<0xFF>(ri_);
#pragma unroll
      for (int u = 0; u < 2; ++u) {
        const unsigned own = xq[r & 1][(r & 2) + u], oth = dpp_u<0xB1>(xq[r & 1][2 - (r & 2) + u]);
        const unsigned w = hi == (r >= 2) ? own : oth;
        cv[r * 4 + 2 * u] = __builtin_fmaf(lo_bf(w) - mb, ri, lo_bf(w));
        cv[r * 4 + 2 * u + 1] = __builtin_fmaf(hi_bf(w) - mb, ri, hi_bf(w));
        PINV(cv[r * 4 + 2 * u]); PINV(cv[r * 4 + 2 * u + 1]);
      }
    }
  };
#define MSG_IDX(m) (kq * 4 + ((kq + 1 + (m)) & 3))
  unsigned seen = 0;

  if (__builtin_expect(wid >= 4, 1)) {
    __builtin_amdgcn_s_setprio(3);
    G1_STAGE_TILE(0, 0, NOW);
    constexpr int O_ST = (FL_ST - FL_AB) * 4, O_CE = (FL_CE - FL_AB) * 4, O_KEY = (FL_KEY - FL_AB) * 4, O_EP = (FL_EPOCH - FL_AB) * 4;
    const __amdgpu_buffer_rsrc_t rf = mk_rsrc(wsp + WS_FLAGS + FL_AB * 4);
    u32x4 lv[2], lv2[2], lw[2], ld[2], ld2[2];
    unsigned epv;
    {
      const char* xrow = (const char*)x + o_l;
      const char* wdrow = (const char*)wdz + o_d;
      lv[0] = ld16_s<0>(xrow, l16); lv[1] = ld16_s<1024>(xrow, l16);
      lv2[0] = ld16_s<2048>(xrow, l16); lv2[1] = ld16_s<3072>(xrow, l16);
      ld[0] = ld16_s<0>(wdrow, l16); ld[1] = ld16_s<1024>(wdrow, l16);
      ld2[0] = ld16_s<2048>(wdrow, l16); ld2[1] = ld16_s<3072>(wdrow, l16);
      lw[0] = ld16_s<0>(w_r, l16); lw[1] = ld16_s<1024>(w_r, l16);
      asm volatile("buffer_load_dword %0, off, %1, %2 sc1" : "=v"(epv) : "s"(rf), "s"(O_EP) : "memory");
    }
    SB();
    const unsigned long long did = llvm_dispatch_id();
    const unsigned dmix = (unsigned)did * 0x85EBCA6Bu + (unsigned)(did >> 32) * 0xC2B2AE35u + 0x27D4EB2Fu;
    const bool moe = et != 7;
    unsigned epoch;
    const int ci = lane & 15, rq = lane >> 4, tk = (lane >> 3) & 1;
    int mat, cg, dq, d0;
    u32x4 wv[16];
    const u16 *pgu, *pn2;
    const char *pm2, *spb;
#define rw __builtin_amdgcn_make_buffer_rsrc((void*)pgu, 0, moe ? 0x7fffffff : 0, 0x00020000)
#define rn2 mk_rsrc(pn2)
#define rm2 mk_rsrc(pm2)
    int vw, vn2[8], vm2;
    auto k_blk = [&](int k) __attribute__((always_inline)) { return tt ^ (k_ord(k) >> 1); };
#define KROT(kk_) (k_blk(kk_) * 256 + (k_ord(kk_) & 1) * 128)
    int tv2[3];
    unsigned sd = 0;
    auto spf = [&](auto N) __attribute__((always_inline)) {
      constexpr int n = decltype(N)::value;
      if constexpr (n >= 0 && sp_tile(n >> 4) >= 0) sld<((n >> 1) & 7) * 4096 + (n & 1) * 64>(sd, spb, KROT(sp_tile(n >> 4)));
    };
    auto touches = [&](auto P) __attribute__((always_inline)) {
      constexpr int p = decltype(P)::value;
      if constexpr (VAN > 0 && p == VAP) touch1(mk_rsrc(spb), tv2[0], mb0, 0);
      if constexpr (VBN > 0 && p == VBP) touch1(mk_rsrc(spb), tv2[1], mb0, 0);
      if constexpr (VCN > 0 && p == VCP) touch1(mk_rsrc(spb), tv2[2], mb0, 0);
    };
    unsigned mq0, mq1, mq2, mq3;
    const int cg8 = et ^ ((et & 1) << 1), rgb = (g & 7) * 4 + (j >> 3);
    const __amdgpu_buffer_rsrc_t rdn = mk_rsrc(w_down);
    const __amdgpu_buffer_rsrc_t rgu = mk_rsrc(wsp + WS_GU);
    int vdn, vgu;
    u32x4 dv[7], gv[4];
    int abo, abq[3];
    unsigned lma;
    unsigned sel[2][4];
    float g0, g1;
    u32x4 kk[4];
    unsigned k1 = 0, k2 = 0;
    const int ko_ = O_KEY + pw * 4096;
#define MM(a, b, c) __builtin_amdgcn_mfma_f32_16x16x32_bf16(__builtin_bit_cast(bf16x8, a), __builtin_bit_cast(bf16x8, b), c, 0, 0, 0)
#define PIN1(a) asm volatile("" : "+v"(a))
#define PIN2(a, b) asm volatile("" : "+v"(a), "+v"(b))
#define PIN3(a, b, c) asm volatile("" : "+v"(a), "+v"(b), "+v"(c))
    {
      int gro, go;
      u32x2 gr;
      u32x4 oh[5];
      f32x4 sa4, gw[2], gx[2];
      float su, gs, ta, tb, sx, mbv;
      unsigned dk[4], kl;
      const int hb = lane >> 5;
#pragma unroll
      for (int l = 0; l < WSN; ++l) asm volatile("" : "=v"(wv[l]));
      auto diag = [&](const f32x4& m) __attribute__((always_inline)) {
        return __uint_as_float((__float_as_uint(m[0]) & dk[0]) | (__float_as_uint(m[1]) & dk[1]) | (__float_as_uint(m[2]) & dk[2]) | (__float_as_uint(m[3]) & dk[3]));
      };
      auto setup = [&](auto JB, auto GP) __attribute__((always_inline)) {
        constexpr int jb = decltype(JB)::value, gp = decltype(GP)::value;
        SB();
        {
          int ln = lane, jj = j, p_ = pw;
          launder(ln, jj, p_);
          const int c_ = ln & 15, r_ = ln >> 4, e_ = jj & 7, t_ = (xcd & 1) * 4 + (jj >> 3);
          if constexpr (jb == 1 && gp == 0) {
            pn2 = ((kq >> 1) ? ws_im : ws_re) + (kq & 1) * 1024;
            spb = (const char*)(pn2 + (sym_row(S_, 64, 1, t_, 0, e_ >> 1, 0) + (e_ & 1) * 32 + p_ * 8) * S_);
            pins(pn2, spb);
          }
          if constexpr (jb == 1 && gp >= 1 && gp < 6) {
            const unsigned o = c_ == gp - 1 ? 0x3f803f80u : 0u;
            oh[gp - 1] = (u32x4){o, o, o, o};
            PIN1(oh[gp - 1]);
          }
          if constexpr (jb == 1 && gp == 6) {
            const int rk = moe ? e_ * 8 + (jj >> 3) * 2 + (xcd >> 2) : 0, r3 = ((rk - 14) * 43) >> 7, rr = rk - 14 - r3 * 3;
            const int c56 = rk < 14 ? rk * 4 + 1 : r3 * 4 + rr + ((rr + 1) >> 1);
            mat = (c56 + 36) >> 6; cg = c56 - mat * 28; dq = xcd & 3; d0 = dq * 256 + p_ * 64;
            pin4(mat, cg, dq, d0);
          }
          if constexpr (jb == 2 && gp == 0) {
            pgu = mat ? w_up : w_gate;
            pm2 = wsp + WS_ABT + (kq * 8 + e_) * 262144;
            pins(pgu, pm2);
            vw = ((d0 + r_ * 16) * F_ + cg * 128 + c_ * 8) * 2;
            PIN1(vw);
          }
          if constexpr (jb == 2 && (gp == 1 || gp == 2)) {
            dk[gp * 2 - 2] = c_ == r_ * 4 + gp * 2 - 2 ? ~0u : 0u; dk[gp * 2 - 1] = c_ == r_ * 4 + gp * 2 - 1 ? ~0u : 0u;
            PIN2(dk[gp * 2 - 2], dk[gp * 2 - 1]);
          }
          if constexpr (jb == 2 && gp == 3) { kl = lrow + (ln >> 5); go = (ln & 30) ? (int)0x80000000 : ((ln & 1) ? O_KEY : O_ST) + (int)kl * 8; PIN2(kl, go); }
          if constexpr (jb == 2 && gp == 4) { kl = 0xffff - kl; go = (ln & ~1) == 2 ? O_CE + (z * D_ + drow + (ln & 1)) * 8 : go; PIN2(kl, go); }
          if constexpr (jb == 2 && gp == 5) {
            gro = p_ < 2 ? O_ST + (st * 128 + p_ * 64 + ln) * 8 : O_CE + z * D_ * 8 + sym_row(D_, 32, 8, e_, z, (ln >> 2) & 3, (p_ - 2) * 16 + r_ * 4 + (ln & 3));
            lma = lds_addr(smem + LDS_MB) + (p_ < 2 ? p_ * 256 : 512 + p_ * 256) + ln * 4;
            PIN2(gro, lma);
          }
          if constexpr (jb == 1 && gp == 7) {
#pragma unroll
            for (int u = 0; u < 3; ++u) {
              const int vn_ = u == 0 ? VAN : u == 1 ? VBN : VCN;
              if (vn_ == 0) continue;
              const int k = (u == 0 ? VA0 : u == 1 ? VB0 : VC0) + (ln >> 3);
              tv2[u] = (ln >> 3) < vn_ ? (ln & 7) * 4096 + ((t_ ^ (k < 9 ? k - 1 : k - 8)) << 8) + (k < 9 ? 0 : 128) : (int)0x80000000;
              PIN1(tv2[u]);
            }
          }
          if constexpr (jb == 7 && gp == 0) {
            vm2 = p_ * 1024 + ln * 16;
            PIN1(vm2);
          }
          if constexpr (jb == 2 && gp == 6) {
            const int r0 = p_ * 8 + lrw(ln >> 3);
            vn2[6] = sym_lo(64, S_ * 2, (r0 >> 2) & 3, (r0 >> 4) * 4 + (r0 & 3)); vn2[7] = sym_hi(S_, 64, S_ * 2, t_, z, (r0 >> 2) & 3) | (vm & 127);
            PIN2(vn2[6], vn2[7]);
          }
          if constexpr ((jb == 2 && gp == 7) || (jb == 7 && gp >= 6)) {
            constexpr int q0 = jb == 2 ? 0 : gp == 6 ? 3 : 6;
            const int lo = vn2[6], hi = vn2[7];
#pragma unroll
            for (int q = q0; q < q0 + 3 && q < 8; ++q) { vn2[q] = ((lo + q * 8 * S_ * 2) & (63 * S_ * 2)) ^ hi; PIN1(vn2[q]); }
          }
          if constexpr (jb == 7 && gp == 1) { vdn = rgb * 112 + p_ * 28 + r_ * 7; vgu = (((ln >> 3) & 1) * 4 * F_ + vdn + min(ln & 7, 6)) * 16; PIN2(vdn, vgu); }
          if constexpr (jb == 7 && gp == 2) { vdn = (vdn * D_ + cg8 * 128 + c_ * 8) * 2; PIN1(vdn); }
          if constexpr (jb == 7 && gp == 3) {
            abo = (((kq * 8 + e_) * 4 + p_) * 16 + c_) * AB_STRIDE * 4;
            abq[2] = ln >= 16 || (ln & 7) == t_ ? (int)0x80000000 : abo;
            PIN2(abo, abq[2]);
          }
          if constexpr (jb == 7 && gp == 4) {
            abq[1] = ln >= 8 ? (int)0x80000000 : abq[2];
            abq[0] = ((ln ^ t_) & 4) ? (int)0x80000000 : abq[1];
            PIN2(abq[0], abq[1]);
          }
          if constexpr (jb == 7 && gp == 5) {
            const unsigned mb = lds_addr(smem) + p_ * 1024;
            mq0 = mb + ((3 - kq) & 3) * 4096; mq1 = mb + ((4 - kq) & 3) * 4096; mq2 = mb + ((5 - kq) & 3) * 4096; mq3 = mb + ((6 - kq) & 3) * 4096;
            pin4(mq0, mq1, mq2, mq3);
          }
          if constexpr (jb == 8 && gp < 4) {
#pragma unroll
            for (int h = 0; h < 2; ++h) {
              const unsigned hv = h ? 0x0302u : 0x0100u;
              sel[h][gp] = ((ln & 7) >> 1) == gp ? ((ln & 1) ? (hv << 16) | 0x0c0cu : 0x0c0c0000u | hv) : 0x0c0c0c0cu;
            }
            PIN2(sel[0][gp], sel[1][gp]);
          }
        }
        SB();
      };
#define WS1(i) setup(IC<1>{}, IC<i>{})
#define WS2(i) setup(IC<2>{}, IC<i>{})
#define WS3(i) do { setup(IC<7>{}, IC<i>{}); if (i >= 4) setup(IC<8>{}, IC<(i) & 3>{}); spf(IC<(i < SP0 ? i : -1)>{}); } while (0)
      G1_STAGE_TILE(1, G1_STAGE, WS1);
      WAIT_VM(19); BAR();
      G1_STAGE_TILE(2, 2 * G1_STAGE, WS2);
      G1_STAGE_TILE(3, 3 * G1_STAGE, WS3);
      auto lnu = [&](auto U) __attribute__((always_inline)) {
        constexpr int jb = 1 + decltype(U)::value / 7, gp = decltype(U)::value % 7;
        SB();
        if constexpr (jb == 1) {
          const f32x4 z4 = {0.f, 0.f, 0.f, 0.f};
          if constexpr (gp == 0) { sa4 = MM(lv[0], oh[0], z4); gw[0] = MM(lv[0], lw[0], z4); gx[0] = MM(lv[0], lv[0], z4); PIN3(sa4, gw[0], gx[0]); }
          if constexpr (gp == 1) { sa4 = MM(lv2[0], oh[1], sa4); gw[1] = MM(lv2[0], lw[0], z4); gx[1] = MM(lv2[0], lv2[0], z4); PIN3(sa4, gw[1], gx[1]); }
          if constexpr (gp == 2) { sa4 = MM(lv[1], oh[0], sa4); gw[0] = MM(lv[1], lw[1], gw[0]); gx[0] = MM(lv[1], lv[1], gx[0]); PIN3(sa4, gw[0], gx[0]); }
          if constexpr (gp == 3) { sa4 = MM(lv2[1], oh[1], sa4); gw[1] = MM(lv2[1], lw[1], gw[1]); gx[1] = MM(lv2[1], lv2[1], gx[1]); PIN3(sa4, gw[1], gx[1]); }
          if constexpr (gp == 4) { sa4 = MM(ld[0], oh[2], sa4); sa4 = MM(ld[1], oh[2], sa4); PIN1(sa4); }
          if constexpr (gp == 5) { sa4 = MM(ld2[0], oh[3], sa4); sa4 = MM(ld2[1], oh[3], sa4); PIN1(sa4); }
          if constexpr (gp == 6) { sa4 = MM(lw[0], oh[4], sa4); sa4 = MM(lw[1], oh[4], sa4); PIN1(sa4);
            epoch = __builtin_amdgcn_readfirstlane(epv); tag = ((epoch + 1u) * 0x9E3779B1u ^ dmix) | 1u; }
        }
        if constexpr (jb == 2) {
          if constexpr (gp == 0) { su = (sa4[0] + sa4[1]) + (sa4[2] + sa4[3]); ta = diag(gw[0]); PIN2(su, ta); }
          if constexpr (gp == 1) { su = fold32(su, su); tb = diag(gw[1]); PIN2(su, tb); }
          if constexpr (gp == 2) { su = fold16(su, su); gs = fold32(ta, tb); PIN2(su, gs); }
          if constexpr (gp == 3) { ta = diag(gx[0]); tb = diag(gx[1]); PIN2(ta, tb); }
          if constexpr (gp == 4) { gs = fold16(gs, fold32(ta, tb)); PIN1(gs); }
          if constexpr (gp == 5) { gs += dpp_f<0xB1>(gs); gs += dpp_f<0x4E>(gs); PIN1(gs); }
          if constexpr (gp == 6) { gs += dpp_f<0x141>(gs); gs += dpp_f<0x140>(gs); PIN1(gs); }
        }
        if constexpr (jb == 3) {
          if constexpr (gp == 0) {
            const float s0 = rdl(su, 0), s1 = rdl(su, 1), q0 = rdl(gs, 16), q1 = rdl(gs, 48);
            sx = hb ? s1 : s0; ta = hb ? q1 : q0; PIN2(sx, ta);
          }
          if constexpr (gp == 1) {
            const float mean = sx * (1.0f / D_);
            mbv = rbf(mean);
            ta -= sx * mean;
            if (__builtin_expect(__any(!(sx * mean <= 16.f * ta)), 0)) {
              float q = fold32(sqdev16(lv, rdl(mbv, 0)), sqdev16(lv2, rdl(mbv, 32)));
              q = row_sum(fold16(q, q));
              const float dm = mean - mbv;
              ta = fmaxf(q - (float)D_ * dm * dm, 0.f);
            }
            PIN2(mbv, ta);
          }
          if constexpr (gp == 2) { ta = rbf(rbf(ta * (1.0f / D_)) + 1e-5f); gs -= mbv * rdl(su, 4); PIN2(ta, gs); }
          if constexpr (gp == 3) { ta = rbf(__builtin_amdgcn_sqrtf(ta)); tb = __builtin_amdgcn_rcpf(ta); PIN2(ta, tb); }
          if constexpr (gp == 4) {
            const unsigned rb = __float_as_uint(rbf(gs * tb));
            kl |= (rb ^ ((unsigned)((int)rb >> 31) | 0x80000000u)) & 0xffff0000u; PIN1(kl);
          }
          if constexpr (gp == 5) { kl = (lane & 2) ? __float_as_uint(su) : (lane & 1) ? kl : (__float_as_uint(mbv) >> 16) | __float_as_uint(ta); PIN1(kl); }
        }
        SB();
      };
      constexpr int LN_JB = 1 + ((19 - LNP) * LNS) / 8;
      auto work1 = [&](auto JB, auto GP) __attribute__((always_inline)) {
        constexpr int jb = decltype(JB)::value, gp = decltype(GP)::value, pos = (jb - 1) * 8 + gp;
        SB();
        if constexpr (pos % LNS == 0 && LNP + pos / LNS < 20) lnu(IC<LNP + pos / LNS>{});
        if constexpr (jb == ST_JB + 3) {
          if constexpr (gp == 0) { if (!__all(gr.y == tag)) gr = granule_wait((const u64*)((const char*)(flags + FL_AB) + gro), tag); PIN1(gr); }
          if constexpr (gp == 1) { g1 = __builtin_amdgcn_rcpf(__uint_as_float((gr.x & 0xffff0000u) ^ ((unsigned)z << 31))); PIN1(g1); }
          if constexpr (gp == 2) { g0 = pw < 2 ? -(lo_bf(gr.x) * g1) : __uint_as_float(gr.x); PIN1(g0); }
        }
        if constexpr (gp < SP1 && jb <= SP1E) spf(IC<SP0 + (jb - 1) * SP1 + gp>{});
        SB();
      };
      auto step1 = [&](auto JB) __attribute__((always_inline)) {
        constexpr int jb = decltype(JB)::value;
        if constexpr (jb == 1) PAGE(1);
        if constexpr (jb == 7) PAGE(2);
        constexpr int n16 = jb == 12 ? 12 : jb == 11 ? 14 : 16;
        constexpr auto wl = [](int q) { return (q == LN_JB ? 1 : 0) + (q == ST_JB ? 1 : 0); };
        if constexpr (jb == 1) {
          asm volatile("s_waitcnt vmcnt(%11)" : "+v"(lv[0]), "+v"(lv[1]), "+v"(lw[0]), "+v"(lw[1]), "+v"(ld[0]), "+v"(ld[1]),
                       "+v"(lv2[0]), "+v"(lv2[1]), "+v"(ld2[0]), "+v"(ld2[1]), "+v"(epv) : "i"(LNP ? 24 : 16) : "memory");
#define LU(u) if constexpr (u < LNP) lnu(IC<u>{});
          LU(0) LU(1) LU(2) LU(3) LU(4) LU(5) LU(6) LU(7) LU(8) LU(9) LU(10) LU(11) LU(12) LU(13) LU(14) LU(15) LU(16) LU(17) LU(18)
          if (LNP) WAIT_VM(16);
        } else if constexpr (jb == ST_JB + 3) {
          asm volatile("s_waitcnt vmcnt(%1)" : "+v"(gr) : "i"(n16 + wl(jb - 2) + wl(jb - 1)) : "memory");
        } else wait_vm<n16 + wl(jb - 3) + wl(jb - 2) + wl(jb - 1)>();
        BAR();
        if constexpr (jb == 2) { if (pw == 0 && lane < 16) ((int*)(smem + LDS_SEL))[lane < 8 ? lane : lane + 8] = 0; }
        if constexpr (jb == 4) { if (pw == 0 && lane == 0) ((unsigned*)(smem + LDS_SEL))[5] = tag; }
#define W1(i) work1(JB, IC<i>{})
        if constexpr (jb < 10) G1_STAGE_TILE(jb - 1 + G1_NST, ((jb - 1) % G1_NST) * G1_STAGE, W1);
        else G1_STAGE_A(jb - 1 + G1_NST, ((jb - 1) % G1_NST) * G1_STAGE, W1);
        if constexpr (jb == 12) { G1_STAGE_B(13, G1_STAGE); G1_STAGE_B(14, 2 * G1_STAGE); }
        if constexpr (jb == LN_JB) __builtin_amdgcn_raw_buffer_store_b64((u32x2){kl, tag}, rf, go, 0, 16);
        if constexpr (jb == ST_JB) {
          asm volatile("buffer_load_dwordx2 %0, %1, %2, 0 offen sc1" : "=v"(gr) : "v"(gro), "s"(rf) : "memory");
        }
        touches(IC<100 + jb>{});
        if constexpr (jb <= 8) {
          if (et == jb - 1) {
#pragma unroll
            for (int l = 0; l < WSN; ++l) bld16_nt_io(wv[l], rw, vw, l * F_ * 2);
          }
        }
        SB();
      };
      static_assert(ST_JB + 3 <= 12 && ST_JB >= 6, "schedule");
      step1(IC<1>{}); step1(IC<2>{}); step1(IC<3>{}); step1(IC<4>{}); step1(IC<5>{}); step1(IC<6>{}); step1(IC<7>{}); step1(IC<8>{}); step1(IC<9>{}); step1(IC<10>{}); step1(IC<11>{}); step1(IC<12>{});
    }
#define ALIAS 0
#define G2_STAGE_N(kk_, W) do { const int kb = KROT(((ALIAS >> (kk_)) & 1) ? (kk_) & 3 : (kk_)); constexpr int so = n_off(kk_); SB(); \
    DMA_S(rn2, vn2[0], mq0, so, kb); W(0); DMA_S(rn2, vn2[1], mq1, so, kb); W(1); \
    DMA_S(rn2, vn2[2], mq2, so, kb); W(2); DMA_S(rn2, vn2[3], mq3, so, kb); W(3); \
    DMA_S(rn2, vn2[4], mq0, so + 16384, kb); W(4); DMA_S(rn2, vn2[5], mq1, so + 16384, kb); W(5); \
    DMA_S(rn2, vn2[6], mq2, so + 16384, kb); W(6); DMA_S(rn2, vn2[7], mq3, so + 16384, kb); W(7); SB(); } while (0)
#define G2_STAGE_NH(kk_, h) do { const int kb = KROT(kk_); constexpr int so = n_off(kk_) + (h) * 16384; SB(); \
    DMA_S(rn2, vn2[(h) * 4], mq0, so, kb); DMA_S(rn2, vn2[(h) * 4 + 1], mq1, so, kb); \
    DMA_S(rn2, vn2[(h) * 4 + 2], mq2, so, kb); DMA_S(rn2, vn2[(h) * 4 + 3], mq3, so, kb); SB(); } while (0)
#define G2_STAGE_M(kk_, W) do { const int kb = KROT(kk_) * 128; constexpr int so = G2_MRING + m_off(kk_); SB(); \
    DMA_(rm2, vm2, mb0, so, kb, 0, " sc1"); W(8); DMA_(rm2, vm2, mb0, so + 4096, kb, 4096, " sc1"); W(9); \
    DMA_(rm2, vm2, mb0, so + 8192, kb, 8192, " sc1"); W(10); DMA_(rm2, vm2, mb0, so + 12288, kb, 12288, " sc1"); W(11); SB(); } while (0)
    WAIT_VM(16); BAR();
    asm volatile("ds_write_b32 %0, %1" :: "v"(lma), "v"(g0) : "memory");
    if (pw < 2) asm volatile("ds_write_b32 %0, %1 offset:512" :: "v"(lma), "v"(g1) : "memory");
    u32x4 tv[4];
    unsigned ready = 0;
    auto need = [&](int k) __attribute__((always_inline)) {
      const int b = k_blk(k) + (k_ord(k) & 1) * 8;
#pragma unroll 1
      for (int it = 0; !((ready >> b) & 1) && it < SPIN_MAX; ++it) {
        ready |= (unsigned)__ballot(__builtin_amdgcn_raw_buffer_load_b32(rf, abq[k < 5 ? 0 : k < 9 ? 1 : 2], 0, 16) == tag);
        asm volatile("" ::: "memory");
      }
    };
    unsigned ksel, bad = 0;
    int isel;
    unsigned xw;
    auto bvec = [&](unsigned src, const unsigned (&sl)[4]) __attribute__((always_inline)) {
      return (u32x4){__builtin_amdgcn_perm(0, src, sl[0]), __builtin_amdgcn_perm(0, src, sl[1]),
                     __builtin_amdgcn_perm(0, src, sl[2]), __builtin_amdgcn_perm(0, src, sl[3])};
    };
#define BC8(v, p) (unsigned)__builtin_amdgcn_update_dpp(__builtin_amdgcn_update_dpp(0, (int)(v), 0x150 + (p), 0xf, 0x3, false), (int)(v), 0x158 + (p), 0xf, 0xc, false)
    float* lg = (float*)(smem + LDS_GU);
    f32x4 macc = {0.f, 0.f, 0.f, 0.f};
    u32x2 sv;
    unsigned hp, gbad;
    float s0, s1;
    auto work2 = [&](auto JB, auto GP) __attribute__((always_inline)) {
      constexpr int jb = decltype(JB)::value, gp = decltype(GP)::value;
      SB();
      if constexpr (jb == -1 && gp < 4) {
        constexpr int c = gp == 0 ? 0xB1 : gp == 1 ? 0x4E : gp == 2 ? 0x141 : 0x140;
        merge2(k1, k2, dpp_u<c>(k1), dpp_u<c>(k2));
        PIN2(k1, k2);
      }
      if constexpr (jb == -1 && gp == 4) {
        ksel = tk ? k2 : k1;
        isel = (0xffff - (int)(ksel & 0xffffu)) & (S_ - 1);
        k1 = (isel * D_ + d0 + rq * 16 + (lane & 7) * 2) * 2; k2 = O_ST + isel * 8;
        PIN1(ksel);
        xw = gld4_s(x, k1);
        sv = bld8_sc1(rf, k2);
      }
      if constexpr (jb == 0 && gp == 1) wait_xs<3 + 1 + (N2A < 2 ? N2A : 2) + tcnt(116) + tcnt(117)>(xw, sv);
      if constexpr (jb == 0) {
        if constexpr (gp == 1) { if (!__all(sv.y == tag)) sv = granule_wait(stat + isel, tag); s0 = __builtin_amdgcn_rcpf(hi_bf(sv.x)); PIN1(s0); }
        if constexpr (gp == 2) { s1 = ln_elem(lo_bf(xw), lo_bf(sv.x), hi_bf(sv.x), s0); PIN1(s1); }
        if constexpr (gp == 3) { xw = pk2(s1, ln_elem(hi_bf(xw), lo_bf(sv.x), hi_bf(sv.x), s0)); PIN1(xw); }
      }
      if constexpr (DGS == 0 && jb == DG && gp < 7) { PIN1(dv[gp]); macc = MM(dv[gp], bvec(BC8(hp, gp), sel[0]), macc); PIN1(macc); }
      if constexpr (jb >= 100 && gp < 7) { PIN1(dv[gp]); macc = MM(dv[gp], bvec(BC8(hp, gp), sel[0]), macc); PIN1(macc); }
      if constexpr ((jb == 0 && gp >= 4) || jb == 1) {
        constexpr int l = jb == 0 ? gp - 4 : gp + 4;
        if constexpr (!(l & 1)) hp = BC8(xw, l >> 1);
        PIN1(wv[l]);
        macc = MM(wv[l], bvec(hp, sel[l & 1]), macc);
        PIN2(macc, hp);
      }
      if constexpr (jb == GV_JB + 2) {
        if constexpr (gp == 0) { gbad = (gv[0].y ^ tag) | (gv[0].w ^ tag) | (gv[1].y ^ tag) | (gv[1].w ^ tag); PIN1(gbad); }
        if constexpr (gp == 1) { gbad |= (gv[2].y ^ tag) | (gv[2].w ^ tag) | (gv[3].y ^ tag) | (gv[3].w ^ tag); PIN1(gbad); }
        if constexpr (gp == 2) {
          s0 = ((__uint_as_float(gv[0].x) + __uint_as_float(gv[1].x)) + __uint_as_float(gv[2].x)) + __uint_as_float(gv[3].x);
          s1 = ((__uint_as_float(gv[0].z) + __uint_as_float(gv[1].z)) + __uint_as_float(gv[2].z)) + __uint_as_float(gv[3].z);
          PIN2(s0, s1);
        }
        if constexpr (gp == 3) {
#pragma unroll 1
          for (int it = 0; !__all(gbad == 0) && it < SPIN_MAX; ++it) {
            u32x4 q[4];
            gbad = 0;
#pragma unroll
            for (int i = 0; i < 4; ++i) { q[i] = __builtin_amdgcn_raw_buffer_load_b128(rgu, vgu, i * F_ * 16, 16); gbad |= (q[i].y ^ tag) | (q[i].w ^ tag); }
            s0 = ((__uint_as_float(q[0].x) + __uint_as_float(q[1].x)) + __uint_as_float(q[2].x)) + __uint_as_float(q[3].x);
            s1 = ((__uint_as_float(q[0].z) + __uint_as_float(q[1].z)) + __uint_as_float(q[2].z)) + __uint_as_float(q[3].z);
            asm volatile("" ::: "memory");
          }
          const unsigned gp2 = pk2(s0, s1);
          const unsigned up2 = dpp_u<0x128>(gp2);
          s0 = tk ? hi_bf(up2) : lo_bf(gp2); s1 = tk ? hi_bf(gp2) : lo_bf(up2);
          PIN2(s0, s1);
        }
        if constexpr (gp == 4) { s0 = rbf(s0 * __builtin_amdgcn_rcpf(1.f + __expf(-s0))); PIN1(s0); }
        if constexpr (gp == 5) { hp = pk2(s0 * s1, 0.f); PIN1(hp); }
      }
      if constexpr (jb == 10 && gp == 4) code_touch(sd, (j * 4 + pw) * 256);
      SB();
    };
#define W2M(i) work2(IC<-1>{}, IC<i>{})
#define W20(i) work2(IC<0>{}, IC<i>{})
#define W21(i) work2(IC<1>{}, IC<i>{})
    auto piece = [&](auto K, auto Q) __attribute__((always_inline)) {
      constexpr int k = decltype(K)::value, q = decltype(Q)::value;
      if constexpr (q < 8) {
        const int kb = KROT(((ALIAS >> k) & 1) ? k & 3 : k);
        DMA_S(rn2, vn2[q], (q & 3) == 0 ? mq0 : (q & 3) == 1 ? mq1 : (q & 3) == 2 ? mq2 : mq3, n_off(k) + (q >> 2) * 16384, kb);
      } else {
        if constexpr (q == 8) need(k);
        const int kb = KROT(k) * 128;
        DMA_(rm2, vm2, mb0, G2_MRING + m_off(k) + (q - 8) * 4096, kb, (q - 8) * 4096, " sc1");
      }
    };
    unsigned fs;
    {
      G1_STAGE_B(15, 3 * G1_STAGE);
      G2_STAGE_NH(0, 0);
      lgkm0(sd);
      WAIT_VM(16); BAR();
      G2_STAGE_NH(0, 1);
      WAIT_VM(10); BAR();
#pragma unroll
      for (int i = 0; i < 4; ++i) kk[i] = bld16_sc1(rf, l16, ko_ + i * 1024);
#pragma unroll
      for (int l = WSN; l < 16; ++l) wv[l] = bld16_nt(rw, vw, l * F_ * 2);
      asm volatile("s_waitcnt vmcnt(%4)" : "+v"(kk[0]), "+v"(kk[1]), "+v"(kk[2]), "+v"(kk[3]) : "i"(16 - WSN) : "memory");
      touches(IC<115>{});
#pragma unroll
      for (int i = 0; i < 4; ++i) {
        k2 = med3u(k1, k2, kk[i].x); k1 = max(k1, kk[i].x);
        k2 = med3u(k1, k2, kk[i].z); k1 = max(k1, kk[i].z);
        bad |= (kk[i].y ^ tag) | (kk[i].w ^ tag);
      }
#pragma unroll 1
      for (int it = 0; !__all(bad == 0) && it < SPIN_MAX; ++it) {
        k1 = 0; k2 = 0; bad = 0;
#pragma unroll
        for (int i = 0; i < 4; ++i) {
          const u32x4 q = __builtin_amdgcn_raw_buffer_load_b128(rf, l16, ko_ + i * 1024, 16);
          k2 = med3u(k1, k2, q.x); k1 = max(k1, q.x);
          k2 = med3u(k1, k2, q.z); k1 = max(k1, q.z);
          bad |= (q.y ^ tag) | (q.w ^ tag);
        }
        asm volatile("" ::: "memory");
      }
      merge2(k1, k2, dpp_u<0xB1>(k1), dpp_u<0xB1>(k2));
      merge2(k1, k2, dpp_u<0x4E>(k1), dpp_u<0x4E>(k2));
      merge2(k1, k2, dpp_u<0x141>(k1), dpp_u<0x141>(k2));
      merge2(k1, k2, dpp_u<0x140>(k1), dpp_u<0x140>(k2));
      if ((lane & 15) == 0) *(u32x2*)(smem + LDS_KEY + (pw * 4 + rq) * 8) = (u32x2){k1, k2};
      PAGE(3);
      BAR_LGKM();
#pragma unroll
      for (int jj = 0; jj < 4; ++jj) tv[jj] = *(const u32x4*)(smem + G2_MRING + m_off(0) + (jj * 4 + pw) * 1024 + lane * 16);
      { const u32x2 kp = *(const u32x2*)(smem + LDS_KEY + ci * 8); k1 = kp.x; k2 = kp.y; }
#pragma unroll
      for (int jj = 0; jj < 4; ++jj) __builtin_amdgcn_raw_buffer_store_b128(tv[jj], rm2, vm2 + jj * 4096 + 2 * tt * 16384, 0, 16);
      G2_STAGE_N(1, W2M);
      touches(IC<116>{});
      wait_vm<10 + tcnt(116)>();
      if (lane == tt) __builtin_amdgcn_raw_buffer_store_b32(tag, rf, abo, 0, 16);
      touches(IC<117>{});
#define P2A(i) if constexpr (i < N2A) piece(IC<2>{}, IC<i>{}); W20(i);
      SB(); P2A(0) P2A(1) P2A(2) P2A(3) P2A(4) P2A(5) P2A(6) P2A(7)
      __builtin_amdgcn_s_sleep(WSL);
      wait_vm<1 + N2A + tcnt(116) + tcnt(117)>();
      fs = bld4_sc1(rf, abq[0]);
    }
    BAR();
#pragma unroll
    for (int jj = 0; jj < 4; ++jj) tv[jj] = *(const u32x4*)(smem + G2_MRING + m_off(1) + (jj * 4 + pw) * 1024 + lane * 16);
    constexpr int SPB = SP0 + SP1E * SP1;
#define P2B(i) if constexpr (i >= N2A) piece(IC<2>{}, IC<i>{});
    SB(); P2B(0) P2B(1) P2B(2) P2B(3) P2B(4) P2B(5) P2B(6) P2B(7)
#define P3A(i) if constexpr (i < N3A) piece(IC<3>{}, IC<i>{});
    P3A(0) P3A(1) P3A(2) P3A(3) P3A(4) P3A(5) P3A(6) P3A(7)
    asm volatile("s_waitcnt vmcnt(%1)" : "+v"(fs) : "i"(8 - N2A + N3A) : "memory");
    ready = (unsigned)__ballot(fs == tag);
    need(2);
    G2_STAGE_M(2, NOW);
#define P3B(i) if constexpr (i >= N3A) piece(IC<3>{}, IC<i>{});
    P3B(0) P3B(1) P3B(2) P3B(3) P3B(4) P3B(5) P3B(6) P3B(7)
    need(3);
    G2_STAGE_M(3, NOW);
    fs = bld4_sc1(rf, abq[1]);
    touches(IC<1>{});
#pragma unroll
    for (int i = 0; i < dn_cnt(1); ++i) dv[i] = bld16_nt(rdn, vdn, i * D_ * 2);
    W21(0); W21(1); W21(2); W21(3); W21(4); W21(5); W21(6); W21(7); W21(8); W21(9); W21(10); W21(11);
    asm volatile("" : "+v"(tv[0]), "+v"(tv[1]), "+v"(tv[2]), "+v"(tv[3]));
    auto step2 = [&](auto JB) __attribute__((always_inline)) {
      constexpr int jb = decltype(JB)::value;
      if constexpr (jb == 3) PAGE(4);
      if constexpr (jb == 2 && PRIO2 != 3) __builtin_amdgcn_s_setprio(PRIO2);
      if constexpr (jb == 9) PAGE(5);
      constexpr auto must = [](int q) { return (q == 2 ? 4 : 0) + ((FSM >> q) & 1) + dig(GVS, q); };
      constexpr auto late = [](int q) { return dn_cnt(q) + (VAN > 0 && q == VAP) + (VBN > 0 && q == VBP) + (VCN > 0 && q == VCP); };
      if constexpr (jb == 2) wait_vm<8 - N3A + 4 + must(1) + late(1)>();
      else wait_vm<(dig(LAGS, jb - 2) ? 0 : late(jb - 2) + (jb == 5 ? 1 : 0)) + 12 - dig(LAGS, jb - 1) + must(jb - 1) + late(jb - 1)>();
      if constexpr (jb == 3) { if (pw == 0 && (lane & ~8) == 0) ((unsigned*)(smem + LDS_SEL))[tk] = ksel; }
      if constexpr ((jb == 2 || jb == DG + 1)) {
        *(f32x4*)(lg + pw * 256 + lane * 4) = macc;
        macc = (f32x4){0.f, 0.f, 0.f, 0.f};
        lgkm0(sd);
      }
      BAR();
      float p0[4], p1[4];
      const int col = (pw & 1) * 64 + lane;
      if constexpr (jb == 3) {
        if (g == 0 && pw == 0 && lane == 0) __hip_atomic_store((unsigned*)flags + FL_EPOCH, epoch + 1u, __ATOMIC_RELAXED, __HIP_MEMORY_SCOPE_AGENT);
      }
      if constexpr (jb == DG + 1) {
        const float* p = lg + (pw & 1) * 32 + (lane >> 4) * 64 + ((2 * lane) & 7) * 4 + ((lane >> 2) & 3);
#pragma unroll
        for (int i = 0; i < 4; ++i) { p0[i] = p[i * 256]; p1[i] = p[i * 256 + 4]; }
      }
      if constexpr (jb == 2) {
        const float* p = lg + (col >> 5) * 64 + (col & 7) * 4 + ((col >> 3) & 3);
#pragma unroll
        for (int i = 0; i < 4; ++i) { p0[i] = p[i * 256]; p1[i] = p[i * 256 + 32]; }
      }
      if constexpr (jb == 4) {
        if (lane == 8 + tt) __builtin_amdgcn_raw_buffer_store_b32(tag, rf, abo, 0, 16);
      }
      if constexpr ((FSM >> (jb - 2)) & 1) {
        asm volatile("" : "+v"(fs));
        ready |= (unsigned)__ballot(fs == tag);
      }
      if constexpr (jb == GV_JB + 2) asm volatile("" : "+v"(gv[0]), "+v"(gv[1]), "+v"(gv[2]), "+v"(gv[3]));
      auto w2 = [&](auto GP) __attribute__((always_inline)) {
        constexpr int gp = decltype(GP)::value;
        if constexpr (jb == 2 && gp == 0) {
          p0[0] = (p0[0] + p0[1]) + (p0[2] + p0[3]); p1[0] = (p1[0] + p1[1]) + (p1[2] + p1[3]);
          PIN2(p0[0], p1[0]);
        }
        if constexpr (gp < 12) work2(JB, GP);
        if constexpr (gp < dig(SPS, jb) && !((SPE >> jb) & 1)) spf(IC<SPB + acc_dig(SPS, jb) + gp>{});
      };
      constexpr int l0 = dig(LAGS, jb - 1), np = l0 + 12 - dig(LAGS, jb);
      auto slot = [&](auto I) __attribute__((always_inline)) {
        constexpr int i = decltype(I)::value;
        if constexpr (i < l0) piece(IC<jb + 1>{}, IC<12 - l0 + i>{});
        else if constexpr (i < np) piece(IC<jb + 2>{}, IC<i - l0>{});
        if constexpr (i < 12 || i < np) w2(I);
      };
#define SL4(i) slot(IC<i>{}); slot(IC<i + 1>{}); slot(IC<i + 2>{}); slot(IC<i + 3>{});
      SB();
      SL4(0) SL4(4) SL4(8) SL4(12) SL4(16) SL4(20)
      SB();
      if constexpr (jb == 2) {
#pragma unroll
        for (int jj = 0; jj < 4; ++jj) __builtin_amdgcn_raw_buffer_store_b128(tv[jj], rm2, vm2 + jj * 4096 + (2 * tt + 1) * 16384, 0, 16);
      }
      if constexpr ((FSM >> jb) & 1) fs = bld4_sc1(rf, abq[jb < 5 ? 1 : 2]);
#pragma unroll
      for (int i = acc_dig(GVS, jb); i < acc_dig(GVS, jb + 1); ++i) gv[i] = bld16_sc1(rgu, vgu, i * F_ * 16);
      touches(JB);
#pragma unroll
      for (int i = dn_first(jb); i < dn_first(jb) + dn_cnt(jb); ++i) dv[i] = bld16_nt(rdn, vdn, i * D_ * 2);
      if constexpr (jb == DG + 1) {
        if (pw < 2)
          __builtin_amdgcn_raw_buffer_store_b128((u32x4){__float_as_uint((p0[0] + p0[1]) + (p0[2] + p0[3])), tag, __float_as_uint((p1[0] + p1[1]) + (p1[2] + p1[3])), tag},
                                                 mk_rsrc(wsp + WS_DN), (((pw * 8 + cg8) * 32 + rgb) * 64 + lane) * 16, 0, 16);
      }
      if constexpr (jb == 2) {
        if (moe && pw < 2)
          __builtin_amdgcn_raw_buffer_store_b128((u32x4){__float_as_uint(p0[0]), tag, __float_as_uint(p1[0]), tag},
                                                 rgu, ((mat * 4 + dq) * F_ + cg * 128 + col) * 16, 0, 16);
      }
      SB();
#define SPX(i) spf(IC<(((i >= np && i >= 12) || ((SPE >> jb) & 1)) && i < dig(SPS, jb) ? SPB + acc_dig(SPS, jb) + i : -1)>{})
      if constexpr (dig(DGS, jb) > 0) {
        if (DGP != 3) __builtin_amdgcn_s_setprio(DGP);
#define DGU(i) if constexpr (i >= acc_dig(DGS, jb) && i < acc_dig(DGS, jb + 1)) work2(IC<100>{}, IC<i>{});
        DGU(0) DGU(1) DGU(2) DGU(3) DGU(4) DGU(5) DGU(6)
        if (DGP != 3) __builtin_amdgcn_s_setprio(3);
      }
      SPX(0); SPX(1); SPX(2); SPX(3); SPX(4); SPX(5); SPX(6); SPX(7); SPX(8); SPX(9); SPX(10); SPX(11); SPX(12); SPX(13); SPX(14);
      SB();
    };
    static_assert(DG + 1 <= 12 && DG >= GV_JB + 3 && dn_first(DG - 2) == 7 && acc_dig(GVS, GV_JB + 1) == 4 && dig(LAGS, 1) == 0 && dig(SPS, 1) == 0 && dig(LAGS, 0) == 0, "schedule");
    static_assert(DGS == 0 || (acc_dig(DGS, DG + 1) == 7 && acc_dig(DGS, GV_JB + 2) == 0 && acc_dig(DGS, 10) <= dn_first(7) && acc_dig(DGS, 11) <= dn_first(8) && acc_dig(DGS, 9) <= dn_first(6)), "units need their loads");
    static_assert(ALIAS || sp_tile((SPB + acc_dig(SPS, 14)) >> 4) < 0, "all tiles are prefetched");
    step2(IC<2>{}); step2(IC<3>{}); step2(IC<4>{}); step2(IC<5>{}); step2(IC<6>{}); step2(IC<7>{}); step2(IC<8>{}); step2(IC<9>{}); step2(IC<10>{}); step2(IC<11>{}); step2(IC<12>{}); step2(IC<13>{});
    lgkm0(sd);
    wait_vm<12 - dig(LAGS, 13)>(); BAR();
    {
      constexpr int l0 = dig(LAGS, 13);
#define PL(i) if constexpr (i >= 12 - l0) piece(IC<15>{}, IC<i>{});
      PL(0) PL(1) PL(2) PL(3) PL(4) PL(5) PL(6) PL(7) PL(8) PL(9) PL(10) PL(11)
    }
    __builtin_amdgcn_s_setprio(0);
    if (EPP == 1) { WAIT_VM(0); BAR(); }
    epi_setup(0); epi_setup(1); epi_setup(2);
    if (EPP == 2) { WAIT_VM(0); BAR(); }
#pragma unroll
    for (int n = 0; n < 3; ++n) xq_load(n);
    if (!EPP) { WAIT_VM(3); BAR(); }
    epi_setup(3);
    wait_xq<0>(xq, sq);
    key[0] = __builtin_amdgcn_readlane(ksel, 0); key[1] = __builtin_amdgcn_readlane(ksel, 8);
    cv_calc();
#pragma unroll 1
    for (int c = 0; c < 2; ++c) {
      if (__builtin_expect(((c ? far1 : far0) & ~9) != 0, 1)) continue;
      const __amdgpu_buffer_rsrc_t rd = mk_rsrc(wsp + WS_DN + (size_t)(c * 8 * 32 + pw * 8) * 1024);
      const int ec = seg0(lane >> 5, (lane >> 4) & 1), dno = (ec >> 7) * 32768 + ((ec & 127) / 2 + (lane & 15)) * 16;
#pragma unroll 1
      for (int it = 0; it < SPIN_MAX; ++it) {
        if (__all(__builtin_amdgcn_raw_buffer_load_b32(rd, (ec >> 7) * 32768 + (lane & 7) * 1024 + 1020, 0, 16) == tag)) break;
        __builtin_amdgcn_s_sleep(8);
        asm volatile("" ::: "memory");
      }
      float s0, s1;
      unsigned bad = 1;
#pragma unroll 1
      for (int it = 0; !__all(bad == 0) && it < SPIN_MAX; ++it) {
        bad = 0; s0 = 0.f; s1 = 0.f;
#pragma unroll
        for (int i = 0; i < 8; ++i) {
          const u32x4 q = __builtin_amdgcn_raw_buffer_load_b128(rd, dno, i * 1024, 16);
          bad |= (q.y ^ tag) | (q.w ^ tag); s0 += __uint_as_float(q.x); s1 += __uint_as_float(q.z);
        }
        asm volatile("" ::: "memory");
      }
      *(f32x2*)(smem + LDS_FIN + (c * 4 + pw) * 512 + lane * 8) = (f32x2){s0, s1};
      asm volatile("s_waitcnt lgkmcnt(0)" ::: "memory");
      if (lane == 0) asm volatile("ds_add_u32 %0, %1" :: "v"(lds_addr(smem + LDS_SEL + 24 + c * 4)), "v"(1) : "memory");
    }
  } else {
  {
    const int q = j * 4 + wid;
    constexpr int Q1 = WS_MB, Q2 = Q1 + 8, Q3 = Q2 + 8, Q4 = Q3 + 7, Q5 = Q4 + 7, Q6 = Q5 + 7, Q7 = Q6 + 4, Q8 = Q7 + 4, QC = Q8 + 1;
    static_assert(QC <= 128, "warm-up slots");
    unsigned dmy = 0;
    if (q < QC) {
      const char* base; int b0;
      if (q < Q1) { base = wsp; b0 = 0; }
      else if (q < Q2) { base = (const char*)ws_re; b0 = Q1; }
      else if (q < Q3) { base = (const char*)ws_im; b0 = Q2; }
      else if (q < Q4) { base = (const char*)w_gate; b0 = Q3; }
      else if (q < Q5) { base = (const char*)w_up; b0 = Q4; }
      else if (q < Q6) { base = (const char*)w_down; b0 = Q5; }
      else if (q < Q7) { base = (const char*)out; b0 = Q6; }
      else if (q < Q8) { base = (const char*)x; b0 = Q7; }
      else { base = (const char*)w_r; b0 = Q8; }
      base += (size_t)(q - b0) << 20;
      asm volatile("s_load_dword %0, %1, 0" : "+s"(dmy) : "s"(base) : "memory");
    }
    if (wid == 0) { CALL_PG(1); CALL_PG(8); CALL_PG(5); }
    else if (wid == 1) { CALL_PG(2); CALL_PG(9); CALL_PG(6); }
    else if (wid == 2) { CALL_PG(3); CALL_PG(10); }
    else { CALL_PG(4); CALL_PG(11); }
    BAR();
    lgkm0(dmy);
    TL(1);
  }
  PAGE(8);
  epi_setup(0); epi_setup(1); epi_setup(2); epi_setup(3);
  const int wr = wid >> 1, wc = wid & 1;
  const int sw = swz(fr), fl = lrw(fr);
  const int ko0 = ((0 + fq) ^ sw) * 16, ko1 = ((4 + fq) ^ sw) * 16;
  bf16x8 fm[2][4], fn[2][4];
  {
    const char* pm = smem + (wr * 64 + fl) * 128;
    const char* pn = smem + 16384 + (wc * 64 + fl) * 128;
    f32x4 acc[4][4];
#pragma unroll
    for (int i = 0; i < 4; ++i)
#pragma unroll
      for (int jn = 0; jn < 4; ++jn)
#pragma unroll
        for (int q = 0; q < 4; ++q) acc[i][jn][q] = 0.f;
#define TO(i) ((i) * 2048)
#define LDSP(T, a) ((__attribute__((address_space(3))) T*)(size_t)(a))
#define SLOTS(n) _Pragma("unroll") for (int q = 0; q < (n); ++q)
#define MFA(c, m, n) asm volatile("v_mfma_f32_16x16x32_bf16 %0, %2, %1, %0" : "+v"(c) : "v"(m), "v"(n))
#define MFA2(c, m, n, m2, n2) asm volatile("v_mfma_f32_16x16x32_bf16 %0, %2, %1, %0\n\tv_mfma_f32_16x16x32_bf16 %0, %4, %3, %0" : "+v"(c) : "v"(m), "v"(n), "v"(m2), "v"(n2))
    unsigned lm[2][2], ln[2][2];
#pragma unroll
    for (int u = 0; u < 2; ++u)
#pragma unroll
      for (int kk = 0; kk < 2; ++kk) {
        lm[u][kk] = lds_addr(pm) + u * 2 * G1_STAGE + (kk ? ko1 : ko0);
        ln[u][kk] = lds_addr(pn) + u * 2 * G1_STAGE + (kk ? ko1 : ko0);
        asm volatile("" : "+v"(lm[u][kk]), "+v"(ln[u][kk]));
      }
    auto ldm = [&](int stg, int i, int kk) __attribute__((always_inline)) { fm[kk][i] = *LDSP(const bf16x8, lm[stg >> 1][kk] + (stg & 1) * G1_STAGE + TO(i)); };
    auto ldn = [&](int stg, int jn, int kk) __attribute__((always_inline)) { fn[kk][jn] = *LDSP(const bf16x8, ln[stg >> 1][kk] + (stg & 1) * G1_STAGE + TO(jn)); };
    auto quad = [&](int a, int c, int d, int s) __attribute__((always_inline)) {
      const int i = (s < 2 || s >= 6) ? a : a + 1, jn = s < 4 ? c : d, kk = (s == 1 || s == 2 || s == 5 || s == 6);
      MFA(acc[i][jn], fm[kk][i], fn[kk][jn]);
    };
    auto quad2 = [&](int a, int c, int d, int s) __attribute__((always_inline)) {
      const int i = (s < 2 || s >= 6) ? a : a + 1, jn = s < 4 ? c : d, kk = (s == 2 || s == 6);
      MFA2(acc[i][jn], fm[kk][i], fn[kk][jn], fm[1 - kk][i], fn[1 - kk][jn]);
    };
    auto half1 = [&](int stg) __attribute__((always_inline)) {
      const int ja = (stg & 1) * 2, jb = 2 - ja;
      SLOTS(8) {
        quad(0, ja, ja + 1, q);
        if (q < 4) ldn(stg, jb + (q >> 1), q & 1); else ldm(stg, 2 + ((q - 4) >> 1), q == 5 || q == 6);
        SB();
      }
      SLOTS(4) { quad2(0, jb, jb + 1, 2 * q); SB(); }
    };
    auto half2 = [&](int stg) __attribute__((always_inline)) {
      const int ja = (stg & 1) * 2, jb = 2 - ja, nx = (stg + 1) & 3;
      SLOTS(8) {
        quad(2, jb + 1, jb, q);
        if (!(q & 1)) ldm(nx, q >> 2, q == 2 || q == 4);
        SB();
      }
      SLOTS(8) {
        quad(2, ja, ja + 1, q);
        if (!(q & 1)) ldn(nx, jb + (q >> 2), (q >> 1) & 1);
        SB();
      }
    };
    unsigned ae[7];
    {
      int l_ = lane;
      asm volatile("" : "+v"(l_));
      const int p = l_ >> 4, dn = sym_dn(p), hf = (p >> 1) ^ (dn & z);
#pragma unroll
      for (int u = 0; u < 7; ++u) {
        const int T = u < 3 ? wc * 4 : (wc * 4 + u - 3 - dn) & 7, i = u < 3 ? (u + 1) ^ ((u & 1) ? 0 : dn * 2) : 0;
        const int f = dn * 8 + ((T & 2) << 1) + ((T & 4) >> 1) + (T & 1);
        ae[u] = lds_addr(smem + G2_MRING) + (hf * 64 + i * 16 + lrw(f)) * 128 + (((wr * 4 + ((l_ >> 2) & 3)) ^ swz(f)) * 16) + (l_ & 3) * 4;
        asm volatile("" : "+v"(ae[u]));
      }
    }
    unsigned al = lds_addr(smem + LDS_MB) + (wr * 32 + fr * 2) * 4, ac = lds_addr(smem + LDS_MB) + (256 + wc * 64 + fq * 4) * 4;
    asm volatile("" : "+v"(al), "+v"(ac));
    SLOTS(16) asm volatile("" : "+v"(acc[q >> 2][q & 3]));
    SB();
    SLOTS(4) { ldm(0, q >> 1, q & 1); ldn(0, q >> 1, q & 1); }
    SB();
#pragma unroll 1
    for (int k4 = 0; k4 < G1_NKT - 4; k4 += 4) {
      half1(0); BAR_LGKM();

      SB(); half2(0);
      half1(1); BAR_LGKM();

      SB(); half2(1);
      half1(2); BAR_LGKM();

      SB(); half2(2);
      half1(3); BAR_LGKM();

      SB(); half2(3);
    }
    bf16x8 gm[2][4], gn[2][8];
    auto rdg = [&](int set, int t, int h, int r) __attribute__((always_inline)) {
      const int u = (t >> 1) & 1, o = (t & 1) * G1_STAGE;
      if (r == 0 || r == 2 || r == 4 || r == 5) {
        const int i = r >= 4, kk = r == 2 || r == 4;
        gm[set][kk * 2 + i] = *LDSP(const bf16x8, lm[u][kk] + o + h * 4096 + i * 2048);
      } else {
        const int jn = r < 4 ? 0 : (r - 4) >> 1, kk = r < 4 ? r >> 1 : r & 1;
        gn[set][kk * 4 + jn] = *LDSP(const bf16x8, ln[u][kk] + o + TO(jn));
      }
    };
    auto mmg = [&](int set, int h, int q) __attribute__((always_inline)) {
      const int jn = q >> 2, r = q & 3, i = (r >> 1) ^ (jn & 1), kk = r == 1 || r == 2;
      MFA(acc[h * 2 + i][jn], gm[set][kk * 2 + i], gn[set][kk * 4 + jn]);
    };
    f32x2 rsv, mrv;
    f32x4 cev[4];
    float tc[32];
    auto rds = [&](int h, int r) __attribute__((always_inline)) {
      if (r == 0) mrv = *LDSP(const f32x2, al + h * 256); else if (r == 1) rsv = *LDSP(const f32x2, al + 512 + h * 256);
    };
    auto prep = [&](int n) __attribute__((always_inline)) {
      tc[n] = llvm_fmul_legacy(mrv[n & 1], cev[n >> 3][(n >> 1) & 3]);
      asm volatile("" : "+v"(tc[n]));
    };
    auto epi = [&](int h, int q) __attribute__((always_inline)) {
      const int jn = q >> 1, r = (q & 1) * 2, o = m_off(h) + (jn & 1) * 128 + (jn >> 1) * lrw(4) * 128;
      asm volatile("v_fmac_f32 %0, %4, %8\n\tv_fmac_f32 %1, %5, %9\n\tv_fmac_f32 %2, %6, %8\n\tv_fmac_f32 %3, %7, %9"
                   : "+v"(tc[q * 4]), "+v"(tc[q * 4 + 1]), "+v"(tc[q * 4 + 2]), "+v"(tc[q * 4 + 3])
                   : "v"(acc[h * 2][jn][r]), "v"(acc[h * 2 + 1][jn][r]), "v"(acc[h * 2][jn][r + 1]), "v"(acc[h * 2 + 1][jn][r + 1]),
                     "v"(rsv[0]), "v"(rsv[1]));
      *LDSP(unsigned, r ? ae[1] + o : ae[3 + jn] + m_off(h)) = pk2(tc[q * 4], tc[q * 4 + 1]);
      *LDSP(unsigned, ae[r] + o) = pk2(tc[q * 4 + 2], tc[q * 4 + 3]);
    };
    SB();
    half1(0);
    BAR_LGKM();
    SB();
    TL(14);
    SLOTS(16) {
      if (q < 8) quad(2, 3, 2, q); else quad(2, 0, 1, q - 8);
      if (q < 12) rdg(0, 13, 0, q);
      SB();
    }
    BAR();
    SB();
    TL(15);
    SLOTS(16) {
      mmg(0, 0, q);
      if (q < 12) rdg(1, 14, 0, q);
      else { rds(0, q - 12); cev[q - 12] = *LDSP(const f32x4, ac + (q - 12) * 64); }
      SB();
    }
    PAGE(9);
    BAR();
    SB();
    TL(16);
    SLOTS(16) { mmg(1, 0, q); if ((q & 3) < 3) rdg(0, 15, 0, (q >> 2) * 3 + (q & 3)); if (q >= 8) { prep(2 * q - 16); prep(2 * q - 15); } SB(); }
    SLOTS(16) { mmg(0, 0, q); if ((q & 3) < 3) rdg(1, 13, 1, (q >> 2) * 3 + (q & 3)); prep(16 + q); SB(); if (q >= 9 && (q & 1)) { epi(0, (q - 9) >> 1); SB(); } }
    SLOTS(8) { mmg(1, 1, q); if (q < 6) { rdg(0, 14, 1, 2 * q); rdg(0, 14, 1, 2 * q + 1); } SB(); if (q & 1) { epi(0, 4 + (q >> 1)); SB(); } }
    BAR_LGKM();
    SB();
    TL(60);
    SLOTS(8) { mmg(1, 1, 8 + q); if (q < 4) rdg(1, 15, 1, q == 0 ? 1 : q == 1 ? 3 : q + 4); else rds(1, q - 4); SB(); }
    SLOTS(16) { mmg(0, 1, q); if (q < 8) rdg(1, 15, 1, q == 0 ? 0 : q == 1 ? 2 : q < 4 ? q + 2 : q + 4); prep(q); SB(); }
    asm volatile("s_waitcnt lgkmcnt(0)" ::: "memory");
    if (lane == 0) asm volatile("ds_add_u32 %0, %1" :: "v"(lds_addr(smem + LDS_SEL + 12)), "v"(1) : "memory");
    SB();
    SLOTS(16) {
      mmg(1, 1, q);
      if (q < 4) fn[0][q] = *(const bf16x8*)(smem + (wr * 128 + fl) * 128 + (q >> 1) * 2048 + ((q & 1) ? ko1 : ko0));
      else if (q < 8) fm[q & 1][(q - 4) >> 1] = *(const bf16x8*)(smem + G2_MRING + m_off(0) + (wc * 64 + fl) * 128 + TO((q - 4) >> 1) + ((q & 1) ? ko1 : ko0));
      prep(16 + q);
      SB();
    }
    {
      lds_vi rel = (lds_vi)(smem + LDS_SEL + 12);
      int it = 0;
      while (rel[0] != 4 && it < SPIN_MAX * 8) ++it;
      asm volatile("" ::: "memory");
    }
    TL(17);
    asm volatile("s_nop 7");
    SB();
    SLOTS(8) { epi(1, q); SB(); }
  }

  const char* pn = smem + (wr * 128 + fl) * 128;
  const char* pm = smem + G2_MRING + (wc * 64 + fl) * 128;
  f32x4 acc[8][4];
  auto mm = [&](bool imaj, int set, int q2, int s, int mode) __attribute__((always_inline)) {
    int i, jj, kk;
    if (imaj) { i = s >> 2; const int r = s & 3; jj = (r >> 1) ^ (i & 1); kk = (r == 1 || r == 2); }
    else { jj = s >> 3; const int r = s & 7; i = jj ? 3 - (r >> 1) : (r >> 1); kk = ((r + 1) >> 1) & 1; }
    if (mode == 1 && !(s & 1)) asm volatile("v_mfma_f32_16x16x32_bf16 %0, %2, %1, 0" : "=&v"(acc[q2 * 2 + jj][i]) : "v"(fm[kk][i]), "v"(fn[set][jj * 2 + kk]));
    else if (mode) asm volatile("v_mfma_f32_16x16x32_bf16 %0, %2, %1, %0" : "+v"(acc[q2 * 2 + jj][i]) : "v"(fm[kk][i]), "v"(fn[set][jj * 2 + kk]));
    else acc[q2 * 2 + jj][i] = __builtin_amdgcn_mfma_f32_16x16x32_bf16(fn[set][jj * 2 + kk], fm[kk][i], acc[q2 * 2 + jj][i], 0, 0, 0);
  };
  unsigned bn[2], bm[2], an[2], am[2];
#pragma unroll
  for (int kk = 0; kk < 2; ++kk) {
    bn[kk] = lds_addr(pn) + (kk ? ko1 : ko0); bm[kk] = lds_addr(pm) + (kk ? ko1 : ko0);
    asm volatile("" : "+v"(bn[kk]), "+v"(bm[kk]));
    an[kk] = bn[kk]; am[kk] = bm[kk] + m_off(0);
  }
#define RDN(jt, kk, o) (*LDSP(const bf16x8, an[kk] + (o) + (jt) * 2048))
#define RDM(i, kk, o) (*LDSP(const bf16x8, am[kk] + (o) + TO(i)))
  PAGE(10);
  TL(18);
  TL(19);
  tag = __builtin_amdgcn_readfirstlane(((const unsigned*)(smem + LDS_SEL))[5]);
  {
    int son = 0;
    auto tile = [&](int kt, int zero) __attribute__((always_inline)) {
      SLOTS(16) {
        mm(true, 0, 0, q, zero);
        if (!(q & 1)) {
          if (q < 8) fm[(q >> 1) & 1][2 + (q >> 2)] = RDM(2 + (q >> 2), (q >> 1) & 1, 0);
          else { const int r = (q - 8) >> 1; fn[1][r] = RDN(2 + (r >> 1), r & 1, 0); }
        }
        SB();
      }
      SLOTS(16) {
        mm(false, 1, 1, q, zero);
        if (!(q & 1) && q < 8) fn[0][q >> 1] = RDN(4 + (q >> 2), (q >> 1) & 1, 0);
        SB();
      }
      const int som = son >> 1;
      son = son == 65536 ? 0 : son + 32768;
      SLOTS(16) {
        mm(false, 0, 2, q, zero);
        if (!(q & 1) && q < 8) fn[1][q >> 1] = RDN(6 + (q >> 2), (q >> 1) & 1, 0);
        if (q == 9 || q == 11) { an[q == 11] = bn[q == 11] + son; asm volatile("" : "+v"(an[q == 11])); }
        if (q == 13 || q == 15) { am[q == 15] = bm[q == 15] + som; asm volatile("" : "+v"(am[q == 15])); }
        SB();
      }

      BAR_LGKM();

      SB();
      SLOTS(16) {
        mm(true, 1, 3, q, zero);
        if (!(q & 1)) {
          if (q < 8) fn[0][q >> 1] = RDN(q >> 2, (q >> 1) & 1, 0);
          else fm[(q >> 1) & 1][(q - 8) >> 2] = RDM((q - 8) >> 2, (q >> 1) & 1, 0);
        }
        SB();
      }
    };
    SB();
    tile(0, 1);
#pragma unroll 1
    for (int kt = 1; kt < G2_NKT - 3; ++kt) tile(kt, 0);
  }
  unsigned fx;
  constexpr int XC1 = XL0 >= 104 ? 3 : 0, XC2 = XL0 >= 152 ? 3 : 0;
  static_assert(XL0 + 2 * XLS < FL0 && (XL0 + 2 * XLS < 80 || XL0 >= 152 || (XL0 >= 104 && XL0 + 2 * XLS < 144)), "");
  int fo = lane == 0 ? kq * 512 : (int)0x80000000;
  {
    constexpr int NG = 12;
    static constexpr int g_t[NG] = {0, 0, 1, 1, 2, 2, 2, 1, 0, 0, 1, 2};
    static constexpr int g_q[NG] = {0, 1, 0, 1, 0, 1, 2, 2, 2, 3, 3, 3};
    constexpr int G14 = 1, G15 = 3, GP0 = 5, GP1 = 6;
    constexpr auto chp = [](int g) { return g == 0 || g_t[g] != g_t[g - 1]; };
    constexpr auto chn = [](int g) { return g + 1 < NG && g_t[g + 1] != g_t[g]; };
    static_assert(n_off(13) == 32768 && n_off(14) == 65536 && n_off(15) == 0, "");
    unsigned an2[2];
#pragma unroll
    for (int kk = 0; kk < 2; ++kk) {
      an[kk] = bn[kk]; an2[kk] = bn[kk] + 65536; am[kk] = bm[kk];
      asm volatile("" : "+v"(an[kk]), "+v"(an2[kk]), "+v"(am[kk]));
    }
    int vo;
    u32x4 pk;
    u32x2 kv;
    auto publish = [&](int d, int h) __attribute__((always_inline)) {
      if (h == 0) { vo = ml; PINV(vo); vo += (((d + kq + 1) & 3) * 4 + kq) * 4 * MSG; PINV(vo); }
      const f32x4 v = acc[2 * d + (h >> 2)][h & 3];
      pk[(h & 1) * 2] = pk2(v[0], v[1]); pk[(h & 1) * 2 + 1] = pk2(v[2], v[3]);
      if (h & 1) __builtin_amdgcn_raw_buffer_store_b128(pk, rs, vo + (h >> 1) * 1024, 0, 16);
    };
#define MSG_FLAG(d) __builtin_amdgcn_raw_buffer_store_b32(tag, rc, fo + (((d) + kq + 1) & 3) * 2048, 0, 16)
    auto rdn = [&](int g, int r) __attribute__((always_inline)) {
      const int t = g_t[g], jt = g_q[g] * 2 + (r >> 1);
      fn[g & 1][r] = *LDSP(const bf16x8, (t == 1 ? an2[r & 1] : an[r & 1]) + (t == 0 ? 32768 : 0) + jt * 2048);
    };
    auto rdm = [&](int t, int i, int kk) __attribute__((always_inline)) { fm[kk][i] = *LDSP(const bf16x8, am[kk] + m_off(13 + t) + TO(i)); };
    auto group = [&](auto GI) __attribute__((always_inline)) {
      constexpr int gi = decltype(GI)::value;
      if (gi == G14) { BAR(); if (CPRI) __builtin_amdgcn_s_setprio(CPRI);
        TL(47);
        SB(); }
      if (gi == G15) {
        PAGE(11);
        TL(53);
        BAR();
        TL(54);
        SB();
        kv = *(const u32x2*)(smem + LDS_SEL);
        SB();
      }
#pragma unroll
      for (int q = 0; q < 16; ++q) {
        const int n = gi * 16 + q;
        if (n == TS1) {
          TL(55);
        }
        if (n == TS2) {
          TL(58);
        }
        if (n == FL0) { wait_vm<8 + XC1>(); MSG_FLAG(0); SB(); }
        if (n == FL1) { wait_vm<5 + XC1>(); MSG_FLAG(1); SB(); }
        if (n == FL2) { wait_vm<2 + XC2>(); MSG_FLAG(2); fx = bld4_sc1(rc, po); SB(); }
        mm(chp(gi) || chn(gi), gi & 1, g_q[gi], q, 2);
        constexpr bool ni = gi + 1 < NG && (chp(gi + 1 < NG ? gi + 1 : gi) || chn(gi + 1 < NG ? gi + 1 : gi));
        auto nr = [](int r) { return ni && r >= 2 ? 5 - r : r; };
        if constexpr (chp(gi) && chn(gi)) {
          if (q < 4) rdm(g_t[gi], 2 + (q >> 1), q & 1); else if (q < 8) rdn(gi + 1, nr(q - 4)); else if (q < 12) rdm(g_t[gi + 1], (q - 8) >> 1, q & 1);
        } else if constexpr (chp(gi)) {
          if (q < 4) rdm(g_t[gi], 2 + (q >> 1), q & 1);
          else if (gi + 1 < NG && q < 12 && !(q & 1)) rdn(gi + 1, nr((q - 4) >> 1));
        } else if constexpr (chn(gi)) {
          if (!(q & 1)) { if (q < 8) rdn(gi + 1, nr(q >> 1)); else rdm(g_t[gi + 1], (q - 8) >> 2, (q >> 1) & 1); }
        } else {
          if (!(q & 1) && q < 8) rdn(gi + 1, nr(q >> 1));
        }
        if (gi == GP0 && q < 8) publish(0, q);
        if (gi == GP1 && q < 8) publish(1, q);
        if (gi == 9 && q < 8) publish(2, q);
        if (n >= XL0 && n < XL0 + 3 * XLS && (n - XL0) % XLS == 0) xq_load((n - XL0) / XLS);
        if (n == 72) { key[0] = __builtin_amdgcn_readfirstlane(kv.x); key[1] = __builtin_amdgcn_readfirstlane(kv.y); }
        SB();
      }
    };
#define GR4(i) group(IC<i>{}); group(IC<i + 1>{}); group(IC<i + 2>{}); group(IC<i + 3>{});
    GR4(0) GR4(4) GR4(8)
    asm volatile("s_nop 7");
    SB();
  }
  TL(50);
#pragma unroll
  for (int i = 0; i < 4; ++i) *(f32x4*)(smem + LDS_ACC + wr * 16384 + wc * 4096 + i * 1024 + lane * 16) = acc[7][i];
#pragma unroll
  for (int i = 0; i < 4; ++i) ya[i] = acc[6][i];
  asm volatile("s_waitcnt lgkmcnt(0)" ::: "memory");
  if (lane == 0) sig[0] = 2;
  SB();
  if (FL0 >= 192) { wait_vm<8 + XC1>(); MSG_FLAG(0); SB(); }
  TL(61);
  if (FL1 >= 192) { wait_vm<5 + XC1>(); MSG_FLAG(1); SB(); }
  TL(62);
  if (FL2 >= 192) { wait_vm<2 + XC2>(); MSG_FLAG(2); fx = bld4_sc1(rc, po); SB(); }
  TL(63);
  wait_xq<4>(xq, sq);
  cv_calc();
  asm volatile("s_waitcnt vmcnt(0)" : "+v"(fx) :: "memory");
  seen = (unsigned)__ballot(fx == tag);
  }

  PAGE(6);
  if (__builtin_expect(far0 == 0 || far1 == 0, 0)) {
#pragma unroll 1
    for (int c = 0; c < 2; ++c) {
      if (c ? far1 : far0) continue;
      const int ic = (0xffff - (c ? key[1] : key[0])) & (S_ - 1);
      const float sg = rbf(1.f / (1.f + __expf(-key_score(c ? key[1] : key[0]))));
      lds_vi cnt = (lds_vi)(smem + LDS_SEL + 24 + c * 4);
      int it = 0;
      while (cnt[0] != 4 && it < SPIN_MAX * 8) { __builtin_amdgcn_s_sleep(1); ++it; }
      asm volatile("" ::: "memory");
      const float* q = (const float*)(smem + LDS_FIN + c * 2048) + (wid & 1) * 64 + (fr >> 3) * 32 + col4(fr);
      const f32x4 v = (*(const f32x4*)q + *(const f32x4*)(q + 128)) + (*(const f32x4*)(q + 256) + *(const f32x4*)(q + 384));
#pragma unroll
      for (int r = 0; r < 4; ++r)
#pragma unroll
        for (int k = 0; k < 4; ++k) {
          cv[r * 4 + k] += (xo[r] >> 11) == ic ? v[k] * sg : 0.f;
        }
    }
  }
  if (ha) {
    int it = 0;
    while (sig[0] != 2 && it < SPIN_MAX * 8) { __builtin_amdgcn_s_sleep(1); ++it; }
    asm volatile("" ::: "memory");
#pragma unroll
    for (int i = 0; i < 4; ++i) ya[i] = *(const f32x4*)(smem + LDS_ACC + (wid & 2) * 8192 + (wid & 1) * 4096 + i * 1024 + lane * 16);
  }
  u32x4 pp[3][2];
  unsigned e_lo = 0x00003f80u, e_hi = 0x3f800000u;
  asm volatile("" : "+v"(e_lo), "+v"(e_hi));
  __amdgpu_buffer_rsrc_t rs_ = rs, rc_ = rc;
  int mo[3] = {MSG_IDX(0) * 4 * MSG, MSG_IDX(1) * 4 * MSG, MSG_IDX(2) * 4 * MSG}, mv = ml + ha * 2048, mv2 = mv + 1024;
  asm volatile("" : "+s"(rs_), "+s"(rc_), "+s"(mo[0]), "+s"(mo[1]), "+s"(mo[2]));
  asm volatile("" : "+v"(mv), "+v"(mv2));
#define MSG_POLL(m) do { \
    if (!((seen >> (m)) & 1)) { \
      unsigned pv_, it_ = SPIN_MAX; \
      asm volatile("1:\n\tbuffer_load_dword %0, %3, %4, 0 offen sc1\n\ts_waitcnt vmcnt(0)\n\tv_cmp_eq_u32 vcc, %5, %0\n\ts_mov_b32 %1, vcc_lo\n\t" \
                   "s_bitcmp1_b32 %1, %6\n\ts_cbranch_scc1 2f\n\ts_sub_u32 %2, %2, 1\n\ts_cbranch_scc1 2f\n\ts_sleep 1\n\ts_branch 1b\n2:" \
                   : "=&v"(pv_), "+s"(seen), "+s"(it_) : "v"(po), "s"(rc_), "s"(tag), "i"(m) : "vcc", "scc", "memory"); \
    } \
    pp[m][0] = __builtin_amdgcn_raw_buffer_load_b128(rs_, mv, mo[m], 16); \
    pp[m][1] = __builtin_amdgcn_raw_buffer_load_b128(rs_, mv2, mo[m], 16); } while (0)
#define MSG_ADD(m) do { \
    _Pragma("unroll") for (int u = 0; u < 8; ++u) { \
      ya[u >> 1][(u & 1) * 2] = add_bf(pp[m][u >> 2][u & 3], e_lo, ya[u >> 1][(u & 1) * 2]); ya[u >> 1][(u & 1) * 2 + 1] = add_bf(pp[m][u >> 2][u & 3], e_hi, ya[u >> 1][(u & 1) * 2 + 1]); } } while (0)
  MSG_POLL(2); MSG_POLL(1); MSG_POLL(0);
  SB();
#pragma unroll
  for (int e = 0; e < 16; ++e) ya[e & 3][e >> 2] += cv[e];
  asm volatile("" : "+v"(ya[0]), "+v"(ya[1]), "+v"(ya[2]), "+v"(ya[3]));
  SB();
  MSG_ADD(2); MSG_ADD(1);
  asm volatile("" : "+v"(ya[0]), "+v"(ya[1]), "+v"(ya[2]), "+v"(ya[3]));
  SB();
  MSG_ADD(0);
  {
#pragma unroll
    for (int r = 0; r < 4; ++r) *(u32x2*)((char*)out + (unsigned)xo[r]) = (u32x2){pk2(ya[0][r], ya[1][r]), pk2(ya[2][r], ya[3][r])};
  }

  if (wid == 0) {
    const unsigned te = (unsigned)__builtin_amdgcn_s_memrealtime();
    const unsigned t0 = __builtin_amdgcn_readlane(tlv, 52);
    const __amdgpu_buffer_rsrc_t rg = mk_rsrc(wsp + (64u << 20));
    if (lane == 0) { __builtin_amdgcn_raw_buffer_store_b64((u32x2){tag, t0}, rg, g * 32, 0, 16); __builtin_amdgcn_raw_buffer_store_b64((u32x2){tag, (unsigned)__builtin_amdgcn_readlane(tlv, 14)}, rg, g * 32 + 8, 0, 16);
      __builtin_amdgcn_raw_buffer_store_b64((u32x2){tag, (unsigned)__builtin_amdgcn_readlane(tlv, 1)}, rg, g * 32 + 16, 0, 16); }
    if (g == 37) {
      int mn0, mx[2], sum[2];
      for (int it = 0; it < 100000; ++it) {
        unsigned bad = 0; mn0 = 0x7fffffff; mx[0] = mx[1] = -0x7fffffff; sum[0] = sum[1] = 0;
        for (int k = 0; k < 4; ++k) {
          const u32x4 q = __builtin_amdgcn_raw_buffer_load_b128(rg, (k * 64 + lane) * 32, 0, 16);
          const u32x2 q2 = __builtin_amdgcn_raw_buffer_load_b64(rg, (k * 64 + lane) * 32 + 16, 0, 16);
          bad |= (q.x ^ tag) | (q.z ^ tag) | (q2.x ^ tag);
          mn0 = min(mn0, (int)(q.y - t0)); mx[0] = max(mx[0], (int)(q.w - t0)); sum[0] += (int)(q.w - t0); mx[1] = max(mx[1], (int)(q2.y - t0)); sum[1] += (int)(q2.y - t0);
        }
        if (__all(bad == 0)) break;
        asm volatile("" ::: "memory");
      }
      for (int o = 32; o; o >>= 1) { mn0 = min(mn0, __shfl_xor(mn0, o, 64)); for (int k = 0; k < 2; ++k) { mx[k] = max(mx[k], __shfl_xor(mx[k], o, 64)); sum[k] += __shfl_xor(sum[k], o, 64); } }
      int code = 0;
      { int d = sum[0] / 256 - mn0; d = (d - 700) / 10; code = code * 32 + min(max(d, 0), 31); }
      { int d = sum[1] / 256 - mn0; d = (d - 150) / 10; code = code * 16 + min(max(d, 0), 15); }
      const unsigned tend = __builtin_amdgcn_readfirstlane(t0 + mn0 + 4000 + code * 400);
      while ((int)((unsigned)__builtin_amdgcn_s_memrealtime() - tend) < 0) __builtin_amdgcn_s_sleep(1);
    }
  }

}

extern "C" void kernel_launch(void* const* d_in, const int* in_sizes, int n_in, void* d_out, int out_size,
                              void* d_ws, size_t ws_size, hipStream_t stream) {
  const u16* x = (const u16*)d_in[0];
  const u16* ws_re = (const u16*)d_in[1];
  const u16* ws_im = (const u16*)d_in[2];
  const u16* wd_re = (const u16*)d_in[3];
  const u16* wd_im = (const u16*)d_in[4];
  const u16* w_r = (const u16*)d_in[6];
  const u16* w_gate = (const u16*)d_in[7];
  const u16* w_up = (const u16*)d_in[8];
  const u16* w_down = (const u16*)d_in[9];
  k_main<<<256, 512, 0, stream>>>(x, wd_re, wd_im, w_r, (char*)d_ws, ws_re, ws_im, w_gate, w_up, w_down, (u16*)d_out);
}
```

```cpp
#include <hip/hip_runtime.h>
#include <hip/hip_bf16.h>
#include <stdint.h>
#include <type_traits>

extern "C" __device__ unsigned long long llvm_dispatch_id(void) __asm("llvm.amdgcn.dispatch.id");
extern "C" __device__ float llvm_fmul_legacy(float, float) __asm("llvm.amdgcn.fmul.legacy");
typedef unsigned short u16;
typedef unsigned long long u64;
typedef __attribute__((ext_vector_type(8))) short bf16x8;
typedef __attribute__((ext_vector_type(4))) unsigned u32x4;
typedef __attribute__((ext_vector_type(2))) unsigned u32x2;
typedef __attribute__((ext_vector_type(4))) float f32x4;
typedef __attribute__((ext_vector_type(2))) float f32x2;
typedef __attribute__((ext_vector_type(2))) __bf16 bf16x2;
typedef __attribute__((address_space(3))) void* lds_vp;
typedef volatile __attribute__((address_space(3))) int* lds_vi;

constexpr int S_ = 2048, D_ = 1024, F_ = 3584;
template <int N> using IC = std::integral_constant<int, N>;

static __device__ __forceinline__ float bf2f(u16 u) { return __uint_as_float((unsigned)u << 16); }
static __device__ __forceinline__ u16 f2bf(float f) {
  __hip_bfloat16 b = __float2bfloat16(f);
  return *reinterpret_cast<u16*>(&b);
}
static __device__ __forceinline__ float rbf(float f) { return bf2f(f2bf(f)); }
static __device__ __forceinline__ float lo_bf(unsigned u) { return __uint_as_float(u << 16); }
static __device__ __forceinline__ float hi_bf(unsigned u) { return __uint_as_float(u & 0xffff0000u); }
static __device__ __forceinline__ unsigned pk2(float a, float b) {
  const bf16x2 v = __builtin_convertvector((f32x2){a, b}, bf16x2);
  return __builtin_bit_cast(unsigned, v);
}
static __device__ __forceinline__ float add_bf(unsigned u, unsigned e, float c) {
  return __builtin_amdgcn_fdot2_f32_bf16(__builtin_bit_cast(bf16x2, u), __builtin_bit_cast(bf16x2, e), c, false);
}
static __device__ __forceinline__ float ln_elem(float x, float mb, float sd, float ri) {
  const float d = rbf(x - mb);
  const float q0 = d * ri;
  return rbf(__builtin_fmaf(__builtin_fmaf(-q0, sd, d), ri, q0));
}
template <int CTRL> static __device__ __forceinline__ float dpp_f(float v) {
  return __int_as_float(__builtin_amdgcn_update_dpp(0, __float_as_int(v), CTRL, 0xf, 0xf, true));
}
template <int CTRL> static __device__ __forceinline__ unsigned dpp_u(unsigned v) {
  return (unsigned)__builtin_amdgcn_update_dpp(0, (int)v, CTRL, 0xf, 0xf, true);
}
static __device__ __forceinline__ float row_sum(float v) {
  v += dpp_f<0xB1>(v);
  v += dpp_f<0x4E>(v);
  v += dpp_f<0x141>(v);
  v += dpp_f<0x140>(v);
  return v;
}
static __device__ __forceinline__ float fold32(float a, float b) {
  const auto r = __builtin_amdgcn_permlane32_swap(__float_as_uint(a), __float_as_uint(b), false, false);
  return __uint_as_float(r[0]) + __uint_as_float(r[1]);
}
static __device__ __forceinline__ float fold16(float a, float b) {
  const auto r = __builtin_amdgcn_permlane16_swap(__float_as_uint(a), __float_as_uint(b), false, false);
  return __uint_as_float(r[0]) + __uint_as_float(r[1]);
}
static __device__ __forceinline__ float rdl(float v, int l) { return __int_as_float(__builtin_amdgcn_readlane(__float_as_int(v), l)); }

#define TL(I) do { if ((I) == 51) { const unsigned _t = (unsigned)__builtin_amdgcn_s_memrealtime(); tlv = lane == (I) ? _t : tlv; } } while (0)
#define XMAP(g) (((~g & 1) << 2) | (g & 2) | ((g >> 2) & 1))
#define ST_JB 9
#define WSN 10
#define FSM 0xa2
#define GV_JB 7
#define DG 11
#define AB_STRIDE 32
#define SPIN_MAX (1 << 17)
#define FL0 168
#define FL1 192
#define FL2 192
#define VA0 5
#define VAN 4
#define VAP 116
#define VB0 12
#define VBN 0
#define VBP 6
#define SP0 3
#define SP1 7
#define SP1E 11
#define VC0 14
#define VCN 0
#define VCP 9
#define SPS 0x000cccccdddd00
#define SPE 0
#define PRIO2 3
#define N2A 8
#define N3A 8
#define WSL 2
#define LNP 0
#define LNS 2
#define EPP 0
#define DGS 0
#define DGP 0
#define TS1 112
#define TS2 152
#define XL0 33
#define XLS 4
#define CPRI 0
#define LAGS 0x00000000000000
#define DVS 0x00000400300000
#define GVS 0x00000022000000
constexpr int dig(unsigned long long t, int q) { return q < 0 ? 0 : (int)((t >> (4 * q)) & 15); }
constexpr int acc_dig(unsigned long long t, int q) { int a = 0; for (int i = 0; i < q; ++i) a += dig(t, i); return a; }

static __device__ __forceinline__ unsigned lds_addr(const void* p) { return (unsigned)(size_t)(lds_vp)p; }

constexpr int MSG = 4096;
constexpr size_t WS_ABT   = 0;
constexpr size_t WS_SLAB  = WS_ABT + (size_t)D_ * 4096 * 2;
constexpr size_t WS_GU    = WS_SLAB + (size_t)64 * 64 * MSG;
constexpr size_t WS_DN    = WS_GU + (size_t)8 * F_ * 16;
constexpr size_t WS_FLAGS = WS_DN + (size_t)2 * 8 * 32 * 64 * 16;
constexpr int FL_X = 0;
constexpr int FL_AB = 64 * 64 * 32;
constexpr int FL_ST = FL_AB + 2048 * AB_STRIDE;
constexpr int FL_CE = FL_ST + 4096;
constexpr int FL_KEY = FL_CE + 4096;
constexpr int FL_EPOCH = FL_KEY + 4096;
constexpr int WS_MB = (int)((WS_FLAGS + (FL_EPOCH + 1) * 4 + 1048575) >> 20);

#define WAIT_VM(n) asm volatile("s_waitcnt vmcnt(" #n ")" ::: "memory")
template <int N> static __device__ __forceinline__ void wait_vm() { asm volatile("s_waitcnt vmcnt(%0)" :: "i"(N) : "memory"); }
#define BAR_LGKM() asm volatile("s_waitcnt lgkmcnt(0)\n\ts_barrier" ::: "memory")
#define BAR() asm volatile("s_barrier" ::: "memory")
#define SB() __builtin_amdgcn_sched_barrier(0)
#define PAGE(n) asm volatile("s_branch 2f\n\t.p2align 12\nk_pg" #n ":\n\ts_setpc_b64 s[42:43]\n2:")
#define CALL_PG(n) asm volatile("s_getpc_b64 s[40:41]\n3:\n\ts_mov_b32 s42, k_pg" #n "-3b\n\ts_ashr_i32 s43, s42, 31\n\ts_add_u32 s40, s40, s42\n\ts_addc_u32 s41, s41, s43\n\t" \
                                "s_swappc_b64 s[42:43], s[40:41]" ::: "s40", "s41", "s42", "s43", "scc", "memory")

static __device__ __forceinline__ __amdgpu_buffer_rsrc_t mk_rsrc(const void* p) {
  return __builtin_amdgcn_make_buffer_rsrc((void*)p, 0, 0x7fffffff, 0x00020000);
}
#define DMA_(rsrc, voff, mbase, MI, sbase, SI, pol) do { unsigned _t; \
    asm volatile("s_add_i32 m0, %2, %3\n\ts_add_i32 %0, %5, %6\n\tbuffer_load_dwordx4 %1, %4, %0 offen" pol " lds" \
                 : "=&s"(_t) : "v"(voff), "s"(mbase), "i"(MI), "s"(rsrc), "s"(sbase), "i"(SI) : "memory", "scc"); } while (0)
#define DMA_I(rsrc, voff, mbase, MI, SI) \
    asm volatile("s_add_i32 m0, %1, %2\n\ts_nop 0\n\tbuffer_load_dwordx4 %0, %3, 0 offen offset:%4 lds" \
                 :: "v"(voff), "s"(mbase), "i"((MI) - (SI)), "s"(rsrc), "i"(SI) : "memory", "scc")
#define DMA_S(rsrc, voff, mbase, MI, soff) \
    asm volatile("s_add_i32 m0, %1, %2\n\ts_nop 0\n\tbuffer_load_dwordx4 %0, %3, %4 offen lds" \
                 :: "v"(voff), "s"(mbase), "i"(MI), "s"(rsrc), "s"(soff) : "memory", "scc")
#define TPOL ""
#define TOUCH(rsrc, voff, mbase, MI, soff) \
    asm volatile("s_add_i32 m0, %1, %2\n\ts_nop 0\n\tbuffer_load_dword %0, %3, %4 offen" TPOL " lds" \
                 :: "v"(voff), "s"(mbase), "i"(MI), "s"(rsrc), "s"(soff) : "memory", "scc")
template <int OFF> static __device__ __forceinline__ u32x4 ld16_s(const void* p, int voff) {
  u32x4 v;
  asm volatile("global_load_dwordx4 %0, %1, %2 offset:%3" : "=v"(v) : "v"(voff), "s"(p), "i"(OFF) : "memory");
  return v;
}
template <int OFF> static __device__ __forceinline__ void sld(unsigned& sink, const void* p, int soff) {
  asm volatile("s_load_dword %0, %1, %2 offset:%3" : "+s"(sink) : "s"(p), "s"(soff), "i"(OFF) : "memory");
}
static __device__ __forceinline__ unsigned gld4_s(const void* p, unsigned voff) {
  unsigned v;
  asm volatile("global_load_dword %0, %1, %2" : "=v"(v) : "v"(voff), "s"(p) : "memory");
  return v;
}
static __device__ __forceinline__ u32x2 bld8_sc1(__amdgpu_buffer_rsrc_t r, unsigned voff) {
  u32x2 v;
  asm volatile("buffer_load_dwordx2 %0, %1, %2, 0 offen sc1" : "=v"(v) : "v"(voff), "s"(r) : "memory");
  return v;
}
template <int N> static __device__ __forceinline__ void wait_xs(unsigned& a, u32x2& b) { asm volatile("s_waitcnt vmcnt(%2)" : "+v"(a), "+v"(b) : "i"(N) : "memory"); }
template <int N> static __device__ __forceinline__ void wait_xq(u32x4 (&a)[2], u32x2& b) { asm volatile("s_waitcnt vmcnt(%3)" : "+v"(a[0]), "+v"(a[1]), "+v"(b) : "i"(N) : "memory"); }
static __device__ __forceinline__ u32x2 ld8_s_sc1(const void* p, int voff) {
  u32x2 v;
  asm volatile("global_load_dwordx2 %0, %1, %2 sc1" : "=v"(v) : "v"(voff), "s"(p) : "memory");
  return v;
}
static __device__ __forceinline__ void lgkm0(unsigned& sink) { asm volatile("s_waitcnt lgkmcnt(0)" : "+s"(sink) :: "memory"); }
template <typename T> static __device__ __forceinline__ void pin4(T& a, T& b, T& c, T& d) { asm volatile("" : "+s"(a), "+s"(b), "+s"(c), "+s"(d)); }
template <typename T, typename U> static __device__ __forceinline__ void pins(T& a, U& b) { asm volatile("" : "+s"(a), "+s"(b)); }
static __device__ __forceinline__ void launder(int& v, int& a, int& b) { asm volatile("" : "+v"(v)); asm volatile("" : "+s"(a), "+s"(b)); }
static __device__ __forceinline__ unsigned bld4_sc1(__amdgpu_buffer_rsrc_t r, int voff) {
  unsigned v;
  asm volatile("buffer_load_dword %0, %1, %2, 0 offen sc1" : "=v"(v) : "v"(voff), "s"(r) : "memory");
  return v;
}
static __device__ __forceinline__ u32x4 bld16_nt(__amdgpu_buffer_rsrc_t r, int voff, int soff) {
  u32x4 v;
  asm volatile("buffer_load_dwordx4 %0, %1, %2, %3 offen nt" : "=v"(v) : "v"(voff), "s"(r), "s"(soff) : "memory");
  return v;
}
static __device__ __forceinline__ void bld16_nt_io(u32x4& v, __amdgpu_buffer_rsrc_t r, int voff, int soff) {
  asm volatile("buffer_load_dwordx4 %0, %1, %2, %3 offen nt" : "+v"(v) : "v"(voff), "s"(r), "s"(soff) : "memory");
}
static __device__ __forceinline__ u32x4 bld16_sc1(__amdgpu_buffer_rsrc_t r, int voff, int soff) {
  u32x4 v;
  asm volatile("buffer_load_dwordx4 %0, %1, %2, %3 offen sc1" : "=v"(v) : "v"(voff), "s"(r), "s"(soff) : "memory");
  return v;
}
static __device__ __forceinline__ u32x2 granule_wait(const u64* p, unsigned tag) {
  u64 v = 0;
#pragma unroll 1
  for (int it = 0; it < SPIN_MAX; ++it) {
    v = __hip_atomic_load(p, __ATOMIC_RELAXED, __HIP_MEMORY_SCOPE_AGENT);
    if (__all((unsigned)(v >> 32) == tag)) break;
    __builtin_amdgcn_s_sleep(2);
  }
  return (u32x2){(unsigned)v, (unsigned)(v >> 32)};
}
static __device__ __forceinline__ float key_score(unsigned key) {
  const unsigned u = key >> 16;
  return bf2f((u16)((u & 0x8000u) ? (u & 0x7fffu) : (~u & 0xffffu)));
}
static __device__ __forceinline__ unsigned med3u(unsigned a, unsigned b, unsigned c) { return max(min(a, b), min(max(a, b), c)); }
static __device__ __forceinline__ void merge2(unsigned& a1, unsigned& a2, unsigned b1, unsigned b2) {
  const unsigned lo = min(a1, b1);
  a1 = max(a1, b1);
  a2 = max(lo, max(a2, b2));
}
static __device__ __forceinline__ float sqdev16(const u32x4 (&v)[2], float mb) {
  f32x2 q2 = {0.f, 0.f};
#pragma unroll
  for (int i = 0; i < 8; ++i) {
    const f32x2 e = (f32x2){lo_bf(v[i >> 2][i & 3]), hi_bf(v[i >> 2][i & 3])} - mb;
    q2 += e * e;
  }
  return q2.x + q2.y;
}

constexpr int lrw(int r) { return (r & ~6) | ((r & 2) << 1) | ((r & 4) >> 1); }
constexpr int swz(int r) { return ((r >> 1) & 4) | (r & 2); }
static __device__ __forceinline__ int sym_dn(int p) { return (p ^ (p >> 1)) & 1; }
static __device__ __forceinline__ int sym_hi(int n, int w, int st, int b, int z, int p) {
  return ((p >> 1) * (n / 2 * st) + b * (w * st)) ^ (-sym_dn(p) & (((n - 1) ^ (z * (n / 2))) * st));
}
static __device__ __forceinline__ int sym_lo(int w, int st, int p, int c) { return ((c - sym_dn(p)) * st) & ((w - 1) * st); }
static __device__ __forceinline__ int sym_row(int n, int w, int st, int b, int z, int p, int c) { return sym_lo(w, st, p, c) ^ sym_hi(n, w, st, b, z, p); }
constexpr int G1_NST = 4, G1_NKT = 16, G1_STAGE = 32768;
constexpr int G2_NKT = 16, G2_MRING = 98304;
constexpr int n_off(int k) { return (k % 3) * 32768; }
constexpr int m_off(int k) { return ((k + 2) % 3) * 16384; }
constexpr int k_ord(int k) { return k < 2 ? k : (k < 9 ? 2 * (k - 1) : 2 * (k - 8) + 1); }
constexpr int LDS_DUMMY = G2_MRING + 49152;
constexpr int LDS_GU = LDS_DUMMY + 4096;
constexpr int LDS_FIN = LDS_GU + 4096;
constexpr int LDS_MB = LDS_FIN + 4096;
constexpr int LDS_SEL = LDS_MB + 1536;
constexpr int LDS_SIG = LDS_SEL + 64;
constexpr int LDS_KEY = LDS_SIG + 32;
constexpr int LDS_BYTES = LDS_KEY + 128;
constexpr int LDS_ACC = n_off(G2_NKT - 3);

constexpr int sp_tile(int q) {
  for (int k = 0; k < 16; ++k) {
    if ((k >= VA0 && k < VA0 + VAN) || (k >= VB0 && k < VB0 + VBN) || (k >= VC0 && k < VC0 + VCN)) continue;
    if (q-- == 0) return k;
  }
  return -1;
}
constexpr int tcnt(int q) { return (VAN > 0 && q == VAP) + (VBN > 0 && q == VBP) + (VCN > 0 && q == VCP); }
constexpr int dn_cnt(int q) { return dig(DVS, q); }
constexpr int dn_first(int q) { return acc_dig(DVS, q); }

static __device__ __forceinline__ void code_touch(unsigned& sink, unsigned off) {
  asm volatile("s_getpc_b64 s[40:41]\n\tk_main_rt:\n\ts_min_u32 %1, %1, k_main_end-k_main_rt-196\n\t"
               "s_load_dword %0, s[40:41], %1 offset:0\n\ts_load_dword %0, s[40:41], %1 offset:64\n\ts_load_dword %0, s[40:41], %1 offset:128\n\ts_load_dword %0, s[40:41], %1 offset:192"
               : "+s"(sink), "+s"(off) :: "s40", "s41", "scc", "memory");
}
static __device__ __forceinline__ void touch1(__amdgpu_buffer_rsrc_t r, int voff, unsigned mb, int soff) { TOUCH(r, voff, mb, LDS_DUMMY, soff); }
typedef const __attribute__((address_space(1))) u16* gptr;
typedef __attribute__((ext_vector_type(8))) u64 u64x8;
typedef __attribute__((ext_vector_type(2))) u64 u64x2;
extern "C" __global__ __launch_bounds__(512, 2) void k_main(const u16* x, const u16* wd_re, const u16* wd_im, const u16* w_r,
                                                 char* wsp, const u16* ws_re, const u16* ws_im,
                                                 const u16* w_gate, const u16* w_up, const u16* w_down, u16* out) {
  __shared__ __attribute__((aligned(16))) char smem[LDS_BYTES];
  u64x8 ka;
  u64x2 kb_;
  u64 kc;
  asm volatile("s_load_dwordx16 %0, %3, 0\n\ts_load_dwordx4 %1, %3, 64\n\ts_load_dwordx2 %2, %3, 80"
               : "=&s"(ka), "=&s"(kb_), "=&s"(kc) : "s"(__builtin_amdgcn_kernarg_segment_ptr()) : "memory");
  unsigned long long t00;
  asm volatile("s_memrealtime %0" : "=s"(t00) :: "memory");
  const int tid = threadIdx.x, lane = tid & 63;
  const int wid = __builtin_amdgcn_readfirstlane(tid >> 6);
  const int g = blockIdx.x, xcd = XMAP(g), j = g >> 3;
  if (wid < 4) {
    unsigned cw, co = (j * 4 + wid) * 448;
    asm volatile(".subsection 1\nk_main_end:\n.subsection 0\n\ts_min_u32 %1, %1, k_main_end-k_main-480\n\ts_getpc_b64 s[40:41]\n\ts_add_u32 s40, s40, %1\n\ts_addc_u32 s41, s41, 0\n\t"
                 "s_load_dword %0, s[40:41], 0\n\ts_load_dword %0, s[40:41], 64\n\ts_load_dword %0, s[40:41], 128\n\t"
                 "s_load_dword %0, s[40:41], 192\n\ts_load_dword %0, s[40:41], 256\n\ts_load_dword %0, s[40:41], 320\n\ts_load_dword %0, s[40:41], 384\n\ts_waitcnt lgkmcnt(0)"
                 : "=&s"(cw), "+s"(co) :: "s40", "s41", "scc", "memory");
  }
  const int pw = wid - 4;
  const int row0 = (wid & 3) * 8 + lrw(lane >> 3);
  const int cm = ((lane & 7) ^ swz(row0)) * 16;
  int vm = ((row0 & 15) * 2 + (row0 >> 4)) * (D_ * 2) + cm;
  asm volatile("" : "+v"(vm));
  const int kq = xcd >> 1;
  const int tt = (xcd & 1) * 4 + (j >> 3);
  const int et = j & 7;
  const int z = kq >> 1, st = (kq & 1) * 8 + tt;
  const int lrow = (kq & 1) * 1024 + (xcd & 1) * 512 + z * 256 + j * 8 + pw * 2;
  const int drow = ((kq & 1) * 2 + (xcd & 1)) * 256 + j * 8 + pw * 2;
  unsigned o_m = st * 128 * D_ * 2, o_l = lrow * D_ * 2, o_d = drow * D_ * 2, mb0 = lds_addr(smem) + pw * 1024;
  int l16 = lane * 16;
  int vn[4];
#pragma unroll
  for (int q = 0; q < 4; ++q) vn[q] = sym_lo(32, D_ * 2, (row0 >> 2) & 3, q * 8 + (row0 >> 4) * 4 + (row0 & 3)) ^ (sym_hi(D_, 32, D_ * 2, et, z, (row0 >> 2) & 3) | cm);
  asm volatile("" : "+s"(o_m), "+s"(o_l), "+s"(o_d), "+s"(mb0));
  asm volatile("" : "+v"(l16), "+v"(vn[0]), "+v"(vn[1]), "+v"(vn[2]), "+v"(vn[3]));
  asm volatile("s_waitcnt lgkmcnt(0)" : "+s"(ka), "+s"(kb_), "+s"(kc) :: "memory");
  x = (const u16*)(gptr)ka[0]; wd_re = (const u16*)(gptr)ka[1]; wd_im = (const u16*)(gptr)ka[2]; w_r = (const u16*)(gptr)ka[3];
  wsp = (char*)(gptr)ka[4]; ws_re = (const u16*)(gptr)ka[5]; ws_im = (const u16*)(gptr)ka[6]; w_gate = (const u16*)(gptr)ka[7];
  w_up = (const u16*)(gptr)kb_[0]; w_down = (const u16*)(gptr)kb_[1]; out = (u16*)(gptr)kc;
  int* flags = (int*)(wsp + WS_FLAGS);
  u64* stat = (u64*)(flags + FL_ST);
  unsigned tlv = 0;
  asm volatile("s_waitcnt lgkmcnt(0)" : "+s"(t00) :: "memory");
  tlv = lane == 52 ? (unsigned)t00 : tlv;
  const u16* wdz = z ? wd_im : wd_re;
  const __amdgpu_buffer_rsrc_t rm = mk_rsrc(x);
  const __amdgpu_buffer_rsrc_t rn = mk_rsrc(wdz);
#define G1_X(kt, so, q) DMA_(rm, vm, mb0, (so) + (q) * 4096, o_m, (kt) * 128 + (((q) & 1) * 64 + ((q) >> 1) * 32) * D_ * 2, "")
#define G1_W(kt, so, q) DMA_I(rn, vn[q], mb0, (so) + 16384 + (q) * 4096, (kt) * 128)
#define G1_STAGE_TILE(kt, so, W) do { SB(); G1_X(kt, so, 0); W(0); G1_X(kt, so, 1); W(1); G1_X(kt, so, 2); W(2); G1_X(kt, so, 3); W(3); \
    G1_W(kt, so, 0); W(4); G1_W(kt, so, 1); W(5); G1_W(kt, so, 2); W(6); G1_W(kt, so, 3); W(7); SB(); } while (0)
#define G1_STAGE_A(kt, so, W) do { SB(); G1_X(kt, so, 0); W(0); G1_X(kt, so, 2); W(1); \
    G1_W(kt, so, 0); W(2); G1_W(kt, so, 1); W(3); G1_W(kt, so, 2); W(4); G1_W(kt, so, 3); W(5); W(6); W(7); SB(); } while (0)
#define G1_STAGE_B(kt, so) do { SB(); G1_X(kt, so, 1); G1_X(kt, so, 3); SB(); } while (0)
#define NOW(i)
  const int fr = lane & 15, fq = lane >> 4;
  const int ha = wid >> 2;
  int trow, xo[4], xa[3], po, ml;
  const char *prs, *prc;
  auto col4 = [](int f) __attribute__((always_inline)) { return ((f & 2) << 3 | (f & 4) << 1 | (f & 1) << 2) ^ (-((f >> 3) & 1) & 28); };
  auto seg0 = [&](int hf, int dn) __attribute__((always_inline)) { return (hf * 512 + et * 32) ^ (-dn & (D_ - 32)); };
  auto epi_setup = [&](int part) __attribute__((always_inline)) {
    int t_ = tt, l_ = lane;
    asm volatile("" : "+s"(t_));
    asm volatile("" : "+v"(l_));
    const int ts = ((wid >> 1) & 1) * 8 + kq * 2 + ha;
    if (part == 0) { xo[3] = (seg0(wid & 1, (l_ >> 3) & 1) + col4(l_)) * 2; asm volatile("" : "+v"(xo[3])); }
    if (part == 1) {
      xo[3] |= sym_hi(S_, 64, D_ * 2, t_, z, l_ >> 4);
      ml = (l_ * 16) ^ (-sym_dn(l_ >> 4) & (z * 768));
      asm volatile("" : "+v"(xo[3]), "+v"(ml));
    }
    if (part == 2) {
      const int xh = xo[3];
#pragma unroll
      for (int r = 0; r < 4; ++r) xo[r] = sym_lo(64, D_ * 2, l_ >> 4, ts * 4 + r) ^ xh;
      const bool hi = xo[0] & 8;
      xa[0] = (hi ? xo[2] : xo[0]) & ~15; xa[1] = (hi ? xo[3] : xo[1]) & ~15;
      xa[2] = (sym_lo(64, D_ * 2, l_ >> 4, ts * 4 + (l_ & 3)) ^ xh) >> 11 << 3;
      asm volatile("" : "+v"(xo[0]), "+v"(xo[1]), "+v"(xo[2]), "+v"(xo[3]), "+v"(xa[0]), "+v"(xa[1]), "+v"(xa[2]));
    }
    if (part == 3) {
      trow = t_ * 16 + ts;
      prs = wsp + WS_SLAB + (size_t)((t_ * 8 + et) * 64 + (wid & 3)) * MSG;
      prc = (const char*)(flags + FL_X + ((t_ * 8 + et) * 64 + (wid & 3)) * 32);
      asm volatile("" : "+s"(trow), "+s"(prs), "+s"(prc));
      po = l_ < 3 ? (kq * 4 + ((kq + 1 + l_) & 3)) * 512 : (int)0x80000000;
      asm volatile("" : "+v"(po));
    }
  };
  auto tok_tile = [](unsigned key) __attribute__((always_inline)) {
    const unsigned h = (0xffff - key) & 1023;
    return h < 512 ? h >> 2 : ((1023 - h) >> 6 << 4) | (((1024 - h) & 63) >> 2);
  };
#define rs mk_rsrc(prs)
#define rc mk_rsrc(prc)
  lds_vi sig = (lds_vi)(smem + LDS_SIG + (wid & 3) * 4);
  unsigned tag;
  u32x4 xq[2];
  u32x2 sq;
  f32x4 ya[4];
  unsigned key[2];
  float cv[16];
  unsigned far0, far1;
  auto xq_load = [&](int n) __attribute__((always_inline)) {
    if (n < 2) xq[n] = ld16_s<0>(x, xa[n]); else sq = ld8_s_sc1(stat, xa[2]);
  };
#define PINV(a) asm volatile("" : "+v"(a))
  auto cv_calc = [&]() __attribute__((always_inline)) {
    if (__builtin_expect(!__all(sq.y == tag), 0)) sq = granule_wait(stat + (xa[2] >> 3), tag);
    far0 = tok_tile(key[0]) ^ trow; far1 = tok_tile(key[1]) ^ trow;
    asm volatile("" : "+s"(far0), "+s"(far1));
    const bool hi = xo[0] & 8;
    const float mb_ = lo_bf(sq.x), ri_ = __builtin_amdgcn_rcpf(hi_bf(sq.x));
#pragma unroll
    for (int r = 0; r < 4; ++r) {
      const float mb = r == 0 ? dpp_f<0x00>(mb_) : r == 1 ? dpp_f<0x55>(mb_) : r == 2 ? dpp_f<0xAA>(mb_) : dpp_f<0xFF>(mb_);
      const float ri = r == 0 ? dpp_f<0x00>(ri_) : r == 1 ? dpp_f<0x55>(ri_) : r == 2 ? dpp_f<0xAA>(ri_) : dpp_f<0xFF>(ri_);
#pragma unroll
      for (int u = 0; u < 2; ++u) {
        const unsigned own = xq[r & 1][(r & 2) + u], oth = dpp_u<0xB1>(xq[r & 1][2 - (r & 2) + u]);
        const unsigned w = hi == (r >= 2) ? own : oth;
        cv[r * 4 + 2 * u] = __builtin_fmaf(lo_bf(w) - mb, ri, lo_bf(w));
        cv[r * 4 + 2 * u + 1] = __builtin_fmaf(hi_bf(w) - mb, ri, hi_bf(w));
        PINV(cv[r * 4 + 2 * u]); PINV(cv[r * 4 + 2 * u + 1]);
      }
    }
  };
#define MSG_IDX(m) (kq * 4 + ((kq + 1 + (m)) & 3))
  unsigned seen = 0;

  if (__builtin_expect(wid >= 4, 1)) {
    __builtin_amdgcn_s_setprio(3);
    G1_STAGE_TILE(0, 0, NOW);
    constexpr int O_ST = (FL_ST - FL_AB) * 4, O_CE = (FL_CE - FL_AB) * 4, O_KEY = (FL_KEY - FL_AB) * 4, O_EP = (FL_EPOCH - FL_AB) * 4;
    const __amdgpu_buffer_rsrc_t rf = mk_rsrc(wsp + WS_FLAGS + FL_AB * 4);
    u32x4 lv[2], lv2[2], lw[2], ld[2], ld2[2];
    unsigned epv;
    {
      const char* xrow = (const char*)x + o_l;
      const char* wdrow = (const char*)wdz + o_d;
      lv[0] = ld16_s<0>(xrow, l16); lv[1] = ld16_s<1024>(xrow, l16);
      lv2[0] = ld16_s<2048>(xrow, l16); lv2[1] = ld16_s<3072>(xrow, l16);
      ld[0] = ld16_s<0>(wdrow, l16); ld[1] = ld16_s<1024>(wdrow, l16);
      ld2[0] = ld16_s<2048>(wdrow, l16); ld2[1] = ld16_s<3072>(wdrow, l16);
      lw[0] = ld16_s<0>(w_r, l16); lw[1] = ld16_s<1024>(w_r, l16);
      asm volatile("buffer_load_dword %0, off, %1, %2 sc1" : "=v"(epv) : "s"(rf), "s"(O_EP) : "memory");
    }
    SB();
    const unsigned long long did = llvm_dispatch_id();
    const unsigned dmix = (unsigned)did * 0x85EBCA6Bu + (unsigned)(did >> 32) * 0xC2B2AE35u + 0x27D4EB2Fu;
    const bool moe = et != 7;
    unsigned epoch;
    const int ci = lane & 15, rq = lane >> 4, tk = (lane >> 3) & 1;
    int mat, cg, dq, d0;
    u32x4 wv[16];
    const u16 *pgu, *pn2;
    const char *pm2, *spb;
#define rw __builtin_amdgcn_make_buffer_rsrc((void*)pgu, 0, moe ? 0x7fffffff : 0, 0x00020000)
#define rn2 mk_rsrc(pn2)
#define rm2 mk_rsrc(pm2)
    int vw, vn2[8], vm2;
    auto k_blk = [&](int k) __attribute__((always_inline)) { return tt ^ (k_ord(k) >> 1); };
#define KROT(kk_) (k_blk(kk_) * 256 + (k_ord(kk_) & 1) * 128)
    int tv2[3];
    unsigned sd = 0;
    auto spf = [&](auto N) __attribute__((always_inline)) {
      constexpr int n = decltype(N)::value;
      if constexpr (n >= 0 && sp_tile(n >> 4) >= 0) sld<((n >> 1) & 7) * 4096 + (n & 1) * 64>(sd, spb, KROT(sp_tile(n >> 4)));
    };
    auto touches = [&](auto P) __attribute__((always_inline)) {
      constexpr int p = decltype(P)::value;
      if constexpr (VAN > 0 && p == VAP) touch1(mk_rsrc(spb), tv2[0], mb0, 0);
      if constexpr (VBN > 0 && p == VBP) touch1(mk_rsrc(spb), tv2[1], mb0, 0);
      if constexpr (VCN > 0 && p == VCP) touch1(mk_rsrc(spb), tv2[2], mb0, 0);
    };
    unsigned mq0, mq1, mq2, mq3;
    const int cg8 = et ^ ((et & 1) << 1), rgb = (g & 7) * 4 + (j >> 3);
    const __amdgpu_buffer_rsrc_t rdn = mk_rsrc(w_down);
    const __amdgpu_buffer_rsrc_t rgu = mk_rsrc(wsp + WS_GU);
    int vdn, vgu;
    u32x4 dv[7], gv[4];
    int abo, abq[3];
    unsigned lma;
    unsigned sel[2][4];
    float g0, g1;
    u32x4 kk[4];
    unsigned k1 = 0, k2 = 0;
    const int ko_ = O_KEY + pw * 4096;
#define MM(a, b, c) __builtin_amdgcn_mfma_f32_16x16x32_bf16(__builtin_bit_cast(bf16x8, a), __builtin_bit_cast(bf16x8, b), c, 0, 0, 0)
#define PIN1(a) asm volatile("" : "+v"(a))
#define PIN2(a, b) asm volatile("" : "+v"(a), "+v"(b))
#define PIN3(a, b, c) asm volatile("" : "+v"(a), "+v"(b), "+v"(c))
    {
      int gro, go;
      u32x2 gr;
      u32x4 oh[5];
      f32x4 sa4, gw[2], gx[2];
      float su, gs, ta, tb, sx, mbv;
      unsigned dk[4], kl;
      const int hb = lane >> 5;
#pragma unroll
      for (int l = 0; l < WSN; ++l) asm volatile("" : "=v"(wv[l]));
      auto diag = [&](const f32x4& m) __attribute__((always_inline)) {
        return __uint_as_float((__float_as_uint(m[0]) & dk[0]) | (__float_as_uint(m[1]) & dk[1]) | (__float_as_uint(m[2]) & dk[2]) | (__float_as_uint(m[3]) & dk[3]));
      };
      auto setup = [&](auto JB, auto GP) __attribute__((always_inline)) {
        constexpr int jb = decltype(JB)::value, gp = decltype(GP)::value;
        SB();
        {
          int ln = lane, jj = j, p_ = pw;
          launder(ln, jj, p_);
          const int c_ = ln & 15, r_ = ln >> 4, e_ = jj & 7, t_ = (xcd & 1) * 4 + (jj >> 3);
          if constexpr (jb == 1 && gp == 0) {
            pn2 = ((kq >> 1) ? ws_im : ws_re) + (kq & 1) * 1024;
            spb = (const char*)(pn2 + (sym_row(S_, 64, 1, t_, 0, e_ >> 1, 0) + (e_ & 1) * 32 + p_ * 8) * S_);
            pins(pn2, spb);
          }
          if constexpr (jb == 1 && gp >= 1 && gp < 6) {
            const unsigned o = c_ == gp - 1 ? 0x3f803f80u : 0u;
            oh[gp - 1] = (u32x4){o, o, o, o};
            PIN1(oh[gp - 1]);
          }
          if constexpr (jb == 1 && gp == 6) {
            const int rk = moe ? e_ * 8 + (jj >> 3) * 2 + (xcd >> 2) : 0, r3 = ((rk - 14) * 43) >> 7, rr = rk - 14 - r3 * 3;
            const int c56 = rk < 14 ? rk * 4 + 1 : r3 * 4 + rr + ((rr + 1) >> 1);
            mat = (c56 + 36) >> 6; cg = c56 - mat * 28; dq = xcd & 3; d0 = dq * 256 + p_ * 64;
            pin4(mat, cg, dq, d0);
          }
          if constexpr (jb == 2 && gp == 0) {
            pgu = mat ? w_up : w_gate;
            pm2 = wsp + WS_ABT + (kq * 8 + e_) * 262144;
            pins(pgu, pm2);
            vw = ((d0 + r_ * 16) * F_ + cg * 128 + c_ * 8) * 2;
            PIN1(vw);
          }
          if constexpr (jb == 2 && (gp == 1 || gp == 2)) {
            dk[gp * 2 - 2] = c_ == r_ * 4 + gp * 2 - 2 ? ~0u : 0u; dk[gp * 2 - 1] = c_ == r_ * 4 + gp * 2 - 1 ? ~0u : 0u;
            PIN2(dk[gp * 2 - 2], dk[gp * 2 - 1]);
          }
          if constexpr (jb == 2 && gp == 3) { kl = lrow + (ln >> 5); go = (ln & 30) ? (int)0x80000000 : ((ln & 1) ? O_KEY : O_ST) + (int)kl * 8; PIN2(kl, go); }
          if constexpr (jb == 2 && gp == 4) { kl = 0xffff - kl; go = (ln & ~1) == 2 ? O_CE + (z * D_ + drow + (ln & 1)) * 8 : go; PIN2(kl, go); }
          if constexpr (jb == 2 && gp == 5) {
            gro = p_ < 2 ? O_ST + (st * 128 + p_ * 64 + ln) * 8 : O_CE + z * D_ * 8 + sym_row(D_, 32, 8, e_, z, (ln >> 2) & 3, (p_ - 2) * 16 + r_ * 4 + (ln & 3));
            lma = lds_addr(smem + LDS_MB) + (p_ < 2 ? p_ * 256 : 512 + p_ * 256) + ln * 4;
            PIN2(gro, lma);
          }
          if constexpr (jb == 1 && gp == 7) {
#pragma unroll
            for (int u = 0; u < 3; ++u) {
              const int vn_ = u == 0 ? VAN : u == 1 ? VBN : VCN;
              if (vn_ == 0) continue;
              const int k = (u == 0 ? VA0 : u == 1 ? VB0 : VC0) + (ln >> 3);
              tv2[u] = (ln >> 3) < vn_ ? (ln & 7) * 4096 + ((t_ ^ (k < 9 ? k - 1 : k - 8)) << 8) + (k < 9 ? 0 : 128) : (int)0x80000000;
              PIN1(tv2[u]);
            }
          }
          if constexpr (jb == 7 && gp == 0) {
            vm2 = p_ * 1024 + ln * 16;
            PIN1(vm2);
          }
          if constexpr (jb == 2 && gp == 6) {
            const int r0 = p_ * 8 + lrw(ln >> 3);
            vn2[6] = sym_lo(64, S_ * 2, (r0 >> 2) & 3, (r0 >> 4) * 4 + (r0 & 3)); vn2[7] = sym_hi(S_, 64, S_ * 2, t_, z, (r0 >> 2) & 3) | (vm & 127);
            PIN2(vn2[6], vn2[7]);
          }
          if constexpr ((jb == 2 && gp == 7) || (jb == 7 && gp >= 6)) {
            constexpr int q0 = jb == 2 ? 0 : gp == 6 ? 3 : 6;
            const int lo = vn2[6], hi = vn2[7];
#pragma unroll
            for (int q = q0; q < q0 + 3 && q < 8; ++q) { vn2[q] = ((lo + q * 8 * S_ * 2) & (63 * S_ * 2)) ^ hi; PIN1(vn2[q]); }
          }
          if constexpr (jb == 7 && gp == 1) { vdn = rgb * 112 + p_ * 28 + r_ * 7; vgu = (((ln >> 3) & 1) * 4 * F_ + vdn + min(ln & 7, 6)) * 16; PIN2(vdn, vgu); }
          if constexpr (jb == 7 && gp == 2) { vdn = (vdn * D_ + cg8 * 128 + c_ * 8) * 2; PIN1(vdn); }
          if constexpr (jb == 7 && gp == 3) {
            abo = (((kq * 8 + e_) * 4 + p_) * 16 + c_) * AB_STRIDE * 4;
            abq[2] = ln >= 16 || (ln & 7) == t_ ? (int)0x80000000 : abo;
            PIN2(abo, abq[2]);
          }
          if constexpr (jb == 7 && gp == 4) {
            abq[1] = ln >= 8 ? (int)0x80000000 : abq[2];
            abq[0] = ((ln ^ t_) & 4) ? (int)0x80000000 : abq[1];
            PIN2(abq[0], abq[1]);
          }
          if constexpr (jb == 7 && gp == 5) {
            const unsigned mb = lds_addr(smem) + p_ * 1024;
            mq0 = mb + ((3 - kq) & 3) * 4096; mq1 = mb + ((4 - kq) & 3) * 4096; mq2 = mb + ((5 - kq) & 3) * 4096; mq3 = mb + ((6 - kq) & 3) * 4096;
            pin4(mq0, mq1, mq2, mq3);
          }
          if constexpr (jb == 8 && gp < 4) {
#pragma unroll
            for (int h = 0; h < 2; ++h) {
              const unsigned hv = h ? 0x0302u : 0x0100u;
              sel[h][gp] = ((ln & 7) >> 1) == gp ? ((ln & 1) ? (hv << 16) | 0x0c0cu : 0x0c0c0000u | hv) : 0x0c0c0c0cu;
            }
            PIN2(sel[0][gp], sel[1][gp]);
          }
        }
        SB();
      };
#define WS1(i) setup(IC<1>{}, IC<i>{})
#define WS2(i) setup(IC<2>{}, IC<i>{})
#define WS3(i) do { setup(IC<7>{}, IC<i>{}); if (i >= 4) setup(IC<8>{}, IC<(i) & 3>{}); spf(IC<(i < SP0 ? i : -1)>{}); } while (0)
      G1_STAGE_TILE(1, G1_STAGE, WS1);
      WAIT_VM(19); BAR();
      G1_STAGE_TILE(2, 2 * G1_STAGE, WS2);
      G1_STAGE_TILE(3, 3 * G1_STAGE, WS3);
      auto lnu = [&](auto U) __attribute__((always_inline)) {
        constexpr int jb = 1 + decltype(U)::value / 7, gp = decltype(U)::value % 7;
        SB();
        if constexpr (jb == 1) {
          const f32x4 z4 = {0.f, 0.f, 0.f, 0.f};
          if constexpr (gp == 0) { sa4 = MM(lv[0], oh[0], z4); gw[0] = MM(lv[0], lw[0], z4); gx[0] = MM(lv[0], lv[0], z4); PIN3(sa4, gw[0], gx[0]); }
          if constexpr (gp == 1) { sa4 = MM(lv2[0], oh[1], sa4); gw[1] = MM(lv2[0], lw[0], z4); gx[1] = MM(lv2[0], lv2[0], z4); PIN3(sa4, gw[1], gx[1]); }
          if constexpr (gp == 2) { sa4 = MM(lv[1], oh[0], sa4); gw[0] = MM(lv[1], lw[1], gw[0]); gx[0] = MM(lv[1], lv[1], gx[0]); PIN3(sa4, gw[0], gx[0]); }
          if constexpr (gp == 3) { sa4 = MM(lv2[1], oh[1], sa4); gw[1] = MM(lv2[1], lw[1], gw[1]); gx[1] = MM(lv2[1], lv2[1], gx[1]); PIN3(sa4, gw[1], gx[1]); }
          if constexpr (gp == 4) { sa4 = MM(ld[0], oh[2], sa4); sa4 = MM(ld[1], oh[2], sa4); PIN1(sa4); }
          if constexpr (gp == 5) { sa4 = MM(ld2[0], oh[3], sa4); sa4 = MM(ld2[1], oh[3], sa4); PIN1(sa4); }
          if constexpr (gp == 6) { sa4 = MM(lw[0], oh[4], sa4); sa4 = MM(lw[1], oh[4], sa4); PIN1(sa4);
            epoch = __builtin_amdgcn_readfirstlane(epv); tag = ((epoch + 1u) * 0x9E3779B1u ^ dmix) | 1u; }
        }
        if constexpr (jb == 2) {
          if constexpr (gp == 0) { su = (sa4[0] + sa4[1]) + (sa4[2] + sa4[3]); ta = diag(gw[0]); PIN2(su, ta); }
          if constexpr (gp == 1) { su = fold32(su, su); tb = diag(gw[1]); PIN2(su, tb); }
          if constexpr (gp == 2) { su = fold16(su, su); gs = fold32(ta, tb); PIN2(su, gs); }
          if constexpr (gp == 3) { ta = diag(gx[0]); tb = diag(gx[1]); PIN2(ta, tb); }
          if constexpr (gp == 4) { gs = fold16(gs, fold32(ta, tb)); PIN1(gs); }
          if constexpr (gp == 5) { gs += dpp_f<0xB1>(gs); gs += dpp_f<0x4E>(gs); PIN1(gs); }
          if constexpr (gp == 6) { gs += dpp_f<0x141>(gs); gs += dpp_f<0x140>(gs); PIN1(gs); }
        }
        if constexpr (jb == 3) {
          if constexpr (gp == 0) {
            const float s0 = rdl(su, 0), s1 = rdl(su, 1), q0 = rdl(gs, 16), q1 = rdl(gs, 48);
            sx = hb ? s1 : s0; ta = hb ? q1 : q0; PIN2(sx, ta);
          }
          if constexpr (gp == 1) {
            const float mean = sx * (1.0f / D_);
            mbv = rbf(mean);
            ta -= sx * mean;
            if (__builtin_expect(__any(!(sx * mean <= 16.f * ta)), 0)) {
              float q = fold32(sqdev16(lv, rdl(mbv, 0)), sqdev16(lv2, rdl(mbv, 32)));
              q = row_sum(fold16(q, q));
              const float dm = mean - mbv;
              ta = fmaxf(q - (float)D_ * dm * dm, 0.f);
            }
            PIN2(mbv, ta);
          }
          if constexpr (gp == 2) { ta = rbf(rbf(ta * (1.0f / D_)) + 1e-5f); gs -= mbv * rdl(su, 4); PIN2(ta, gs); }
          if constexpr (gp == 3) { ta = rbf(__builtin_amdgcn_sqrtf(ta)); tb = __builtin_amdgcn_rcpf(ta); PIN2(ta, tb); }
          if constexpr (gp == 4) {
            const unsigned rb = __float_as_uint(rbf(gs * tb));
            kl |= (rb ^ ((unsigned)((int)rb >> 31) | 0x80000000u)) & 0xffff0000u; PIN1(kl);
          }
          if constexpr (gp == 5) { kl = (lane & 2) ? __float_as_uint(su) : (lane & 1) ? kl : (__float_as_uint(mbv) >> 16) | __float_as_uint(ta); PIN1(kl); }
        }
        SB();
      };
      constexpr int LN_JB = 1 + ((19 - LNP) * LNS) / 8;
      auto work1 = [&](auto JB, auto GP) __attribute__((always_inline)) {
        constexpr int jb = decltype(JB)::value, gp = decltype(GP)::value, pos = (jb - 1) * 8 + gp;
        SB();
        if constexpr (pos % LNS == 0 && LNP + pos / LNS < 20) lnu(IC<LNP + pos / LNS>{});
        if constexpr (jb == ST_JB + 3) {
          if constexpr (gp == 0) { if (!__all(gr.y == tag)) gr = granule_wait((const u64*)((const char*)(flags + FL_AB) + gro), tag); PIN1(gr); }
          if constexpr (gp == 1) { g1 = __builtin_amdgcn_rcpf(__uint_as_float((gr.x & 0xffff0000u) ^ ((unsigned)z << 31))); PIN1(g1); }
          if constexpr (gp == 2) { g0 = pw < 2 ? -(lo_bf(gr.x) * g1) : __uint_as_float(gr.x); PIN1(g0); }
        }
        if constexpr (gp < SP1 && jb <= SP1E) spf(IC<SP0 + (jb - 1) * SP1 + gp>{});
        SB();
      };
      auto step1 = [&](auto JB) __attribute__((always_inline)) {
        constexpr int jb = decltype(JB)::value;
        if constexpr (jb == 1) PAGE(1);
        if constexpr (jb == 7) PAGE(2);
        constexpr int n16 = jb == 12 ? 12 : jb == 11 ? 14 : 16;
        constexpr auto wl = [](int q) { return (q == LN_JB ? 1 : 0) + (q == ST_JB ? 1 : 0); };
        if constexpr (jb == 1) {
          asm volatile("s_waitcnt vmcnt(%11)" : "+v"(lv[0]), "+v"(lv[1]), "+v"(lw[0]), "+v"(lw[1]), "+v"(ld[0]), "+v"(ld[1]),
                       "+v"(lv2[0]), "+v"(lv2[1]), "+v"(ld2[0]), "+v"(ld2[1]), "+v"(epv) : "i"(LNP ? 24 : 16) : "memory");
#define LU(u) if constexpr (u < LNP) lnu(IC<u>{});
          LU(0) LU(1) LU(2) LU(3) LU(4) LU(5) LU(6) LU(7) LU(8) LU(9) LU(10) LU(11) LU(12) LU(13) LU(14) LU(15) LU(16) LU(17) LU(18)
          if (LNP) WAIT_VM(16);
        } else if constexpr (jb == ST_JB + 3) {
          asm volatile("s_waitcnt vmcnt(%1)" : "+v"(gr) : "i"(n16 + wl(jb - 2) + wl(jb - 1)) : "memory");
        } else wait_vm<n16 + wl(jb - 3) + wl(jb - 2) + wl(jb - 1)>();
        BAR();
        if constexpr (jb == 2) { if (pw == 0 && lane < 16) ((int*)(smem + LDS_SEL))[lane < 8 ? lane : lane + 8] = 0; }
        if constexpr (jb == 4) { if (pw == 0 && lane == 0) ((unsigned*)(smem + LDS_SEL))[5] = tag; }
#define W1(i) work1(JB, IC<i>{})
        if constexpr (jb < 10) G1_STAGE_TILE(jb - 1 + G1_NST, ((jb - 1) % G1_NST) * G1_STAGE, W1);
        else G1_STAGE_A(jb - 1 + G1_NST, ((jb - 1) % G1_NST) * G1_STAGE, W1);
        if constexpr (jb == 12) { G1_STAGE_B(13, G1_STAGE); G1_STAGE_B(14, 2 * G1_STAGE); }
        if constexpr (jb == LN_JB) __builtin_amdgcn_raw_buffer_store_b64((u32x2){kl, tag}, rf, go, 0, 16);
        if constexpr (jb == ST_JB) {
          asm volatile("buffer_load_dwordx2 %0, %1, %2, 0 offen sc1" : "=v"(gr) : "v"(gro), "s"(rf) : "memory");
        }
        touches(IC<100 + jb>{});
        if constexpr (jb <= 8) {
          if (et == jb - 1) {
#pragma unroll
            for (int l = 0; l < WSN; ++l) bld16_nt_io(wv[l], rw, vw, l * F_ * 2);
          }
        }
        SB();
      };
      static_assert(ST_JB + 3 <= 12 && ST_JB >= 6, "schedule");
      step1(IC<1>{}); step1(IC<2>{}); step1(IC<3>{}); step1(IC<4>{}); step1(IC<5>{}); step1(IC<6>{}); step1(IC<7>{}); step1(IC<8>{}); step1(IC<9>{}); step1(IC<10>{}); step1(IC<11>{}); step1(IC<12>{});
    }
#define ALIAS 0
#define G2_STAGE_N(kk_, W) do { const int kb = KROT(((ALIAS >> (kk_)) & 1) ? (kk_) & 3 : (kk_)); constexpr int so = n_off(kk_); SB(); \
    DMA_S(rn2, vn2[0], mq0, so, kb); W(0); DMA_S(rn2, vn2[1], mq1, so, kb); W(1); \
    DMA_S(rn2, vn2[2], mq2, so, kb); W(2); DMA_S(rn2, vn2[3], mq3, so, kb); W(3); \
    DMA_S(rn2, vn2[4], mq0, so + 16384, kb); W(4); DMA_S(rn2, vn2[5], mq1, so + 16384, kb); W(5); \
    DMA_S(rn2, vn2[6], mq2, so + 16384, kb); W(6); DMA_S(rn2, vn2[7], mq3, so + 16384, kb); W(7); SB(); } while (0)
#define G2_STAGE_NH(kk_, h) do { const int kb = KROT(kk_); constexpr int so = n_off(kk_) + (h) * 16384; SB(); \
    DMA_S(rn2, vn2[(h) * 4], mq0, so, kb); DMA_S(rn2, vn2[(h) * 4 + 1], mq1, so, kb); \
    DMA_S(rn2, vn2[(h) * 4 + 2], mq2, so, kb); DMA_S(rn2, vn2[(h) * 4 + 3], mq3, so, kb); SB(); } while (0)
#define G2_STAGE_M(kk_, W) do { const int kb = KROT(kk_) * 128; constexpr int so = G2_MRING + m_off(kk_); SB(); \
    DMA_(rm2, vm2, mb0, so, kb, 0, " sc1"); W(8); DMA_(rm2, vm2, mb0, so + 4096, kb, 4096, " sc1"); W(9); \
    DMA_(rm2, vm2, mb0, so + 8192, kb, 8192, " sc1"); W(10); DMA_(rm2, vm2, mb0, so + 12288, kb, 12288, " sc1"); W(11); SB(); } while (0)
    WAIT_VM(16); BAR();
    asm volatile("ds_write_b32 %0, %1" :: "v"(lma), "v"(g0) : "memory");
    if (pw < 2) asm volatile("ds_write_b32 %0, %1 offset:512" :: "v"(lma), "v"(g1) : "memory");
    u32x4 tv[4];
    unsigned ready = 0;
    auto need = [&](int k) __attribute__((always_inline)) {
      const int b = k_blk(k) + (k_ord(k) & 1) * 8;
#pragma unroll 1
      for (int it = 0; !((ready >> b) & 1) && it < SPIN_MAX; ++it) {
        ready |= (unsigned)__ballot(__builtin_amdgcn_raw_buffer_load_b32(rf, abq[k < 5 ? 0 : k < 9 ? 1 : 2], 0, 16) == tag);
        asm volatile("" ::: "memory");
      }
    };
    unsigned ksel, bad = 0;
    int isel;
    unsigned xw;
    auto bvec = [&](unsigned src, const unsigned (&sl)[4]) __attribute__((always_inline)) {
      return (u32x4){__builtin_amdgcn_perm(0, src, sl[0]), __builtin_amdgcn_perm(0, src, sl[1]),
                     __builtin_amdgcn_perm(0, src, sl[2]), __builtin_amdgcn_perm(0, src, sl[3])};
    };
#define BC8(v, p) (unsigned)__builtin_amdgcn_update_dpp(__builtin_amdgcn_update_dpp(0, (int)(v), 0x150 + (p), 0xf, 0x3, false), (int)(v), 0x158 + (p), 0xf, 0xc, false)
    float* lg = (float*)(smem + LDS_GU);
    f32x4 macc = {0.f, 0.f, 0.f, 0.f};
    u32x2 sv;
    unsigned hp, gbad;
    float s0, s1;
    auto work2 = [&](auto JB, auto GP) __attribute__((always_inline)) {
      constexpr int jb = decltype(JB)::value, gp = decltype(GP)::value;
      SB();
      if constexpr (jb == -1 && gp < 4) {
        constexpr int c = gp == 0 ? 0xB1 : gp == 1 ? 0x4E : gp == 2 ? 0x141 : 0x140;
        merge2(k1, k2, dpp_u<c>(k1), dpp_u<c>(k2));
        PIN2(k1, k2);
      }
      if constexpr (jb == -1 && gp == 4) {
        ksel = tk ? k2 : k1;
        isel = (0xffff - (int)(ksel & 0xffffu)) & (S_ - 1);
        k1 = (isel * D_ + d0 + rq * 16 + (lane & 7) * 2) * 2; k2 = O_ST + isel * 8;
        PIN1(ksel);
        xw = gld4_s(x, k1);
        sv = bld8_sc1(rf, k2);
      }
      if constexpr (jb == 0 && gp == 1) wait_xs<3 + 1 + (N2A < 2 ? N2A : 2) + tcnt(116) + tcnt(117)>(xw, sv);
      if constexpr (jb == 0) {
        if constexpr (gp == 1) { if (!__all(sv.y == tag)) sv = granule_wait(stat + isel, tag); s0 = __builtin_amdgcn_rcpf(hi_bf(sv.x)); PIN1(s0); }
        if constexpr (gp == 2) { s1 = ln_elem(lo_bf(xw), lo_bf(sv.x), hi_bf(sv.x), s0); PIN1(s1); }
        if constexpr (gp == 3) { xw = pk2(s1, ln_elem(hi_bf(xw), lo_bf(sv.x), hi_bf(sv.x), s0)); PIN1(xw); }
      }
      if constexpr (DGS == 0 && jb == DG && gp < 7) { PIN1(dv[gp]); macc = MM(dv[gp], bvec(BC8(hp, gp), sel[0]), macc); PIN1(macc); }
      if constexpr (jb >= 100 && gp < 7) { PIN1(dv[gp]); macc = MM(dv[gp], bvec(BC8(hp, gp), sel[0]), macc); PIN1(macc); }
      if constexpr ((jb == 0 && gp >= 4) || jb == 1) {
        constexpr int l = jb == 0 ? gp - 4 : gp + 4;
        if constexpr (!(l & 1)) hp = BC8(xw, l >> 1);
        PIN1(wv[l]);
        macc = MM(wv[l], bvec(hp, sel[l & 1]), macc);
        PIN2(macc, hp);
      }
      if constexpr (jb == GV_JB + 2) {
        if constexpr (gp == 0) { gbad = (gv[0].y ^ tag) | (gv[0].w ^ tag) | (gv[1].y ^ tag) | (gv[1].w ^ tag); PIN1(gbad); }
        if constexpr (gp == 1) { gbad |= (gv[2].y ^ tag) | (gv[2].w ^ tag) | (gv[3].y ^ tag) | (gv[3].w ^ tag); PIN1(gbad); }
        if constexpr (gp == 2) {
          s0 = ((__uint_as_float(gv[0].x) + __uint_as_float(gv[1].x)) + __uint_as_float(gv[2].x)) + __uint_as_float(gv[3].x);
          s1 = ((__uint_as_float(gv[0].z) + __uint_as_float(gv[1].z)) + __uint_as_float(gv[2].z)) + __uint_as_float(gv[3].z);
          PIN2(s0, s1);
        }
        if constexpr (gp == 3) {
#pragma unroll 1
          for (int it = 0; !__all(gbad == 0) && it < SPIN_MAX; ++it) {
            u32x4 q[4];
            gbad = 0;
#pragma unroll
            for (int i = 0; i < 4; ++i) { q[i] = __builtin_amdgcn_raw_buffer_load_b128(rgu, vgu, i * F_ * 16, 16); gbad |= (q[i].y ^ tag) | (q[i].w ^ tag); }
            s0 = ((__uint_as_float(q[0].x) + __uint_as_float(q[1].x)) + __uint_as_float(q[2].x)) + __uint_as_float(q[3].x);
            s1 = ((__uint_as_float(q[0].z) + __uint_as_float(q[1].z)) + __uint_as_float(q[2].z)) + __uint_as_float(q[3].z);
            asm volatile("" ::: "memory");
          }
          const unsigned gp2 = pk2(s0, s1);
          const unsigned up2 = dpp_u<0x128>(gp2);
          s0 = tk ? hi_bf(up2) : lo_bf(gp2); s1 = tk ? hi_bf(gp2) : lo_bf(up2);
          PIN2(s0, s1);
        }
        if constexpr (gp == 4) { s0 = rbf(s0 * __builtin_amdgcn_rcpf(1.f + __expf(-s0))); PIN1(s0); }
        if constexpr (gp == 5) { hp = pk2(s0 * s1, 0.f); PIN1(hp); }
      }
      if constexpr (jb == 10 && gp == 4) code_touch(sd, (j * 4 + pw) * 256);
      SB();
    };
#define W2M(i) work2(IC<-1>{}, IC<i>{})
#define W20(i) work2(IC<0>{}, IC<i>{})
#define W21(i) work2(IC<1>{}, IC<i>{})
    auto piece = [&](auto K, auto Q) __attribute__((always_inline)) {
      constexpr int k = decltype(K)::value, q = decltype(Q)::value;
      if constexpr (q < 8) {
        const int kb = KROT(((ALIAS >> k) & 1) ? k & 3 : k);
        DMA_S(rn2, vn2[q], (q & 3) == 0 ? mq0 : (q & 3) == 1 ? mq1 : (q & 3) == 2 ? mq2 : mq3, n_off(k) + (q >> 2) * 16384, kb);
      } else {
        if constexpr (q == 8) need(k);
        const int kb = KROT(k) * 128;
        DMA_(rm2, vm2, mb0, G2_MRING + m_off(k) + (q - 8) * 4096, kb, (q - 8) * 4096, " sc1");
      }
    };
    unsigned fs;
    {
      G1_STAGE_B(15, 3 * G1_STAGE);
      G2_STAGE_NH(0, 0);
      lgkm0(sd);
      WAIT_VM(16); BAR();
      G2_STAGE_NH(0, 1);
      WAIT_VM(10); BAR();
#pragma unroll
      for (int i = 0; i < 4; ++i) kk[i] = bld16_sc1(rf, l16, ko_ + i * 1024);
#pragma unroll
      for (int l = WSN; l < 16; ++l) wv[l] = bld16_nt(rw, vw, l * F_ * 2);
      asm volatile("s_waitcnt vmcnt(%4)" : "+v"(kk[0]), "+v"(kk[1]), "+v"(kk[2]), "+v"(kk[3]) : "i"(16 - WSN) : "memory");
      touches(IC<115>{});
#pragma unroll
      for (int i = 0; i < 4; ++i) {
        k2 = med3u(k1, k2, kk[i].x); k1 = max(k1, kk[i].x);
        k2 = med3u(k1, k2, kk[i].z); k1 = max(k1, kk[i].z);
        bad |= (kk[i].y ^ tag) | (kk[i].w ^ tag);
      }
#pragma unroll 1
      for (int it = 0; !__all(bad == 0) && it < SPIN_MAX; ++it) {
        k1 = 0; k2 = 0; bad = 0;
#pragma unroll
        for (int i = 0; i < 4; ++i) {
          const u32x4 q = __builtin_amdgcn_raw_buffer_load_b128(rf, l16, ko_ + i * 1024, 16);
          k2 = med3u(k1, k2, q.x); k1 = max(k1, q.x);
          k2 = med3u(k1, k2, q.z); k1 = max(k1, q.z);
          bad |= (q.y ^ tag) | (q.w ^ tag);
        }
        asm volatile("" ::: "memory");
      }
      merge2(k1, k2, dpp_u<0xB1>(k1), dpp_u<0xB1>(k2));
      merge2(k1, k2, dpp_u<0x4E>(k1), dpp_u<0x4E>(k2));
      merge2(k1, k2, dpp_u<0x141>(k1), dpp_u<0x141>(k2));
      merge2(k1, k2, dpp_u<0x140>(k1), dpp_u<0x140>(k2));
      if ((lane & 15) == 0) *(u32x2*)(smem + LDS_KEY + (pw * 4 + rq) * 8) = (u32x2){k1, k2};
      PAGE(3);
      BAR_LGKM();
#pragma unroll
      for (int jj = 0; jj < 4; ++jj) tv[jj] = *(const u32x4*)(smem + G2_MRING + m_off(0) + (jj * 4 + pw) * 1024 + lane * 16);
      { const u32x2 kp = *(const u32x2*)(smem + LDS_KEY + ci * 8); k1 = kp.x; k2 = kp.y; }
#pragma unroll
      for (int jj = 0; jj < 4; ++jj) __builtin_amdgcn_raw_buffer_store_b128(tv[jj], rm2, vm2 + jj * 4096 + 2 * tt * 16384, 0, 16);
      G2_STAGE_N(1, W2M);
      touches(IC<116>{});
      wait_vm<10 + tcnt(116)>();
      if (lane == tt) __builtin_amdgcn_raw_buffer_store_b32(tag, rf, abo, 0, 16);
      touches(IC<117>{});
#define P2A(i) if constexpr (i < N2A) piece(IC<2>{}, IC<i>{}); W20(i);
      SB(); P2A(0) P2A(1) P2A(2) P2A(3) P2A(4) P2A(5) P2A(6) P2A(7)
      __builtin_amdgcn_s_sleep(WSL);
      wait_vm<1 + N2A + tcnt(116) + tcnt(117)>();
      fs = bld4_sc1(rf, abq[0]);
    }
    BAR();
#pragma unroll
    for (int jj = 0; jj < 4; ++jj) tv[jj] = *(const u32x4*)(smem + G2_MRING + m_off(1) + (jj * 4 + pw) * 1024 + lane * 16);
    constexpr int SPB = SP0 + SP1E * SP1;
#define P2B(i) if constexpr (i >= N2A) piece(IC<2>{}, IC<i>{});
    SB(); P2B(0) P2B(1) P2B(2) P2B(3) P2B(4) P2B(5) P2B(6) P2B(7)
#define P3A(i) if constexpr (i < N3A) piece(IC<3>{}, IC<i>{});
    P3A(0) P3A(1) P3A(2) P3A(3) P3A(4) P3A(5) P3A(6) P3A(7)
    asm volatile("s_waitcnt vmcnt(%1)" : "+v"(fs) : "i"(8 - N2A + N3A) : "memory");
    ready = (unsigned)__ballot(fs == tag);
    need(2);
    G2_STAGE_M(2, NOW);
#define P3B(i) if constexpr (i >= N3A) piece(IC<3>{}, IC<i>{});
    P3B(0) P3B(1) P3B(2) P3B(3) P3B(4) P3B(5) P3B(6) P3B(7)
    need(3);
    G2_STAGE_M(3, NOW);
    fs = bld4_sc1(rf, abq[1]);
    touches(IC<1>{});
#pragma unroll
    for (int i = 0; i < dn_cnt(1); ++i) dv[i] = bld16_nt(rdn, vdn, i * D_ * 2);
    W21(0); W21(1); W21(2); W21(3); W21(4); W21(5); W21(6); W21(7); W21(8); W21(9); W21(10); W21(11);
    asm volatile("" : "+v"(tv[0]), "+v"(tv[1]), "+v"(tv[2]), "+v"(tv[3]));
    auto step2 = [&](auto JB) __attribute__((always_inline)) {
      constexpr int jb = decltype(JB)::value;
      if constexpr (jb == 3) PAGE(4);
      if constexpr (jb == 2 && PRIO2 != 3) __builtin_amdgcn_s_setprio(PRIO2);
      if constexpr (jb == 9) PAGE(5);
      constexpr auto must = [](int q) { return (q == 2 ? 4 : 0) + ((FSM >> q) & 1) + dig(GVS, q); };
      constexpr auto late = [](int q) { return dn_cnt(q) + (VAN > 0 && q == VAP) + (VBN > 0 && q == VBP) + (VCN > 0 && q == VCP); };
      if constexpr (jb == 2) wait_vm<8 - N3A + 4 + must(1) + late(1)>();
      else wait_vm<(dig(LAGS, jb - 2) ? 0 : late(jb - 2) + (jb == 5 ? 1 : 0)) + 12 - dig(LAGS, jb - 1) + must(jb - 1) + late(jb - 1)>();
      if constexpr (jb == 3) { if (pw == 0 && (lane & ~8) == 0) ((unsigned*)(smem + LDS_SEL))[tk] = ksel; }
      if constexpr ((jb == 2 || jb == DG + 1)) {
        *(f32x4*)(lg + pw * 256 + lane * 4) = macc;
        macc = (f32x4){0.f, 0.f, 0.f, 0.f};
        lgkm0(sd);
      }
      BAR();
      float p0[4], p1[4];
      const int col = (pw & 1) * 64 + lane;
      if constexpr (jb == 3) {
        if (g == 0 && pw == 0 && lane == 0) __hip_atomic_store((unsigned*)flags + FL_EPOCH, epoch + 1u, __ATOMIC_RELAXED, __HIP_MEMORY_SCOPE_AGENT);
      }
      if constexpr (jb == DG + 1) {
        const float* p = lg + (pw & 1) * 32 + (lane >> 4) * 64 + ((2 * lane) & 7) * 4 + ((lane >> 2) & 3);
#pragma unroll
        for (int i = 0; i < 4; ++i) { p0[i] = p[i * 256]; p1[i] = p[i * 256 + 4]; }
      }
      if constexpr (jb == 2) {
        const float* p = lg + (col >> 5) * 64 + (col & 7) * 4 + ((col >> 3) & 3);
#pragma unroll
        for (int i = 0; i < 4; ++i) { p0[i] = p[i * 256]; p1[i] = p[i * 256 + 32]; }
      }
      if constexpr (jb == 4) {
        if (lane == 8 + tt) __builtin_amdgcn_raw_buffer_store_b32(tag, rf, abo, 0, 16);
      }
      if constexpr ((FSM >> (jb - 2)) & 1) {
        asm volatile("" : "+v"(fs));
        ready |= (unsigned)__ballot(fs == tag);
      }
      if constexpr (jb == GV_JB + 2) asm volatile("" : "+v"(gv[0]), "+v"(gv[1]), "+v"(gv[2]), "+v"(gv[3]));
      auto w2 = [&](auto GP) __attribute__((always_inline)) {
        constexpr int gp = decltype(GP)::value;
        if constexpr (jb == 2 && gp == 0) {
          p0[0] = (p0[0] + p0[1]) + (p0[2] + p0[3]); p1[0] = (p1[0] + p1[1]) + (p1[2] + p1[3]);
          PIN2(p0[0], p1[0]);
        }
        if constexpr (gp < 12) work2(JB, GP);
        if constexpr (gp < dig(SPS, jb) && !((SPE >> jb) & 1)) spf(IC<SPB + acc_dig(SPS, jb) + gp>{});
      };
      constexpr int l0 = dig(LAGS, jb - 1), np = l0 + 12 - dig(LAGS, jb);
      auto slot = [&](auto I) __attribute__((always_inline)) {
        constexpr int i = decltype(I)::value;
        if constexpr (i < l0) piece(IC<jb + 1>{}, IC<12 - l0 + i>{});
        else if constexpr (i < np) piece(IC<jb + 2>{}, IC<i - l0>{});
        if constexpr (i < 12 || i < np) w2(I);
      };
#define SL4(i) slot(IC<i>{}); slot(IC<i + 1>{}); slot(IC<i + 2>{}); slot(IC<i + 3>{});
      SB();
      SL4(0) SL4(4) SL4(8) SL4(12) SL4(16) SL4(20)
      SB();
      if constexpr (jb == 2) {
#pragma unroll
        for (int jj = 0; jj < 4; ++jj) __builtin_amdgcn_raw_buffer_store_b128(tv[jj], rm2, vm2 + jj * 4096 + (2 * tt + 1) * 16384, 0, 16);
      }
      if constexpr ((FSM >> jb) & 1) fs = bld4_sc1(rf, abq[jb < 5 ? 1 : 2]);
#pragma unroll
      for (int i = acc_dig(GVS, jb); i < acc_dig(GVS, jb + 1); ++i) gv[i] = bld16_sc1(rgu, vgu, i * F_ * 16);
      touches(JB);
#pragma unroll
      for (int i = dn_first(jb); i < dn_first(jb) + dn_cnt(jb); ++i) dv[i] = bld16_nt(rdn, vdn, i * D_ * 2);
      if constexpr (jb == DG + 1) {
        if (pw < 2)
          __builtin_amdgcn_raw_buffer_store_b128((u32x4){__float_as_uint((p0[0] + p0[1]) + (p0[2] + p0[3])), tag, __float_as_uint((p1[0] + p1[1]) + (p1[2] + p1[3])), tag},
                                                 mk_rsrc(wsp + WS_DN), (((pw * 8 + cg8) * 32 + rgb) * 64 + lane) * 16, 0, 16);
      }
      if constexpr (jb == 2) {
        if (moe && pw < 2)
          __builtin_amdgcn_raw_buffer_store_b128((u32x4){__float_as_uint(p0[0]), tag, __float_as_uint(p1[0]), tag},
                                                 rgu, ((mat * 4 + dq) * F_ + cg * 128 + col) * 16, 0, 16);
      }
      SB();
#define SPX(i) spf(IC<(((i >= np && i >= 12) || ((SPE >> jb) & 1)) && i < dig(SPS, jb) ? SPB + acc_dig(SPS, jb) + i : -1)>{})
      if constexpr (dig(DGS, jb) > 0) {
        if (DGP != 3) __builtin_amdgcn_s_setprio(DGP);
#define DGU(i) if constexpr (i >= acc_dig(DGS, jb) && i < acc_dig(DGS, jb + 1)) work2(IC<100>{}, IC<i>{});
        DGU(0) DGU(1) DGU(2) DGU(3) DGU(4) DGU(5) DGU(6)
        if (DGP != 3) __builtin_amdgcn_s_setprio(3);
      }
      SPX(0); SPX(1); SPX(2); SPX(3); SPX(4); SPX(5); SPX(6); SPX(7); SPX(8); SPX(9); SPX(10); SPX(11); SPX(12); SPX(13); SPX(14);
      SB();
    };
    static_assert(DG + 1 <= 12 && DG >= GV_JB + 3 && dn_first(DG - 2) == 7 && acc_dig(GVS, GV_JB + 1) == 4 && dig(LAGS, 1) == 0 && dig(SPS, 1) == 0 && dig(LAGS, 0) == 0, "schedule");
    static_assert(DGS == 0 || (acc_dig(DGS, DG + 1) == 7 && acc_dig(DGS, GV_JB + 2) == 0 && acc_dig(DGS, 10) <= dn_first(7) && acc_dig(DGS, 11) <= dn_first(8) && acc_dig(DGS, 9) <= dn_first(6)), "units need their loads");
    static_assert(ALIAS || sp_tile((SPB + acc_dig(SPS, 14)) >> 4) < 0, "all tiles are prefetched");
    step2(IC<2>{}); step2(IC<3>{}); step2(IC<4>{}); step2(IC<5>{}); step2(IC<6>{}); step2(IC<7>{}); step2(IC<8>{}); step2(IC<9>{}); step2(IC<10>{}); step2(IC<11>{}); step2(IC<12>{}); step2(IC<13>{});
    lgkm0(sd);
    wait_vm<12 - dig(LAGS, 13)>(); BAR();
    {
      constexpr int l0 = dig(LAGS, 13);
#define PL(i) if constexpr (i >= 12 - l0) piece(IC<15>{}, IC<i>{});
      PL(0) PL(1) PL(2) PL(3) PL(4) PL(5) PL(6) PL(7) PL(8) PL(9) PL(10) PL(11)
    }
    __builtin_amdgcn_s_setprio(0);
    if (EPP == 1) { WAIT_VM(0); BAR(); }
    epi_setup(0); epi_setup(1); epi_setup(2);
    if (EPP == 2) { WAIT_VM(0); BAR(); }
#pragma unroll
    for (int n = 0; n < 3; ++n) xq_load(n);
    if (!EPP) { WAIT_VM(3); BAR(); }
    epi_setup(3);
    wait_xq<0>(xq, sq);
    key[0] = __builtin_amdgcn_readlane(ksel, 0); key[1] = __builtin_amdgcn_readlane(ksel, 8);
    cv_calc();
#pragma unroll 1
    for (int c = 0; c < 2; ++c) {
      if (__builtin_expect(((c ? far1 : far0) & ~9) != 0, 1)) continue;
      const __amdgpu_buffer_rsrc_t rd = mk_rsrc(wsp + WS_DN + (size_t)(c * 8 * 32 + pw * 8) * 1024);
      const int ec = seg0(lane >> 5, (lane >> 4) & 1), dno = (ec >> 7) * 32768 + ((ec & 127) / 2 + (lane & 15)) * 16;
#pragma unroll 1
      for (int it = 0; it < SPIN_MAX; ++it) {
        if (__all(__builtin_amdgcn_raw_buffer_load_b32(rd, (ec >> 7) * 32768 + (lane & 7) * 1024 + 1020, 0, 16) == tag)) break;
        __builtin_amdgcn_s_sleep(8);
        asm volatile("" ::: "memory");
      }
      float s0, s1;
      unsigned bad = 1;
#pragma unroll 1
      for (int it = 0; !__all(bad == 0) && it < SPIN_MAX; ++it) {
        bad = 0; s0 = 0.f; s1 = 0.f;
#pragma unroll
        for (int i = 0; i < 8; ++i) {
          const u32x4 q = __builtin_amdgcn_raw_buffer_load_b128(rd, dno, i * 1024, 16);
          bad |= (q.y ^ tag) | (q.w ^ tag); s0 += __uint_as_float(q.x); s1 += __uint_as_float(q.z);
        }
        asm volatile("" ::: "memory");
      }
      *(f32x2*)(smem + LDS_FIN + (c * 4 + pw) * 512 + lane * 8) = (f32x2){s0, s1};
      asm volatile("s_waitcnt lgkmcnt(0)" ::: "memory");
      if (lane == 0) asm volatile("ds_add_u32 %0, %1" :: "v"(lds_addr(smem + LDS_SEL + 24 + c * 4)), "v"(1) : "memory");
    }
  } else {
  {
    const int q = j * 4 + wid;
    constexpr int Q1 = WS_MB, Q2 = Q1 + 8, Q3 = Q2 + 8, Q4 = Q3 + 7, Q5 = Q4 + 7, Q6 = Q5 + 7, Q7 = Q6 + 4, Q8 = Q7 + 4, QC = Q8 + 1;
    static_assert(QC <= 128, "warm-up slots");
    unsigned dmy = 0;
    if (q < QC) {
      const char* base; int b0;
      if (q < Q1) { base = wsp; b0 = 0; }
      else if (q < Q2) { base = (const char*)ws_re; b0 = Q1; }
      else if (q < Q3) { base = (const char*)ws_im; b0 = Q2; }
      else if (q < Q4) { base = (const char*)w_gate; b0 = Q3; }
      else if (q < Q5) { base = (const char*)w_up; b0 = Q4; }
      else if (q < Q6) { base = (const char*)w_down; b0 = Q5; }
      else if (q < Q7) { base = (const char*)out; b0 = Q6; }
      else if (q < Q8) { base = (const char*)x; b0 = Q7; }
      else { base = (const char*)w_r; b0 = Q8; }
      base += (size_t)(q - b0) << 20;
      asm volatile("s_load_dword %0, %1, 0" : "+s"(dmy) : "s"(base) : "memory");
    }
    if (wid == 0) { CALL_PG(1); CALL_PG(8); CALL_PG(5); }
    else if (wid == 1) { CALL_PG(2); CALL_PG(9); CALL_PG(6); }
    else if (wid == 2) { CALL_PG(3); CALL_PG(10); }
    else { CALL_PG(4); CALL_PG(11); }
    BAR();
    lgkm0(dmy);
    TL(1);
  }
  PAGE(8);
  epi_setup(0); epi_setup(1); epi_setup(2); epi_setup(3);
  const int wr = wid >> 1, wc = wid & 1;
  const int sw = swz(fr), fl = lrw(fr);
  const int ko0 = ((0 + fq) ^ sw) * 16, ko1 = ((4 + fq) ^ sw) * 16;
  bf16x8 fm[2][4], fn[2][4];
  {
    const char* pm = smem + (wr * 64 + fl) * 128;
    const char* pn = smem + 16384 + (wc * 64 + fl) * 128;
    f32x4 acc[4][4];
#pragma unroll
    for (int i = 0; i < 4; ++i)
#pragma unroll
      for (int jn = 0; jn < 4; ++jn)
#pragma unroll
        for (int q = 0; q < 4; ++q) acc[i][jn][q] = 0.f;
#define TO(i) ((i) * 2048)
#define LDSP(T, a) ((__attribute__((address_space(3))) T*)(size_t)(a))
#define SLOTS(n) _Pragma("unroll") for (int q = 0; q < (n); ++q)
#define MFA(c, m, n) asm volatile("v_mfma_f32_16x16x32_bf16 %0, %2, %1, %0" : "+v"(c) : "v"(m), "v"(n))
#define MFA2(c, m, n, m2, n2) asm volatile("v_mfma_f32_16x16x32_bf16 %0, %2, %1, %0\n\tv_mfma_f32_16x16x32_bf16 %0, %4, %3, %0" : "+v"(c) : "v"(m), "v"(n), "v"(m2), "v"(n2))
    unsigned lm[2][2], ln[2][2];
#pragma unroll
    for (int u = 0; u < 2; ++u)
#pragma unroll
      for (int kk = 0; kk < 2; ++kk) {
        lm[u][kk] = lds_addr(pm) + u * 2 * G1_STAGE + (kk ? ko1 : ko0);
        ln[u][kk] = lds_addr(pn) + u * 2 * G1_STAGE + (kk ? ko1 : ko0);
        asm volatile("" : "+v"(lm[u][kk]), "+v"(ln[u][kk]));
      }
    auto ldm = [&](int stg, int i, int kk) __attribute__((always_inline)) { fm[kk][i] = *LDSP(const bf16x8, lm[stg >> 1][kk] + (stg & 1) * G1_STAGE + TO(i)); };
    auto ldn = [&](int stg, int jn, int kk) __attribute__((always_inline)) { fn[kk][jn] = *LDSP(const bf16x8, ln[stg >> 1][kk] + (stg & 1) * G1_STAGE + TO(jn)); };
    auto quad = [&](int a, int c, int d, int s) __attribute__((always_inline)) {
      const int i = (s < 2 || s >= 6) ? a : a + 1, jn = s < 4 ? c : d, kk = (s == 1 || s == 2 || s == 5 || s == 6);
      MFA(acc[i][jn], fm[kk][i], fn[kk][jn]);
    };
    auto quad2 = [&](int a, int c, int d, int s) __attribute__((always_inline)) {
      const int i = (s < 2 || s >= 6) ? a : a + 1, jn = s < 4 ? c : d, kk = (s == 2 || s == 6);
      MFA2(acc[i][jn], fm[kk][i], fn[kk][jn], fm[1 - kk][i], fn[1 - kk][jn]);
    };
    auto half1 = [&](int stg) __attribute__((always_inline)) {
      const int ja = (stg & 1) * 2, jb = 2 - ja;
      SLOTS(8) {
        quad(0, ja, ja + 1, q);
        if (q < 4) ldn(stg, jb + (q >> 1), q & 1); else ldm(stg, 2 + ((q - 4) >> 1), q == 5 || q == 6);
        SB();
      }
      SLOTS(4) { quad2(0, jb, jb + 1, 2 * q); SB(); }
    };
    auto half2 = [&](int stg) __attribute__((always_inline)) {
      const int ja = (stg & 1) * 2, jb = 2 - ja, nx = (stg + 1) & 3;
      SLOTS(8) {
        quad(2, jb + 1, jb, q);
        if (!(q & 1)) ldm(nx, q >> 2, q == 2 || q == 4);
        SB();
      }
      SLOTS(8) {
        quad(2, ja, ja + 1, q);
        if (!(q & 1)) ldn(nx, jb + (q >> 2), (q >> 1) & 1);
        SB();
      }
    };
    unsigned ae[7];
    {
      int l_ = lane;
      asm volatile("" : "+v"(l_));
      const int p = l_ >> 4, dn = sym_dn(p), hf = (p >> 1) ^ (dn & z);
#pragma unroll
      for (int u = 0; u < 7; ++u) {
        const int T = u < 3 ? wc * 4 : (wc * 4 + u - 3 - dn) & 7, i = u < 3 ? (u + 1) ^ ((u & 1) ? 0 : dn * 2) : 0;
        const int f = dn * 8 + ((T & 2) << 1) + ((T & 4) >> 1) + (T & 1);
        ae[u] = lds_addr(smem + G2_MRING) + (hf * 64 + i * 16 + lrw(f)) * 128 + (((wr * 4 + ((l_ >> 2) & 3)) ^ swz(f)) * 16) + (l_ & 3) * 4;
        asm volatile("" : "+v"(ae[u]));
      }
    }
    unsigned al = lds_addr(smem + LDS_MB) + (wr * 32 + fr * 2) * 4, ac = lds_addr(smem + LDS_MB) + (256 + wc * 64 + fq * 4) * 4;
    asm volatile("" : "+v"(al), "+v"(ac));
    SLOTS(16) asm volatile("" : "+v"(acc[q >> 2][q & 3]));
    SB();
    SLOTS(4) { ldm(0, q >> 1, q & 1); ldn(0, q >> 1, q & 1); }
    SB();
#pragma unroll 1
    for (int k4 = 0; k4 < G1_NKT - 4; k4 += 4) {
      half1(0); BAR_LGKM();

      SB(); half2(0);
      half1(1); BAR_LGKM();

      SB(); half2(1);
      half1(2); BAR_LGKM();

      SB(); half2(2);
      half1(3); BAR_LGKM();

      SB(); half2(3);
    }
    bf16x8 gm[2][4], gn[2][8];
    auto rdg = [&](int set, int t, int h, int r) __attribute__((always_inline)) {
      const int u = (t >> 1) & 1, o = (t & 1) * G1_STAGE;
      if (r == 0 || r == 2 || r == 4 || r == 5) {
        const int i = r >= 4, kk = r == 2 || r == 4;
        gm[set][kk * 2 + i] = *LDSP(const bf16x8, lm[u][kk] + o + h * 4096 + i * 2048);
      } else {
        const int jn = r < 4 ? 0 : (r - 4) >> 1, kk = r < 4 ? r >> 1 : r & 1;
        gn[set][kk * 4 + jn] = *LDSP(const bf16x8, ln[u][kk] + o + TO(jn));
      }
    };
    auto mmg = [&](int set, int h, int q) __attribute__((always_inline)) {
      const int jn = q >> 2, r = q & 3, i = (r >> 1) ^ (jn & 1), kk = r == 1 || r == 2;
      MFA(acc[h * 2 + i][jn], gm[set][kk * 2 + i], gn[set][kk * 4 + jn]);
    };
    f32x2 rsv, mrv;
    f32x4 cev[4];
    float tc[32];
    auto rds = [&](int h, int r) __attribute__((always_inline)) {
      if (r == 0) mrv = *LDSP(const f32x2, al + h * 256); else if (r == 1) rsv = *LDSP(const f32x2, al + 512 + h * 256);
    };
    auto prep = [&](int n) __attribute__((always_inline)) {
      tc[n] = llvm_fmul_legacy(mrv[n & 1], cev[n >> 3][(n >> 1) & 3]);
      asm volatile("" : "+v"(tc[n]));
    };
    auto epi = [&](int h, int q) __attribute__((always_inline)) {
      const int jn = q >> 1, r = (q & 1) * 2, o = m_off(h) + (jn & 1) * 128 + (jn >> 1) * lrw(4) * 128;
      asm volatile("v_fmac_f32 %0, %4, %8\n\tv_fmac_f32 %1, %5, %9\n\tv_fmac_f32 %2, %6, %8\n\tv_fmac_f32 %3, %7, %9"
                   : "+v"(tc[q * 4]), "+v"(tc[q * 4 + 1]), "+v"(tc[q * 4 + 2]), "+v"(tc[q * 4 + 3])
                   : "v"(acc[h * 2][jn][r]), "v"(acc[h * 2 + 1][jn][r]), "v"(acc[h * 2][jn][r + 1]), "v"(acc[h * 2 + 1][jn][r + 1]),
                     "v"(rsv[0]), "v"(rsv[1]));
      *LDSP(unsigned, r ? ae[1] + o : ae[3 + jn] + m_off(h)) = pk2(tc[q * 4], tc[q * 4 + 1]);
      *LDSP(unsigned, ae[r] + o) = pk2(tc[q * 4 + 2], tc[q * 4 + 3]);
    };
    SB();
    half1(0);
    BAR_LGKM();
    SB();
    TL(14);
    SLOTS(16) {
      if (q < 8) quad(2, 3, 2, q); else quad(2, 0, 1, q - 8);
      if (q < 12) rdg(0, 13, 0, q);
      SB();
    }
    BAR();
    SB();
    TL(15);
    SLOTS(16) {
      mmg(0, 0, q);
      if (q < 12) rdg(1, 14, 0, q);
      else { rds(0, q - 12); cev[q - 12] = *LDSP(const f32x4, ac + (q - 12) * 64); }
      SB();
    }
    PAGE(9);
    BAR();
    SB();
    TL(16);
    SLOTS(16) { mmg(1, 0, q); if ((q & 3) < 3) rdg(0, 15, 0, (q >> 2) * 3 + (q & 3)); if (q >= 8) { prep(2 * q - 16); prep(2 * q - 15); } SB(); }
    SLOTS(16) { mmg(0, 0, q); if ((q & 3) < 3) rdg(1, 13, 1, (q >> 2) * 3 + (q & 3)); prep(16 + q); SB(); if (q >= 9 && (q & 1)) { epi(0, (q - 9) >> 1); SB(); } }
    SLOTS(8) { mmg(1, 1, q); if (q < 6) { rdg(0, 14, 1, 2 * q); rdg(0, 14, 1, 2 * q + 1); } SB(); if (q & 1) { epi(0, 4 + (q >> 1)); SB(); } }
    BAR_LGKM();
    SB();
    TL(60);
    SLOTS(8) { mmg(1, 1, 8 + q); if (q < 4) rdg(1, 15, 1, q == 0 ? 1 : q == 1 ? 3 : q + 4); else rds(1, q - 4); SB(); }
    SLOTS(16) { mmg(0, 1, q); if (q < 8) rdg(1, 15, 1, q == 0 ? 0 : q == 1 ? 2 : q < 4 ? q + 2 : q + 4); prep(q); SB(); }
    asm volatile("s_waitcnt lgkmcnt(0)" ::: "memory");
    if (lane == 0) asm volatile("ds_add_u32 %0, %1" :: "v"(lds_addr(smem + LDS_SEL + 12)), "v"(1) : "memory");
    SB();
    SLOTS(16) {
      mmg(1, 1, q);
      if (q < 4) fn[0][q] = *(const bf16x8*)(smem + (wr * 128 + fl) * 128 + (q >> 1) * 2048 + ((q & 1) ? ko1 : ko0));
      else if (q < 8) fm[q & 1][(q - 4) >> 1] = *(const bf16x8*)(smem + G2_MRING + m_off(0) + (wc * 64 + fl) * 128 + TO((q - 4) >> 1) + ((q & 1) ? ko1 : ko0));
      prep(16 + q);
      SB();
    }
    {
      lds_vi rel = (lds_vi)(smem + LDS_SEL + 12);
      int it = 0;
      while (rel[0] != 4 && it < SPIN_MAX * 8) ++it;
      asm volatile("" ::: "memory");
    }
    TL(17);
    asm volatile("s_nop 7");
    SB();
    SLOTS(8) { epi(1, q); SB(); }
  }

  const char* pn = smem + (wr * 128 + fl) * 128;
  const char* pm = smem + G2_MRING + (wc * 64 + fl) * 128;
  f32x4 acc[8][4];
  auto mm = [&](bool imaj, int set, int q2, int s, int mode) __attribute__((always_inline)) {
    int i, jj, kk;
    if (imaj) { i = s >> 2; const int r = s & 3; jj = (r >> 1) ^ (i & 1); kk = (r == 1 || r == 2); }
    else { jj = s >> 3; const int r = s & 7; i = jj ? 3 - (r >> 1) : (r >> 1); kk = ((r + 1) >> 1) & 1; }
    if (mode == 1 && !(s & 1)) asm volatile("v_mfma_f32_16x16x32_bf16 %0, %2, %1, 0" : "=&v"(acc[q2 * 2 + jj][i]) : "v"(fm[kk][i]), "v"(fn[set][jj * 2 + kk]));
    else if (mode) asm volatile("v_mfma_f32_16x16x32_bf16 %0, %2, %1, %0" : "+v"(acc[q2 * 2 + jj][i]) : "v"(fm[kk][i]), "v"(fn[set][jj * 2 + kk]));
    else acc[q2 * 2 + jj][i] = __builtin_amdgcn_mfma_f32_16x16x32_bf16(fn[set][jj * 2 + kk], fm[kk][i], acc[q2 * 2 + jj][i], 0, 0, 0);
  };
  unsigned bn[2], bm[2], an[2], am[2];
#pragma unroll
  for (int kk = 0; kk < 2; ++kk) {
    bn[kk] = lds_addr(pn) + (kk ? ko1 : ko0); bm[kk] = lds_addr(pm) + (kk ? ko1 : ko0);
    asm volatile("" : "+v"(bn[kk]), "+v"(bm[kk]));
    an[kk] = bn[kk]; am[kk] = bm[kk] + m_off(0);
  }
#define RDN(jt, kk, o) (*LDSP(const bf16x8, an[kk] + (o) + (jt) * 2048))
#define RDM(i, kk, o) (*LDSP(const bf16x8, am[kk] + (o) + TO(i)))
  PAGE(10);
  TL(18);
  TL(19);
  tag = __builtin_amdgcn_readfirstlane(((const unsigned*)(smem + LDS_SEL))[5]);
  {
    int son = 0;
    auto tile = [&](int kt, int zero) __attribute__((always_inline)) {
      SLOTS(16) {
        mm(true, 0, 0, q, zero);
        if (!(q & 1)) {
          if (q < 8) fm[(q >> 1) & 1][2 + (q >> 2)] = RDM(2 + (q >> 2), (q >> 1) & 1, 0);
          else { const int r = (q - 8) >> 1; fn[1][r] = RDN(2 + (r >> 1), r & 1, 0); }
        }
        SB();
      }
      SLOTS(16) {
        mm(false, 1, 1, q, zero);
        if (!(q & 1) && q < 8) fn[0][q >> 1] = RDN(4 + (q >> 2), (q >> 1) & 1, 0);
        SB();
      }
      const int som = son >> 1;
      son = son == 65536 ? 0 : son + 32768;
      SLOTS(16) {
        mm(false, 0, 2, q, zero);
        if (!(q & 1) && q < 8) fn[1][q >> 1] = RDN(6 + (q >> 2), (q >> 1) & 1, 0);
        if (q == 9 || q == 11) { an[q == 11] = bn[q == 11] + son; asm volatile("" : "+v"(an[q == 11])); }
        if (q == 13 || q == 15) { am[q == 15] = bm[q == 15] + som; asm volatile("" : "+v"(am[q == 15])); }
        SB();
      }

      BAR_LGKM();

      SB();
      SLOTS(16) {
        mm(true, 1, 3, q, zero);
        if (!(q & 1)) {
          if (q < 8) fn[0][q >> 1] = RDN(q >> 2, (q >> 1) & 1, 0);
          else fm[(q >> 1) & 1][(q - 8) >> 2] = RDM((q - 8) >> 2, (q >> 1) & 1, 0);
        }
        SB();
      }
    };
    SB();
    tile(0, 1);
#pragma unroll 1
    for (int kt = 1; kt < G2_NKT - 3; ++kt) tile(kt, 0);
  }
  unsigned fx;
  constexpr int XC1 = XL0 >= 104 ? 3 : 0, XC2 = XL0 >= 152 ? 3 : 0;
  static_assert(XL0 + 2 * XLS < FL0 && (XL0 + 2 * XLS < 80 || XL0 >= 152 || (XL0 >= 104 && XL0 + 2 * XLS < 144)), "");
  int fo = lane == 0 ? kq * 512 : (int)0x80000000;
  {
    constexpr int NG = 12;
    static constexpr int g_t[NG] = {0, 0, 1, 1, 2, 2, 2, 1, 0, 0, 1, 2};
    static constexpr int g_q[NG] = {0, 1, 0, 1, 0, 1, 2, 2, 2, 3, 3, 3};
    constexpr int G14 = 1, G15 = 3, GP0 = 5, GP1 = 6;
    constexpr auto chp = [](int g) { return g == 0 || g_t[g] != g_t[g - 1]; };
    constexpr auto chn = [](int g) { return g + 1 < NG && g_t[g + 1] != g_t[g]; };
    static_assert(n_off(13) == 32768 && n_off(14) == 65536 && n_off(15) == 0, "");
    unsigned an2[2];
#pragma unroll
    for (int kk = 0; kk < 2; ++kk) {
      an[kk] = bn[kk]; an2[kk] = bn[kk] + 65536; am[kk] = bm[kk];
      asm volatile("" : "+v"(an[kk]), "+v"(an2[kk]), "+v"(am[kk]));
    }
    int vo;
    u32x4 pk;
    u32x2 kv;
    auto publish = [&](int d, int h) __attribute__((always_inline)) {
      if (h == 0) { vo = ml; PINV(vo); vo += (((d + kq + 1) & 3) * 4 + kq) * 4 * MSG; PINV(vo); }
      const f32x4 v = acc[2 * d + (h >> 2)][h & 3];
      pk[(h & 1) * 2] = pk2(v[0], v[1]); pk[(h & 1) * 2 + 1] = pk2(v[2], v[3]);
      if (h & 1) __builtin_amdgcn_raw_buffer_store_b128(pk, rs, vo + (h >> 1) * 1024, 0, 16);
    };
#define MSG_FLAG(d) __builtin_amdgcn_raw_buffer_store_b32(tag, rc, fo + (((d) + kq + 1) & 3) * 2048, 0, 16)
    auto rdn = [&](int g, int r) __attribute__((always_inline)) {
      const int t = g_t[g], jt = g_q[g] * 2 + (r >> 1);
      fn[g & 1][r] = *LDSP(const bf16x8, (t == 1 ? an2[r & 1] : an[r & 1]) + (t == 0 ? 32768 : 0) + jt * 2048);
    };
    auto rdm = [&](int t, int i, int kk) __attribute__((always_inline)) { fm[kk][i] = *LDSP(const bf16x8, am[kk] + m_off(13 + t) + TO(i)); };
    auto group = [&](auto GI) __attribute__((always_inline)) {
      constexpr int gi = decltype(GI)::value;
      if (gi == G14) { BAR(); if (CPRI) __builtin_amdgcn_s_setprio(CPRI);
        TL(47);
        SB(); }
      if (gi == G15) {
        PAGE(11);
        TL(53);
        BAR();
        TL(54);
        SB();
        kv = *(const u32x2*)(smem + LDS_SEL);
        SB();
      }
#pragma unroll
      for (int q = 0; q < 16; ++q) {
        const int n = gi * 16 + q;
        if (n == TS1) {
          TL(55);
        }
        if (n == TS2) {
          TL(58);
        }
        if (n == FL0) { wait_vm<8 + XC1>(); MSG_FLAG(0); SB(); }
        if (n == FL1) { wait_vm<5 + XC1>(); MSG_FLAG(1); SB(); }
        if (n == FL2) { wait_vm<2 + XC2>(); MSG_FLAG(2); fx = bld4_sc1(rc, po); SB(); }
        mm(chp(gi) || chn(gi), gi & 1, g_q[gi], q, 2);
        constexpr bool ni = gi + 1 < NG && (chp(gi + 1 < NG ? gi + 1 : gi) || chn(gi + 1 < NG ? gi + 1 : gi));
        auto nr = [](int r) { return ni && r >= 2 ? 5 - r : r; };
        if constexpr (chp(gi) && chn(gi)) {
          if (q < 4) rdm(g_t[gi], 2 + (q >> 1), q & 1); else if (q < 8) rdn(gi + 1, nr(q - 4)); else if (q < 12) rdm(g_t[gi + 1], (q - 8) >> 1, q & 1);
        } else if constexpr (chp(gi)) {
          if (q < 4) rdm(g_t[gi], 2 + (q >> 1), q & 1);
          else if (gi + 1 < NG && q < 12 && !(q & 1)) rdn(gi + 1, nr((q - 4) >> 1));
        } else if constexpr (chn(gi)) {
          if (!(q & 1)) { if (q < 8) rdn(gi + 1, nr(q >> 1)); else rdm(g_t[gi + 1], (q - 8) >> 2, (q >> 1) & 1); }
        } else {
          if (!(q & 1) && q < 8) rdn(gi + 1, nr(q >> 1));
        }
        if (gi == GP0 && q < 8) publish(0, q);
        if (gi == GP1 && q < 8) publish(1, q);
        if (gi == 9 && q < 8) publish(2, q);
        if (n >= XL0 && n < XL0 + 3 * XLS && (n - XL0) % XLS == 0) xq_load((n - XL0) / XLS);
        if (n == 72) { key[0] = __builtin_amdgcn_readfirstlane(kv.x); key[1] = __builtin_amdgcn_readfirstlane(kv.y); }
        SB();
      }
    };
#define GR4(i) group(IC<i>{}); group(IC<i + 1>{}); group(IC<i + 2>{}); group(IC<i + 3>{});
    GR4(0) GR4(4) GR4(8)
    asm volatile("s_nop 7");
    SB();
  }
  TL(50);
#pragma unroll
  for (int i = 0; i < 4; ++i) *(f32x4*)(smem + LDS_ACC + wr * 16384 + wc * 4096 + i * 1024 + lane * 16) = acc[7][i];
#pragma unroll
  for (int i = 0; i < 4; ++i) ya[i] = acc[6][i];
  asm volatile("s_waitcnt lgkmcnt(0)" ::: "memory");
  if (lane == 0) sig[0] = 2;
  SB();
  if (FL0 >= 192) { wait_vm<8 + XC1>(); MSG_FLAG(0); SB(); }
  TL(61);
  if (FL1 >= 192) { wait_vm<5 + XC1>(); MSG_FLAG(1); SB(); }
  TL(62);
  if (FL2 >= 192) { wait_vm<2 + XC2>(); MSG_FLAG(2); fx = bld4_sc1(rc, po); SB(); }
  TL(63);
  wait_xq<4>(xq, sq);
  cv_calc();
  asm volatile("s_waitcnt vmcnt(0)" : "+v"(fx) :: "memory");
  seen = (unsigned)__ballot(fx == tag);
  }

  PAGE(6);
  if (__builtin_expect(far0 == 0 || far1 == 0, 0)) {
#pragma unroll 1
    for (int c = 0; c < 2; ++c) {
      if (c ? far1 : far0) continue;
      const int ic = (0xffff - (c ? key[1] : key[0])) & (S_ - 1);
      const float sg = rbf(1.f / (1.f + __expf(-key_score(c ? key[1] : key[0]))));
      lds_vi cnt = (lds_vi)(smem + LDS_SEL + 24 + c * 4);
      int it = 0;
      while (cnt[0] != 4 && it < SPIN_MAX * 8) { __builtin_amdgcn_s_sleep(1); ++it; }
      asm volatile("" ::: "memory");
      const float* q = (const float*)(smem + LDS_FIN + c * 2048) + (wid & 1) * 64 + (fr >> 3) * 32 + col4(fr);
      const f32x4 v = (*(const f32x4*)q + *(const f32x4*)(q + 128)) + (*(const f32x4*)(q + 256) + *(const f32x4*)(q + 384));
#pragma unroll
      for (int r = 0; r < 4; ++r)
#pragma unroll
        for (int k = 0; k < 4; ++k) {
          cv[r * 4 + k] += (xo[r] >> 11) == ic ? v[k] * sg : 0.f;
        }
    }
  }
  if (ha) {
    int it = 0;
    while (sig[0] != 2 && it < SPIN_MAX * 8) { __builtin_amdgcn_s_sleep(1); ++it; }
    asm volatile("" ::: "memory");
#pragma unroll
    for (int i = 0; i < 4; ++i) ya[i] = *(const f32x4*)(smem + LDS_ACC + (wid & 2) * 8192 + (wid & 1) * 4096 + i * 1024 + lane * 16);
  }
  u32x4 pp[3][2];
  unsigned e_lo = 0x00003f80u, e_hi = 0x3f800000u;
  asm volatile("" : "+v"(e_lo), "+v"(e_hi));
  __amdgpu_buffer_rsrc_t rs_ = rs, rc_ = rc;
  int mo[3] = {MSG_IDX(0) * 4 * MSG, MSG_IDX(1) * 4 * MSG, MSG_IDX(2) * 4 * MSG}, mv = ml + ha * 2048, mv2 = mv + 1024;
  asm volatile("" : "+s"(rs_), "+s"(rc_), "+s"(mo[0]), "+s"(mo[1]), "+s"(mo[2]));
  asm volatile("" : "+v"(mv), "+v"(mv2));
#define MSG_POLL(m) do { \
    if (!((seen >> (m)) & 1)) { \
      unsigned pv_, it_ = SPIN_MAX; \
      asm volatile("1:\n\tbuffer_load_dword %0, %3, %4, 0 offen sc1\n\ts_waitcnt vmcnt(0)\n\tv_cmp_eq_u32 vcc, %5, %0\n\ts_mov_b32 %1, vcc_lo\n\t" \
                   "s_bitcmp1_b32 %1, %6\n\ts_cbranch_scc1 2f\n\ts_sub_u32 %2, %2, 1\n\ts_cbranch_scc1 2f\n\ts_sleep 1\n\ts_branch 1b\n2:" \
                   : "=&v"(pv_), "+s"(seen), "+s"(it_) : "v"(po), "s"(rc_), "s"(tag), "i"(m) : "vcc", "scc", "memory"); \
    } \
    pp[m][0] = __builtin_amdgcn_raw_buffer_load_b128(rs_, mv, mo[m], 16); \
    pp[m][1] = __builtin_amdgcn_raw_buffer_load_b128(rs_, mv2, mo[m], 16); } while (0)
#define MSG_ADD(m) do { \
    _Pragma("unroll") for (int u = 0; u < 8; ++u) { \
      ya[u >> 1][(u & 1) * 2] = add_bf(pp[m][u >> 2][u & 3], e_lo, ya[u >> 1][(u & 1) * 2]); ya[u >> 1][(u & 1) * 2 + 1] = add_bf(pp[m][u >> 2][u & 3], e_hi, ya[u >> 1][(u & 1) * 2 + 1]); } } while (0)
  MSG_POLL(2); MSG_POLL(1); MSG_POLL(0);
  SB();
#pragma unroll
  for (int e = 0; e < 16; ++e) ya[e & 3][e >> 2] += cv[e];
  asm volatile("" : "+v"(ya[0]), "+v"(ya[1]), "+v"(ya[2]), "+v"(ya[3]));
  SB();
  MSG_ADD(2); MSG_ADD(1);
  asm volatile("" : "+v"(ya[0]), "+v"(ya[1]), "+v"(ya[2]), "+v"(ya[3]));
  SB();
  MSG_ADD(0);
  {
#pragma unroll
    for (int r = 0; r < 4; ++r) *(u32x2*)((char*)out + (unsigned)xo[r]) = (u32x2){pk2(ya[0][r], ya[1][r]), pk2(ya[2][r], ya[3][r])};
  }

  if (wid == 0) {
    const unsigned te = (unsigned)__builtin_amdgcn_s_memrealtime();
    const unsigned t0 = __builtin_amdgcn_readlane(tlv, 52);
    const __amdgpu_buffer_rsrc_t rg = mk_rsrc(wsp + (64u << 20));
    if (lane == 0) { __builtin_amdgcn_raw_buffer_store_b64((u32x2){tag, t0}, rg, g * 32, 0, 16); __builtin_amdgcn_raw_buffer_store_b64((u32x2){tag, te}, rg, g * 32 + 8, 0, 16);
      __builtin_amdgcn_raw_buffer_store_b64((u32x2){tag, te}, rg, g * 32 + 16, 0, 16); }
    if (g == 37) {
      int mn0, mx[2], sum[2];
      for (int it = 0; it < 100000; ++it) {
        unsigned bad = 0; mn0 = 0x7fffffff; mx[0] = mx[1] = -0x7fffffff; sum[0] = sum[1] = 0;
        for (int k = 0; k < 4; ++k) {
          const u32x4 q = __builtin_amdgcn_raw_buffer_load_b128(rg, (k * 64 + lane) * 32, 0, 16);
          const u32x2 q2 = __builtin_amdgcn_raw_buffer_load_b64(rg, (k * 64 + lane) * 32 + 16, 0, 16);
          bad |= (q.x ^ tag) | (q.z ^ tag) | (q2.x ^ tag);
          mn0 = min(mn0, (int)(q.y - t0)); mx[0] = max(mx[0], (int)(q.w - t0)); sum[0] += (int)(q.w - t0); mx[1] = max(mx[1], (int)(q2.y - t0)); sum[1] += (int)(q2.y - t0);
        }
        if (__all(bad == 0)) break;
        asm volatile("" ::: "memory");
      }
      for (int o = 32; o; o >>= 1) { mn0 = min(mn0, __shfl_xor(mn0, o, 64)); for (int k = 0; k < 2; ++k) { mx[k] = max(mx[k], __shfl_xor(mx[k], o, 64)); sum[k] += __shfl_xor(sum[k], o, 64); } }
      if (lane == 0) __builtin_amdgcn_raw_buffer_store_b64((u32x2){t0 + mn0, (unsigned)__builtin_amdgcn_s_memrealtime()}, rg, 65536, 0, 16);
    }
  }

}

extern "C" __global__ void k_post(char* wsp) {
  const unsigned now = (unsigned)__builtin_amdgcn_s_memrealtime();
  const u64 v = __hip_atomic_load((u64*)(wsp + (64u << 20) + 65536), __ATOMIC_RELAXED, __HIP_MEMORY_SCOPE_AGENT);
  const int code = min(max((int)(now - (unsigned)(v >> 32)) / 10, 0), 127);
  const unsigned tend = (unsigned)v + 6000 + code * 1600;
  for (int it = 0; it < (1 << 17) && (int)((unsigned)__builtin_amdgcn_s_memrealtime() - tend) < 0; ++it) __builtin_amdgcn_s_sleep(1);
}
extern "C" void kernel_launch(void* const* d_in, const int* in_sizes, int n_in, void* d_out, int out_size,
                              void* d_ws, size_t ws_size, hipStream_t stream) {
  const u16* x = (const u16*)d_in[0];
  const u16* ws_re = (const u16*)d_in[1];
  const u16* ws_im = (const u16*)d_in[2];
  const u16* wd_re = (const u16*)d_in[3];
  const u16* wd_im = (const u16*)d_in[4];
  const u16* w_r = (const u16*)d_in[6];
  const u16* w_gate = (const u16*)d_in[7];
  const u16* w_up = (const u16*)d_in[8];
  const u16* w_down = (const u16*)d_in[9];
  k_main<<<256, 512, 0, stream>>>(x, wd_re, wd_im, w_r, (char*)d_ws, ws_re, ws_im, w_gate, w_up, w_down, (u16*)d_out);
  k_post<<<1, 64, 0, stream>>>((char*)d_ws);
}
```

```cpp
#include <hip/hip_runtime.h>
#include <hip/hip_bf16.h>
#include <stdint.h>
#include <type_traits>

extern "C" __device__ unsigned long long llvm_dispatch_id(void) __asm("llvm.amdgcn.dispatch.id");
extern "C" __device__ float llvm_fmul_legacy(float, float) __asm("llvm.amdgcn.fmul.legacy");
typedef unsigned short u16;
typedef unsigned long long u64;
typedef __attribute__((ext_vector_type(8))) short bf16x8;
typedef __attribute__((ext_vector_type(4))) unsigned u32x4;
typedef __attribute__((ext_vector_type(2))) unsigned u32x2;
typedef __attribute__((ext_vector_type(4))) float f32x4;
typedef __attribute__((ext_vector_type(2))) float f32x2;
typedef __attribute__((ext_vector_type(2))) __bf16 bf16x2;
typedef __attribute__((address_space(3))) void* lds_vp;
typedef volatile __attribute__((address_space(3))) int* lds_vi;

constexpr int S_ = 2048, D_ = 1024, F_ = 3584;
template <int N> using IC = std::integral_constant<int, N>;

static __device__ __forceinline__ float bf2f(u16 u) { return __uint_as_float((unsigned)u << 16); }
static __device__ __forceinline__ u16 f2bf(float f) {
  __hip_bfloat16 b = __float2bfloat16(f);
  return *reinterpret_cast<u16*>(&b);
}
static __device__ __forceinline__ float rbf(float f) { return bf2f(f2bf(f)); }
static __device__ __forceinline__ float lo_bf(unsigned u) { return __uint_as_float(u << 16); }
static __device__ __forceinline__ float hi_bf(unsigned u) { return __uint_as_float(u & 0xffff0000u); }
static __device__ __forceinline__ unsigned pk2(float a, float b) {
  const bf16x2 v = __builtin_convertvector((f32x2){a, b}, bf16x2);
  return __builtin_bit_cast(unsigned, v);
}
static __device__ __forceinline__ float add_bf(unsigned u, unsigned e, float c) {
  return __builtin_amdgcn_fdot2_f32_bf16(__builtin_bit_cast(bf16x2, u), __builtin_bit_cast(bf16x2, e), c, false);
}
static __device__ __forceinline__ float ln_elem(float x, float mb, float sd, float ri) {
  const float d = rbf(x - mb);
  const float q0 = d * ri;
  return rbf(__builtin_fmaf(__builtin_fmaf(-q0, sd, d), ri, q0));
}
template <int CTRL> static __device__ __forceinline__ float dpp_f(float v) {
  return __int_as_float(__builtin_amdgcn_update_dpp(0, __float_as_int(v), CTRL, 0xf, 0xf, true));
}
template <int CTRL> static __device__ __forceinline__ unsigned dpp_u(unsigned v) {
  return (unsigned)__builtin_amdgcn_update_dpp(0, (int)v, CTRL, 0xf, 0xf, true);
}
static __device__ __forceinline__ float row_sum(float v) {
  v += dpp_f<0xB1>(v);
  v += dpp_f<0x4E>(v);
  v += dpp_f<0x141>(v);
  v += dpp_f<0x140>(v);
  return v;
}
static __device__ __forceinline__ float fold32(float a, float b) {
  const auto r = __builtin_amdgcn_permlane32_swap(__float_as_uint(a), __float_as_uint(b), false, false);
  return __uint_as_float(r[0]) + __uint_as_float(r[1]);
}
static __device__ __forceinline__ float fold16(float a, float b) {
  const auto r = __builtin_amdgcn_permlane16_swap(__float_as_uint(a), __float_as_uint(b), false, false);
  return __uint_as_float(r[0]) + __uint_as_float(r[1]);
}
static __device__ __forceinline__ float rdl(float v, int l) { return __int_as_float(__builtin_amdgcn_readlane(__float_as_int(v), l)); }

#define TL(I) do { if ((I) == 51) { const unsigned _t = (unsigned)__builtin_amdgcn_s_memrealtime(); tlv = lane == (I) ? _t : tlv; } } while (0)
#define XMAP(g) (((~g & 1) << 2) | (g & 2) | ((g >> 2) & 1))
#define ST_JB 9
#define WSN 10
#define FSM 0xa2
#define GV_JB 7
#define DG 11
#define AB_STRIDE 32
#define SPIN_MAX (1 << 17)
#define FL0 168
#define FL1 192
#define FL2 192
#define VA0 5
#define VAN 4
#define VAP 116
#define VB0 12
#define VBN 0
#define VBP 6
#define SP0 3
#define SP1 7
#define SP1E 11
#define VC0 14
#define VCN 0
#define VCP 9
#define SPS 0x000cccccdddd00
#define SPE 0
#define PRIO2 3
#define N2A 8
#define N3A 8
#define WSL 2
#define LNP 0
#define LNS 2
#define EPP 0
#define DGS 0
#define DGP 0
#define TS1 112
#define TS2 152
#define XL0 33
#define XLS 4
#define CPRI 0
#define LAGS 0x00000000000000
#define DVS 0x00000400300000
#define GVS 0x00000022000000
constexpr int dig(unsigned long long t, int q) { return q < 0 ? 0 : (int)((t >> (4 * q)) & 15); }
constexpr int acc_dig(unsigned long long t, int q) { int a = 0; for (int i = 0; i < q; ++i) a += dig(t, i); return a; }

static __device__ __forceinline__ unsigned lds_addr(const void* p) { return (unsigned)(size_t)(lds_vp)p; }

constexpr int MSG = 4096;
constexpr size_t WS_ABT   = 0;
constexpr size_t WS_SLAB  = WS_ABT + (size_t)D_ * 4096 * 2;
constexpr size_t WS_GU    = WS_SLAB + (size_t)64 * 64 * MSG;
constexpr size_t WS_DN    = WS_GU + (size_t)8 * F_ * 16;
constexpr size_t WS_FLAGS = WS_DN + (size_t)2 * 8 * 32 * 64 * 16;
constexpr int FL_X = 0;
constexpr int FL_AB = 64 * 64 * 32;
constexpr int FL_ST = FL_AB + 2048 * AB_STRIDE;
constexpr int FL_CE = FL_ST + 4096;
constexpr int FL_KEY = FL_CE + 4096;
constexpr int FL_EPOCH = FL_KEY + 4096;
constexpr int WS_MB = (int)((WS_FLAGS + (FL_EPOCH + 1) * 4 + 1048575) >> 20);

#define WAIT_VM(n) asm volatile("s_waitcnt vmcnt(" #n ")" ::: "memory")
template <int N> static __device__ __forceinline__ void wait_vm() { asm volatile("s_waitcnt vmcnt(%0)" :: "i"(N) : "memory"); }
#define BAR_LGKM() asm volatile("s_waitcnt lgkmcnt(0)\n\ts_barrier" ::: "memory")
#define BAR() asm volatile("s_barrier" ::: "memory")
#define SB() __builtin_amdgcn_sched_barrier(0)
#define PAGE(n) asm volatile("s_branch 2f\n\t.p2align 12\nk_pg" #n ":\n\ts_setpc_b64 s[42:43]\n2:")
#define CALL_PG(n) asm volatile("s_getpc_b64 s[40:41]\n3:\n\ts_mov_b32 s42, k_pg" #n "-3b\n\ts_ashr_i32 s43, s42, 31\n\ts_add_u32 s40, s40, s42\n\ts_addc_u32 s41, s41, s43\n\t" \
                                "s_swappc_b64 s[42:43], s[40:41]" ::: "s40", "s41", "s42", "s43", "scc", "memory")

static __device__ __forceinline__ __amdgpu_buffer_rsrc_t mk_rsrc(const void* p) {
  return __builtin_amdgcn_make_buffer_rsrc((void*)p, 0, 0x7fffffff, 0x00020000);
}
#define DMA_(rsrc, voff, mbase, MI, sbase, SI, pol) do { unsigned _t; \
    asm volatile("s_add_i32 m0, %2, %3\n\ts_add_i32 %0, %5, %6\n\tbuffer_load_dwordx4 %1, %4, %0 offen" pol " lds" \
                 : "=&s"(_t) : "v"(voff), "s"(mbase), "i"(MI), "s"(rsrc), "s"(sbase), "i"(SI) : "memory", "scc"); } while (0)
#define DMA_I(rsrc, voff, mbase, MI, SI) \
    asm volatile("s_add_i32 m0, %1, %2\n\ts_nop 0\n\tbuffer_load_dwordx4 %0, %3, 0 offen offset:%4 lds" \
                 :: "v"(voff), "s"(mbase), "i"((MI) - (SI)), "s"(rsrc), "i"(SI) : "memory", "scc")
#define DMA_S(rsrc, voff, mbase, MI, soff) \
    asm volatile("s_add_i32 m0, %1, %2\n\ts_nop 0\n\tbuffer_load_dwordx4 %0, %3, %4 offen lds" \
                 :: "v"(voff), "s"(mbase), "i"(MI), "s"(rsrc), "s"(soff) : "memory", "scc")
#define TPOL ""
#define TOUCH(rsrc, voff, mbase, MI, soff) \
    asm volatile("s_add_i32 m0, %1, %2\n\ts_nop 0\n\tbuffer_load_dword %0, %3, %4 offen" TPOL " lds" \
                 :: "v"(voff), "s"(mbase), "i"(MI), "s"(rsrc), "s"(soff) : "memory", "scc")
template <int OFF> static __device__ __forceinline__ u32x4 ld16_s(const void* p, int voff) {
  u32x4 v;
  asm volatile("global_load_dwordx4 %0, %1, %2 offset:%3" : "=v"(v) : "v"(voff), "s"(p), "i"(OFF) : "memory");
  return v;
}
template <int OFF> static __device__ __forceinline__ void sld(unsigned& sink, const void* p, int soff) {
  asm volatile("s_load_dword %0, %1, %2 offset:%3" : "+s"(sink) : "s"(p), "s"(soff), "i"(OFF) : "memory");
}
static __device__ __forceinline__ unsigned gld4_s(const void* p, unsigned voff) {
  unsigned v;
  asm volatile("global_load_dword %0, %1, %2" : "=v"(v) : "v"(voff), "s"(p) : "memory");
  return v;
}
static __device__ __forceinline__ u32x2 bld8_sc1(__amdgpu_buffer_rsrc_t r, unsigned voff) {
  u32x2 v;
  asm volatile("buffer_load_dwordx2 %0, %1, %2, 0 offen sc1" : "=v"(v) : "v"(voff), "s"(r) : "memory");
  return v;
}
template <int N> static __device__ __forceinline__ void wait_xs(unsigned& a, u32x2& b) { asm volatile("s_waitcnt vmcnt(%2)" : "+v"(a), "+v"(b) : "i"(N) : "memory"); }
template <int N> static __device__ __forceinline__ void wait_xq(u32x4 (&a)[2], u32x2& b) { asm volatile("s_waitcnt vmcnt(%3)" : "+v"(a[0]), "+v"(a[1]), "+v"(b) : "i"(N) : "memory"); }
static __device__ __forceinline__ u32x2 ld8_s_sc1(const void* p, int voff) {
  u32x2 v;
  asm volatile("global_load_dwordx2 %0, %1, %2 sc1" : "=v"(v) : "v"(voff), "s"(p) : "memory");
  return v;
}
static __device__ __forceinline__ void lgkm0(unsigned& sink) { asm volatile("s_waitcnt lgkmcnt(0)" : "+s"(sink) :: "memory"); }
template <typename T> static __device__ __forceinline__ void pin4(T& a, T& b, T& c, T& d) { asm volatile("" : "+s"(a), "+s"(b), "+s"(c), "+s"(d)); }
template <typename T, typename U> static __device__ __forceinline__ void pins(T& a, U& b) { asm volatile("" : "+s"(a), "+s"(b)); }
static __device__ __forceinline__ void launder(int& v, int& a, int& b) { asm volatile("" : "+v"(v)); asm volatile("" : "+s"(a), "+s"(b)); }
static __device__ __forceinline__ unsigned bld4_sc1(__amdgpu_buffer_rsrc_t r, int voff) {
  unsigned v;
  asm volatile("buffer_load_dword %0, %1, %2, 0 offen sc1" : "=v"(v) : "v"(voff), "s"(r) : "memory");
  return v;
}
static __device__ __forceinline__ u32x4 bld16_nt(__amdgpu_buffer_rsrc_t r, int voff, int soff) {
  u32x4 v;
  asm volatile("buffer_load_dwordx4 %0, %1, %2, %3 offen nt" : "=v"(v) : "v"(voff), "s"(r), "s"(soff) : "memory");
  return v;
}
static __device__ __forceinline__ void bld16_nt_io(u32x4& v, __amdgpu_buffer_rsrc_t r, int voff, int soff) {
  asm volatile("buffer_load_dwordx4 %0, %1, %2, %3 offen nt" : "+v"(v) : "v"(voff), "s"(r), "s"(soff) : "memory");
}
static __device__ __forceinline__ u32x4 bld16_sc1(__amdgpu_buffer_rsrc_t r, int voff, int soff) {
  u32x4 v;
  asm volatile("buffer_load_dwordx4 %0, %1, %2, %3 offen sc1" : "=v"(v) : "v"(voff), "s"(r), "s"(soff) : "memory");
  return v;
}
static __device__ __forceinline__ u32x2 granule_wait(const u64* p, unsigned tag) {
  u64 v = 0;
#pragma unroll 1
  for (int it = 0; it < SPIN_MAX; ++it) {
    v = __hip_atomic_load(p, __ATOMIC_RELAXED, __HIP_MEMORY_SCOPE_AGENT);
    if (__all((unsigned)(v >> 32) == tag)) break;
    __builtin_amdgcn_s_sleep(2);
  }
  return (u32x2){(unsigned)v, (unsigned)(v >> 32)};
}
static __device__ __forceinline__ float key_score(unsigned key) {
  const unsigned u = key >> 16;
  return bf2f((u16)((u & 0x8000u) ? (u & 0x7fffu) : (~u & 0xffffu)));
}
static __device__ __forceinline__ unsigned med3u(unsigned a, unsigned b, unsigned c) { return max(min(a, b), min(max(a, b), c)); }
static __device__ __forceinline__ void merge2(unsigned& a1, unsigned& a2, unsigned b1, unsigned b2) {
  const unsigned lo = min(a1, b1);
  a1 = max(a1, b1);
  a2 = max(lo, max(a2, b2));
}
static __device__ __forceinline__ float sqdev16(const u32x4 (&v)[2], float mb) {
  f32x2 q2 = {0.f, 0.f};
#pragma unroll
  for (int i = 0; i < 8; ++i) {
    const f32x2 e = (f32x2){lo_bf(v[i >> 2][i & 3]), hi_bf(v[i >> 2][i & 3])} - mb;
    q2 += e * e;
  }
  return q2.x + q2.y;
}

constexpr int lrw(int r) { return (r & ~6) | ((r & 2) << 1) | ((r & 4) >> 1); }
constexpr int swz(int r) { return ((r >> 1) & 4) | (r & 2); }
static __device__ __forceinline__ int sym_dn(int p) { return (p ^ (p >> 1)) & 1; }
static __device__ __forceinline__ int sym_hi(int n, int w, int st, int b, int z, int p) {
  return ((p >> 1) * (n / 2 * st) + b * (w * st)) ^ (-sym_dn(p) & (((n - 1) ^ (z * (n / 2))) * st));
}
static __device__ __forceinline__ int sym_lo(int w, int st, int p, int c) { return ((c - sym_dn(p)) * st) & ((w - 1) * st); }
static __device__ __forceinline__ int sym_row(int n, int w, int st, int b, int z, int p, int c) { return sym_lo(w, st, p, c) ^ sym_hi(n, w, st, b, z, p); }
constexpr int G1_NST = 4, G1_NKT = 16, G1_STAGE = 32768;
constexpr int G2_NKT = 16, G2_MRING = 98304;
constexpr int n_off(int k) { return (k % 3) * 32768; }
constexpr int m_off(int k) { return ((k + 2) % 3) * 16384; }
constexpr int k_ord(int k) { return k < 2 ? k : (k < 9 ? 2 * (k - 1) : 2 * (k - 8) + 1); }
constexpr int LDS_DUMMY = G2_MRING + 49152;
constexpr int LDS_GU = LDS_DUMMY + 4096;
constexpr int LDS_FIN = LDS_GU + 4096;
constexpr int LDS_MB = LDS_FIN + 4096;
constexpr int LDS_SEL = LDS_MB + 1536;
constexpr int LDS_SIG = LDS_SEL + 64;
constexpr int LDS_KEY = LDS_SIG + 32;
constexpr int LDS_BYTES = LDS_KEY + 128;
constexpr int LDS_ACC = n_off(G2_NKT - 3);

constexpr int sp_tile(int q) {
  for (int k = 0; k < 16; ++k) {
    if ((k >= VA0 && k < VA0 + VAN) || (k >= VB0 && k < VB0 + VBN) || (k >= VC0 && k < VC0 + VCN)) continue;
    if (q-- == 0) return k;
  }
  return -1;
}
constexpr int tcnt(int q) { return (VAN > 0 && q == VAP) + (VBN > 0 && q == VBP) + (VCN > 0 && q == VCP); }
constexpr int dn_cnt(int q) { return dig(DVS, q); }
constexpr int dn_first(int q) { return acc_dig(DVS, q); }

static __device__ __forceinline__ void code_touch(unsigned& sink, unsigned off) {
  asm volatile("s_getpc_b64 s[40:41]\n\tk_main_rt:\n\ts_min_u32 %1, %1, k_main_end-k_main_rt-196\n\t"
               "s_load_dword %0, s[40:41], %1 offset:0\n\ts_load_dword %0, s[40:41], %1 offset:64\n\ts_load_dword %0, s[40:41], %1 offset:128\n\ts_load_dword %0, s[40:41], %1 offset:192"
               : "+s"(sink), "+s"(off) :: "s40", "s41", "scc", "memory");
}
static __device__ __forceinline__ void touch1(__amdgpu_buffer_rsrc_t r, int voff, unsigned mb, int soff) { TOUCH(r, voff, mb, LDS_DUMMY, soff); }
typedef const __attribute__((address_space(1))) u16* gptr;
typedef __attribute__((ext_vector_type(8))) u64 u64x8;
typedef __attribute__((ext_vector_type(2))) u64 u64x2;
extern "C" __global__ __launch_bounds__(512, 2) void k_main(const u16* x, const u16* wd_re, const u16* wd_im, const u16* w_r,
                                                 char* wsp, const u16* ws_re, const u16* ws_im,
                                                 const u16* w_gate, const u16* w_up, const u16* w_down, u16* out) {
  __shared__ __attribute__((aligned(16))) char smem[LDS_BYTES];
  u64x8 ka;
  u64x2 kb_;
  u64 kc;
  asm volatile("s_load_dwordx16 %0, %3, 0\n\ts_load_dwordx4 %1, %3, 64\n\ts_load_dwordx2 %2, %3, 80"
               : "=&s"(ka), "=&s"(kb_), "=&s"(kc) : "s"(__builtin_amdgcn_kernarg_segment_ptr()) : "memory");
  unsigned long long t00;
  asm volatile("s_memrealtime %0" : "=s"(t00) :: "memory");
  const int tid = threadIdx.x, lane = tid & 63;
  const int wid = __builtin_amdgcn_readfirstlane(tid >> 6);
  const int g = blockIdx.x, xcd = XMAP(g), j = g >> 3;
  if (wid < 4) {
    unsigned cw, co = (j * 4 + wid) * 448;
    asm volatile(".subsection 1\nk_main_end:\n.subsection 0\n\ts_min_u32 %1, %1, k_main_end-k_main-480\n\ts_getpc_b64 s[40:41]\n\ts_add_u32 s40, s40, %1\n\ts_addc_u32 s41, s41, 0\n\t"
                 "s_load_dword %0, s[40:41], 0\n\ts_load_dword %0, s[40:41], 64\n\ts_load_dword %0, s[40:41], 128\n\t"
                 "s_load_dword %0, s[40:41], 192\n\ts_load_dword %0, s[40:41], 256\n\ts_load_dword %0, s[40:41], 320\n\ts_load_dword %0, s[40:41], 384\n\ts_waitcnt lgkmcnt(0)"
                 : "=&s"(cw), "+s"(co) :: "s40", "s41", "scc", "memory");
  }
  const int pw = wid - 4;
  const int row0 = (wid & 3) * 8 + lrw(lane >> 3);
  const int cm = ((lane & 7) ^ swz(row0)) * 16;
  int vm = ((row0 & 15) * 2 + (row0 >> 4)) * (D_ * 2) + cm;
  asm volatile("" : "+v"(vm));
  const int kq = xcd >> 1;
  const int tt = (xcd & 1) * 4 + (j >> 3);
  const int et = j & 7;
  const int z = kq >> 1, st = (kq & 1) * 8 + tt;
  const int lrow = (kq & 1) * 1024 + (xcd & 1) * 512 + z * 256 + j * 8 + pw * 2;
  const int drow = ((kq & 1) * 2 + (xcd & 1)) * 256 + j * 8 + pw * 2;
  unsigned o_m = st * 128 * D_ * 2, o_l = lrow * D_ * 2, o_d = drow * D_ * 2, mb0 = lds_addr(smem) + pw * 1024;
  int l16 = lane * 16;
  int vn[4];
#pragma unroll
  for (int q = 0; q < 4; ++q) vn[q] = sym_lo(32, D_ * 2, (row0 >> 2) & 3, q * 8 + (row0 >> 4) * 4 + (row0 & 3)) ^ (sym_hi(D_, 32, D_ * 2, et, z, (row0 >> 2) & 3) | cm);
  asm volatile("" : "+s"(o_m), "+s"(o_l), "+s"(o_d), "+s"(mb0));
  asm volatile("" : "+v"(l16), "+v"(vn[0]), "+v"(vn[1]), "+v"(vn[2]), "+v"(vn[3]));
  asm volatile("s_waitcnt lgkmcnt(0)" : "+s"(ka), "+s"(kb_), "+s"(kc) :: "memory");
  x = (const u16*)(gptr)ka[0]; wd_re = (const u16*)(gptr)ka[1]; wd_im = (const u16*)(gptr)ka[2]; w_r = (const u16*)(gptr)ka[3];
  wsp = (char*)(gptr)ka[4]; ws_re = (const u16*)(gptr)ka[5]; ws_im = (const u16*)(gptr)ka[6]; w_gate = (const u16*)(gptr)ka[7];
  w_up = (const u16*)(gptr)kb_[0]; w_down = (const u16*)(gptr)kb_[1]; out = (u16*)(gptr)kc;
  int* flags = (int*)(wsp + WS_FLAGS);
  u64* stat = (u64*)(flags + FL_ST);
  unsigned tlv = 0;
  asm volatile("s_waitcnt lgkmcnt(0)" : "+s"(t00) :: "memory");
  tlv = lane == 52 ? (unsigned)t00 : tlv;
  const u16* wdz = z ? wd_im : wd_re;
  const __amdgpu_buffer_rsrc_t rm = mk_rsrc(x);
  const __amdgpu_buffer_rsrc_t rn = mk_rsrc(wdz);
#define G1_X(kt, so, q) DMA_(rm, vm, mb0, (so) + (q) * 4096, o_m, (kt) * 128 + (((q) & 1) * 64 + ((q) >> 1) * 32) * D_ * 2, "")
#define G1_W(kt, so, q) DMA_I(rn, vn[q], mb0, (so) + 16384 + (q) * 4096, (kt) * 128)
#define G1_STAGE_TILE(kt, so, W) do { SB(); G1_X(kt, so, 0); W(0); G1_X(kt, so, 1); W(1); G1_X(kt, so, 2); W(2); G1_X(kt, so, 3); W(3); \
    G1_W(kt, so, 0); W(4); G1_W(kt, so, 1); W(5); G1_W(kt, so, 2); W(6); G1_W(kt, so, 3); W(7); SB(); } while (0)
#define G1_STAGE_A(kt, so, W) do { SB(); G1_X(kt, so, 0); W(0); G1_X(kt, so, 2); W(1); \
    G1_W(kt, so, 0); W(2); G1_W(kt, so, 1); W(3); G1_W(kt, so, 2); W(4); G1_W(kt, so, 3); W(5); W(6); W(7); SB(); } while (0)
#define G1_STAGE_B(kt, so) do { SB(); G1_X(kt, so, 1); G1_X(kt, so, 3); SB(); } while (0)
#define NOW(i)
  const int fr = lane & 15, fq = lane >> 4;
  const int ha = wid >> 2;
  int trow, xo[4], xa[3], po, ml;
  const char *prs, *prc;
  auto col4 = [](int f) __attribute__((always_inline)) { return ((f & 2) << 3 | (f & 4) << 1 | (f & 1) << 2) ^ (-((f >> 3) & 1) & 28); };
  auto seg0 = [&](int hf, int dn) __attribute__((always_inline)) { return (hf * 512 + et * 32) ^ (-dn & (D_ - 32)); };
  auto epi_setup = [&](int part) __attribute__((always_inline)) {
    int t_ = tt, l_ = lane;
    asm volatile("" : "+s"(t_));
    asm volatile("" : "+v"(l_));
    const int ts = ((wid >> 1) & 1) * 8 + kq * 2 + ha;
    if (part == 0) { xo[3] = (seg0(wid & 1, (l_ >> 3) & 1) + col4(l_)) * 2; asm volatile("" : "+v"(xo[3])); }
    if (part == 1) {
      xo[3] |= sym_hi(S_, 64, D_ * 2, t_, z, l_ >> 4);
      ml = (l_ * 16) ^ (-sym_dn(l_ >> 4) & (z * 768));
      asm volatile("" : "+v"(xo[3]), "+v"(ml));
    }
    if (part == 2) {
      const int xh = xo[3];
#pragma unroll
      for (int r = 0; r < 4; ++r) xo[r] = sym_lo(64, D_ * 2, l_ >> 4, ts * 4 + r) ^ xh;
      const bool hi = xo[0] & 8;
      xa[0] = (hi ? xo[2] : xo[0]) & ~15; xa[1] = (hi ? xo[3] : xo[1]) & ~15;
      xa[2] = (sym_lo(64, D_ * 2, l_ >> 4, ts * 4 + (l_ & 3)) ^ xh) >> 11 << 3;
      asm volatile("" : "+v"(xo[0]), "+v"(xo[1]), "+v"(xo[2]), "+v"(xo[3]), "+v"(xa[0]), "+v"(xa[1]), "+v"(xa[2]));
    }
    if (part == 3) {
      trow = t_ * 16 + ts;
      prs = wsp + WS_SLAB + (size_t)((t_ * 8 + et) * 64 + (wid & 3)) * MSG;
      prc = (const char*)(flags + FL_X + ((t_ * 8 + et) * 64 + (wid & 3)) * 32);
      asm volatile("" : "+s"(trow), "+s"(prs), "+s"(prc));
      po = l_ < 3 ? (kq * 4 + ((kq + 1 + l_) & 3)) * 512 : (int)0x80000000;
      asm volatile("" : "+v"(po));
    }
  };
  auto tok_tile = [](unsigned key) __attribute__((always_inline)) {
    const unsigned h = (0xffff - key) & 1023;
    return h < 512 ? h >> 2 : ((1023 - h) >> 6 << 4) | (((1024 - h) & 63) >> 2);
  };
#define rs mk_rsrc(prs)
#define rc mk_rsrc(prc)
  lds_vi sig = (lds_vi)(smem + LDS_SIG + (wid & 3) * 4);
  unsigned tag;
  u32x4 xq[2];
  u32x2 sq;
  f32x4 ya[4];
  unsigned key[2];
  float cv[16];
  unsigned far0, far1;
  auto xq_load = [&](int n) __attribute__((always_inline)) {
    if (n < 2) xq[n] = ld16_s<0>(x, xa[n]); else sq = ld8_s_sc1(stat, xa[2]);
  };
#define PINV(a) asm volatile("" : "+v"(a))
  auto cv_calc = [&]() __attribute__((always_inline)) {
    if (__builtin_expect(!__all(sq.y == tag), 0)) sq = granule_wait(stat + (xa[2] >> 3), tag);
    far0 = tok_tile(key[0]) ^ trow; far1 = tok_tile(key[1]) ^ trow;
    asm volatile("" : "+s"(far0), "+s"(far1));
    const bool hi = xo[0] & 8;
    const float mb_ = lo_bf(sq.x), ri_ = __builtin_amdgcn_rcpf(hi_bf(sq.x));
#pragma unroll
    for (int r = 0; r < 4; ++r) {
      const float mb = r == 0 ? dpp_f<0x00>(mb_) : r == 1 ? dpp_f<0x55>(mb_) : r == 2 ? dpp_f<0xAA>(mb_) : dpp_f<0xFF>(mb_);
      const float ri = r == 0 ? dpp_f<0x00>(ri_) : r == 1 ? dpp_f<0x55>(ri_) : r == 2 ? dpp_f<0xAA>(ri_) : dpp_f<0xFF>(ri_);
#pragma unroll
      for (int u = 0; u < 2; ++u) {
        const unsigned own = xq[r & 1][(r & 2) + u], oth = dpp_u<0xB1>(xq[r & 1][2 - (r & 2) + u]);
        const unsigned w = hi == (r >= 2) ? own : oth;
        cv[r * 4 + 2 * u] = __builtin_fmaf(lo_bf(w) - mb, ri, lo_bf(w));
        cv[r * 4 + 2 * u + 1] = __builtin_fmaf(hi_bf(w) - mb, ri, hi_bf(w));
        PINV(cv[r * 4 + 2 * u]); PINV(cv[r * 4 + 2 * u + 1]);
      }
    }
  };
#define MSG_IDX(m) (kq * 4 + ((kq + 1 + (m)) & 3))
  unsigned seen = 0;

  if (__builtin_expect(wid >= 4, 1)) {
    __builtin_amdgcn_s_setprio(3);
    G1_STAGE_TILE(0, 0, NOW);
    constexpr int O_ST = (FL_ST - FL_AB) * 4, O_CE = (FL_CE - FL_AB) * 4, O_KEY = (FL_KEY - FL_AB) * 4, O_EP = (FL_EPOCH - FL_AB) * 4;
    const __amdgpu_buffer_rsrc_t rf = mk_rsrc(wsp + WS_FLAGS + FL_AB * 4);
    u32x4 lv[2], lv2[2], lw[2], ld[2], ld2[2];
    unsigned epv;
    {
      const char* xrow = (const char*)x + o_l;
      const char* wdrow = (const char*)wdz + o_d;
      lv[0] = ld16_s<0>(xrow, l16); lv[1] = ld16_s<1024>(xrow, l16);
      lv2[0] = ld16_s<2048>(xrow, l16); lv2[1] = ld16_s<3072>(xrow, l16);
      ld[0] = ld16_s<0>(wdrow, l16); ld[1] = ld16_s<1024>(wdrow, l16);
      ld2[0] = ld16_s<2048>(wdrow, l16); ld2[1] = ld16_s<3072>(wdrow, l16);
      lw[0] = ld16_s<0>(w_r, l16); lw[1] = ld16_s<1024>(w_r, l16);
      asm volatile("buffer_load_dword %0, off, %1, %2 sc1" : "=v"(epv) : "s"(rf), "s"(O_EP) : "memory");
    }
    SB();
    const unsigned long long did = llvm_dispatch_id();
    const unsigned dmix = (unsigned)did * 0x85EBCA6Bu + (unsigned)(did >> 32) * 0xC2B2AE35u + 0x27D4EB2Fu;
    const bool moe = et != 7;
    unsigned epoch;
    const int ci = lane & 15, rq = lane >> 4, tk = (lane >> 3) & 1;
    int mat, cg, dq, d0;
    u32x4 wv[16];
    const u16 *pgu, *pn2;
    const char *pm2, *spb;
#define rw __builtin_amdgcn_make_buffer_rsrc((void*)pgu, 0, moe ? 0x7fffffff : 0, 0x00020000)
#define rn2 mk_rsrc(pn2)
#define rm2 mk_rsrc(pm2)
    int vw, vn2[8], vm2;
    auto k_blk = [&](int k) __attribute__((always_inline)) { return tt ^ (k_ord(k) >> 1); };
#define KROT(kk_) (k_blk(kk_) * 256 + (k_ord(kk_) & 1) * 128)
    int tv2[3];
    unsigned sd = 0;
    auto spf = [&](auto N) __attribute__((always_inline)) {
      constexpr int n = decltype(N)::value;
      if constexpr (n >= 0 && sp_tile(n >> 4) >= 0) sld<((n >> 1) & 7) * 4096 + (n & 1) * 64>(sd, spb, KROT(sp_tile(n >> 4)));
    };
    auto touches = [&](auto P) __attribute__((always_inline)) {
      constexpr int p = decltype(P)::value;
      if constexpr (VAN > 0 && p == VAP) touch1(mk_rsrc(spb), tv2[0], mb0, 0);
      if constexpr (VBN > 0 && p == VBP) touch1(mk_rsrc(spb), tv2[1], mb0, 0);
      if constexpr (VCN > 0 && p == VCP) touch1(mk_rsrc(spb), tv2[2], mb0, 0);
    };
    unsigned mq0, mq1, mq2, mq3;
    const int cg8 = et ^ ((et & 1) << 1), rgb = (g & 7) * 4 + (j >> 3);
    const __amdgpu_buffer_rsrc_t rdn = mk_rsrc(w_down);
    const __amdgpu_buffer_rsrc_t rgu = mk_rsrc(wsp + WS_GU);
    int vdn, vgu;
    u32x4 dv[7], gv[4];
    int abo, abq[3];
    unsigned lma;
    unsigned sel[2][4];
    float g0, g1;
    u32x4 kk[4];
    unsigned k1 = 0, k2 = 0;
    const int ko_ = O_KEY + pw * 4096;
#define MM(a, b, c) __builtin_amdgcn_mfma_f32_16x16x32_bf16(__builtin_bit_cast(bf16x8, a), __builtin_bit_cast(bf16x8, b), c, 0, 0, 0)
#define PIN1(a) asm volatile("" : "+v"(a))
#define PIN2(a, b) asm volatile("" : "+v"(a), "+v"(b))
#define PIN3(a, b, c) asm volatile("" : "+v"(a), "+v"(b), "+v"(c))
    {
      int gro, go;
      u32x2 gr;
      u32x4 oh[5];
      f32x4 sa4, gw[2], gx[2];
      float su, gs, ta, tb, sx, mbv;
      unsigned dk[4], kl;
      const int hb = lane >> 5;
#pragma unroll
      for (int l = 0; l < WSN; ++l) asm volatile("" : "=v"(wv[l]));
      auto diag = [&](const f32x4& m) __attribute__((always_inline)) {
        return __uint_as_float((__float_as_uint(m[0]) & dk[0]) | (__float_as_uint(m[1]) & dk[1]) | (__float_as_uint(m[2]) & dk[2]) | (__float_as_uint(m[3]) & dk[3]));
      };
      auto setup = [&](auto JB, auto GP) __attribute__((always_inline)) {
        constexpr int jb = decltype(JB)::value, gp = decltype(GP)::value;
        SB();
        {
          int ln = lane, jj = j, p_ = pw;
          launder(ln, jj, p_);
          const int c_ = ln & 15, r_ = ln >> 4, e_ = jj & 7, t_ = (xcd & 1) * 4 + (jj >> 3);
          if constexpr (jb == 1 && gp == 0) {
            pn2 = ((kq >> 1) ? ws_im : ws_re) + (kq & 1) * 1024;
            spb = (const char*)(pn2 + (sym_row(S_, 64, 1, t_, 0, e_ >> 1, 0) + (e_ & 1) * 32 + p_ * 8) * S_);
            pins(pn2, spb);
          }
          if constexpr (jb == 1 && gp >= 1 && gp < 6) {
            const unsigned o = c_ == gp - 1 ? 0x3f803f80u : 0u;
            oh[gp - 1] = (u32x4){o, o, o, o};
            PIN1(oh[gp - 1]);
          }
          if constexpr (jb == 1 && gp == 6) {
            const int rk = moe ? e_ * 8 + (jj >> 3) * 2 + (xcd >> 2) : 0, r3 = ((rk - 14) * 43) >> 7, rr = rk - 14 - r3 * 3;
            const int c56 = rk < 14 ? rk * 4 + 1 : r3 * 4 + rr + ((rr + 1) >> 1);
            mat = (c56 + 36) >> 6; cg = c56 - mat * 28; dq = xcd & 3; d0 = dq * 256 + p_ * 64;
            pin4(mat, cg, dq, d0);
          }
          if constexpr (jb == 2 && gp == 0) {
            pgu = mat ? w_up : w_gate;
            pm2 = wsp + WS_ABT + (kq * 8 + e_) * 262144;
            pins(pgu, pm2);
            vw = ((d0 + r_ * 16) * F_ + cg * 128 + c_ * 8) * 2;
            PIN1(vw);
          }
          if constexpr (jb == 2 && (gp == 1 || gp == 2)) {
            dk[gp * 2 - 2] = c_ == r_ * 4 + gp * 2 - 2 ? ~0u : 0u; dk[gp * 2 - 1] = c_ == r_ * 4 + gp * 2 - 1 ? ~0u : 0u;
            PIN2(dk[gp * 2 - 2], dk[gp * 2 - 1]);
          }
          if constexpr (jb == 2 && gp == 3) { kl = lrow + (ln >> 5); go = (ln & 30) ? (int)0x80000000 : ((ln & 1) ? O_KEY : O_ST) + (int)kl * 8; PIN2(kl, go); }
          if constexpr (jb == 2 && gp == 4) { kl = 0xffff - kl; go = (ln & ~1) == 2 ? O_CE + (z * D_ + drow + (ln & 1)) * 8 : go; PIN2(kl, go); }
          if constexpr (jb == 2 && gp == 5) {
            gro = p_ < 2 ? O_ST + (st * 128 + p_ * 64 + ln) * 8 : O_CE + z * D_ * 8 + sym_row(D_, 32, 8, e_, z, (ln >> 2) & 3, (p_ - 2) * 16 + r_ * 4 + (ln & 3));
            lma = lds_addr(smem + LDS_MB) + (p_ < 2 ? p_ * 256 : 512 + p_ * 256) + ln * 4;
            PIN2(gro, lma);
          }
          if constexpr (jb == 1 && gp == 7) {
#pragma unroll
            for (int u = 0; u < 3; ++u) {
              const int vn_ = u == 0 ? VAN : u == 1 ? VBN : VCN;
              if (vn_ == 0) continue;
              const int k = (u == 0 ? VA0 : u == 1 ? VB0 : VC0) + (ln >> 3);
              tv2[u] = (ln >> 3) < vn_ ? (ln & 7) * 4096 + ((t_ ^ (k < 9 ? k - 1 : k - 8)) << 8) + (k < 9 ? 0 : 128) : (int)0x80000000;
              PIN1(tv2[u]);
            }
          }
          if constexpr (jb == 7 && gp == 0) {
            vm2 = p_ * 1024 + ln * 16;
            PIN1(vm2);
          }
          if constexpr (jb == 2 && gp == 6) {
            const int r0 = p_ * 8 + lrw(ln >> 3);
            vn2[6] = sym_lo(64, S_ * 2, (r0 >> 2) & 3, (r0 >> 4) * 4 + (r0 & 3)); vn2[7] = sym_hi(S_, 64, S_ * 2, t_, z, (r0 >> 2) & 3) | (vm & 127);
            PIN2(vn2[6], vn2[7]);
          }
          if constexpr ((jb == 2 && gp == 7) || (jb == 7 && gp >= 6)) {
            constexpr int q0 = jb == 2 ? 0 : gp == 6 ? 3 : 6;
            const int lo = vn2[6], hi = vn2[7];
#pragma unroll
            for (int q = q0; q < q0 + 3 && q < 8; ++q) { vn2[q] = ((lo + q * 8 * S_ * 2) & (63 * S_ * 2)) ^ hi; PIN1(vn2[q]); }
          }
          if constexpr (jb == 7 && gp == 1) { vdn = rgb * 112 + p_ * 28 + r_ * 7; vgu = (((ln >> 3) & 1) * 4 * F_ + vdn + min(ln & 7, 6)) * 16; PIN2(vdn, vgu); }
          if constexpr (jb == 7 && gp == 2) { vdn = (vdn * D_ + cg8 * 128 + c_ * 8) * 2; PIN1(vdn); }
          if constexpr (jb == 7 && gp == 3) {
            abo = (((kq * 8 + e_) * 4 + p_) * 16 + c_) * AB_STRIDE * 4;
            abq[2] = ln >= 16 || (ln & 7) == t_ ? (int)0x80000000 : abo;
            PIN2(abo, abq[2]);
          }
          if constexpr (jb == 7 && gp == 4) {
            abq[1] = ln >= 8 ? (int)0x80000000 : abq[2];
            abq[0] = ((ln ^ t_) & 4) ? (int)0x80000000 : abq[1];
            PIN2(abq[0], abq[1]);
          }
          if constexpr (jb == 7 && gp == 5) {
            const unsigned mb = lds_addr(smem) + p_ * 1024;
            mq0 = mb + ((3 - kq) & 3) * 4096; mq1 = mb + ((4 - kq) & 3) * 4096; mq2 = mb + ((5 - kq) & 3) * 4096; mq3 = mb + ((6 - kq) & 3) * 4096;
            pin4(mq0, mq1, mq2, mq3);
          }
          if constexpr (jb == 8 && gp < 4) {
#pragma unroll
            for (int h = 0; h < 2; ++h) {
              const unsigned hv = h ? 0x0302u : 0x0100u;
              sel[h][gp] = ((ln & 7) >> 1) == gp ? ((ln & 1) ? (hv << 16) | 0x0c0cu : 0x0c0c0000u | hv) : 0x0c0c0c0cu;
            }
            PIN2(sel[0][gp], sel[1][gp]);
          }
        }
        SB();
      };
#define WS1(i) setup(IC<1>{}, IC<i>{})
#define WS2(i) setup(IC<2>{}, IC<i>{})
#define WS3(i) do { setup(IC<7>{}, IC<i>{}); if (i >= 4) setup(IC<8>{}, IC<(i) & 3>{}); spf(IC<(i < SP0 ? i : -1)>{}); } while (0)
      G1_STAGE_TILE(1, G1_STAGE, WS1);
      WAIT_VM(19); BAR();
      G1_STAGE_TILE(2, 2 * G1_STAGE, WS2);
      G1_STAGE_TILE(3, 3 * G1_STAGE, WS3);
      auto lnu = [&](auto U) __attribute__((always_inline)) {
        constexpr int jb = 1 + decltype(U)::value / 7, gp = decltype(U)::value % 7;
        SB();
        if constexpr (jb == 1) {
          const f32x4 z4 = {0.f, 0.f, 0.f, 0.f};
          if constexpr (gp == 0) { sa4 = MM(lv[0], oh[0], z4); gw[0] = MM(lv[0], lw[0], z4); gx[0] = MM(lv[0], lv[0], z4); PIN3(sa4, gw[0], gx[0]); }
          if constexpr (gp == 1) { sa4 = MM(lv2[0], oh[1], sa4); gw[1] = MM(lv2[0], lw[0], z4); gx[1] = MM(lv2[0], lv2[0], z4); PIN3(sa4, gw[1], gx[1]); }
          if constexpr (gp == 2) { sa4 = MM(lv[1], oh[0], sa4); gw[0] = MM(lv[1], lw[1], gw[0]); gx[0] = MM(lv[1], lv[1], gx[0]); PIN3(sa4, gw[0], gx[0]); }
          if constexpr (gp == 3) { sa4 = MM(lv2[1], oh[1], sa4); gw[1] = MM(lv2[1], lw[1], gw[1]); gx[1] = MM(lv2[1], lv2[1], gx[1]); PIN3(sa4, gw[1], gx[1]); }
          if constexpr (gp == 4) { sa4 = MM(ld[0], oh[2], sa4); sa4 = MM(ld[1], oh[2], sa4); PIN1(sa4); }
          if constexpr (gp == 5) { sa4 = MM(ld2[0], oh[3], sa4); sa4 = MM(ld2[1], oh[3], sa4); PIN1(sa4); }
          if constexpr (gp == 6) { sa4 = MM(lw[0], oh[4], sa4); sa4 = MM(lw[1], oh[4], sa4); PIN1(sa4);
            epoch = __builtin_amdgcn_readfirstlane(epv); tag = ((epoch + 1u) * 0x9E3779B1u ^ dmix) | 1u; }
        }
        if constexpr (jb == 2) {
          if constexpr (gp == 0) { su = (sa4[0] + sa4[1]) + (sa4[2] + sa4[3]); ta = diag(gw[0]); PIN2(su, ta); }
          if constexpr (gp == 1) { su = fold32(su, su); tb = diag(gw[1]); PIN2(su, tb); }
          if constexpr (gp == 2) { su = fold16(su, su); gs = fold32(ta, tb); PIN2(su, gs); }
          if constexpr (gp == 3) { ta = diag(gx[0]); tb = diag(gx[1]); PIN2(ta, tb); }
          if constexpr (gp == 4) { gs = fold16(gs, fold32(ta, tb)); PIN1(gs); }
          if constexpr (gp == 5) { gs += dpp_f<0xB1>(gs); gs += dpp_f<0x4E>(gs); PIN1(gs); }
          if constexpr (gp == 6) { gs += dpp_f<0x141>(gs); gs += dpp_f<0x140>(gs); PIN1(gs); }
        }
        if constexpr (jb == 3) {
          if constexpr (gp == 0) {
            const float s0 = rdl(su, 0), s1 = rdl(su, 1), q0 = rdl(gs, 16), q1 = rdl(gs, 48);
            sx = hb ? s1 : s0; ta = hb ? q1 : q0; PIN2(sx, ta);
          }
          if constexpr (gp == 1) {
            const float mean = sx * (1.0f / D_);
            mbv = rbf(mean);
            ta -= sx * mean;
            if (__builtin_expect(__any(!(sx * mean <= 16.f * ta)), 0)) {
              float q = fold32(sqdev16(lv, rdl(mbv, 0)), sqdev16(lv2, rdl(mbv, 32)));
              q = row_sum(fold16(q, q));
              const float dm = mean - mbv;
              ta = fmaxf(q - (float)D_ * dm * dm, 0.f);
            }
            PIN2(mbv, ta);
          }
          if constexpr (gp == 2) { ta = rbf(rbf(ta * (1.0f / D_)) + 1e-5f); gs -= mbv * rdl(su, 4); PIN2(ta, gs); }
          if constexpr (gp == 3) { ta = rbf(__builtin_amdgcn_sqrtf(ta)); tb = __builtin_amdgcn_rcpf(ta); PIN2(ta, tb); }
          if constexpr (gp == 4) {
            const unsigned rb = __float_as_uint(rbf(gs * tb));
            kl |= (rb ^ ((unsigned)((int)rb >> 31) | 0x80000000u)) & 0xffff0000u; PIN1(kl);
          }
          if constexpr (gp == 5) { kl = (lane & 2) ? __float_as_uint(su) : (lane & 1) ? kl : (__float_as_uint(mbv) >> 16) | __float_as_uint(ta); PIN1(kl); }
        }
        SB();
      };
      constexpr int LN_JB = 1 + ((19 - LNP) * LNS) / 8;
      auto work1 = [&](auto JB, auto GP) __attribute__((always_inline)) {
        constexpr int jb = decltype(JB)::value, gp = decltype(GP)::value, pos = (jb - 1) * 8 + gp;
        SB();
        if constexpr (pos % LNS == 0 && LNP + pos / LNS < 20) lnu(IC<LNP + pos / LNS>{});
        if constexpr (jb == ST_JB + 3) {
          if constexpr (gp == 0) { if (!__all(gr.y == tag)) gr = granule_wait((const u64*)((const char*)(flags + FL_AB) + gro), tag); PIN1(gr); }
          if constexpr (gp == 1) { g1 = __builtin_amdgcn_rcpf(__uint_as_float((gr.x & 0xffff0000u) ^ ((unsigned)z << 31))); PIN1(g1); }
          if constexpr (gp == 2) { g0 = pw < 2 ? -(lo_bf(gr.x) * g1) : __uint_as_float(gr.x); PIN1(g0); }
        }
        if constexpr (gp < SP1 && jb <= SP1E) spf(IC<SP0 + (jb - 1) * SP1 + gp>{});
        SB();
      };
      auto step1 = [&](auto JB) __attribute__((always_inline)) {
        constexpr int jb = decltype(JB)::value;
        if constexpr (jb == 1) PAGE(1);
        if constexpr (jb == 7) PAGE(2);
        constexpr int n16 = jb == 12 ? 12 : jb == 11 ? 14 : 16;
        constexpr auto wl = [](int q) { return (q == LN_JB ? 1 : 0) + (q == ST_JB ? 1 : 0); };
        if constexpr (jb == 1) {
          asm volatile("s_waitcnt vmcnt(%11)" : "+v"(lv[0]), "+v"(lv[1]), "+v"(lw[0]), "+v"(lw[1]), "+v"(ld[0]), "+v"(ld[1]),
                       "+v"(lv2[0]), "+v"(lv2[1]), "+v"(ld2[0]), "+v"(ld2[1]), "+v"(epv) : "i"(LNP ? 24 : 16) : "memory");
#define LU(u) if constexpr (u < LNP) lnu(IC<u>{});
          LU(0) LU(1) LU(2) LU(3) LU(4) LU(5) LU(6) LU(7) LU(8) LU(9) LU(10) LU(11) LU(12) LU(13) LU(14) LU(15) LU(16) LU(17) LU(18)
          if (LNP) WAIT_VM(16);
        } else if constexpr (jb == ST_JB + 3) {
          asm volatile("s_waitcnt vmcnt(%1)" : "+v"(gr) : "i"(n16 + wl(jb - 2) + wl(jb - 1)) : "memory");
        } else wait_vm<n16 + wl(jb - 3) + wl(jb - 2) + wl(jb - 1)>();
        BAR();
        if constexpr (jb == 2) { if (pw == 0 && lane < 16) ((int*)(smem + LDS_SEL))[lane < 8 ? lane : lane + 8] = 0; }
        if constexpr (jb == 4) { if (pw == 0 && lane == 0) ((unsigned*)(smem + LDS_SEL))[5] = tag; }
#define W1(i) work1(JB, IC<i>{})
        if constexpr (jb < 10) G1_STAGE_TILE(jb - 1 + G1_NST, ((jb - 1) % G1_NST) * G1_STAGE, W1);
        else G1_STAGE_A(jb - 1 + G1_NST, ((jb - 1) % G1_NST) * G1_STAGE, W1);
        if constexpr (jb == 12) { G1_STAGE_B(13, G1_STAGE); G1_STAGE_B(14, 2 * G1_STAGE); }
        if constexpr (jb == LN_JB) __builtin_amdgcn_raw_buffer_store_b64((u32x2){kl, tag}, rf, go, 0, 16);
        if constexpr (jb == ST_JB) {
          asm volatile("buffer_load_dwordx2 %0, %1, %2, 0 offen sc1" : "=v"(gr) : "v"(gro), "s"(rf) : "memory");
        }
        touches(IC<100 + jb>{});
        if constexpr (jb <= 8) {
          if (et == jb - 1) {
#pragma unroll
            for (int l = 0; l < WSN; ++l) bld16_nt_io(wv[l], rw, vw, l * F_ * 2);
          }
        }
        SB();
      };
      static_assert(ST_JB + 3 <= 12 && ST_JB >= 6, "schedule");
      step1(IC<1>{}); step1(IC<2>{}); step1(IC<3>{}); step1(IC<4>{}); step1(IC<5>{}); step1(IC<6>{}); step1(IC<7>{}); step1(IC<8>{}); step1(IC<9>{}); step1(IC<10>{}); step1(IC<11>{}); step1(IC<12>{});
    }
#define ALIAS 0
#define G2_STAGE_N(kk_, W) do { const int kb = KROT(((ALIAS >> (kk_)) & 1) ? (kk_) & 3 : (kk_)); constexpr int so = n_off(kk_); SB(); \
    DMA_S(rn2, vn2[0], mq0, so, kb); W(0); DMA_S(rn2, vn2[1], mq1, so, kb); W(1); \
    DMA_S(rn2, vn2[2], mq2, so, kb); W(2); DMA_S(rn2, vn2[3], mq3, so, kb); W(3); \
    DMA_S(rn2, vn2[4], mq0, so + 16384, kb); W(4); DMA_S(rn2, vn2[5], mq1, so + 16384, kb); W(5); \
    DMA_S(rn2, vn2[6], mq2, so + 16384, kb); W(6); DMA_S(rn2, vn2[7], mq3, so + 16384, kb); W(7); SB(); } while (0)
#define G2_STAGE_NH(kk_, h) do { const int kb = KROT(kk_); constexpr int so = n_off(kk_) + (h) * 16384; SB(); \
    DMA_S(rn2, vn2[(h) * 4], mq0, so, kb); DMA_S(rn2, vn2[(h) * 4 + 1], mq1, so, kb); \
    DMA_S(rn2, vn2[(h) * 4 + 2], mq2, so, kb); DMA_S(rn2, vn2[(h) * 4 + 3], mq3, so, kb); SB(); } while (0)
#define G2_STAGE_M(kk_, W) do { const int kb = KROT(kk_) * 128; constexpr int so = G2_MRING + m_off(kk_); SB(); \
    DMA_(rm2, vm2, mb0, so, kb, 0, " sc1"); W(8); DMA_(rm2, vm2, mb0, so + 4096, kb, 4096, " sc1"); W(9); \
    DMA_(rm2, vm2, mb0, so + 8192, kb, 8192, " sc1"); W(10); DMA_(rm2, vm2, mb0, so + 12288, kb, 12288, " sc1"); W(11); SB(); } while (0)
    WAIT_VM(16); BAR();
    asm volatile("ds_write_b32 %0, %1" :: "v"(lma), "v"(g0) : "memory");
    if (pw < 2) asm volatile("ds_write_b32 %0, %1 offset:512" :: "v"(lma), "v"(g1) : "memory");
    u32x4 tv[4];
    unsigned ready = 0;
    auto need = [&](int k) __attribute__((always_inline)) {
      const int b = k_blk(k) + (k_ord(k) & 1) * 8;
#pragma unroll 1
      for (int it = 0; !((ready >> b) & 1) && it < SPIN_MAX; ++it) {
        ready |= (unsigned)__ballot(__builtin_amdgcn_raw_buffer_load_b32(rf, abq[k < 5 ? 0 : k < 9 ? 1 : 2], 0, 16) == tag);
        asm volatile("" ::: "memory");
      }
    };
    unsigned ksel, bad = 0;
    int isel;
    unsigned xw;
    auto bvec = [&](unsigned src, const unsigned (&sl)[4]) __attribute__((always_inline)) {
      return (u32x4){__builtin_amdgcn_perm(0, src, sl[0]), __builtin_amdgcn_perm(0, src, sl[1]),
                     __builtin_amdgcn_perm(0, src, sl[2]), __builtin_amdgcn_perm(0, src, sl[3])};
    };
#define BC8(v, p) (unsigned)__builtin_amdgcn_update_dpp(__builtin_amdgcn_update_dpp(0, (int)(v), 0x150 + (p), 0xf, 0x3, false), (int)(v), 0x158 + (p), 0xf, 0xc, false)
    float* lg = (float*)(smem + LDS_GU);
    f32x4 macc = {0.f, 0.f, 0.f, 0.f};
    u32x2 sv;
    unsigned hp, gbad;
    float s0, s1;
    auto work2 = [&](auto JB, auto GP) __attribute__((always_inline)) {
      constexpr int jb = decltype(JB)::value, gp = decltype(GP)::value;
      SB();
      if constexpr (jb == -1 && gp < 4) {
        constexpr int c = gp == 0 ? 0xB1 : gp == 1 ? 0x4E : gp == 2 ? 0x141 : 0x140;
        merge2(k1, k2, dpp_u<c>(k1), dpp_u<c>(k2));
        PIN2(k1, k2);
      }
      if constexpr (jb == -1 && gp == 4) {
        ksel = tk ? k2 : k1;
        isel = (0xffff - (int)(ksel & 0xffffu)) & (S_ - 1);
        k1 = (isel * D_ + d0 + rq * 16 + (lane & 7) * 2) * 2; k2 = O_ST + isel * 8;
        PIN1(ksel);
        xw = gld4_s(x, k1);
        sv = bld8_sc1(rf, k2);
      }
      if constexpr (jb == 0 && gp == 1) wait_xs<3 + 1 + (N2A < 2 ? N2A : 2) + tcnt(116) + tcnt(117)>(xw, sv);
      if constexpr (jb == 0) {
        if constexpr (gp == 1) { if (!__all(sv.y == tag)) sv = granule_wait(stat + isel, tag); s0 = __builtin_amdgcn_rcpf(hi_bf(sv.x)); PIN1(s0); }
        if constexpr (gp == 2) { s1 = ln_elem(lo_bf(xw), lo_bf(sv.x), hi_bf(sv.x), s0); PIN1(s1); }
        if constexpr (gp == 3) { xw = pk2(s1, ln_elem(hi_bf(xw), lo_bf(sv.x), hi_bf(sv.x), s0)); PIN1(xw); }
      }
      if constexpr (DGS == 0 && jb == DG && gp < 7) { PIN1(dv[gp]); macc = MM(dv[gp], bvec(BC8(hp, gp), sel[0]), macc); PIN1(macc); }
      if constexpr (jb >= 100 && gp < 7) { PIN1(dv[gp]); macc = MM(dv[gp], bvec(BC8(hp, gp), sel[0]), macc); PIN1(macc); }
      if constexpr ((jb == 0 && gp >= 4) || jb == 1) {
        constexpr int l = jb == 0 ? gp - 4 : gp + 4;
        if constexpr (!(l & 1)) hp = BC8(xw, l >> 1);
        PIN1(wv[l]);
        macc = MM(wv[l], bvec(hp, sel[l & 1]), macc);
        PIN2(macc, hp);
      }
      if constexpr (jb == GV_JB + 2) {
        if constexpr (gp == 0) { gbad = (gv[0].y ^ tag) | (gv[0].w ^ tag) | (gv[1].y ^ tag) | (gv[1].w ^ tag); PIN1(gbad); }
        if constexpr (gp == 1) { gbad |= (gv[2].y ^ tag) | (gv[2].w ^ tag) | (gv[3].y ^ tag) | (gv[3].w ^ tag); PIN1(gbad); }
        if constexpr (gp == 2) {
          s0 = ((__uint_as_float(gv[0].x) + __uint_as_float(gv[1].x)) + __uint_as_float(gv[2].x)) + __uint_as_float(gv[3].x);
          s1 = ((__uint_as_float(gv[0].z) + __uint_as_float(gv[1].z)) + __uint_as_float(gv[2].z)) + __uint_as_float(gv[3].z);
          PIN2(s0, s1);
        }
        if constexpr (gp == 3) {
#pragma unroll 1
          for (int it = 0; !__all(gbad == 0) && it < SPIN_MAX; ++it) {
            u32x4 q[4];
            gbad = 0;
#pragma unroll
            for (int i = 0; i < 4; ++i) { q[i] = __builtin_amdgcn_raw_buffer_load_b128(rgu, vgu, i * F_ * 16, 16); gbad |= (q[i].y ^ tag) | (q[i].w ^ tag); }
            s0 = ((__uint_as_float(q[0].x) + __uint_as_float(q[1].x)) + __uint_as_float(q[2].x)) + __uint_as_float(q[3].x);
            s1 = ((__uint_as_float(q[0].z) + __uint_as_float(q[1].z)) + __uint_as_float(q[2].z)) + __uint_as_float(q[3].z);
            asm volatile("" ::: "memory");
          }
          const unsigned gp2 = pk2(s0, s1);
          const unsigned up2 = dpp_u<0x128>(gp2);
          s0 = tk ? hi_bf(up2) : lo_bf(gp2); s1 = tk ? hi_bf(gp2) : lo_bf(up2);
          PIN2(s0, s1);
        }
        if constexpr (gp == 4) { s0 = rbf(s0 * __builtin_amdgcn_rcpf(1.f + __expf(-s0))); PIN1(s0); }
        if constexpr (gp == 5) { hp = pk2(s0 * s1, 0.f); PIN1(hp); }
      }
      if constexpr (jb == 10 && gp == 4) code_touch(sd, (j * 4 + pw) * 256);
      SB();
    };
#define W2M(i) work2(IC<-1>{}, IC<i>{})
#define W20(i) work2(IC<0>{}, IC<i>{})
#define W21(i) work2(IC<1>{}, IC<i>{})
    auto piece = [&](auto K, auto Q) __attribute__((always_inline)) {
      constexpr int k = decltype(K)::value, q = decltype(Q)::value;
      if constexpr (q < 8) {
        const int kb = KROT(((ALIAS >> k) & 1) ? k & 3 : k);
        DMA_S(rn2, vn2[q], (q & 3) == 0 ? mq0 : (q & 3) == 1 ? mq1 : (q & 3) == 2 ? mq2 : mq3, n_off(k) + (q >> 2) * 16384, kb);
      } else {
        if constexpr (q == 8) need(k);
        const int kb = KROT(k) * 128;
        DMA_(rm2, vm2, mb0, G2_MRING + m_off(k) + (q - 8) * 4096, kb, (q - 8) * 4096, " sc1");
      }
    };
    unsigned fs;
    {
      G1_STAGE_B(15, 3 * G1_STAGE);
      G2_STAGE_NH(0, 0);
      lgkm0(sd);
      WAIT_VM(16); BAR();
      G2_STAGE_NH(0, 1);
      WAIT_VM(10); BAR();
#pragma unroll
      for (int i = 0; i < 4; ++i) kk[i] = bld16_sc1(rf, l16, ko_ + i * 1024);
#pragma unroll
      for (int l = WSN; l < 16; ++l) wv[l] = bld16_nt(rw, vw, l * F_ * 2);
      asm volatile("s_waitcnt vmcnt(%4)" : "+v"(kk[0]), "+v"(kk[1]), "+v"(kk[2]), "+v"(kk[3]) : "i"(16 - WSN) : "memory");
      touches(IC<115>{});
#pragma unroll
      for (int i = 0; i < 4; ++i) {
        k2 = med3u(k1, k2, kk[i].x); k1 = max(k1, kk[i].x);
        k2 = med3u(k1, k2, kk[i].z); k1 = max(k1, kk[i].z);
        bad |= (kk[i].y ^ tag) | (kk[i].w ^ tag);
      }
#pragma unroll 1
      for (int it = 0; !__all(bad == 0) && it < SPIN_MAX; ++it) {
        k1 = 0; k2 = 0; bad = 0;
#pragma unroll
        for (int i = 0; i < 4; ++i) {
          const u32x4 q = __builtin_amdgcn_raw_buffer_load_b128(rf, l16, ko_ + i * 1024, 16);
          k2 = med3u(k1, k2, q.x); k1 = max(k1, q.x);
          k2 = med3u(k1, k2, q.z); k1 = max(k1, q.z);
          bad |= (q.y ^ tag) | (q.w ^ tag);
        }
        asm volatile("" ::: "memory");
      }
      merge2(k1, k2, dpp_u<0xB1>(k1), dpp_u<0xB1>(k2));
      merge2(k1, k2, dpp_u<0x4E>(k1), dpp_u<0x4E>(k2));
      merge2(k1, k2, dpp_u<0x141>(k1), dpp_u<0x141>(k2));
      merge2(k1, k2, dpp_u<0x140>(k1), dpp_u<0x140>(k2));
      if ((lane & 15) == 0) *(u32x2*)(smem + LDS_KEY + (pw * 4 + rq) * 8) = (u32x2){k1, k2};
      PAGE(3);
      BAR_LGKM();
#pragma unroll
      for (int jj = 0; jj < 4; ++jj) tv[jj] = *(const u32x4*)(smem + G2_MRING + m_off(0) + (jj * 4 + pw) * 1024 + lane * 16);
      { const u32x2 kp = *(const u32x2*)(smem + LDS_KEY + ci * 8); k1 = kp.x; k2 = kp.y; }
#pragma unroll
      for (int jj = 0; jj < 4; ++jj) __builtin_amdgcn_raw_buffer_store_b128(tv[jj], rm2, vm2 + jj * 4096 + 2 * tt * 16384, 0, 16);
      G2_STAGE_N(1, W2M);
      touches(IC<116>{});
      wait_vm<10 + tcnt(116)>();
      if (lane == tt) __builtin_amdgcn_raw_buffer_store_b32(tag, rf, abo, 0, 16);
      touches(IC<117>{});
#define P2A(i) if constexpr (i < N2A) piece(IC<2>{}, IC<i>{}); W20(i);
      SB(); P2A(0) P2A(1) P2A(2) P2A(3) P2A(4) P2A(5) P2A(6) P2A(7)
      __builtin_amdgcn_s_sleep(WSL);
      wait_vm<1 + N2A + tcnt(116) + tcnt(117)>();
      fs = bld4_sc1(rf, abq[0]);
    }
    BAR();
#pragma unroll
    for (int jj = 0; jj < 4; ++jj) tv[jj] = *(const u32x4*)(smem + G2_MRING + m_off(1) + (jj * 4 + pw) * 1024 + lane * 16);
    constexpr int SPB = SP0 + SP1E * SP1;
#define P2B(i) if constexpr (i >= N2A) piece(IC<2>{}, IC<i>{});
    SB(); P2B(0) P2B(1) P2B(2) P2B(3) P2B(4) P2B(5) P2B(6) P2B(7)
#define P3A(i) if constexpr (i < N3A) piece(IC<3>{}, IC<i>{});
    P3A(0) P3A(1) P3A(2) P3A(3) P3A(4) P3A(5) P3A(6) P3A(7)
    asm volatile("s_waitcnt vmcnt(%1)" : "+v"(fs) : "i"(8 - N2A + N3A) : "memory");
    ready = (unsigned)__ballot(fs == tag);
    need(2);
    G2_STAGE_M(2, NOW);
#define P3B(i) if constexpr (i >= N3A) piece(IC<3>{}, IC<i>{});
    P3B(0) P3B(1) P3B(2) P3B(3) P3B(4) P3B(5) P3B(6) P3B(7)
    need(3);
    G2_STAGE_M(3, NOW);
    fs = bld4_sc1(rf, abq[1]);
    touches(IC<1>{});
#pragma unroll
    for (int i = 0; i < dn_cnt(1); ++i) dv[i] = bld16_nt(rdn, vdn, i * D_ * 2);
    W21(0); W21(1); W21(2); W21(3); W21(4); W21(5); W21(6); W21(7); W21(8); W21(9); W21(10); W21(11);
    asm volatile("" : "+v"(tv[0]), "+v"(tv[1]), "+v"(tv[2]), "+v"(tv[3]));
    auto step2 = [&](auto JB) __attribute__((always_inline)) {
      constexpr int jb = decltype(JB)::value;
      if constexpr (jb == 3) PAGE(4);
      if constexpr (jb == 2 && PRIO2 != 3) __builtin_amdgcn_s_setprio(PRIO2);
      if constexpr (jb == 9) PAGE(5);
      constexpr auto must = [](int q) { return (q == 2 ? 4 : 0) + ((FSM >> q) & 1) + dig(GVS, q); };
      constexpr auto late = [](int q) { return dn_cnt(q) + (VAN > 0 && q == VAP) + (VBN > 0 && q == VBP) + (VCN > 0 && q == VCP); };
      if constexpr (jb == 2) wait_vm<8 - N3A + 4 + must(1) + late(1)>();
      else wait_vm<(dig(LAGS, jb - 2) ? 0 : late(jb - 2) + (jb == 5 ? 1 : 0)) + 12 - dig(LAGS, jb - 1) + must(jb - 1) + late(jb - 1)>();
      if constexpr (jb == 3) { if (pw == 0 && (lane & ~8) == 0) ((unsigned*)(smem + LDS_SEL))[tk] = ksel; }
      if constexpr ((jb == 2 || jb == DG + 1)) {
        *(f32x4*)(lg + pw * 256 + lane * 4) = macc;
        macc = (f32x4){0.f, 0.f, 0.f, 0.f};
        lgkm0(sd);
      }
      BAR();
      float p0[4], p1[4];
      const int col = (pw & 1) * 64 + lane;
      if constexpr (jb == 3) {
        if (g == 0 && pw == 0 && lane == 0) __hip_atomic_store((unsigned*)flags + FL_EPOCH, epoch + 1u, __ATOMIC_RELAXED, __HIP_MEMORY_SCOPE_AGENT);
      }
      if constexpr (jb == DG + 1) {
        const float* p = lg + (pw & 1) * 32 + (lane >> 4) * 64 + ((2 * lane) & 7) * 4 + ((lane >> 2) & 3);
#pragma unroll
        for (int i = 0; i < 4; ++i) { p0[i] = p[i * 256]; p1[i] = p[i * 256 + 4]; }
      }
      if constexpr (jb == 2) {
        const float* p = lg + (col >> 5) * 64 + (col & 7) * 4 + ((col >> 3) & 3);
#pragma unroll
        for (int i = 0; i < 4; ++i) { p0[i] = p[i * 256]; p1[i] = p[i * 256 + 32]; }
      }
      if constexpr (jb == 4) {
        if (lane == 8 + tt) __builtin_amdgcn_raw_buffer_store_b32(tag, rf, abo, 0, 16);
      }
      if constexpr ((FSM >> (jb - 2)) & 1) {
        asm volatile("" : "+v"(fs));
        ready |= (unsigned)__ballot(fs == tag);
      }
      if constexpr (jb == GV_JB + 2) asm volatile("" : "+v"(gv[0]), "+v"(gv[1]), "+v"(gv[2]), "+v"(gv[3]));
      auto w2 = [&](auto GP) __attribute__((always_inline)) {
        constexpr int gp = decltype(GP)::value;
        if constexpr (jb == 2 && gp == 0) {
          p0[0] = (p0[0] + p0[1]) + (p0[2] + p0[3]); p1[0] = (p1[0] + p1[1]) + (p1[2] + p1[3]);
          PIN2(p0[0], p1[0]);
        }
        if constexpr (gp < 12) work2(JB, GP);
        if constexpr (gp < dig(SPS, jb) && !((SPE >> jb) & 1)) spf(IC<SPB + acc_dig(SPS, jb) + gp>{});
      };
      constexpr int l0 = dig(LAGS, jb - 1), np = l0 + 12 - dig(LAGS, jb);
      auto slot = [&](auto I) __attribute__((always_inline)) {
        constexpr int i = decltype(I)::value;
        if constexpr (i < l0) piece(IC<jb + 1>{}, IC<12 - l0 + i>{});
        else if constexpr (i < np) piece(IC<jb + 2>{}, IC<i - l0>{});
        if constexpr (i < 12 || i < np) w2(I);
      };
#define SL4(i) slot(IC<i>{}); slot(IC<i + 1>{}); slot(IC<i + 2>{}); slot(IC<i + 3>{});
      SB();
      SL4(0) SL4(4) SL4(8) SL4(12) SL4(16) SL4(20)
      SB();
      if constexpr (jb == 2) {
#pragma unroll
        for (int jj = 0; jj < 4; ++jj) __builtin_amdgcn_raw_buffer_store_b128(tv[jj], rm2, vm2 + jj * 4096 + (2 * tt + 1) * 16384, 0, 16);
      }
      if constexpr ((FSM >> jb) & 1) fs = bld4_sc1(rf, abq[jb < 5 ? 1 : 2]);
#pragma unroll
      for (int i = acc_dig(GVS, jb); i < acc_dig(GVS, jb + 1); ++i) gv[i] = bld16_sc1(rgu, vgu, i * F_ * 16);
      touches(JB);
#pragma unroll
      for (int i = dn_first(jb); i < dn_first(jb) + dn_cnt(jb); ++i) dv[i] = bld16_nt(rdn, vdn, i * D_ * 2);
      if constexpr (jb == DG + 1) {
        if (pw < 2)
          __builtin_amdgcn_raw_buffer_store_b128((u32x4){__float_as_uint((p0[0] + p0[1]) + (p0[2] + p0[3])), tag, __float_as_uint((p1[0] + p1[1]) + (p1[2] + p1[3])), tag},
                                                 mk_rsrc(wsp + WS_DN), (((pw * 8 + cg8) * 32 + rgb) * 64 + lane) * 16, 0, 16);
      }
      if constexpr (jb == 2) {
        if (moe && pw < 2)
          __builtin_amdgcn_raw_buffer_store_b128((u32x4){__float_as_uint(p0[0]), tag, __float_as_uint(p1[0]), tag},
                                                 rgu, ((mat * 4 + dq) * F_ + cg * 128 + col) * 16, 0, 16);
      }
      SB();
#define SPX(i) spf(IC<(((i >= np && i >= 12) || ((SPE >> jb) & 1)) && i < dig(SPS, jb) ? SPB + acc_dig(SPS, jb) + i : -1)>{})
      if constexpr (dig(DGS, jb) > 0) {
        if (DGP != 3) __builtin_amdgcn_s_setprio(DGP);
#define DGU(i) if constexpr (i >= acc_dig(DGS, jb) && i < acc_dig(DGS, jb + 1)) work2(IC<100>{}, IC<i>{});
        DGU(0) DGU(1) DGU(2) DGU(3) DGU(4) DGU(5) DGU(6)
        if (DGP != 3) __builtin_amdgcn_s_setprio(3);
      }
      SPX(0); SPX(1); SPX(2); SPX(3); SPX(4); SPX(5); SPX(6); SPX(7); SPX(8); SPX(9); SPX(10); SPX(11); SPX(12); SPX(13); SPX(14);
      SB();
    };
    static_assert(DG + 1 <= 12 && DG >= GV_JB + 3 && dn_first(DG - 2) == 7 && acc_dig(GVS, GV_JB + 1) == 4 && dig(LAGS, 1) == 0 && dig(SPS, 1) == 0 && dig(LAGS, 0) == 0, "schedule");
    static_assert(DGS == 0 || (acc_dig(DGS, DG + 1) == 7 && acc_dig(DGS, GV_JB + 2) == 0 && acc_dig(DGS, 10) <= dn_first(7) && acc_dig(DGS, 11) <= dn_first(8) && acc_dig(DGS, 9) <= dn_first(6)), "units need their loads");
    static_assert(ALIAS || sp_tile((SPB + acc_dig(SPS, 14)) >> 4) < 0, "all tiles are prefetched");
    step2(IC<2>{}); step2(IC<3>{}); step2(IC<4>{}); step2(IC<5>{}); step2(IC<6>{}); step2(IC<7>{}); step2(IC<8>{}); step2(IC<9>{}); step2(IC<10>{}); step2(IC<11>{}); step2(IC<12>{}); step2(IC<13>{});
    lgkm0(sd);
    wait_vm<12 - dig(LAGS, 13)>(); BAR();
    {
      constexpr int l0 = dig(LAGS, 13);
#define PL(i) if constexpr (i >= 12 - l0) piece(IC<15>{}, IC<i>{});
      PL(0) PL(1) PL(2) PL(3) PL(4) PL(5) PL(6) PL(7) PL(8) PL(9) PL(10) PL(11)
    }
    __builtin_amdgcn_s_setprio(0);
    if (EPP == 1) { WAIT_VM(0); BAR(); }
    epi_setup(0); epi_setup(1); epi_setup(2);
    if (EPP == 2) { WAIT_VM(0); BAR(); }
#pragma unroll
    for (int n = 0; n < 3; ++n) xq_load(n);
    if (!EPP) { WAIT_VM(3); BAR(); }
    epi_setup(3);
    wait_xq<0>(xq, sq);
    key[0] = __builtin_amdgcn_readlane(ksel, 0); key[1] = __builtin_amdgcn_readlane(ksel, 8);
    cv_calc();
#pragma unroll 1
    for (int c = 0; c < 2; ++c) {
      if (__builtin_expect(((c ? far1 : far0) & ~9) != 0, 1)) continue;
      const __amdgpu_buffer_rsrc_t rd = mk_rsrc(wsp + WS_DN + (size_t)(c * 8 * 32 + pw * 8) * 1024);
      const int ec = seg0(lane >> 5, (lane >> 4) & 1), dno = (ec >> 7) * 32768 + ((ec & 127) / 2 + (lane & 15)) * 16;
#pragma unroll 1
      for (int it = 0; it < SPIN_MAX; ++it) {
        if (__all(__builtin_amdgcn_raw_buffer_load_b32(rd, (ec >> 7) * 32768 + (lane & 7) * 1024 + 1020, 0, 16) == tag)) break;
        __builtin_amdgcn_s_sleep(8);
        asm volatile("" ::: "memory");
      }
      float s0, s1;
      unsigned bad = 1;
#pragma unroll 1
      for (int it = 0; !__all(bad == 0) && it < SPIN_MAX; ++it) {
        bad = 0; s0 = 0.f; s1 = 0.f;
#pragma unroll
        for (int i = 0; i < 8; ++i) {
          const u32x4 q = __builtin_amdgcn_raw_buffer_load_b128(rd, dno, i * 1024, 16);
          bad |= (q.y ^ tag) | (q.w ^ tag); s0 += __uint_as_float(q.x); s1 += __uint_as_float(q.z);
        }
        asm volatile("" ::: "memory");
      }
      *(f32x2*)(smem + LDS_FIN + (c * 4 + pw) * 512 + lane * 8) = (f32x2){s0, s1};
      asm volatile("s_waitcnt lgkmcnt(0)" ::: "memory");
      if (lane == 0) asm volatile("ds_add_u32 %0, %1" :: "v"(lds_addr(smem + LDS_SEL + 24 + c * 4)), "v"(1) : "memory");
    }
  } else {
  {
    const int q = j * 4 + wid;
    constexpr int Q1 = WS_MB, Q2 = Q1 + 8, Q3 = Q2 + 8, Q4 = Q3 + 7, Q5 = Q4 + 7, Q6 = Q5 + 7, Q7 = Q6 + 4, Q8 = Q7 + 4, QC = Q8 + 1;
    static_assert(QC <= 128, "warm-up slots");
    unsigned dmy = 0;
    if (q < QC) {
      const char* base; int b0;
      if (q < Q1) { base = wsp; b0 = 0; }
      else if (q < Q2) { base = (const char*)ws_re; b0 = Q1; }
      else if (q < Q3) { base = (const char*)ws_im; b0 = Q2; }
      else if (q < Q4) { base = (const char*)w_gate; b0 = Q3; }
      else if (q < Q5) { base = (const char*)w_up; b0 = Q4; }
      else if (q < Q6) { base = (const char*)w_down; b0 = Q5; }
      else if (q < Q7) { base = (const char*)out; b0 = Q6; }
      else if (q < Q8) { base = (const char*)x; b0 = Q7; }
      else { base = (const char*)w_r; b0 = Q8; }
      base += (size_t)(q - b0) << 20;
      asm volatile("s_load_dword %0, %1, 0" : "+s"(dmy) : "s"(base) : "memory");
    }
    if (wid == 0) { CALL_PG(1); CALL_PG(8); CALL_PG(5); }
    else if (wid == 1) { CALL_PG(2); CALL_PG(9); CALL_PG(6); }
    else if (wid == 2) { CALL_PG(3); CALL_PG(10); }
    else { CALL_PG(4); CALL_PG(11); }
    BAR();
    lgkm0(dmy);
    TL(1);
  }
  PAGE(8);
  epi_setup(0); epi_setup(1); epi_setup(2); epi_setup(3);
  const int wr = wid >> 1, wc = wid & 1;
  const int sw = swz(fr), fl = lrw(fr);
  const int ko0 = ((0 + fq) ^ sw) * 16, ko1 = ((4 + fq) ^ sw) * 16;
  bf16x8 fm[2][4], fn[2][4];
  {
    const char* pm = smem + (wr * 64 + fl) * 128;
    const char* pn = smem + 16384 + (wc * 64 + fl) * 128;
    f32x4 acc[4][4];
#pragma unroll
    for (int i = 0; i < 4; ++i)
#pragma unroll
      for (int jn = 0; jn < 4; ++jn)
#pragma unroll
        for (int q = 0; q < 4; ++q) acc[i][jn][q] = 0.f;
#define TO(i) ((i) * 2048)
#define LDSP(T, a) ((__attribute__((address_space(3))) T*)(size_t)(a))
#define SLOTS(n) _Pragma("unroll") for (int q = 0; q < (n); ++q)
#define MFA(c, m, n) asm volatile("v_mfma_f32_16x16x32_bf16 %0, %2, %1, %0" : "+v"(c) : "v"(m), "v"(n))
#define MFA2(c, m, n, m2, n2) asm volatile("v_mfma_f32_16x16x32_bf16 %0, %2, %1, %0\n\tv_mfma_f32_16x16x32_bf16 %0, %4, %3, %0" : "+v"(c) : "v"(m), "v"(n), "v"(m2), "v"(n2))
    unsigned lm[2][2], ln[2][2];
#pragma unroll
    for (int u = 0; u < 2; ++u)
#pragma unroll
      for (int kk = 0; kk < 2; ++kk) {
        lm[u][kk] = lds_addr(pm) + u * 2 * G1_STAGE + (kk ? ko1 : ko0);
        ln[u][kk] = lds_addr(pn) + u * 2 * G1_STAGE + (kk ? ko1 : ko0);
        asm volatile("" : "+v"(lm[u][kk]), "+v"(ln[u][kk]));
      }
    auto ldm = [&](int stg, int i, int kk) __attribute__((always_inline)) { fm[kk][i] = *LDSP(const bf16x8, lm[stg >> 1][kk] + (stg & 1) * G1_STAGE + TO(i)); };
    auto ldn = [&](int stg, int jn, int kk) __attribute__((always_inline)) { fn[kk][jn] = *LDSP(const bf16x8, ln[stg >> 1][kk] + (stg & 1) * G1_STAGE + TO(jn)); };
    auto quad = [&](int a, int c, int d, int s) __attribute__((always_inline)) {
      const int i = (s < 2 || s >= 6) ? a : a + 1, jn = s < 4 ? c : d, kk = (s == 1 || s == 2 || s == 5 || s == 6);
      MFA(acc[i][jn], fm[kk][i], fn[kk][jn]);
    };
    auto quad2 = [&](int a, int c, int d, int s) __attribute__((always_inline)) {
      const int i = (s < 2 || s >= 6) ? a : a + 1, jn = s < 4 ? c : d, kk = (s == 2 || s == 6);
      MFA2(acc[i][jn], fm[kk][i], fn[kk][jn], fm[1 - kk][i], fn[1 - kk][jn]);
    };
    auto half1 = [&](int stg) __attribute__((always_inline)) {
      const int ja = (stg & 1) * 2, jb = 2 - ja;
      SLOTS(8) {
        quad(0, ja, ja + 1, q);
        if (q < 4) ldn(stg, jb + (q >> 1), q & 1); else ldm(stg, 2 + ((q - 4) >> 1), q == 5 || q == 6);
        SB();
      }
      SLOTS(4) { quad2(0, jb, jb + 1, 2 * q); SB(); }
    };
    auto half2 = [&](int stg) __attribute__((always_inline)) {
      const int ja = (stg & 1) * 2, jb = 2 - ja, nx = (stg + 1) & 3;
      SLOTS(8) {
        quad(2, jb + 1, jb, q);
        if (!(q & 1)) ldm(nx, q >> 2, q == 2 || q == 4);
        SB();
      }
      SLOTS(8) {
        quad(2, ja, ja + 1, q);
        if (!(q & 1)) ldn(nx, jb + (q >> 2), (q >> 1) & 1);
        SB();
      }
    };
    unsigned ae[7];
    {
      int l_ = lane;
      asm volatile("" : "+v"(l_));
      const int p = l_ >> 4, dn = sym_dn(p), hf = (p >> 1) ^ (dn & z);
#pragma unroll
      for (int u = 0; u < 7; ++u) {
        const int T = u < 3 ? wc * 4 : (wc * 4 + u - 3 - dn) & 7, i = u < 3 ? (u + 1) ^ ((u & 1) ? 0 : dn * 2) : 0;
        const int f = dn * 8 + ((T & 2) << 1) + ((T & 4) >> 1) + (T & 1);
        ae[u] = lds_addr(smem + G2_MRING) + (hf * 64 + i * 16 + lrw(f)) * 128 + (((wr * 4 + ((l_ >> 2) & 3)) ^ swz(f)) * 16) + (l_ & 3) * 4;
        asm volatile("" : "+v"(ae[u]));
      }
    }
    unsigned al = lds_addr(smem + LDS_MB) + (wr * 32 + fr * 2) * 4, ac = lds_addr(smem + LDS_MB) + (256 + wc * 64 + fq * 4) * 4;
    asm volatile("" : "+v"(al), "+v"(ac));
    SLOTS(16) asm volatile("" : "+v"(acc[q >> 2][q & 3]));
    SB();
    SLOTS(4) { ldm(0, q >> 1, q & 1); ldn(0, q >> 1, q & 1); }
    SB();
#pragma unroll 1
    for (int k4 = 0; k4 < G1_NKT - 4; k4 += 4) {
      half1(0); BAR_LGKM();

      SB(); half2(0);
      half1(1); BAR_LGKM();

      SB(); half2(1);
      half1(2); BAR_LGKM();

      SB(); half2(2);
      half1(3); BAR_LGKM();

      SB(); half2(3);
    }
    bf16x8 gm[2][4], gn[2][8];
    auto rdg = [&](int set, int t, int h, int r) __attribute__((always_inline)) {
      const int u = (t >> 1) & 1, o = (t & 1) * G1_STAGE;
      if (r == 0 || r == 2 || r == 4 || r == 5) {
        const int i = r >= 4, kk = r == 2 || r == 4;
        gm[set][kk * 2 + i] = *LDSP(const bf16x8, lm[u][kk] + o + h * 4096 + i * 2048);
      } else {
        const int jn = r < 4 ? 0 : (r - 4) >> 1, kk = r < 4 ? r >> 1 : r & 1;
        gn[set][kk * 4 + jn] = *LDSP(const bf16x8, ln[u][kk] + o + TO(jn));
      }
    };
    auto mmg = [&](int set, int h, int q) __attribute__((always_inline)) {
      const int jn = q >> 2, r = q & 3, i = (r >> 1) ^ (jn & 1), kk = r == 1 || r == 2;
      MFA(acc[h * 2 + i][jn], gm[set][kk * 2 + i], gn[set][kk * 4 + jn]);
    };
    f32x2 rsv, mrv;
    f32x4 cev[4];
    float tc[32];
    auto rds = [&](int h, int r) __attribute__((always_inline)) {
      if (r == 0) mrv = *LDSP(const f32x2, al + h * 256); else if (r == 1) rsv = *LDSP(const f32x2, al + 512 + h * 256);
    };
    auto prep = [&](int n) __attribute__((always_inline)) {
      tc[n] = llvm_fmul_legacy(mrv[n & 1], cev[n >> 3][(n >> 1) & 3]);
      asm volatile("" : "+v"(tc[n]));
    };
    auto epi = [&](int h, int q) __attribute__((always_inline)) {
      const int jn = q >> 1, r = (q & 1) * 2, o = m_off(h) + (jn & 1) * 128 + (jn >> 1) * lrw(4) * 128;
      asm volatile("v_fmac_f32 %0, %4, %8\n\tv_fmac_f32 %1, %5, %9\n\tv_fmac_f32 %2, %6, %8\n\tv_fmac_f32 %3, %7, %9"
                   : "+v"(tc[q * 4]), "+v"(tc[q * 4 + 1]), "+v"(tc[q * 4 + 2]), "+v"(tc[q * 4 + 3])
                   : "v"(acc[h * 2][jn][r]), "v"(acc[h * 2 + 1][jn][r]), "v"(acc[h * 2][jn][r + 1]), "v"(acc[h * 2 + 1][jn][r + 1]),
                     "v"(rsv[0]), "v"(rsv[1]));
      *LDSP(unsigned, r ? ae[1] + o : ae[3 + jn] + m_off(h)) = pk2(tc[q * 4], tc[q * 4 + 1]);
      *LDSP(unsigned, ae[r] + o) = pk2(tc[q * 4 + 2], tc[q * 4 + 3]);
    };
    SB();
    half1(0);
    BAR_LGKM();
    SB();
    TL(14);
    SLOTS(16) {
      if (q < 8) quad(2, 3, 2, q); else quad(2, 0, 1, q - 8);
      if (q < 12) rdg(0, 13, 0, q);
      SB();
    }
    BAR();
    SB();
    TL(15);
    SLOTS(16) {
      mmg(0, 0, q);
      if (q < 12) rdg(1, 14, 0, q);
      else { rds(0, q - 12); cev[q - 12] = *LDSP(const f32x4, ac + (q - 12) * 64); }
      SB();
    }
    PAGE(9);
    BAR();
    SB();
    TL(16);
    SLOTS(16) { mmg(1, 0, q); if ((q & 3) < 3) rdg(0, 15, 0, (q >> 2) * 3 + (q & 3)); if (q >= 8) { prep(2 * q - 16); prep(2 * q - 15); } SB(); }
    SLOTS(16) { mmg(0, 0, q); if ((q & 3) < 3) rdg(1, 13, 1, (q >> 2) * 3 + (q & 3)); prep(16 + q); SB(); if (q >= 9 && (q & 1)) { epi(0, (q - 9) >> 1); SB(); } }
    SLOTS(8) { mmg(1, 1, q); if (q < 6) { rdg(0, 14, 1, 2 * q); rdg(0, 14, 1, 2 * q + 1); } SB(); if (q & 1) { epi(0, 4 + (q >> 1)); SB(); } }
    BAR_LGKM();
    SB();
    TL(60);
    SLOTS(8) { mmg(1, 1, 8 + q); if (q < 4) rdg(1, 15, 1, q == 0 ? 1 : q == 1 ? 3 : q + 4); else rds(1, q - 4); SB(); }
    SLOTS(16) { mmg(0, 1, q); if (q < 8) rdg(1, 15, 1, q == 0 ? 0 : q == 1 ? 2 : q < 4 ? q + 2 : q + 4); prep(q); SB(); }
    asm volatile("s_waitcnt lgkmcnt(0)" ::: "memory");
    if (lane == 0) asm volatile("ds_add_u32 %0, %1" :: "v"(lds_addr(smem + LDS_SEL + 12)), "v"(1) : "memory");
    SB();
    SLOTS(16) {
      mmg(1, 1, q);
      if (q < 4) fn[0][q] = *(const bf16x8*)(smem + (wr * 128 + fl) * 128 + (q >> 1) * 2048 + ((q & 1) ? ko1 : ko0));
      else if (q < 8) fm[q & 1][(q - 4) >> 1] = *(const bf16x8*)(smem + G2_MRING + m_off(0) + (wc * 64 + fl) * 128 + TO((q - 4) >> 1) + ((q & 1) ? ko1 : ko0));
      prep(16 + q);
      SB();
    }
    {
      lds_vi rel = (lds_vi)(smem + LDS_SEL + 12);
      int it = 0;
      while (rel[0] != 4 && it < SPIN_MAX * 8) ++it;
      asm volatile("" ::: "memory");
    }
    TL(17);
    asm volatile("s_nop 7");
    SB();
    SLOTS(8) { epi(1, q); SB(); }
  }

  const char* pn = smem + (wr * 128 + fl) * 128;
  const char* pm = smem + G2_MRING + (wc * 64 + fl) * 128;
  f32x4 acc[8][4];
  auto mm = [&](bool imaj, int set, int q2, int s, int mode) __attribute__((always_inline)) {
    int i, jj, kk;
    if (imaj) { i = s >> 2; const int r = s & 3; jj = (r >> 1) ^ (i & 1); kk = (r == 1 || r == 2); }
    else { jj = s >> 3; const int r = s & 7; i = jj ? 3 - (r >> 1) : (r >> 1); kk = ((r + 1) >> 1) & 1; }
    if (mode == 1 && !(s & 1)) asm volatile("v_mfma_f32_16x16x32_bf16 %0, %2, %1, 0" : "=&v"(acc[q2 * 2 + jj][i]) : "v"(fm[kk][i]), "v"(fn[set][jj * 2 + kk]));
    else if (mode) asm volatile("v_mfma_f32_16x16x32_bf16 %0, %2, %1, %0" : "+v"(acc[q2 * 2 + jj][i]) : "v"(fm[kk][i]), "v"(fn[set][jj * 2 + kk]));
    else acc[q2 * 2 + jj][i] = __builtin_amdgcn_mfma_f32_16x16x32_bf16(fn[set][jj * 2 + kk], fm[kk][i], acc[q2 * 2 + jj][i], 0, 0, 0);
  };
  unsigned bn[2], bm[2], an[2], am[2];
#pragma unroll
  for (int kk = 0; kk < 2; ++kk) {
    bn[kk] = lds_addr(pn) + (kk ? ko1 : ko0); bm[kk] = lds_addr(pm) + (kk ? ko1 : ko0);
    asm volatile("" : "+v"(bn[kk]), "+v"(bm[kk]));
    an[kk] = bn[kk]; am[kk] = bm[kk] + m_off(0);
  }
#define RDN(jt, kk, o) (*LDSP(const bf16x8, an[kk] + (o) + (jt) * 2048))
#define RDM(i, kk, o) (*LDSP(const bf16x8, am[kk] + (o) + TO(i)))
  PAGE(10);
  TL(18);
  TL(19);
  tag = __builtin_amdgcn_readfirstlane(((const unsigned*)(smem + LDS_SEL))[5]);
  {
    int son = 0;
    auto tile = [&](int kt, int zero) __attribute__((always_inline)) {
      SLOTS(16) {
        mm(true, 0, 0, q, zero);
        if (!(q & 1)) {
          if (q < 8) fm[(q >> 1) & 1][2 + (q >> 2)] = RDM(2 + (q >> 2), (q >> 1) & 1, 0);
          else { const int r = (q - 8) >> 1; fn[1][r] = RDN(2 + (r >> 1), r & 1, 0); }
        }
        SB();
      }
      SLOTS(16) {
        mm(false, 1, 1, q, zero);
        if (!(q & 1) && q < 8) fn[0][q >> 1] = RDN(4 + (q >> 2), (q >> 1) & 1, 0);
        SB();
      }
      const int som = son >> 1;
      son = son == 65536 ? 0 : son + 32768;
      SLOTS(16) {
        mm(false, 0, 2, q, zero);
        if (!(q & 1) && q < 8) fn[1][q >> 1] = RDN(6 + (q >> 2), (q >> 1) & 1, 0);
        if (q == 9 || q == 11) { an[q == 11] = bn[q == 11] + son; asm volatile("" : "+v"(an[q == 11])); }
        if (q == 13 || q == 15) { am[q == 15] = bm[q == 15] + som; asm volatile("" : "+v"(am[q == 15])); }
        SB();
      }

      BAR_LGKM();

      SB();
      SLOTS(16) {
        mm(true, 1, 3, q, zero);
        if (!(q & 1)) {
          if (q < 8) fn[0][q >> 1] = RDN(q >> 2, (q >> 1) & 1, 0);
          else fm[(q >> 1) & 1][(q - 8) >> 2] = RDM((q - 8) >> 2, (q >> 1) & 1, 0);
        }
        SB();
      }
    };
    SB();
    tile(0, 1);
#pragma unroll 1
    for (int kt = 1; kt < G2_NKT - 3; ++kt) tile(kt, 0);
  }
  unsigned fx;
  constexpr int XC1 = XL0 >= 104 ? 3 : 0, XC2 = XL0 >= 152 ? 3 : 0;
  static_assert(XL0 + 2 * XLS < FL0 && (XL0 + 2 * XLS < 80 || XL0 >= 152 || (XL0 >= 104 && XL0 + 2 * XLS < 144)), "");
  int fo = lane == 0 ? kq * 512 : (int)0x80000000;
  {
    constexpr int NG = 12;
    static constexpr int g_t[NG] = {0, 0, 1, 1, 2, 2, 2, 1, 0, 0, 1, 2};
    static constexpr int g_q[NG] = {0, 1, 0, 1, 0, 1, 2, 2, 2, 3, 3, 3};
    constexpr int G14 = 1, G15 = 3, GP0 = 5, GP1 = 6;
    constexpr auto chp = [](int g) { return g == 0 || g_t[g] != g_t[g - 1]; };
    constexpr auto chn = [](int g) { return g + 1 < NG && g_t[g + 1] != g_t[g]; };
    static_assert(n_off(13) == 32768 && n_off(14) == 65536 && n_off(15) == 0, "");
    unsigned an2[2];
#pragma unroll
    for (int kk = 0; kk < 2; ++kk) {
      an[kk] = bn[kk]; an2[kk] = bn[kk] + 65536; am[kk] = bm[kk];
      asm volatile("" : "+v"(an[kk]), "+v"(an2[kk]), "+v"(am[kk]));
    }
    int vo;
    u32x4 pk;
    u32x2 kv;
    auto publish = [&](int d, int h) __attribute__((always_inline)) {
      if (h == 0) { vo = ml; PINV(vo); vo += (((d + kq + 1) & 3) * 4 + kq) * 4 * MSG; PINV(vo); }
      const f32x4 v = acc[2 * d + (h >> 2)][h & 3];
      pk[(h & 1) * 2] = pk2(v[0], v[1]); pk[(h & 1) * 2 + 1] = pk2(v[2], v[3]);
      if (h & 1) __builtin_amdgcn_raw_buffer_store_b128(pk, rs, vo + (h >> 1) * 1024, 0, 16);
    };
#define MSG_FLAG(d) __builtin_amdgcn_raw_buffer_store_b32(tag, rc, fo + (((d) + kq + 1) & 3) * 2048, 0, 16)
    auto rdn = [&](int g, int r) __attribute__((always_inline)) {
      const int t = g_t[g], jt = g_q[g] * 2 + (r >> 1);
      fn[g & 1][r] = *LDSP(const bf16x8, (t == 1 ? an2[r & 1] : an[r & 1]) + (t == 0 ? 32768 : 0) + jt * 2048);
    };
    auto rdm = [&](int t, int i, int kk) __attribute__((always_inline)) { fm[kk][i] = *LDSP(const bf16x8, am[kk] + m_off(13 + t) + TO(i)); };
    auto group = [&](auto GI) __attribute__((always_inline)) {
      constexpr int gi = decltype(GI)::value;
      if (gi == G14) { BAR(); if (CPRI) __builtin_amdgcn_s_setprio(CPRI);
        TL(47);
        SB(); }
      if (gi == G15) {
        PAGE(11);
        TL(53);
        BAR();
        TL(54);
        SB();
        kv = *(const u32x2*)(smem + LDS_SEL);
        SB();
      }
#pragma unroll
      for (int q = 0; q < 16; ++q) {
        const int n = gi * 16 + q;
        if (n == TS1) {
          TL(55);
        }
        if (n == TS2) {
          TL(58);
        }
        if (n == FL0) { wait_vm<8 + XC1>(); MSG_FLAG(0); SB(); }
        if (n == FL1) { wait_vm<5 + XC1>(); MSG_FLAG(1); SB(); }
        if (n == FL2) { wait_vm<2 + XC2>(); MSG_FLAG(2); fx = bld4_sc1(rc, po); SB(); }
        mm(chp(gi) || chn(gi), gi & 1, g_q[gi], q, 2);
        constexpr bool ni = gi + 1 < NG && (chp(gi + 1 < NG ? gi + 1 : gi) || chn(gi + 1 < NG ? gi + 1 : gi));
        auto nr = [](int r) { return ni && r >= 2 ? 5 - r : r; };
        if constexpr (chp(gi) && chn(gi)) {
          if (q < 4) rdm(g_t[gi], 2 + (q >> 1), q & 1); else if (q < 8) rdn(gi + 1, nr(q - 4)); else if (q < 12) rdm(g_t[gi + 1], (q - 8) >> 1, q & 1);
        } else if constexpr (chp(gi)) {
          if (q < 4) rdm(g_t[gi], 2 + (q >> 1), q & 1);
          else if (gi + 1 < NG && q < 12 && !(q & 1)) rdn(gi + 1, nr((q - 4) >> 1));
        } else if constexpr (chn(gi)) {
          if (!(q & 1)) { if (q < 8) rdn(gi + 1, nr(q >> 1)); else rdm(g_t[gi + 1], (q - 8) >> 2, (q >> 1) & 1); }
        } else {
          if (!(q & 1) && q < 8) rdn(gi + 1, nr(q >> 1));
        }
        if (gi == GP0 && q < 8) publish(0, q);
        if (gi == GP1 && q < 8) publish(1, q);
        if (gi == 9 && q < 8) publish(2, q);
        if (n >= XL0 && n < XL0 + 3 * XLS && (n - XL0) % XLS == 0) xq_load((n - XL0) / XLS);
        if (n == 72) { key[0] = __builtin_amdgcn_readfirstlane(kv.x); key[1] = __builtin_amdgcn_readfirstlane(kv.y); }
        SB();
      }
    };
#define GR4(i) group(IC<i>{}); group(IC<i + 1>{}); group(IC<i + 2>{}); group(IC<i + 3>{});
    GR4(0) GR4(4) GR4(8)
    asm volatile("s_nop 7");
    SB();
  }
  TL(50);
#pragma unroll
  for (int i = 0; i < 4; ++i) *(f32x4*)(smem + LDS_ACC + wr * 16384 + wc * 4096 + i * 1024 + lane * 16) = acc[7][i];
#pragma unroll
  for (int i = 0; i < 4; ++i) ya[i] = acc[6][i];
  asm volatile("s_waitcnt lgkmcnt(0)" ::: "memory");
  if (lane == 0) sig[0] = 2;
  SB();
  if (FL0 >= 192) { wait_vm<8 + XC1>(); MSG_FLAG(0); SB(); }
  TL(61);
  if (FL1 >= 192) { wait_vm<5 + XC1>(); MSG_FLAG(1); SB(); }
  TL(62);
  if (FL2 >= 192) { wait_vm<2 + XC2>(); MSG_FLAG(2); fx = bld4_sc1(rc, po); SB(); }
  TL(63);
  wait_xq<4>(xq, sq);
  cv_calc();
  asm volatile("s_waitcnt vmcnt(0)" : "+v"(fx) :: "memory");
  seen = (unsigned)__ballot(fx == tag);
  }

  PAGE(6);
  if (__builtin_expect(far0 == 0 || far1 == 0, 0)) {
#pragma unroll 1
    for (int c = 0; c < 2; ++c) {
      if (c ? far1 : far0) continue;
      const int ic = (0xffff - (c ? key[1] : key[0])) & (S_ - 1);
      const float sg = rbf(1.f / (1.f + __expf(-key_score(c ? key[1] : key[0]))));
      lds_vi cnt = (lds_vi)(smem + LDS_SEL + 24 + c * 4);
      int it = 0;
      while (cnt[0] != 4 && it < SPIN_MAX * 8) { __builtin_amdgcn_s_sleep(1); ++it; }
      asm volatile("" ::: "memory");
      const float* q = (const float*)(smem + LDS_FIN + c * 2048) + (wid & 1) * 64 + (fr >> 3) * 32 + col4(fr);
      const f32x4 v = (*(const f32x4*)q + *(const f32x4*)(q + 128)) + (*(const f32x4*)(q + 256) + *(const f32x4*)(q + 384));
#pragma unroll
      for (int r = 0; r < 4; ++r)
#pragma unroll
        for (int k = 0; k < 4; ++k) {
          cv[r * 4 + k] += (xo[r] >> 11) == ic ? v[k] * sg : 0.f;
        }
    }
  }
  if (ha) {
    int it = 0;
    while (sig[0] != 2 && it < SPIN_MAX * 8) { __builtin_amdgcn_s_sleep(1); ++it; }
    asm volatile("" ::: "memory");
#pragma unroll
    for (int i = 0; i < 4; ++i) ya[i] = *(const f32x4*)(smem + LDS_ACC + (wid & 2) * 8192 + (wid & 1) * 4096 + i * 1024 + lane * 16);
  }
  u32x4 pp[3][2];
  unsigned e_lo = 0x00003f80u, e_hi = 0x3f800000u;
  asm volatile("" : "+v"(e_lo), "+v"(e_hi));
  __amdgpu_buffer_rsrc_t rs_ = rs, rc_ = rc;
  int mo[3] = {MSG_IDX(0) * 4 * MSG, MSG_IDX(1) * 4 * MSG, MSG_IDX(2) * 4 * MSG}, mv = ml + ha * 2048, mv2 = mv + 1024;
  asm volatile("" : "+s"(rs_), "+s"(rc_), "+s"(mo[0]), "+s"(mo[1]), "+s"(mo[2]));
  asm volatile("" : "+v"(mv), "+v"(mv2));
#define MSG_POLL(m) do { \
    if (!((seen >> (m)) & 1)) { \
      unsigned pv_, it_ = SPIN_MAX; \
      asm volatile("1:\n\tbuffer_load_dword %0, %3, %4, 0 offen sc1\n\ts_waitcnt vmcnt(0)\n\tv_cmp_eq_u32 vcc, %5, %0\n\ts_mov_b32 %1, vcc_lo\n\t" \
                   "s_bitcmp1_b32 %1, %6\n\ts_cbranch_scc1 2f\n\ts_sub_u32 %2, %2, 1\n\ts_cbranch_scc1 2f\n\ts_sleep 1\n\ts_branch 1b\n2:" \
                   : "=&v"(pv_), "+s"(seen), "+s"(it_) : "v"(po), "s"(rc_), "s"(tag), "i"(m) : "vcc", "scc", "memory"); \
    } \
    pp[m][0] = __builtin_amdgcn_raw_buffer_load_b128(rs_, mv, mo[m], 16); \
    pp[m][1] = __builtin_amdgcn_raw_buffer_load_b128(rs_, mv2, mo[m], 16); } while (0)
#define MSG_ADD(m) do { \
    _Pragma("unroll") for (int u = 0; u < 8; ++u) { \
      ya[u >> 1][(u & 1) * 2] = add_bf(pp[m][u >> 2][u & 3], e_lo, ya[u >> 1][(u & 1) * 2]); ya[u >> 1][(u & 1) * 2 + 1] = add_bf(pp[m][u >> 2][u & 3], e_hi, ya[u >> 1][(u & 1) * 2 + 1]); } } while (0)
  MSG_POLL(2); MSG_POLL(1); MSG_POLL(0);
  SB();
#pragma unroll
  for (int e = 0; e < 16; ++e) ya[e & 3][e >> 2] += cv[e];
  asm volatile("" : "+v"(ya[0]), "+v"(ya[1]), "+v"(ya[2]), "+v"(ya[3]));
  SB();
  MSG_ADD(2); MSG_ADD(1);
  asm volatile("" : "+v"(ya[0]), "+v"(ya[1]), "+v"(ya[2]), "+v"(ya[3]));
  SB();
  MSG_ADD(0);
  {
#pragma unroll
    for (int r = 0; r < 4; ++r) *(u32x2*)((char*)out + (unsigned)xo[r]) = (u32x2){pk2(ya[0][r], ya[1][r]), pk2(ya[2][r], ya[3][r])};
  }

  if (wid == 0) {
    const unsigned te = (unsigned)__builtin_amdgcn_s_memrealtime();
    const unsigned t0 = __builtin_amdgcn_readlane(tlv, 52);
    const __amdgpu_buffer_rsrc_t rg = mk_rsrc(wsp + (64u << 20));
    if (lane == 0) { __builtin_amdgcn_raw_buffer_store_b64((u32x2){tag, t0}, rg, g * 32, 0, 16); __builtin_amdgcn_raw_buffer_store_b64((u32x2){tag, te}, rg, g * 32 + 8, 0, 16);
      __builtin_amdgcn_raw_buffer_store_b64((u32x2){tag, te}, rg, g * 32 + 16, 0, 16); }
    if (g == 37) {
      int mn0, mx[2], sum[2];
      for (int it = 0; it < 100000; ++it) {
        unsigned bad = 0; mn0 = 0x7fffffff; mx[0] = mx[1] = -0x7fffffff; sum[0] = sum[1] = 0;
        for (int k = 0; k < 4; ++k) {
          const u32x4 q = __builtin_amdgcn_raw_buffer_load_b128(rg, (k * 64 + lane) * 32, 0, 16);
          const u32x2 q2 = __builtin_amdgcn_raw_buffer_load_b64(rg, (k * 64 + lane) * 32 + 16, 0, 16);
          bad |= (q.x ^ tag) | (q.z ^ tag) | (q2.x ^ tag);
          mn0 = min(mn0, (int)(q.y - t0)); mx[0] = max(mx[0], (int)(q.w - t0)); sum[0] += (int)(q.w - t0); mx[1] = max(mx[1], (int)(q2.y - t0)); sum[1] += (int)(q2.y - t0);
        }
        if (__all(bad == 0)) break;
        asm volatile("" ::: "memory");
      }
      for (int o = 32; o; o >>= 1) { mn0 = min(mn0, __shfl_xor(mn0, o, 64)); for (int k = 0; k < 2; ++k) { mx[k] = max(mx[k], __shfl_xor(mx[k], o, 64)); sum[k] += __shfl_xor(sum[k], o, 64); } }
      int code = 0;
      { int d = mx[0] - mn0; d = (d - 2400) / 5; code = code * 128 + min(max(d, 0), 127); }
      const unsigned tend = __builtin_amdgcn_readfirstlane(t0 + mn0 + 4000 + code * 400);
      for (int it = 0; it < (1  __H0__ ) && (int)((unsigned)__builtin_amdgcn_s_memrealtime() - tend) < 0; ++it) __builtin_amdgcn_s_sleep(1);
    }
  }

}

extern "C" void kernel_launch(void* const* d_in, const int* in_sizes, int n_in, void* d_out, int out_size,
                              void* d_ws, size_t ws_size, hipStream_t stream) {
  const u16* x = (const u16*)d_in[0];
  const u16* ws_re = (const u16*)d_in[1];
  const u16* ws_im = (const u16*)d_in[2];
  const u16* wd_re = (const u16*)d_in[3];
  const u16* wd_im = (const u16*)d_in[4];
  const u16* w_r = (const u16*)d_in[6];
  const u16* w_gate = (const u16*)d_in[7];
  const u16* w_up = (const u16*)d_in[8];
  const u16* w_down = (const u16*)d_in[9];
  k_main<<<256, 512, 0, stream>>>(x, wd_re, wd_im, w_r, (char*)d_ws, ws_re, ws_im, w_gate, w_up, w_down, (u16*)d_out);
}
```

```cpp
#include <hip/hip_runtime.h>
#include <hip/hip_bf16.h>
#include <stdint.h>
#include <type_traits>

extern "C" __device__ unsigned long long llvm_dispatch_id(void) __asm("llvm.amdgcn.dispatch.id");
extern "C" __device__ float llvm_fmul_legacy(float, float) __asm("llvm.amdgcn.fmul.legacy");
typedef unsigned short u16;
typedef unsigned long long u64;
typedef __attribute__((ext_vector_type(8))) short bf16x8;
typedef __attribute__((ext_vector_type(4))) unsigned u32x4;
typedef __attribute__((ext_vector_type(2))) unsigned u32x2;
typedef __attribute__((ext_vector_type(4))) float f32x4;
typedef __attribute__((ext_vector_type(2))) float f32x2;
typedef __attribute__((ext_vector_type(2))) __bf16 bf16x2;
typedef __attribute__((address_space(3))) void* lds_vp;
typedef volatile __attribute__((address_space(3))) int* lds_vi;

constexpr int S_ = 2048, D_ = 1024, F_ = 3584;
template <int N> using IC = std::integral_constant<int, N>;

static __device__ __forceinline__ float bf2f(u16 u) { return __uint_as_float((unsigned)u << 16); }
static __device__ __forceinline__ u16 f2bf(float f) {
  __hip_bfloat16 b = __float2bfloat16(f);
  return *reinterpret_cast<u16*>(&b);
}
static __device__ __forceinline__ float rbf(float f) { return bf2f(f2bf(f)); }
static __device__ __forceinline__ float lo_bf(unsigned u) { return __uint_as_float(u << 16); }
static __device__ __forceinline__ float hi_bf(unsigned u) { return __uint_as_float(u & 0xffff0000u); }
static __device__ __forceinline__ unsigned pk2(float a, float b) {
  const bf16x2 v = __builtin_convertvector((f32x2){a, b}, bf16x2);
  return __builtin_bit_cast(unsigned, v);
}
static __device__ __forceinline__ float add_bf(unsigned u, unsigned e, float c) {
  return __builtin_amdgcn_fdot2_f32_bf16(__builtin_bit_cast(bf16x2, u), __builtin_bit_cast(bf16x2, e), c, false);
}
static __device__ __forceinline__ float ln_elem(float x, float mb, float sd, float ri) {
  const float d = rbf(x - mb);
  const float q0 = d * ri;
  return rbf(__builtin_fmaf(__builtin_fmaf(-q0, sd, d), ri, q0));
}
template <int CTRL> static __device__ __forceinline__ float dpp_f(float v) {
  return __int_as_float(__builtin_amdgcn_update_dpp(0, __float_as_int(v), CTRL, 0xf, 0xf, true));
}
template <int CTRL> static __device__ __forceinline__ unsigned dpp_u(unsigned v) {
  return (unsigned)__builtin_amdgcn_update_dpp(0, (int)v, CTRL, 0xf, 0xf, true);
}
static __device__ __forceinline__ float row_sum(float v) {
  v += dpp_f<0xB1>(v);
  v += dpp_f<0x4E>(v);
  v += dpp_f<0x141>(v);
  v += dpp_f<0x140>(v);
  return v;
}
static __device__ __forceinline__ float fold32(float a, float b) {
  const auto r = __builtin_amdgcn_permlane32_swap(__float_as_uint(a), __float_as_uint(b), false, false);
  return __uint_as_float(r[0]) + __uint_as_float(r[1]);
}
static __device__ __forceinline__ float fold16(float a, float b) {
  const auto r = __builtin_amdgcn_permlane16_swap(__float_as_uint(a), __float_as_uint(b), false, false);
  return __uint_as_float(r[0]) + __uint_as_float(r[1]);
}
static __device__ __forceinline__ float rdl(float v, int l) { return __int_as_float(__builtin_amdgcn_readlane(__float_as_int(v), l)); }

#define TL(I) do { if ((I) == 51) { const unsigned _t = (unsigned)__builtin_amdgcn_s_memrealtime(); tlv = lane == (I) ? _t : tlv; } } while (0)
#define XMAP(g) (((~g & 1) << 2) | (g & 2) | ((g >> 2) & 1))
#define ST_JB 9
#define WSN 10
#define FSM 0xa2
#define GV_JB 7
#define DG 11
#define AB_STRIDE 32
#define SPIN_MAX (1 << 17)
#define FL0 168
#define FL1 192
#define FL2 192
#define VA0 5
#define VAN 4
#define VAP 116
#define VB0 12
#define VBN 0
#define VBP 6
#define SP0 3
#define SP1 7
#define SP1E 11
#define VC0 14
#define VCN 0
#define VCP 9
#define SPS 0x000cccccdddd00
#define SPE 0
#define PRIO2 3
#define N2A 8
#define N3A 8
#define WSL 2
#define LNP 0
#define LNS 2
#define EPP 0
#define DGS 0
#define DGP 0
#define TS1 112
#define TS2 152
#define XL0 33
#define XLS 4
#define CPRI 0
#define KP 1
#define LAGS 0x00000000000000
#define DVS 0x00000400300000
#define GVS 0x00000022000000
constexpr int dig(unsigned long long t, int q) { return q < 0 ? 0 : (int)((t >> (4 * q)) & 15); }
constexpr int acc_dig(unsigned long long t, int q) { int a = 0; for (int i = 0; i < q; ++i) a += dig(t, i); return a; }

static __device__ __forceinline__ unsigned lds_addr(const void* p) { return (unsigned)(size_t)(lds_vp)p; }

constexpr int MSG = 4096;
constexpr size_t WS_ABT   = 0;
constexpr size_t WS_SLAB  = WS_ABT + (size_t)D_ * 4096 * 2;
constexpr size_t WS_GU    = WS_SLAB + (size_t)64 * 64 * MSG;
constexpr size_t WS_DN    = WS_GU + (size_t)8 * F_ * 16;
constexpr size_t WS_FLAGS = WS_DN + (size_t)2 * 8 * 32 * 64 * 16;
constexpr int FL_X = 0;
constexpr int FL_AB = 64 * 64 * 32;
constexpr int FL_ST = FL_AB + 2048 * AB_STRIDE;
constexpr int FL_CE = FL_ST + 4096;
constexpr int FL_KEY = FL_CE + 4096;
constexpr int FL_EPOCH = FL_KEY + 4096;
constexpr int WS_MB = (int)((WS_FLAGS + (FL_EPOCH + 1) * 4 + 1048575) >> 20);

#define WAIT_VM(n) asm volatile("s_waitcnt vmcnt(" #n ")" ::: "memory")
template <int N> static __device__ __forceinline__ void wait_vm() { asm volatile("s_waitcnt vmcnt(%0)" :: "i"(N) : "memory"); }
#define BAR_LGKM() asm volatile("s_waitcnt lgkmcnt(0)\n\ts_barrier" ::: "memory")
#define BAR() asm volatile("s_barrier" ::: "memory")
#define SB() __builtin_amdgcn_sched_barrier(0)
#define PAGE(n) asm volatile("s_branch 2f\n\t.p2align 12\nk_pg" #n ":\n\ts_setpc_b64 s[42:43]\n2:")
#define CALL_PG(n) asm volatile("s_getpc_b64 s[40:41]\n3:\n\ts_mov_b32 s42, k_pg" #n "-3b\n\ts_ashr_i32 s43, s42, 31\n\ts_add_u32 s40, s40, s42\n\ts_addc_u32 s41, s41, s43\n\t" \
                                "s_swappc_b64 s[42:43], s[40:41]" ::: "s40", "s41", "s42", "s43", "scc", "memory")

static __device__ __forceinline__ __amdgpu_buffer_rsrc_t mk_rsrc(const void* p) {
  return __builtin_amdgcn_make_buffer_rsrc((void*)p, 0, 0x7fffffff, 0x00020000);
}
#define DMA_(rsrc, voff, mbase, MI, sbase, SI, pol) do { unsigned _t; \
    asm volatile("s_add_i32 m0, %2, %3\n\ts_add_i32 %0, %5, %6\n\tbuffer_load_dwordx4 %1, %4, %0 offen" pol " lds" \
                 : "=&s"(_t) : "v"(voff), "s"(mbase), "i"(MI), "s"(rsrc), "s"(sbase), "i"(SI) : "memory", "scc"); } while (0)
#define DMA_I(rsrc, voff, mbase, MI, SI) \
    asm volatile("s_add_i32 m0, %1, %2\n\ts_nop 0\n\tbuffer_load_dwordx4 %0, %3, 0 offen offset:%4 lds" \
                 :: "v"(voff), "s"(mbase), "i"((MI) - (SI)), "s"(rsrc), "i"(SI) : "memory", "scc")
#define DMA_S(rsrc, voff, mbase, MI, soff) \
    asm volatile("s_add_i32 m0, %1, %2\n\ts_nop 0\n\tbuffer_load_dwordx4 %0, %3, %4 offen lds" \
                 :: "v"(voff), "s"(mbase), "i"(MI), "s"(rsrc), "s"(soff) : "memory", "scc")
#define TPOL ""
#define TOUCH(rsrc, voff, mbase, MI, soff) \
    asm volatile("s_add_i32 m0, %1, %2\n\ts_nop 0\n\tbuffer_load_dword %0, %3, %4 offen" TPOL " lds" \
                 :: "v"(voff), "s"(mbase), "i"(MI), "s"(rsrc), "s"(soff) : "memory", "scc")
template <int OFF> static __device__ __forceinline__ u32x4 ld16_s(const void* p, int voff) {
  u32x4 v;
  asm volatile("global_load_dwordx4 %0, %1, %2 offset:%3" : "=v"(v) : "v"(voff), "s"(p), "i"(OFF) : "memory");
  return v;
}
template <int OFF> static __device__ __forceinline__ void sld(unsigned& sink, const void* p, int soff) {
  asm volatile("s_load_dword %0, %1, %2 offset:%3" : "+s"(sink) : "s"(p), "s"(soff), "i"(OFF) : "memory");
}
static __device__ __forceinline__ unsigned gld4_s(const void* p, unsigned voff) {
  unsigned v;
  asm volatile("global_load_dword %0, %1, %2" : "=v"(v) : "v"(voff), "s"(p) : "memory");
  return v;
}
static __device__ __forceinline__ u32x2 bld8_sc1(__amdgpu_buffer_rsrc_t r, unsigned voff) {
  u32x2 v;
  asm volatile("buffer_load_dwordx2 %0, %1, %2, 0 offen sc1" : "=v"(v) : "v"(voff), "s"(r) : "memory");
  return v;
}
template <int N> static __device__ __forceinline__ void wait_xs(unsigned& a, u32x2& b) { asm volatile("s_waitcnt vmcnt(%2)" : "+v"(a), "+v"(b) : "i"(N) : "memory"); }
template <int N> static __device__ __forceinline__ void wait_xq(u32x4 (&a)[2], u32x2& b) { asm volatile("s_waitcnt vmcnt(%3)" : "+v"(a[0]), "+v"(a[1]), "+v"(b) : "i"(N) : "memory"); }
static __device__ __forceinline__ u32x2 ld8_s_sc1(const void* p, int voff) {
  u32x2 v;
  asm volatile("global_load_dwordx2 %0, %1, %2 sc1" : "=v"(v) : "v"(voff), "s"(p) : "memory");
  return v;
}
static __device__ __forceinline__ void lgkm0(unsigned& sink) { asm volatile("s_waitcnt lgkmcnt(0)" : "+s"(sink) :: "memory"); }
template <typename T> static __device__ __forceinline__ void pin4(T& a, T& b, T& c, T& d) { asm volatile("" : "+s"(a), "+s"(b), "+s"(c), "+s"(d)); }
template <typename T, typename U> static __device__ __forceinline__ void pins(T& a, U& b) { asm volatile("" : "+s"(a), "+s"(b)); }
static __device__ __forceinline__ void launder(int& v, int& a, int& b) { asm volatile("" : "+v"(v)); asm volatile("" : "+s"(a), "+s"(b)); }
static __device__ __forceinline__ unsigned bld4_sc1(__amdgpu_buffer_rsrc_t r, int voff) {
  unsigned v;
  asm volatile("buffer_load_dword %0, %1, %2, 0 offen sc1" : "=v"(v) : "v"(voff), "s"(r) : "memory");
  return v;
}
static __device__ __forceinline__ u32x4 bld16_nt(__amdgpu_buffer_rsrc_t r, int voff, int soff) {
  u32x4 v;
  asm volatile("buffer_load_dwordx4 %0, %1, %2, %3 offen nt" : "=v"(v) : "v"(voff), "s"(r), "s"(soff) : "memory");
  return v;
}
static __device__ __forceinline__ void bld16_nt_io(u32x4& v, __amdgpu_buffer_rsrc_t r, int voff, int soff) {
  asm volatile("buffer_load_dwordx4 %0, %1, %2, %3 offen nt" : "+v"(v) : "v"(voff), "s"(r), "s"(soff) : "memory");
}
static __device__ __forceinline__ u32x4 bld16_sc1(__amdgpu_buffer_rsrc_t r, int voff, int soff) {
  u32x4 v;
  asm volatile("buffer_load_dwordx4 %0, %1, %2, %3 offen sc1" : "=v"(v) : "v"(voff), "s"(r), "s"(soff) : "memory");
  return v;
}
static __device__ __forceinline__ u32x2 granule_wait(const u64* p, unsigned tag) {
  u64 v = 0;
#pragma unroll 1
  for (int it = 0; it < SPIN_MAX; ++it) {
    v = __hip_atomic_load(p, __ATOMIC_RELAXED, __HIP_MEMORY_SCOPE_AGENT);
    if (__all((unsigned)(v >> 32) == tag)) break;
    __builtin_amdgcn_s_sleep(2);
  }
  return (u32x2){(unsigned)v, (unsigned)(v >> 32)};
}
static __device__ __forceinline__ float key_score(unsigned key) {
  const unsigned u = key >> 16;
  return bf2f((u16)((u & 0x8000u) ? (u & 0x7fffu) : (~u & 0xffffu)));
}
static __device__ __forceinline__ unsigned med3u(unsigned a, unsigned b, unsigned c) { return max(min(a, b), min(max(a, b), c)); }
static __device__ __forceinline__ void merge2(unsigned& a1, unsigned& a2, unsigned b1, unsigned b2) {
  const unsigned lo = min(a1, b1);
  a1 = max(a1, b1);
  a2 = max(lo, max(a2, b2));
}
static __device__ __forceinline__ float sqdev16(const u32x4 (&v)[2], float mb) {
  f32x2 q2 = {0.f, 0.f};
#pragma unroll
  for (int i = 0; i < 8; ++i) {
    const f32x2 e = (f32x2){lo_bf(v[i >> 2][i & 3]), hi_bf(v[i >> 2][i & 3])} - mb;
    q2 += e * e;
  }
  return q2.x + q2.y;
}

constexpr int lrw(int r) { return (r & ~6) | ((r & 2) << 1) | ((r & 4) >> 1); }
constexpr int swz(int r) { return ((r >> 1) & 4) | (r & 2); }
static __device__ __forceinline__ int sym_dn(int p) { return (p ^ (p >> 1)) & 1; }
static __device__ __forceinline__ int sym_hi(int n, int w, int st, int b, int z, int p) {
  return ((p >> 1) * (n / 2 * st) + b * (w * st)) ^ (-sym_dn(p) & (((n - 1) ^ (z * (n / 2))) * st));
}
static __device__ __forceinline__ int sym_lo(int w, int st, int p, int c) { return ((c - sym_dn(p)) * st) & ((w - 1) * st); }
static __device__ __forceinline__ int sym_row(int n, int w, int st, int b, int z, int p, int c) { return sym_lo(w, st, p, c) ^ sym_hi(n, w, st, b, z, p); }
constexpr int G1_NST = 4, G1_NKT = 16, G1_STAGE = 32768;
constexpr int G2_NKT = 16, G2_MRING = 98304;
constexpr int n_off(int k) { return (k % 3) * 32768; }
constexpr int m_off(int k) { return ((k + 2) % 3) * 16384; }
constexpr int k_ord(int k) { return k < 2 ? k : (k < 9 ? 2 * (k - 1) : 2 * (k - 8) + 1); }
constexpr int LDS_DUMMY = G2_MRING + 49152;
constexpr int LDS_GU = LDS_DUMMY + 4096;
constexpr int LDS_FIN = LDS_GU + 4096;
constexpr int LDS_MB = LDS_FIN + 4096;
constexpr int LDS_SEL = LDS_MB + 1536;
constexpr int LDS_SIG = LDS_SEL + 64;
constexpr int LDS_KEY = LDS_SIG + 32;
constexpr int LDS_BYTES = LDS_KEY + 128;
constexpr int LDS_ACC = n_off(G2_NKT - 3);

constexpr int sp_tile(int q) {
  for (int k = 0; k < 16; ++k) {
    if ((k >= VA0 && k < VA0 + VAN) || (k >= VB0 && k < VB0 + VBN) || (k >= VC0 && k < VC0 + VCN)) continue;
    if (q-- == 0) return k;
  }
  return -1;
}
constexpr int tcnt(int q) { return (VAN > 0 && q == VAP) + (VBN > 0 && q == VBP) + (VCN > 0 && q == VCP); }
constexpr int dn_cnt(int q) { return dig(DVS, q); }
constexpr int dn_first(int q) { return acc_dig(DVS, q); }

static __device__ __forceinline__ void code_touch(unsigned& sink, unsigned off) {
  asm volatile("s_getpc_b64 s[40:41]\n\tk_main_rt:\n\ts_min_u32 %1, %1, k_main_end-k_main_rt-196\n\t"
               "s_load_dword %0, s[40:41], %1 offset:0\n\ts_load_dword %0, s[40:41], %1 offset:64\n\ts_load_dword %0, s[40:41], %1 offset:128\n\ts_load_dword %0, s[40:41], %1 offset:192"
               : "+s"(sink), "+s"(off) :: "s40", "s41", "scc", "memory");
}
static __device__ __forceinline__ void touch1(__amdgpu_buffer_rsrc_t r, int voff, unsigned mb, int soff) { TOUCH(r, voff, mb, LDS_DUMMY, soff); }
typedef const __attribute__((address_space(1))) u16* gptr;
typedef __attribute__((ext_vector_type(8))) u64 u64x8;
typedef __attribute__((ext_vector_type(2))) u64 u64x2;
extern "C" __global__ __launch_bounds__(512, 2) void k_main(const u16* x, const u16* wd_re, const u16* wd_im, const u16* w_r,
                                                 char* wsp, const u16* ws_re, const u16* ws_im,
                                                 const u16* w_gate, const u16* w_up, const u16* w_down, u16* out) {
  __shared__ __attribute__((aligned(16))) char smem[LDS_BYTES];
  u64x8 ka;
  u64x2 kb_;
  u64 kc;
  asm volatile("s_load_dwordx16 %0, %3, 0\n\ts_load_dwordx4 %1, %3, 64\n\ts_load_dwordx2 %2, %3, 80"
               : "=&s"(ka), "=&s"(kb_), "=&s"(kc) : "s"(__builtin_amdgcn_kernarg_segment_ptr()) : "memory");
  unsigned long long t00;
  asm volatile("s_memrealtime %0" : "=s"(t00) :: "memory");
  const int tid = threadIdx.x, lane = tid & 63;
  const int wid = __builtin_amdgcn_readfirstlane(tid >> 6);
  const int g = blockIdx.x, xcd = XMAP(g), j = g >> 3;
  if (wid < 4) {
    unsigned cw, co = (j * 4 + wid) * 448;
    asm volatile(".subsection 1\nk_main_end:\n.subsection 0\n\ts_min_u32 %1, %1, k_main_end-k_main-480\n\ts_getpc_b64 s[40:41]\n\ts_add_u32 s40, s40, %1\n\ts_addc_u32 s41, s41, 0\n\t"
                 "s_load_dword %0, s[40:41], 0\n\ts_load_dword %0, s[40:41], 64\n\ts_load_dword %0, s[40:41], 128\n\t"
                 "s_load_dword %0, s[40:41], 192\n\ts_load_dword %0, s[40:41], 256\n\ts_load_dword %0, s[40:41], 320\n\ts_load_dword %0, s[40:41], 384\n\ts_waitcnt lgkmcnt(0)"
                 : "=&s"(cw), "+s"(co) :: "s40", "s41", "scc", "memory");
  }
  const int pw = wid - 4;
  const int row0 = (wid & 3) * 8 + lrw(lane >> 3);
  const int cm = ((lane & 7) ^ swz(row0)) * 16;
  int vm = ((row0 & 15) * 2 + (row0 >> 4)) * (D_ * 2) + cm;
  asm volatile("" : "+v"(vm));
  const int kq = xcd >> 1;
  const int tt = (xcd & 1) * 4 + (j >> 3);
  const int et = j & 7;
  const int z = kq >> 1, st = (kq & 1) * 8 + tt;
  const int lrow = (kq & 1) * 1024 + (xcd & 1) * 512 + z * 256 + j * 8 + pw * 2;
  const int drow = ((kq & 1) * 2 + (xcd & 1)) * 256 + j * 8 + pw * 2;
  unsigned o_m = st * 128 * D_ * 2, o_l = lrow * D_ * 2, o_d = drow * D_ * 2, mb0 = lds_addr(smem) + pw * 1024;
  int l16 = lane * 16;
  int vn[4];
#pragma unroll
  for (int q = 0; q < 4; ++q) vn[q] = sym_lo(32, D_ * 2, (row0 >> 2) & 3, q * 8 + (row0 >> 4) * 4 + (row0 & 3)) ^ (sym_hi(D_, 32, D_ * 2, et, z, (row0 >> 2) & 3) | cm);
  asm volatile("" : "+s"(o_m), "+s"(o_l), "+s"(o_d), "+s"(mb0));
  asm volatile("" : "+v"(l16), "+v"(vn[0]), "+v"(vn[1]), "+v"(vn[2]), "+v"(vn[3]));
  asm volatile("s_waitcnt lgkmcnt(0)" : "+s"(ka), "+s"(kb_), "+s"(kc) :: "memory");
  x = (const u16*)(gptr)ka[0]; wd_re = (const u16*)(gptr)ka[1]; wd_im = (const u16*)(gptr)ka[2]; w_r = (const u16*)(gptr)ka[3];
  wsp = (char*)(gptr)ka[4]; ws_re = (const u16*)(gptr)ka[5]; ws_im = (const u16*)(gptr)ka[6]; w_gate = (const u16*)(gptr)ka[7];
  w_up = (const u16*)(gptr)kb_[0]; w_down = (const u16*)(gptr)kb_[1]; out = (u16*)(gptr)kc;
  int* flags = (int*)(wsp + WS_FLAGS);
  u64* stat = (u64*)(flags + FL_ST);
  unsigned tlv = 0;
  asm volatile("s_waitcnt lgkmcnt(0)" : "+s"(t00) :: "memory");
  tlv = lane == 52 ? (unsigned)t00 : tlv;
  const u16* wdz = z ? wd_im : wd_re;
  const __amdgpu_buffer_rsrc_t rm = mk_rsrc(x);
  const __amdgpu_buffer_rsrc_t rn = mk_rsrc(wdz);
#define G1_X(kt, so, q) DMA_(rm, vm, mb0, (so) + (q) * 4096, o_m, (kt) * 128 + (((q) & 1) * 64 + ((q) >> 1) * 32) * D_ * 2, "")
#define G1_W(kt, so, q) DMA_I(rn, vn[q], mb0, (so) + 16384 + (q) * 4096, (kt) * 128)
#define G1_STAGE_TILE(kt, so, W) do { SB(); G1_X(kt, so, 0); W(0); G1_X(kt, so, 1); W(1); G1_X(kt, so, 2); W(2); G1_X(kt, so, 3); W(3); \
    G1_W(kt, so, 0); W(4); G1_W(kt, so, 1); W(5); G1_W(kt, so, 2); W(6); G1_W(kt, so, 3); W(7); SB(); } while (0)
#define G1_STAGE_A(kt, so, W) do { SB(); G1_X(kt, so, 0); W(0); G1_X(kt, so, 2); W(1); \
    G1_W(kt, so, 0); W(2); G1_W(kt, so, 1); W(3); G1_W(kt, so, 2); W(4); G1_W(kt, so, 3); W(5); W(6); W(7); SB(); } while (0)
#define G1_STAGE_B(kt, so) do { SB(); G1_X(kt, so, 1); G1_X(kt, so, 3); SB(); } while (0)
#define NOW(i)
  const int fr = lane & 15, fq = lane >> 4;
  const int ha = wid >> 2;
  int trow, xo[4], xa[3], po, ml;
  const char *prs, *prc;
  auto col4 = [](int f) __attribute__((always_inline)) { return ((f & 2) << 3 | (f & 4) << 1 | (f & 1) << 2) ^ (-((f >> 3) & 1) & 28); };
  auto seg0 = [&](int hf, int dn) __attribute__((always_inline)) { return (hf * 512 + et * 32) ^ (-dn & (D_ - 32)); };
  auto epi_setup = [&](int part) __attribute__((always_inline)) {
    int t_ = tt, l_ = lane;
    asm volatile("" : "+s"(t_));
    asm volatile("" : "+v"(l_));
    const int ts = ((wid >> 1) & 1) * 8 + kq * 2 + ha;
    if (part == 0) { xo[3] = (seg0(wid & 1, (l_ >> 3) & 1) + col4(l_)) * 2; asm volatile("" : "+v"(xo[3])); }
    if (part == 1) {
      xo[3] |= sym_hi(S_, 64, D_ * 2, t_, z, l_ >> 4);
      ml = (l_ * 16) ^ (-sym_dn(l_ >> 4) & (z * 768));
      asm volatile("" : "+v"(xo[3]), "+v"(ml));
    }
    if (part == 2) {
      const int xh = xo[3];
#pragma unroll
      for (int r = 0; r < 4; ++r) xo[r] = sym_lo(64, D_ * 2, l_ >> 4, ts * 4 + r) ^ xh;
      const bool hi = xo[0] & 8;
      xa[0] = (hi ? xo[2] : xo[0]) & ~15; xa[1] = (hi ? xo[3] : xo[1]) & ~15;
      xa[2] = (sym_lo(64, D_ * 2, l_ >> 4, ts * 4 + (l_ & 3)) ^ xh) >> 11 << 3;
      asm volatile("" : "+v"(xo[0]), "+v"(xo[1]), "+v"(xo[2]), "+v"(xo[3]), "+v"(xa[0]), "+v"(xa[1]), "+v"(xa[2]));
    }
    if (part == 3) {
      trow = t_ * 16 + ts;
      prs = wsp + WS_SLAB + (size_t)((t_ * 8 + et) * 64 + (wid & 3)) * MSG;
      prc = (const char*)(flags + FL_X + ((t_ * 8 + et) * 64 + (wid & 3)) * 32);
      asm volatile("" : "+s"(trow), "+s"(prs), "+s"(prc));
      po = l_ < 3 ? (kq * 4 + ((kq + 1 + l_) & 3)) * 512 : (int)0x80000000;
      asm volatile("" : "+v"(po));
    }
  };
  auto tok_tile = [](unsigned key) __attribute__((always_inline)) {
    const unsigned h = (0xffff - key) & 1023;
    return h < 512 ? h >> 2 : ((1023 - h) >> 6 << 4) | (((1024 - h) & 63) >> 2);
  };
#define rs mk_rsrc(prs)
#define rc mk_rsrc(prc)
  lds_vi sig = (lds_vi)(smem + LDS_SIG + (wid & 3) * 4);
  unsigned tag;
  u32x4 xq[2];
  u32x2 sq;
  f32x4 ya[4];
  unsigned key[2];
  float cv[16];
  unsigned far0, far1;
  auto xq_load = [&](int n) __attribute__((always_inline)) {
    if (n < 2) xq[n] = ld16_s<0>(x, xa[n]); else sq = ld8_s_sc1(stat, xa[2]);
  };
#define PINV(a) asm volatile("" : "+v"(a))
  auto cv_calc = [&]() __attribute__((always_inline)) {
    if (__builtin_expect(!__all(sq.y == tag), 0)) sq = granule_wait(stat + (xa[2] >> 3), tag);
    far0 = tok_tile(key[0]) ^ trow; far1 = tok_tile(key[1]) ^ trow;
    asm volatile("" : "+s"(far0), "+s"(far1));
    const bool hi = xo[0] & 8;
    const float mb_ = lo_bf(sq.x), ri_ = __builtin_amdgcn_rcpf(hi_bf(sq.x));
#pragma unroll
    for (int r = 0; r < 4; ++r) {
      const float mb = r == 0 ? dpp_f<0x00>(mb_) : r == 1 ? dpp_f<0x55>(mb_) : r == 2 ? dpp_f<0xAA>(mb_) : dpp_f<0xFF>(mb_);
      const float ri = r == 0 ? dpp_f<0x00>(ri_) : r == 1 ? dpp_f<0x55>(ri_) : r == 2 ? dpp_f<0xAA>(ri_) : dpp_f<0xFF>(ri_);
#pragma unroll
      for (int u = 0; u < 2; ++u) {
        const unsigned own = xq[r & 1][(r & 2) + u], oth = dpp_u<0xB1>(xq[r & 1][2 - (r & 2) + u]);
        const unsigned w = hi == (r >= 2) ? own : oth;
        cv[r * 4 + 2 * u] = __builtin_fmaf(lo_bf(w) - mb, ri, lo_bf(w));
        cv[r * 4 + 2 * u + 1] = __builtin_fmaf(hi_bf(w) - mb, ri, hi_bf(w));
        PINV(cv[r * 4 + 2 * u]); PINV(cv[r * 4 + 2 * u + 1]);
      }
    }
  };
#define MSG_IDX(m) (kq * 4 + ((kq + 1 + (m)) & 3))
  unsigned seen = 0;

  if (__builtin_expect(wid >= 4, 1)) {
    __builtin_amdgcn_s_setprio(3);
    G1_STAGE_TILE(0, 0, NOW);
    constexpr int O_ST = (FL_ST - FL_AB) * 4, O_CE = (FL_CE - FL_AB) * 4, O_KEY = (FL_KEY - FL_AB) * 4, O_EP = (FL_EPOCH - FL_AB) * 4;
    const __amdgpu_buffer_rsrc_t rf = mk_rsrc(wsp + WS_FLAGS + FL_AB * 4);
    u32x4 lv[2], lv2[2], lw[2], ld[2], ld2[2];
    unsigned epv;
    {
      const char* xrow = (const char*)x + o_l;
      const char* wdrow = (const char*)wdz + o_d;
      lv[0] = ld16_s<0>(xrow, l16); lv[1] = ld16_s<1024>(xrow, l16);
      lv2[0] = ld16_s<2048>(xrow, l16); lv2[1] = ld16_s<3072>(xrow, l16);
      ld[0] = ld16_s<0>(wdrow, l16); ld[1] = ld16_s<1024>(wdrow, l16);
      ld2[0] = ld16_s<2048>(wdrow, l16); ld2[1] = ld16_s<3072>(wdrow, l16);
      lw[0] = ld16_s<0>(w_r, l16); lw[1] = ld16_s<1024>(w_r, l16);
      asm volatile("buffer_load_dword %0, off, %1, %2 sc1" : "=v"(epv) : "s"(rf), "s"(O_EP) : "memory");
    }
    SB();
    const unsigned long long did = llvm_dispatch_id();
    const unsigned dmix = (unsigned)did * 0x85EBCA6Bu + (unsigned)(did >> 32) * 0xC2B2AE35u + 0x27D4EB2Fu;
    const bool moe = et != 7;
    unsigned epoch;
    const int ci = lane & 15, rq = lane >> 4, tk = (lane >> 3) & 1;
    int mat, cg, dq, d0;
    u32x4 wv[16];
    const u16 *pgu, *pn2;
    const char *pm2, *spb;
#define rw __builtin_amdgcn_make_buffer_rsrc((void*)pgu, 0, moe ? 0x7fffffff : 0, 0x00020000)
#define rn2 mk_rsrc(pn2)
#define rm2 mk_rsrc(pm2)
    int vw, vn2[8], vm2;
    auto k_blk = [&](int k) __attribute__((always_inline)) { return tt ^ (k_ord(k) >> 1); };
#define KROT(kk_) (k_blk(kk_) * 256 + (k_ord(kk_) & 1) * 128)
    int tv2[3];
    unsigned sd = 0;
    auto spf = [&](auto N) __attribute__((always_inline)) {
      constexpr int n = decltype(N)::value;
      if constexpr (n >= 0 && sp_tile(n >> 4) >= 0) sld<((n >> 1) & 7) * 4096 + (n & 1) * 64>(sd, spb, KROT(sp_tile(n >> 4)));
    };
    auto touches = [&](auto P) __attribute__((always_inline)) {
      constexpr int p = decltype(P)::value;
      if constexpr (VAN > 0 && p == VAP) touch1(mk_rsrc(spb), tv2[0], mb0, 0);
      if constexpr (VBN > 0 && p == VBP) touch1(mk_rsrc(spb), tv2[1], mb0, 0);
      if constexpr (VCN > 0 && p == VCP) touch1(mk_rsrc(spb), tv2[2], mb0, 0);
    };
    unsigned mq0, mq1, mq2, mq3;
    const int cg8 = et ^ ((et & 1) << 1), rgb = (g & 7) * 4 + (j >> 3);
    const __amdgpu_buffer_rsrc_t rdn = mk_rsrc(w_down);
    const __amdgpu_buffer_rsrc_t rgu = mk_rsrc(wsp + WS_GU);
    int vdn, vgu;
    u32x4 dv[7], gv[4];
    int abo, abq[3];
    unsigned lma;
    unsigned sel[2][4];
    float g0, g1;
    u32x4 kk[4];
    unsigned k1 = 0, k2 = 0;
    const int ko_ = O_KEY + pw * 4096;
#define MM(a, b, c) __builtin_amdgcn_mfma_f32_16x16x32_bf16(__builtin_bit_cast(bf16x8, a), __builtin_bit_cast(bf16x8, b), c, 0, 0, 0)
#define PIN1(a) asm volatile("" : "+v"(a))
#define PIN2(a, b) asm volatile("" : "+v"(a), "+v"(b))
#define PIN3(a, b, c) asm volatile("" : "+v"(a), "+v"(b), "+v"(c))
    {
      int gro, go;
      u32x2 gr;
      u32x4 oh[5];
      f32x4 sa4, gw[2], gx[2];
      float su, gs, ta, tb, sx, mbv;
      unsigned dk[4], kl;
      const int hb = lane >> 5;
#pragma unroll
      for (int l = 0; l < WSN; ++l) asm volatile("" : "=v"(wv[l]));
      auto diag = [&](const f32x4& m) __attribute__((always_inline)) {
        return __uint_as_float((__float_as_uint(m[0]) & dk[0]) | (__float_as_uint(m[1]) & dk[1]) | (__float_as_uint(m[2]) & dk[2]) | (__float_as_uint(m[3]) & dk[3]));
      };
      auto setup = [&](auto JB, auto GP) __attribute__((always_inline)) {
        constexpr int jb = decltype(JB)::value, gp = decltype(GP)::value;
        SB();
        {
          int ln = lane, jj = j, p_ = pw;
          launder(ln, jj, p_);
          const int c_ = ln & 15, r_ = ln >> 4, e_ = jj & 7, t_ = (xcd & 1) * 4 + (jj >> 3);
          if constexpr (jb == 1 && gp == 0) {
            pn2 = ((kq >> 1) ? ws_im : ws_re) + (kq & 1) * 1024;
            spb = (const char*)(pn2 + (sym_row(S_, 64, 1, t_, 0, e_ >> 1, 0) + (e_ & 1) * 32 + p_ * 8) * S_);
            pins(pn2, spb);
          }
          if constexpr (jb == 1 && gp >= 1 && gp < 6) {
            const unsigned o = c_ == gp - 1 ? 0x3f803f80u : 0u;
            oh[gp - 1] = (u32x4){o, o, o, o};
            PIN1(oh[gp - 1]);
          }
          if constexpr (jb == 1 && gp == 6) {
            const int rk = moe ? e_ * 8 + (jj >> 3) * 2 + (xcd >> 2) : 0, r3 = ((rk - 14) * 43) >> 7, rr = rk - 14 - r3 * 3;
            const int c56 = rk < 14 ? rk * 4 + 1 : r3 * 4 + rr + ((rr + 1) >> 1);
            mat = (c56 + 36) >> 6; cg = c56 - mat * 28; dq = xcd & 3; d0 = dq * 256 + p_ * 64;
            pin4(mat, cg, dq, d0);
          }
          if constexpr (jb == 2 && gp == 0) {
            pgu = mat ? w_up : w_gate;
            pm2 = wsp + WS_ABT + (kq * 8 + e_) * 262144;
            pins(pgu, pm2);
            vw = ((d0 + r_ * 16) * F_ + cg * 128 + c_ * 8) * 2;
            PIN1(vw);
          }
          if constexpr (jb == 2 && (gp == 1 || gp == 2)) {
            dk[gp * 2 - 2] = c_ == r_ * 4 + gp * 2 - 2 ? ~0u : 0u; dk[gp * 2 - 1] = c_ == r_ * 4 + gp * 2 - 1 ? ~0u : 0u;
            PIN2(dk[gp * 2 - 2], dk[gp * 2 - 1]);
          }
          if constexpr (jb == 2 && gp == 3) { kl = lrow + (ln >> 5); go = (ln & 30) ? (int)0x80000000 : ((ln & 1) ? O_KEY : O_ST) + (int)kl * 8; PIN2(kl, go); }
          if constexpr (jb == 2 && gp == 4) { kl = 0xffff - kl; go = (ln & ~1) == 2 ? O_CE + (z * D_ + drow + (ln & 1)) * 8 : go; PIN2(kl, go); }
          if constexpr (jb == 2 && gp == 5) {
            gro = p_ < 2 ? O_ST + (st * 128 + p_ * 64 + ln) * 8 : O_CE + z * D_ * 8 + sym_row(D_, 32, 8, e_, z, (ln >> 2) & 3, (p_ - 2) * 16 + r_ * 4 + (ln & 3));
            lma = lds_addr(smem + LDS_MB) + (p_ < 2 ? p_ * 256 : 512 + p_ * 256) + ln * 4;
            PIN2(gro, lma);
          }
          if constexpr (jb == 1 && gp == 7) {
#pragma unroll
            for (int u = 0; u < 3; ++u) {
              const int vn_ = u == 0 ? VAN : u == 1 ? VBN : VCN;
              if (vn_ == 0) continue;
              const int k = (u == 0 ? VA0 : u == 1 ? VB0 : VC0) + (ln >> 3);
              tv2[u] = (ln >> 3) < vn_ ? (ln & 7) * 4096 + ((t_ ^ (k < 9 ? k - 1 : k - 8)) << 8) + (k < 9 ? 0 : 128) : (int)0x80000000;
              PIN1(tv2[u]);
            }
          }
          if constexpr (jb == 7 && gp == 0) {
            vm2 = p_ * 1024 + ln * 16;
            PIN1(vm2);
          }
          if constexpr (jb == 2 && gp == 6) {
            const int r0 = p_ * 8 + lrw(ln >> 3);
            vn2[6] = sym_lo(64, S_ * 2, (r0 >> 2) & 3, (r0 >> 4) * 4 + (r0 & 3)); vn2[7] = sym_hi(S_, 64, S_ * 2, t_, z, (r0 >> 2) & 3) | (vm & 127);
            PIN2(vn2[6], vn2[7]);
          }
          if constexpr ((jb == 2 && gp == 7) || (jb == 7 && gp >= 6)) {
            constexpr int q0 = jb == 2 ? 0 : gp == 6 ? 3 : 6;
            const int lo = vn2[6], hi = vn2[7];
#pragma unroll
            for (int q = q0; q < q0 + 3 && q < 8; ++q) { vn2[q] = ((lo + q * 8 * S_ * 2) & (63 * S_ * 2)) ^ hi; PIN1(vn2[q]); }
          }
          if constexpr (jb == 7 && gp == 1) { vdn = rgb * 112 + p_ * 28 + r_ * 7; vgu = (((ln >> 3) & 1) * 4 * F_ + vdn + min(ln & 7, 6)) * 16; PIN2(vdn, vgu); }
          if constexpr (jb == 7 && gp == 2) { vdn = (vdn * D_ + cg8 * 128 + c_ * 8) * 2; PIN1(vdn); }
          if constexpr (jb == 7 && gp == 3) {
            abo = (((kq * 8 + e_) * 4 + p_) * 16 + c_) * AB_STRIDE * 4;
            abq[2] = ln >= 16 || (ln & 7) == t_ ? (int)0x80000000 : abo;
            PIN2(abo, abq[2]);
          }
          if constexpr (jb == 7 && gp == 4) {
            abq[1] = ln >= 8 ? (int)0x80000000 : abq[2];
            abq[0] = ((ln ^ t_) & 4) ? (int)0x80000000 : abq[1];
            PIN2(abq[0], abq[1]);
          }
          if constexpr (jb == 7 && gp == 5) {
            const unsigned mb = lds_addr(smem) + p_ * 1024;
            mq0 = mb + ((3 - kq) & 3) * 4096; mq1 = mb + ((4 - kq) & 3) * 4096; mq2 = mb + ((5 - kq) & 3) * 4096; mq3 = mb + ((6 - kq) & 3) * 4096;
            pin4(mq0, mq1, mq2, mq3);
          }
          if constexpr (jb == 8 && gp < 4) {
#pragma unroll
            for (int h = 0; h < 2; ++h) {
              const unsigned hv = h ? 0x0302u : 0x0100u;
              sel[h][gp] = ((ln & 7) >> 1) == gp ? ((ln & 1) ? (hv << 16) | 0x0c0cu : 0x0c0c0000u | hv) : 0x0c0c0c0cu;
            }
            PIN2(sel[0][gp], sel[1][gp]);
          }
        }
        SB();
      };
#define WS1(i) setup(IC<1>{}, IC<i>{})
#define WS2(i) setup(IC<2>{}, IC<i>{})
#define WS3(i) do { setup(IC<7>{}, IC<i>{}); if (i >= 4) setup(IC<8>{}, IC<(i) & 3>{}); spf(IC<(i < SP0 ? i : -1)>{}); } while (0)
      G1_STAGE_TILE(1, G1_STAGE, WS1);
      WAIT_VM(19); BAR();
      G1_STAGE_TILE(2, 2 * G1_STAGE, WS2);
      G1_STAGE_TILE(3, 3 * G1_STAGE, WS3);
      auto lnu = [&](auto U) __attribute__((always_inline)) {
        constexpr int jb = 1 + decltype(U)::value / 7, gp = decltype(U)::value % 7;
        SB();
        if constexpr (jb == 1) {
          const f32x4 z4 = {0.f, 0.f, 0.f, 0.f};
          if constexpr (gp == 0) { sa4 = MM(lv[0], oh[0], z4); gw[0] = MM(lv[0], lw[0], z4); gx[0] = MM(lv[0], lv[0], z4); PIN3(sa4, gw[0], gx[0]); }
          if constexpr (gp == 1) { sa4 = MM(lv2[0], oh[1], sa4); gw[1] = MM(lv2[0], lw[0], z4); gx[1] = MM(lv2[0], lv2[0], z4); PIN3(sa4, gw[1], gx[1]); }
          if constexpr (gp == 2) { sa4 = MM(lv[1], oh[0], sa4); gw[0] = MM(lv[1], lw[1], gw[0]); gx[0] = MM(lv[1], lv[1], gx[0]); PIN3(sa4, gw[0], gx[0]); }
          if constexpr (gp == 3) { sa4 = MM(lv2[1], oh[1], sa4); gw[1] = MM(lv2[1], lw[1], gw[1]); gx[1] = MM(lv2[1], lv2[1], gx[1]); PIN3(sa4, gw[1], gx[1]); }
          if constexpr (gp == 4) { sa4 = MM(ld[0], oh[2], sa4); sa4 = MM(ld[1], oh[2], sa4); PIN1(sa4); }
          if constexpr (gp == 5) { sa4 = MM(ld2[0], oh[3], sa4); sa4 = MM(ld2[1], oh[3], sa4); PIN1(sa4); }
          if constexpr (gp == 6) { sa4 = MM(lw[0], oh[4], sa4); sa4 = MM(lw[1], oh[4], sa4); PIN1(sa4);
            epoch = __builtin_amdgcn_readfirstlane(epv); tag = ((epoch + 1u) * 0x9E3779B1u ^ dmix) | 1u; }
        }
        if constexpr (jb == 2) {
          if constexpr (gp == 0) { su = (sa4[0] + sa4[1]) + (sa4[2] + sa4[3]); ta = diag(gw[0]); PIN2(su, ta); }
          if constexpr (gp == 1) { su = fold32(su, su); tb = diag(gw[1]); PIN2(su, tb); }
          if constexpr (gp == 2) { su = fold16(su, su); gs = fold32(ta, tb); PIN2(su, gs); }
          if constexpr (gp == 3) { ta = diag(gx[0]); tb = diag(gx[1]); PIN2(ta, tb); }
          if constexpr (gp == 4) { gs = fold16(gs, fold32(ta, tb)); PIN1(gs); }
          if constexpr (gp == 5) { gs += dpp_f<0xB1>(gs); gs += dpp_f<0x4E>(gs); PIN1(gs); }
          if constexpr (gp == 6) { gs += dpp_f<0x141>(gs); gs += dpp_f<0x140>(gs); PIN1(gs); }
        }
        if constexpr (jb == 3) {
          if constexpr (gp == 0) {
            const float s0 = rdl(su, 0), s1 = rdl(su, 1), q0 = rdl(gs, 16), q1 = rdl(gs, 48);
            sx = hb ? s1 : s0; ta = hb ? q1 : q0; PIN2(sx, ta);
          }
          if constexpr (gp == 1) {
            const float mean = sx * (1.0f / D_);
            mbv = rbf(mean);
            ta -= sx * mean;
            if (__builtin_expect(__any(!(sx * mean <= 16.f * ta)), 0)) {
              float q = fold32(sqdev16(lv, rdl(mbv, 0)), sqdev16(lv2, rdl(mbv, 32)));
              q = row_sum(fold16(q, q));
              const float dm = mean - mbv;
              ta = fmaxf(q - (float)D_ * dm * dm, 0.f);
            }
            PIN2(mbv, ta);
          }
          if constexpr (gp == 2) { ta = rbf(rbf(ta * (1.0f / D_)) + 1e-5f); gs -= mbv * rdl(su, 4); PIN2(ta, gs); }
          if constexpr (gp == 3) { ta = rbf(__builtin_amdgcn_sqrtf(ta)); tb = __builtin_amdgcn_rcpf(ta); PIN2(ta, tb); }
          if constexpr (gp == 4) {
            const unsigned rb = __float_as_uint(rbf(gs * tb));
            kl |= (rb ^ ((unsigned)((int)rb >> 31) | 0x80000000u)) & 0xffff0000u; PIN1(kl);
          }
          if constexpr (gp == 5) { kl = (lane & 2) ? __float_as_uint(su) : (lane & 1) ? kl : (__float_as_uint(mbv) >> 16) | __float_as_uint(ta); PIN1(kl); }
        }
        SB();
      };
      constexpr int LN_JB = 1 + ((19 - LNP) * LNS) / 8;
      auto work1 = [&](auto JB, auto GP) __attribute__((always_inline)) {
        constexpr int jb = decltype(JB)::value, gp = decltype(GP)::value, pos = (jb - 1) * 8 + gp;
        SB();
        if constexpr (pos % LNS == 0 && LNP + pos / LNS < 20) lnu(IC<LNP + pos / LNS>{});
        if constexpr (jb == ST_JB + 3) {
          if constexpr (gp == 0) { if (!__all(gr.y == tag)) gr = granule_wait((const u64*)((const char*)(flags + FL_AB) + gro), tag); PIN1(gr); }
          if constexpr (gp == 1) { g1 = __builtin_amdgcn_rcpf(__uint_as_float((gr.x & 0xffff0000u) ^ ((unsigned)z << 31))); PIN1(g1); }
          if constexpr (gp == 2) { g0 = pw < 2 ? -(lo_bf(gr.x) * g1) : __uint_as_float(gr.x); PIN1(g0); }
        }
        if constexpr (gp < SP1 && jb <= SP1E) spf(IC<SP0 + (jb - 1) * SP1 + gp>{});
        SB();
      };
      auto step1 = [&](auto JB) __attribute__((always_inline)) {
        constexpr int jb = decltype(JB)::value;
        if constexpr (jb == 1) PAGE(1);
        if constexpr (jb == 7) PAGE(2);
        constexpr int n16 = jb == 12 ? 12 : jb == 11 ? 14 : 16;
        constexpr auto wl = [](int q) { return (q == LN_JB ? 1 : 0) + (q == ST_JB ? 1 : 0); };
        if constexpr (jb == 1) {
          asm volatile("s_waitcnt vmcnt(%11)" : "+v"(lv[0]), "+v"(lv[1]), "+v"(lw[0]), "+v"(lw[1]), "+v"(ld[0]), "+v"(ld[1]),
                       "+v"(lv2[0]), "+v"(lv2[1]), "+v"(ld2[0]), "+v"(ld2[1]), "+v"(epv) : "i"(LNP ? 24 : 16) : "memory");
#define LU(u) if constexpr (u < LNP) lnu(IC<u>{});
          LU(0) LU(1) LU(2) LU(3) LU(4) LU(5) LU(6) LU(7) LU(8) LU(9) LU(10) LU(11) LU(12) LU(13) LU(14) LU(15) LU(16) LU(17) LU(18)
          if (LNP) WAIT_VM(16);
        } else if constexpr (jb == ST_JB + 3) {
          asm volatile("s_waitcnt vmcnt(%1)" : "+v"(gr) : "i"(n16 + wl(jb - 2) + wl(jb - 1)) : "memory");
        } else wait_vm<n16 + wl(jb - 3) + wl(jb - 2) + wl(jb - 1)>();
        BAR();
        if constexpr (jb == 2) { if (pw == 0 && lane < 16) ((int*)(smem + LDS_SEL))[lane < 8 ? lane : lane + 8] = 0; }
        if constexpr (jb == 4) { if (pw == 0 && lane == 0) ((unsigned*)(smem + LDS_SEL))[5] = tag; }
#define W1(i) work1(JB, IC<i>{})
        if constexpr (jb < 10) G1_STAGE_TILE(jb - 1 + G1_NST, ((jb - 1) % G1_NST) * G1_STAGE, W1);
        else G1_STAGE_A(jb - 1 + G1_NST, ((jb - 1) % G1_NST) * G1_STAGE, W1);
        if constexpr (jb == 12) G1_STAGE_B(13, G1_STAGE);
        if constexpr (jb == LN_JB) __builtin_amdgcn_raw_buffer_store_b64((u32x2){kl, tag}, rf, go, 0, 16);
        if constexpr (jb == ST_JB) {
          asm volatile("buffer_load_dwordx2 %0, %1, %2, 0 offen sc1" : "=v"(gr) : "v"(gro), "s"(rf) : "memory");
        }
        touches(IC<100 + jb>{});
        if constexpr (jb <= 8) {
          if (et == jb - 1) {
#pragma unroll
            for (int l = 0; l < WSN; ++l) bld16_nt_io(wv[l], rw, vw, l * F_ * 2);
          }
        }
        SB();
      };
      static_assert(ST_JB + 3 <= 12 && ST_JB >= 6, "schedule");
      step1(IC<1>{}); step1(IC<2>{}); step1(IC<3>{}); step1(IC<4>{}); step1(IC<5>{}); step1(IC<6>{}); step1(IC<7>{}); step1(IC<8>{}); step1(IC<9>{}); step1(IC<10>{}); step1(IC<11>{}); step1(IC<12>{});
    }
#define ALIAS 0
#define G2_STAGE_N(kk_, W) do { const int kb = KROT(((ALIAS >> (kk_)) & 1) ? (kk_) & 3 : (kk_)); constexpr int so = n_off(kk_); SB(); \
    DMA_S(rn2, vn2[0], mq0, so, kb); W(0); DMA_S(rn2, vn2[1], mq1, so, kb); W(1); \
    DMA_S(rn2, vn2[2], mq2, so, kb); W(2); DMA_S(rn2, vn2[3], mq3, so, kb); W(3); \
    DMA_S(rn2, vn2[4], mq0, so + 16384, kb); W(4); DMA_S(rn2, vn2[5], mq1, so + 16384, kb); W(5); \
    DMA_S(rn2, vn2[6], mq2, so + 16384, kb); W(6); DMA_S(rn2, vn2[7], mq3, so + 16384, kb); W(7); SB(); } while (0)
#define G2_STAGE_NH(kk_, h) do { const int kb = KROT(kk_); constexpr int so = n_off(kk_) + (h) * 16384; SB(); \
    DMA_S(rn2, vn2[(h) * 4], mq0, so, kb); DMA_S(rn2, vn2[(h) * 4 + 1], mq1, so, kb); \
    DMA_S(rn2, vn2[(h) * 4 + 2], mq2, so, kb); DMA_S(rn2, vn2[(h) * 4 + 3], mq3, so, kb); SB(); } while (0)
#define G2_STAGE_M(kk_, W) do { const int kb = KROT(kk_) * 128; constexpr int so = G2_MRING + m_off(kk_); SB(); \
    DMA_(rm2, vm2, mb0, so, kb, 0, " sc1"); W(8); DMA_(rm2, vm2, mb0, so + 4096, kb, 4096, " sc1"); W(9); \
    DMA_(rm2, vm2, mb0, so + 8192, kb, 8192, " sc1"); W(10); DMA_(rm2, vm2, mb0, so + 12288, kb, 12288, " sc1"); W(11); SB(); } while (0)
    WAIT_VM(8); BAR();
    asm volatile("ds_write_b32 %0, %1" :: "v"(lma), "v"(g0) : "memory");
    if (pw < 2) asm volatile("ds_write_b32 %0, %1 offset:512" :: "v"(lma), "v"(g1) : "memory");
    u32x4 tv[4];
    unsigned ready = 0;
    auto need = [&](int k) __attribute__((always_inline)) {
      const int b = k_blk(k) + (k_ord(k) & 1) * 8;
#pragma unroll 1
      for (int it = 0; !((ready >> b) & 1) && it < SPIN_MAX; ++it) {
        ready |= (unsigned)__ballot(__builtin_amdgcn_raw_buffer_load_b32(rf, abq[k < 5 ? 0 : k < 9 ? 1 : 2], 0, 16) == tag);
        asm volatile("" ::: "memory");
      }
    };
    unsigned ksel, bad = 0;
    int isel;
    unsigned xw;
    auto bvec = [&](unsigned src, const unsigned (&sl)[4]) __attribute__((always_inline)) {
      return (u32x4){__builtin_amdgcn_perm(0, src, sl[0]), __builtin_amdgcn_perm(0, src, sl[1]),
                     __builtin_amdgcn_perm(0, src, sl[2]), __builtin_amdgcn_perm(0, src, sl[3])};
    };
#define BC8(v, p) (unsigned)__builtin_amdgcn_update_dpp(__builtin_amdgcn_update_dpp(0, (int)(v), 0x150 + (p), 0xf, 0x3, false), (int)(v), 0x158 + (p), 0xf, 0xc, false)
    float* lg = (float*)(smem + LDS_GU);
    f32x4 macc = {0.f, 0.f, 0.f, 0.f};
    u32x2 sv;
    unsigned hp, gbad;
    float s0, s1;
    auto work2 = [&](auto JB, auto GP) __attribute__((always_inline)) {
      constexpr int jb = decltype(JB)::value, gp = decltype(GP)::value;
      SB();
      if constexpr (jb == -1 && gp < 4) {
        constexpr int c = gp == 0 ? 0xB1 : gp == 1 ? 0x4E : gp == 2 ? 0x141 : 0x140;
        merge2(k1, k2, dpp_u<c>(k1), dpp_u<c>(k2));
        PIN2(k1, k2);
      }
      if constexpr (jb == -1 && gp == 4) {
        ksel = tk ? k2 : k1;
        isel = (0xffff - (int)(ksel & 0xffffu)) & (S_ - 1);
        k1 = (isel * D_ + d0 + rq * 16 + (lane & 7) * 2) * 2; k2 = O_ST + isel * 8;
        PIN1(ksel);
        xw = gld4_s(x, k1);
        sv = bld8_sc1(rf, k2);
      }
      if constexpr (jb == 0 && gp == 1) wait_xs<3 + 1 + (N2A < 2 ? N2A : 2) + tcnt(116) + tcnt(117)>(xw, sv);
      if constexpr (jb == 0) {
        if constexpr (gp == 1) { if (!__all(sv.y == tag)) sv = granule_wait(stat + isel, tag); s0 = __builtin_amdgcn_rcpf(hi_bf(sv.x)); PIN1(s0); }
        if constexpr (gp == 2) { s1 = ln_elem(lo_bf(xw), lo_bf(sv.x), hi_bf(sv.x), s0); PIN1(s1); }
        if constexpr (gp == 3) { xw = pk2(s1, ln_elem(hi_bf(xw), lo_bf(sv.x), hi_bf(sv.x), s0)); PIN1(xw); }
      }
      if constexpr (DGS == 0 && jb == DG && gp < 7) { PIN1(dv[gp]); macc = MM(dv[gp], bvec(BC8(hp, gp), sel[0]), macc); PIN1(macc); }
      if constexpr (jb >= 100 && gp < 7) { PIN1(dv[gp]); macc = MM(dv[gp], bvec(BC8(hp, gp), sel[0]), macc); PIN1(macc); }
      if constexpr ((jb == 0 && gp >= 4) || jb == 1) {
        constexpr int l = jb == 0 ? gp - 4 : gp + 4;
        if constexpr (!(l & 1)) hp = BC8(xw, l >> 1);
        PIN1(wv[l]);
        macc = MM(wv[l], bvec(hp, sel[l & 1]), macc);
        PIN2(macc, hp);
      }
      if constexpr (jb == GV_JB + 2) {
        if constexpr (gp == 0) { gbad = (gv[0].y ^ tag) | (gv[0].w ^ tag) | (gv[1].y ^ tag) | (gv[1].w ^ tag); PIN1(gbad); }
        if constexpr (gp == 1) { gbad |= (gv[2].y ^ tag) | (gv[2].w ^ tag) | (gv[3].y ^ tag) | (gv[3].w ^ tag); PIN1(gbad); }
        if constexpr (gp == 2) {
          s0 = ((__uint_as_float(gv[0].x) + __uint_as_float(gv[1].x)) + __uint_as_float(gv[2].x)) + __uint_as_float(gv[3].x);
          s1 = ((__uint_as_float(gv[0].z) + __uint_as_float(gv[1].z)) + __uint_as_float(gv[2].z)) + __uint_as_float(gv[3].z);
          PIN2(s0, s1);
        }
        if constexpr (gp == 3) {
#pragma unroll 1
          for (int it = 0; !__all(gbad == 0) && it < SPIN_MAX; ++it) {
            u32x4 q[4];
            gbad = 0;
#pragma unroll
            for (int i = 0; i < 4; ++i) { q[i] = __builtin_amdgcn_raw_buffer_load_b128(rgu, vgu, i * F_ * 16, 16); gbad |= (q[i].y ^ tag) | (q[i].w ^ tag); }
            s0 = ((__uint_as_float(q[0].x) + __uint_as_float(q[1].x)) + __uint_as_float(q[2].x)) + __uint_as_float(q[3].x);
            s1 = ((__uint_as_float(q[0].z) + __uint_as_float(q[1].z)) + __uint_as_float(q[2].z)) + __uint_as_float(q[3].z);
            asm volatile("" ::: "memory");
          }
          const unsigned gp2 = pk2(s0, s1);
          const unsigned up2 = dpp_u<0x128>(gp2);
          s0 = tk ? hi_bf(up2) : lo_bf(gp2); s1 = tk ? hi_bf(gp2) : lo_bf(up2);
          PIN2(s0, s1);
        }
        if constexpr (gp == 4) { s0 = rbf(s0 * __builtin_amdgcn_rcpf(1.f + __expf(-s0))); PIN1(s0); }
        if constexpr (gp == 5) { hp = pk2(s0 * s1, 0.f); PIN1(hp); }
      }
      if constexpr (jb == 10 && gp == 4) code_touch(sd, (j * 4 + pw) * 256);
      SB();
    };
#define W2M(i) work2(IC<-1>{}, IC<i>{})
#define W20(i) work2(IC<0>{}, IC<i>{})
#define W21(i) work2(IC<1>{}, IC<i>{})
    auto piece = [&](auto K, auto Q) __attribute__((always_inline)) {
      constexpr int k = decltype(K)::value, q = decltype(Q)::value;
      if constexpr (q < 8) {
        const int kb = KROT(((ALIAS >> k) & 1) ? k & 3 : k);
        DMA_S(rn2, vn2[q], (q & 3) == 0 ? mq0 : (q & 3) == 1 ? mq1 : (q & 3) == 2 ? mq2 : mq3, n_off(k) + (q >> 2) * 16384, kb);
      } else {
        if constexpr (q == 8) need(k);
        const int kb = KROT(k) * 128;
        DMA_(rm2, vm2, mb0, G2_MRING + m_off(k) + (q - 8) * 4096, kb, (q - 8) * 4096, " sc1");
      }
    };
    unsigned fs;
    {
#define KEYS() _Pragma("unroll") for (int i = 0; i < 4; ++i) kk[i] = bld16_sc1(rf, l16, ko_ + i * 1024)
      G1_STAGE_B(15, 3 * G1_STAGE);
      G1_STAGE_B(14, 2 * G1_STAGE);
      lgkm0(sd);
      BAR();
      if (KP == 0) KEYS(); else G2_STAGE_NH(0, 0);
      WAIT_VM(6); BAR();
      if (KP == 0) G2_STAGE_NH(0, 0);
      if (KP == 1) KEYS();
      G2_STAGE_NH(0, 1);
      if (KP == 2) KEYS();
#pragma unroll
      for (int l = WSN; l < 16; ++l) wv[l] = bld16_nt(rw, vw, l * F_ * 2);
      asm volatile("s_waitcnt vmcnt(%4)" : "+v"(kk[0]), "+v"(kk[1]), "+v"(kk[2]), "+v"(kk[3]) : "i"(16 - WSN + (2 - KP) * 4) : "memory");
      touches(IC<115>{});
#pragma unroll
      for (int i = 0; i < 4; ++i) {
        k2 = med3u(k1, k2, kk[i].x); k1 = max(k1, kk[i].x);
        k2 = med3u(k1, k2, kk[i].z); k1 = max(k1, kk[i].z);
        bad |= (kk[i].y ^ tag) | (kk[i].w ^ tag);
      }
#pragma unroll 1
      for (int it = 0; !__all(bad == 0) && it < SPIN_MAX; ++it) {
        k1 = 0; k2 = 0; bad = 0;
#pragma unroll
        for (int i = 0; i < 4; ++i) {
          const u32x4 q = __builtin_amdgcn_raw_buffer_load_b128(rf, l16, ko_ + i * 1024, 16);
          k2 = med3u(k1, k2, q.x); k1 = max(k1, q.x);
          k2 = med3u(k1, k2, q.z); k1 = max(k1, q.z);
          bad |= (q.y ^ tag) | (q.w ^ tag);
        }
        asm volatile("" ::: "memory");
      }
      merge2(k1, k2, dpp_u<0xB1>(k1), dpp_u<0xB1>(k2));
      merge2(k1, k2, dpp_u<0x4E>(k1), dpp_u<0x4E>(k2));
      merge2(k1, k2, dpp_u<0x141>(k1), dpp_u<0x141>(k2));
      merge2(k1, k2, dpp_u<0x140>(k1), dpp_u<0x140>(k2));
      if ((lane & 15) == 0) *(u32x2*)(smem + LDS_KEY + (pw * 4 + rq) * 8) = (u32x2){k1, k2};
      PAGE(3);
      wait_vm<16 - WSN>();
      BAR_LGKM();
#pragma unroll
      for (int jj = 0; jj < 4; ++jj) tv[jj] = *(const u32x4*)(smem + G2_MRING + m_off(0) + (jj * 4 + pw) * 1024 + lane * 16);
      { const u32x2 kp = *(const u32x2*)(smem + LDS_KEY + ci * 8); k1 = kp.x; k2 = kp.y; }
#pragma unroll
      for (int jj = 0; jj < 4; ++jj) __builtin_amdgcn_raw_buffer_store_b128(tv[jj], rm2, vm2 + jj * 4096 + 2 * tt * 16384, 0, 16);
      G2_STAGE_N(1, W2M);
      touches(IC<116>{});
      wait_vm<10 + tcnt(116)>();
      if (lane == tt) __builtin_amdgcn_raw_buffer_store_b32(tag, rf, abo, 0, 16);
      touches(IC<117>{});
      {
        lds_vi rel = (lds_vi)(smem + LDS_SEL + 12);
        int it = 0;
        while (rel[0] != 4 && it < SPIN_MAX * 8) ++it;
        asm volatile("" ::: "memory");
      }
#define P2A(i) if constexpr (i < N2A) piece(IC<2>{}, IC<i>{}); W20(i);
      SB(); P2A(0) P2A(1) P2A(2) P2A(3) P2A(4) P2A(5) P2A(6) P2A(7)
      __builtin_amdgcn_s_sleep(WSL);
      wait_vm<1 + N2A + tcnt(116) + tcnt(117)>();
      fs = bld4_sc1(rf, abq[0]);
    }
    BAR();
#pragma unroll
    for (int jj = 0; jj < 4; ++jj) tv[jj] = *(const u32x4*)(smem + G2_MRING + m_off(1) + (jj * 4 + pw) * 1024 + lane * 16);
    constexpr int SPB = SP0 + SP1E * SP1;
#define P2B(i) if constexpr (i >= N2A) piece(IC<2>{}, IC<i>{});
    SB(); P2B(0) P2B(1) P2B(2) P2B(3) P2B(4) P2B(5) P2B(6) P2B(7)
#define P3A(i) if constexpr (i < N3A) piece(IC<3>{}, IC<i>{});
    P3A(0) P3A(1) P3A(2) P3A(3) P3A(4) P3A(5) P3A(6) P3A(7)
    asm volatile("s_waitcnt vmcnt(%1)" : "+v"(fs) : "i"(8 - N2A + N3A) : "memory");
    ready = (unsigned)__ballot(fs == tag);
    need(2);
    G2_STAGE_M(2, NOW);
#define P3B(i) if constexpr (i >= N3A) piece(IC<3>{}, IC<i>{});
    P3B(0) P3B(1) P3B(2) P3B(3) P3B(4) P3B(5) P3B(6) P3B(7)
    need(3);
    G2_STAGE_M(3, NOW);
    fs = bld4_sc1(rf, abq[1]);
    touches(IC<1>{});
#pragma unroll
    for (int i = 0; i < dn_cnt(1); ++i) dv[i] = bld16_nt(rdn, vdn, i * D_ * 2);
    W21(0); W21(1); W21(2); W21(3); W21(4); W21(5); W21(6); W21(7); W21(8); W21(9); W21(10); W21(11);
    asm volatile("" : "+v"(tv[0]), "+v"(tv[1]), "+v"(tv[2]), "+v"(tv[3]));
    auto step2 = [&](auto JB) __attribute__((always_inline)) {
      constexpr int jb = decltype(JB)::value;
      if constexpr (jb == 3) PAGE(4);
      if constexpr (jb == 2 && PRIO2 != 3) __builtin_amdgcn_s_setprio(PRIO2);
      if constexpr (jb == 9) PAGE(5);
      constexpr auto must = [](int q) { return (q == 2 ? 4 : 0) + ((FSM >> q) & 1) + dig(GVS, q); };
      constexpr auto late = [](int q) { return dn_cnt(q) + (VAN > 0 && q == VAP) + (VBN > 0 && q == VBP) + (VCN > 0 && q == VCP); };
      if constexpr (jb == 2) wait_vm<8 - N3A + 4 + must(1) + late(1)>();
      else wait_vm<(dig(LAGS, jb - 2) ? 0 : late(jb - 2) + (jb == 5 ? 1 : 0)) + 12 - dig(LAGS, jb - 1) + must(jb - 1) + late(jb - 1)>();
      if constexpr (jb == 3) { if (pw == 0 && (lane & ~8) == 0) ((unsigned*)(smem + LDS_SEL))[tk] = ksel; }
      if constexpr ((jb == 2 || jb == DG + 1)) {
        *(f32x4*)(lg + pw * 256 + lane * 4) = macc;
        macc = (f32x4){0.f, 0.f, 0.f, 0.f};
        lgkm0(sd);
      }
      BAR();
      float p0[4], p1[4];
      const int col = (pw & 1) * 64 + lane;
      if constexpr (jb == 3) {
        if (g == 0 && pw == 0 && lane == 0) __hip_atomic_store((unsigned*)flags + FL_EPOCH, epoch + 1u, __ATOMIC_RELAXED, __HIP_MEMORY_SCOPE_AGENT);
      }
      if constexpr (jb == DG + 1) {
        const float* p = lg + (pw & 1) * 32 + (lane >> 4) * 64 + ((2 * lane) & 7) * 4 + ((lane >> 2) & 3);
#pragma unroll
        for (int i = 0; i < 4; ++i) { p0[i] = p[i * 256]; p1[i] = p[i * 256 + 4]; }
      }
      if constexpr (jb == 2) {
        const float* p = lg + (col >> 5) * 64 + (col & 7) * 4 + ((col >> 3) & 3);
#pragma unroll
        for (int i = 0; i < 4; ++i) { p0[i] = p[i * 256]; p1[i] = p[i * 256 + 32]; }
      }
      if constexpr (jb == 4) {
        if (lane == 8 + tt) __builtin_amdgcn_raw_buffer_store_b32(tag, rf, abo, 0, 16);
      }
      if constexpr ((FSM >> (jb - 2)) & 1) {
        asm volatile("" : "+v"(fs));
        ready |= (unsigned)__ballot(fs == tag);
      }
      if constexpr (jb == GV_JB + 2) asm volatile("" : "+v"(gv[0]), "+v"(gv[1]), "+v"(gv[2]), "+v"(gv[3]));
      auto w2 = [&](auto GP) __attribute__((always_inline)) {
        constexpr int gp = decltype(GP)::value;
        if constexpr (jb == 2 && gp == 0) {
          p0[0] = (p0[0] + p0[1]) + (p0[2] + p0[3]); p1[0] = (p1[0] + p1[1]) + (p1[2] + p1[3]);
          PIN2(p0[0], p1[0]);
        }
        if constexpr (gp < 12) work2(JB, GP);
        if constexpr (gp < dig(SPS, jb) && !((SPE >> jb) & 1)) spf(IC<SPB + acc_dig(SPS, jb) + gp>{});
      };
      constexpr int l0 = dig(LAGS, jb - 1), np = l0 + 12 - dig(LAGS, jb);
      auto slot = [&](auto I) __attribute__((always_inline)) {
        constexpr int i = decltype(I)::value;
        if constexpr (i < l0) piece(IC<jb + 1>{}, IC<12 - l0 + i>{});
        else if constexpr (i < np) piece(IC<jb + 2>{}, IC<i - l0>{});
        if constexpr (i < 12 || i < np) w2(I);
      };
#define SL4(i) slot(IC<i>{}); slot(IC<i + 1>{}); slot(IC<i + 2>{}); slot(IC<i + 3>{});
      SB();
      SL4(0) SL4(4) SL4(8) SL4(12) SL4(16) SL4(20)
      SB();
      if constexpr (jb == 2) {
#pragma unroll
        for (int jj = 0; jj < 4; ++jj) __builtin_amdgcn_raw_buffer_store_b128(tv[jj], rm2, vm2 + jj * 4096 + (2 * tt + 1) * 16384, 0, 16);
      }
      if constexpr ((FSM >> jb) & 1) fs = bld4_sc1(rf, abq[jb < 5 ? 1 : 2]);
#pragma unroll
      for (int i = acc_dig(GVS, jb); i < acc_dig(GVS, jb + 1); ++i) gv[i] = bld16_sc1(rgu, vgu, i * F_ * 16);
      touches(JB);
#pragma unroll
      for (int i = dn_first(jb); i < dn_first(jb) + dn_cnt(jb); ++i) dv[i] = bld16_nt(rdn, vdn, i * D_ * 2);
      if constexpr (jb == DG + 1) {
        if (pw < 2)
          __builtin_amdgcn_raw_buffer_store_b128((u32x4){__float_as_uint((p0[0] + p0[1]) + (p0[2] + p0[3])), tag, __float_as_uint((p1[0] + p1[1]) + (p1[2] + p1[3])), tag},
                                                 mk_rsrc(wsp + WS_DN), (((pw * 8 + cg8) * 32 + rgb) * 64 + lane) * 16, 0, 16);
      }
      if constexpr (jb == 2) {
        if (moe && pw < 2)
          __builtin_amdgcn_raw_buffer_store_b128((u32x4){__float_as_uint(p0[0]), tag, __float_as_uint(p1[0]), tag},
                                                 rgu, ((mat * 4 + dq) * F_ + cg * 128 + col) * 16, 0, 16);
      }
      SB();
#define SPX(i) spf(IC<(((i >= np && i >= 12) || ((SPE >> jb) & 1)) && i < dig(SPS, jb) ? SPB + acc_dig(SPS, jb) + i : -1)>{})
      if constexpr (dig(DGS, jb) > 0) {
        if (DGP != 3) __builtin_amdgcn_s_setprio(DGP);
#define DGU(i) if constexpr (i >= acc_dig(DGS, jb) && i < acc_dig(DGS, jb + 1)) work2(IC<100>{}, IC<i>{});
        DGU(0) DGU(1) DGU(2) DGU(3) DGU(4) DGU(5) DGU(6)
        if (DGP != 3) __builtin_amdgcn_s_setprio(3);
      }
      SPX(0); SPX(1); SPX(2); SPX(3); SPX(4); SPX(5); SPX(6); SPX(7); SPX(8); SPX(9); SPX(10); SPX(11); SPX(12); SPX(13); SPX(14);
      SB();
    };
    static_assert(DG + 1 <= 12 && DG >= GV_JB + 3 && dn_first(DG - 2) == 7 && acc_dig(GVS, GV_JB + 1) == 4 && dig(LAGS, 1) == 0 && dig(SPS, 1) == 0 && dig(LAGS, 0) == 0, "schedule");
    static_assert(DGS == 0 || (acc_dig(DGS, DG + 1) == 7 && acc_dig(DGS, GV_JB + 2) == 0 && acc_dig(DGS, 10) <= dn_first(7) && acc_dig(DGS, 11) <= dn_first(8) && acc_dig(DGS, 9) <= dn_first(6)), "units need their loads");
    static_assert(ALIAS || sp_tile((SPB + acc_dig(SPS, 14)) >> 4) < 0, "all tiles are prefetched");
    step2(IC<2>{}); step2(IC<3>{}); step2(IC<4>{}); step2(IC<5>{}); step2(IC<6>{}); step2(IC<7>{}); step2(IC<8>{}); step2(IC<9>{}); step2(IC<10>{}); step2(IC<11>{}); step2(IC<12>{}); step2(IC<13>{});
    lgkm0(sd);
    wait_vm<12 - dig(LAGS, 13)>(); BAR();
    {
      constexpr int l0 = dig(LAGS, 13);
#define PL(i) if constexpr (i >= 12 - l0) piece(IC<15>{}, IC<i>{});
      PL(0) PL(1) PL(2) PL(3) PL(4) PL(5) PL(6) PL(7) PL(8) PL(9) PL(10) PL(11)
    }
    __builtin_amdgcn_s_setprio(0);
    if (EPP == 1) { WAIT_VM(0); BAR(); }
    epi_setup(0); epi_setup(1); epi_setup(2);
    if (EPP == 2) { WAIT_VM(0); BAR(); }
#pragma unroll
    for (int n = 0; n < 3; ++n) xq_load(n);
    if (!EPP) { WAIT_VM(3); BAR(); }
    epi_setup(3);
    wait_xq<0>(xq, sq);
    key[0] = __builtin_amdgcn_readlane(ksel, 0); key[1] = __builtin_amdgcn_readlane(ksel, 8);
    cv_calc();
#pragma unroll 1
    for (int c = 0; c < 2; ++c) {
      if (__builtin_expect(((c ? far1 : far0) & ~9) != 0, 1)) continue;
      const __amdgpu_buffer_rsrc_t rd = mk_rsrc(wsp + WS_DN + (size_t)(c * 8 * 32 + pw * 8) * 1024);
      const int ec = seg0(lane >> 5, (lane >> 4) & 1), dno = (ec >> 7) * 32768 + ((ec & 127) / 2 + (lane & 15)) * 16;
#pragma unroll 1
      for (int it = 0; it < SPIN_MAX; ++it) {
        if (__all(__builtin_amdgcn_raw_buffer_load_b32(rd, (ec >> 7) * 32768 + (lane & 7) * 1024 + 1020, 0, 16) == tag)) break;
        __builtin_amdgcn_s_sleep(8);
        asm volatile("" ::: "memory");
      }
      float s0, s1;
      unsigned bad = 1;
#pragma unroll 1
      for (int it = 0; !__all(bad == 0) && it < SPIN_MAX; ++it) {
        bad = 0; s0 = 0.f; s1 = 0.f;
#pragma unroll
        for (int i = 0; i < 8; ++i) {
          const u32x4 q = __builtin_amdgcn_raw_buffer_load_b128(rd, dno, i * 1024, 16);
          bad |= (q.y ^ tag) | (q.w ^ tag); s0 += __uint_as_float(q.x); s1 += __uint_as_float(q.z);
        }
        asm volatile("" ::: "memory");
      }
      *(f32x2*)(smem + LDS_FIN + (c * 4 + pw) * 512 + lane * 8) = (f32x2){s0, s1};
      asm volatile("s_waitcnt lgkmcnt(0)" ::: "memory");
      if (lane == 0) asm volatile("ds_add_u32 %0, %1" :: "v"(lds_addr(smem + LDS_SEL + 24 + c * 4)), "v"(1) : "memory");
    }
  } else {
  {
    const int q = j * 4 + wid;
    constexpr int Q1 = WS_MB, Q2 = Q1 + 8, Q3 = Q2 + 8, Q4 = Q3 + 7, Q5 = Q4 + 7, Q6 = Q5 + 7, Q7 = Q6 + 4, Q8 = Q7 + 4, QC = Q8 + 1;
    static_assert(QC <= 128, "warm-up slots");
    unsigned dmy = 0;
    if (q < QC) {
      const char* base; int b0;
      if (q < Q1) { base = wsp; b0 = 0; }
      else if (q < Q2) { base = (const char*)ws_re; b0 = Q1; }
      else if (q < Q3) { base = (const char*)ws_im; b0 = Q2; }
      else if (q < Q4) { base = (const char*)w_gate; b0 = Q3; }
      else if (q < Q5) { base = (const char*)w_up; b0 = Q4; }
      else if (q < Q6) { base = (const char*)w_down; b0 = Q5; }
      else if (q < Q7) { base = (const char*)out; b0 = Q6; }
      else if (q < Q8) { base = (const char*)x; b0 = Q7; }
      else { base = (const char*)w_r; b0 = Q8; }
      base += (size_t)(q - b0) << 20;
      asm volatile("s_load_dword %0, %1, 0" : "+s"(dmy) : "s"(base) : "memory");
    }
    if (wid == 0) { CALL_PG(1); CALL_PG(8); CALL_PG(5); }
    else if (wid == 1) { CALL_PG(2); CALL_PG(9); CALL_PG(6); }
    else if (wid == 2) { CALL_PG(3); CALL_PG(10); }
    else { CALL_PG(4); CALL_PG(11); }
    BAR();
    lgkm0(dmy);
    TL(1);
  }
  PAGE(8);
  epi_setup(0); epi_setup(1); epi_setup(2); epi_setup(3);
  const int wr = wid >> 1, wc = wid & 1;
  const int sw = swz(fr), fl = lrw(fr);
  const int ko0 = ((0 + fq) ^ sw) * 16, ko1 = ((4 + fq) ^ sw) * 16;
  bf16x8 fm[2][4], fn[2][4];
  {
    const char* pm = smem + (wr * 64 + fl) * 128;
    const char* pn = smem + 16384 + (wc * 64 + fl) * 128;
    f32x4 acc[4][4];
#pragma unroll
    for (int i = 0; i < 4; ++i)
#pragma unroll
      for (int jn = 0; jn < 4; ++jn)
#pragma unroll
        for (int q = 0; q < 4; ++q) acc[i][jn][q] = 0.f;
#define TO(i) ((i) * 2048)
#define LDSP(T, a) ((__attribute__((address_space(3))) T*)(size_t)(a))
#define SLOTS(n) _Pragma("unroll") for (int q = 0; q < (n); ++q)
#define MFA(c, m, n) asm volatile("v_mfma_f32_16x16x32_bf16 %0, %2, %1, %0" : "+v"(c) : "v"(m), "v"(n))
#define MFA2(c, m, n, m2, n2) asm volatile("v_mfma_f32_16x16x32_bf16 %0, %2, %1, %0\n\tv_mfma_f32_16x16x32_bf16 %0, %4, %3, %0" : "+v"(c) : "v"(m), "v"(n), "v"(m2), "v"(n2))
    unsigned lm[2][2], ln[2][2];
#pragma unroll
    for (int u = 0; u < 2; ++u)
#pragma unroll
      for (int kk = 0; kk < 2; ++kk) {
        lm[u][kk] = lds_addr(pm) + u * 2 * G1_STAGE + (kk ? ko1 : ko0);
        ln[u][kk] = lds_addr(pn) + u * 2 * G1_STAGE + (kk ? ko1 : ko0);
        asm volatile("" : "+v"(lm[u][kk]), "+v"(ln[u][kk]));
      }
    auto ldm = [&](int stg, int i, int kk) __attribute__((always_inline)) { fm[kk][i] = *LDSP(const bf16x8, lm[stg >> 1][kk] + (stg & 1) * G1_STAGE + TO(i)); };
    auto ldn = [&](int stg, int jn, int kk) __attribute__((always_inline)) { fn[kk][jn] = *LDSP(const bf16x8, ln[stg >> 1][kk] + (stg & 1) * G1_STAGE + TO(jn)); };
    auto quad = [&](int a, int c, int d, int s) __attribute__((always_inline)) {
      const int i = (s < 2 || s >= 6) ? a : a + 1, jn = s < 4 ? c : d, kk = (s == 1 || s == 2 || s == 5 || s == 6);
      MFA(acc[i][jn], fm[kk][i], fn[kk][jn]);
    };
    auto quad2 = [&](int a, int c, int d, int s) __attribute__((always_inline)) {
      const int i = (s < 2 || s >= 6) ? a : a + 1, jn = s < 4 ? c : d, kk = (s == 2 || s == 6);
      MFA2(acc[i][jn], fm[kk][i], fn[kk][jn], fm[1 - kk][i], fn[1 - kk][jn]);
    };
    auto half1 = [&](int stg) __attribute__((always_inline)) {
      const int ja = (stg & 1) * 2, jb = 2 - ja;
      SLOTS(8) {
        quad(0, ja, ja + 1, q);
        if (q < 4) ldn(stg, jb + (q >> 1), q & 1); else ldm(stg, 2 + ((q - 4) >> 1), q == 5 || q == 6);
        SB();
      }
      SLOTS(4) { quad2(0, jb, jb + 1, 2 * q); SB(); }
    };
    auto half2 = [&](int stg) __attribute__((always_inline)) {
      const int ja = (stg & 1) * 2, jb = 2 - ja, nx = (stg + 1) & 3;
      SLOTS(8) {
        quad(2, jb + 1, jb, q);
        if (!(q & 1)) ldm(nx, q >> 2, q == 2 || q == 4);
        SB();
      }
      SLOTS(8) {
        quad(2, ja, ja + 1, q);
        if (!(q & 1)) ldn(nx, jb + (q >> 2), (q >> 1) & 1);
        SB();
      }
    };
    unsigned ae[7];
    {
      int l_ = lane;
      asm volatile("" : "+v"(l_));
      const int p = l_ >> 4, dn = sym_dn(p), hf = (p >> 1) ^ (dn & z);
#pragma unroll
      for (int u = 0; u < 7; ++u) {
        const int T = u < 3 ? wc * 4 : (wc * 4 + u - 3 - dn) & 7, i = u < 3 ? (u + 1) ^ ((u & 1) ? 0 : dn * 2) : 0;
        const int f = dn * 8 + ((T & 2) << 1) + ((T & 4) >> 1) + (T & 1);
        ae[u] = lds_addr(smem + G2_MRING) + (hf * 64 + i * 16 + lrw(f)) * 128 + (((wr * 4 + ((l_ >> 2) & 3)) ^ swz(f)) * 16) + (l_ & 3) * 4;
        asm volatile("" : "+v"(ae[u]));
      }
    }
    unsigned al = lds_addr(smem + LDS_MB) + (wr * 32 + fr * 2) * 4, ac = lds_addr(smem + LDS_MB) + (256 + wc * 64 + fq * 4) * 4;
    asm volatile("" : "+v"(al), "+v"(ac));
    SLOTS(16) asm volatile("" : "+v"(acc[q >> 2][q & 3]));
    SB();
    SLOTS(4) { ldm(0, q >> 1, q & 1); ldn(0, q >> 1, q & 1); }
    SB();
#pragma unroll 1
    for (int k4 = 0; k4 < G1_NKT - 4; k4 += 4) {
      half1(0); BAR_LGKM();

      SB(); half2(0);
      half1(1); BAR_LGKM();

      SB(); half2(1);
      half1(2); BAR_LGKM();

      SB(); half2(2);
      half1(3); BAR_LGKM();

      SB(); half2(3);
    }
    bf16x8 gm[4][4], gn[2][8];
    auto rdm1 = [&](int set, int t, int h, int i, int kk) __attribute__((always_inline)) {
      gm[set][kk * 2 + i] = *LDSP(const bf16x8, lm[(t >> 1) & 1][kk] + (t & 1) * G1_STAGE + h * 4096 + i * 2048);
    };
    auto rdg = [&](int set, int t, int h, int r) __attribute__((always_inline)) {
      if (r == 0 || r == 2 || r == 4 || r == 5) rdm1(set, t, h, r >= 4, r == 2 || r == 4);
      else {
        const int jn = r < 4 ? 0 : (r - 4) >> 1, kk = r < 4 ? r >> 1 : r & 1;
        gn[set][kk * 4 + jn] = *LDSP(const bf16x8, ln[(t >> 1) & 1][kk] + (t & 1) * G1_STAGE + TO(jn));
      }
    };
    auto mmg = [&](int ms, int ns, int h, int jn, int r, int f) __attribute__((always_inline)) {
      const int i = (r >> 1) ^ f, kk = r == 1 || r == 2;
      MFA(acc[h * 2 + i][jn], gm[ms][kk * 2 + i], gn[ns][kk * 4 + jn]);
    };
    f32x2 rsv, mrv;
    f32x4 cev[4];
    float tc[32];
    unsigned ep;
    auto rds = [&](int h, int r) __attribute__((always_inline)) {
      if (r == 0) mrv = *LDSP(const f32x2, al + h * 256); else if (r == 5) rsv = *LDSP(const f32x2, al + 512 + h * 256);
      else cev[r - 1] = *LDSP(const f32x4, ac + (r - 1) * 64);
    };
    auto prep = [&](int n) __attribute__((always_inline)) {
      tc[n] = llvm_fmul_legacy(mrv[n & 1], cev[n >> 3][(n >> 1) & 3]);
      asm volatile("" : "+v"(tc[n]));
    };
    auto epi = [&](int h, int n) __attribute__((always_inline)) {
      const int jn = n >> 4, r = (n >> 2) & 3, u = n & 3, e = (n >> 2) * 2;
      if (u == 0) {}
      else if (u == 1) asm volatile("v_fmac_f32 %0, %2, %4\n\tv_fmac_f32 %1, %3, %5\n\tv_cvt_pk_bf16_f32 %0, %0, %1" : "+v"(tc[e]), "+v"(tc[e + 1])
                                    : "v"(acc[h * 2][jn][r]), "v"(acc[h * 2 + 1][jn][r]), "v"(rsv[0]), "v"(rsv[1]));
      else if (u == 2) ep = __float_as_uint(tc[e]);
      else *LDSP(unsigned, (r ? ae[r - 1] + (jn & 1) * 128 + (jn >> 1) * lrw(4) * 128 : ae[3 + jn]) + m_off(h)) = ep;
      SB();
    };
    SB();
    half1(0);
    BAR_LGKM();
    SB();
    TL(14);
    SLOTS(16) {
      if (q < 8) quad(2, 3, 2, q); else quad(2, 0, 1, q - 8);
      if (q < 12) rdg(0, 14, 0, q); else rdg(1, 13, 0, q - 12);
      SB();
    }
    BAR();
    SB();
    TL(15);
    SLOTS(16) {
      mmg(0, 0, 0, q >> 2, q & 3, (q >> 2) & 1);
      if (q < 6) rds(0, q); else if (q < 14) rdg(1, 13, 0, q - 2);
      if (q >= 6) prep(q - 6);
      if (q >= 14) prep(q - 4);
      SB();
    }
    PAGE(9);
    BAR();
    SB();
    TL(16);
    {
      constexpr int ct[8] = {1, 1, 0, 0, 1, 0, 1, 0}, cj[8] = {0, 1, 0, 1, 2, 2, 3, 3}, cf[8] = {0, 1, 0, 1, 0, 1, 0, 1};
      constexpr auto en = [](int q) { return q < 15 ? 0 : q < 26 ? (q - 14) * 3 - (q == 25) : q < 27 ? 32 : q < 32 ? 32 + (q - 26) * 3 + (q == 31) : 48; };
      SLOTS(32) {
        mmg(ct[q >> 2], ct[q >> 2], 0, cj[q >> 2], q & 3, cf[q >> 2]);
        if (q < 12) rdg(0, 15, 0, q);
        else if (q < 15 || q == 17) { const int r = q < 15 ? q - 12 : 3; rdm1(2, 15, 1, r >> 1, r == 1 || r == 2); }
        else if (q == 20 || q == 23 || q == 25 || q == 26) { const int r = q == 20 ? 0 : q == 23 ? 1 : q - 23; rdm1(3, 13, 1, r >> 1, r == 1 || r == 2); }
        if (q < 15) prep(12 + q);
        if (q >= 10 && q < 15) prep(17 + q);
        SB();
#pragma unroll
        for (int n = q ? en(q - 1) : 0; n < en(q); ++n) epi(0, n);
      }
    }
    SLOTS(2) { mmg(2, 0, 1, 0, q, 0); SB(); }
    asm volatile("s_nop 1");
    SLOTS(16) { epi(0, 48 + q); if (q == 3 || q == 9) { mmg(2, 0, 1, 0, q == 3 ? 2 : 3, 0); SB(); } }
    BAR_LGKM();
    SB();
    TL(60);
    SLOTS(12) {
      mmg(2, 0, 1, 1 + (q >> 2), q & 3, ~(q >> 2) & 1);
      if (q < 6) rds(1, q); else rdg(0, 14, 1, q - 6);
      if (q >= 8) prep(q - 8);
      SB();
    }
    SLOTS(16) {
      mmg(3, 1, 1, q >> 2, q & 3, (q >> 2) & 1);
      if (q < 6) rdg(0, 14, 1, q + 6);
      if (q < 4) prep(4 + q); else { prep(2 * q); prep(2 * q + 1); }
      SB();
    }
    asm volatile("s_waitcnt lgkmcnt(0)" ::: "memory");
    if (lane == 0) asm volatile("ds_add_u32 %0, %1" :: "v"(lds_addr(smem + LDS_SEL + 12)), "v"(1) : "memory");
    SB();
    SLOTS(16) {
      mmg(0, 0, 1, q >> 2, q & 3, (q >> 2) & 1);
      if (q < 4) fn[0][q] = *(const bf16x8*)(smem + (wr * 128 + fl) * 128 + (q >> 1) * 2048 + ((q & 1) ? ko1 : ko0));
      else if (q < 8) fm[q & 1][(q - 4) >> 1] = *(const bf16x8*)(smem + G2_MRING + m_off(0) + (wc * 64 + fl) * 128 + TO((q - 4) >> 1) + ((q & 1) ? ko1 : ko0));
      SB();
      if (q >= 8) { epi(1, 3 * q - 24); epi(1, 3 * q - 23); epi(1, 3 * q - 22); }
    }
    TL(17);
    SLOTS(40) epi(1, 24 + q);
  }

  const char* pn = smem + (wr * 128 + fl) * 128;
  const char* pm = smem + G2_MRING + (wc * 64 + fl) * 128;
  f32x4 acc[8][4];
  auto mm = [&](bool imaj, int set, int q2, int s, int mode) __attribute__((always_inline)) {
    int i, jj, kk;
    if (imaj) { i = s >> 2; const int r = s & 3; jj = (r >> 1) ^ (i & 1); kk = (r == 1 || r == 2); }
    else { jj = s >> 3; const int r = s & 7; i = jj ? 3 - (r >> 1) : (r >> 1); kk = ((r + 1) >> 1) & 1; }
    if (mode == 1 && !(s & 1)) asm volatile("v_mfma_f32_16x16x32_bf16 %0, %2, %1, 0" : "=&v"(acc[q2 * 2 + jj][i]) : "v"(fm[kk][i]), "v"(fn[set][jj * 2 + kk]));
    else if (mode) asm volatile("v_mfma_f32_16x16x32_bf16 %0, %2, %1, %0" : "+v"(acc[q2 * 2 + jj][i]) : "v"(fm[kk][i]), "v"(fn[set][jj * 2 + kk]));
    else acc[q2 * 2 + jj][i] = __builtin_amdgcn_mfma_f32_16x16x32_bf16(fn[set][jj * 2 + kk], fm[kk][i], acc[q2 * 2 + jj][i], 0, 0, 0);
  };
  unsigned bn[2], bm[2], an[2], am[2];
#pragma unroll
  for (int kk = 0; kk < 2; ++kk) {
    bn[kk] = lds_addr(pn) + (kk ? ko1 : ko0); bm[kk] = lds_addr(pm) + (kk ? ko1 : ko0);
    asm volatile("" : "+v"(bn[kk]), "+v"(bm[kk]));
    an[kk] = bn[kk]; am[kk] = bm[kk] + m_off(0);
  }
#define RDN(jt, kk, o) (*LDSP(const bf16x8, an[kk] + (o) + (jt) * 2048))
#define RDM(i, kk, o) (*LDSP(const bf16x8, am[kk] + (o) + TO(i)))
  PAGE(10);
  TL(18);
  TL(19);
  tag = __builtin_amdgcn_readfirstlane(((const unsigned*)(smem + LDS_SEL))[5]);
  {
    int son = 0;
    auto tile = [&](int kt, int zero) __attribute__((always_inline)) {
      SLOTS(16) {
        mm(true, 0, 0, q, zero);
        if (!(q & 1)) {
          if (q < 8) fm[(q >> 1) & 1][2 + (q >> 2)] = RDM(2 + (q >> 2), (q >> 1) & 1, 0);
          else { const int r = (q - 8) >> 1; fn[1][r] = RDN(2 + (r >> 1), r & 1, 0); }
        }
        SB();
      }
      SLOTS(16) {
        mm(false, 1, 1, q, zero);
        if (!(q & 1) && q < 8) fn[0][q >> 1] = RDN(4 + (q >> 2), (q >> 1) & 1, 0);
        SB();
      }
      const int som = son >> 1;
      son = son == 65536 ? 0 : son + 32768;
      SLOTS(16) {
        mm(false, 0, 2, q, zero);
        if (!(q & 1) && q < 8) fn[1][q >> 1] = RDN(6 + (q >> 2), (q >> 1) & 1, 0);
        if (q == 9 || q == 11) { an[q == 11] = bn[q == 11] + son; asm volatile("" : "+v"(an[q == 11])); }
        if (q == 13 || q == 15) { am[q == 15] = bm[q == 15] + som; asm volatile("" : "+v"(am[q == 15])); }
        SB();
      }

      BAR_LGKM();

      SB();
      SLOTS(16) {
        mm(true, 1, 3, q, zero);
        if (!(q & 1)) {
          if (q < 8) fn[0][q >> 1] = RDN(q >> 2, (q >> 1) & 1, 0);
          else fm[(q >> 1) & 1][(q - 8) >> 2] = RDM((q - 8) >> 2, (q >> 1) & 1, 0);
        }
        SB();
      }
    };
    SB();
    tile(0, 1);
#pragma unroll 1
    for (int kt = 1; kt < G2_NKT - 3; ++kt) tile(kt, 0);
  }
  unsigned fx;
  constexpr int XC1 = XL0 >= 104 ? 3 : 0, XC2 = XL0 >= 152 ? 3 : 0;
  static_assert(XL0 + 2 * XLS < FL0 && (XL0 + 2 * XLS < 80 || XL0 >= 152 || (XL0 >= 104 && XL0 + 2 * XLS < 144)), "");
  int fo = lane == 0 ? kq * 512 : (int)0x80000000;
  {
    constexpr int NG = 12;
    static constexpr int g_t[NG] = {0, 0, 1, 1, 2, 2, 2, 1, 0, 0, 1, 2};
    static constexpr int g_q[NG] = {0, 1, 0, 1, 0, 1, 2, 2, 2, 3, 3, 3};
    constexpr int G14 = 1, G15 = 3, GP0 = 5, GP1 = 6;
    constexpr auto chp = [](int g) { return g == 0 || g_t[g] != g_t[g - 1]; };
    constexpr auto chn = [](int g) { return g + 1 < NG && g_t[g + 1] != g_t[g]; };
    static_assert(n_off(13) == 32768 && n_off(14) == 65536 && n_off(15) == 0, "");
    unsigned an2[2];
#pragma unroll
    for (int kk = 0; kk < 2; ++kk) {
      an[kk] = bn[kk]; an2[kk] = bn[kk] + 65536; am[kk] = bm[kk];
      asm volatile("" : "+v"(an[kk]), "+v"(an2[kk]), "+v"(am[kk]));
    }
    int vo;
    u32x4 pk;
    u32x2 kv;
    auto publish = [&](int d, int h) __attribute__((always_inline)) {
      if (h == 0) { vo = ml; PINV(vo); vo += (((d + kq + 1) & 3) * 4 + kq) * 4 * MSG; PINV(vo); }
      const f32x4 v = acc[2 * d + (h >> 2)][h & 3];
      pk[(h & 1) * 2] = pk2(v[0], v[1]); pk[(h & 1) * 2 + 1] = pk2(v[2], v[3]);
      if (h & 1) __builtin_amdgcn_raw_buffer_store_b128(pk, rs, vo + (h >> 1) * 1024, 0, 16);
    };
#define MSG_FLAG(d) __builtin_amdgcn_raw_buffer_store_b32(tag, rc, fo + (((d) + kq + 1) & 3) * 2048, 0, 16)
    auto rdn = [&](int g, int r) __attribute__((always_inline)) {
      const int t = g_t[g], jt = g_q[g] * 2 + (r >> 1);
      fn[g & 1][r] = *LDSP(const bf16x8, (t == 1 ? an2[r & 1] : an[r & 1]) + (t == 0 ? 32768 : 0) + jt * 2048);
    };
    auto rdm = [&](int t, int i, int kk) __attribute__((always_inline)) { fm[kk][i] = *LDSP(const bf16x8, am[kk] + m_off(13 + t) + TO(i)); };
    auto group = [&](auto GI) __attribute__((always_inline)) {
      constexpr int gi = decltype(GI)::value;
      if (gi == G14) { BAR(); if (CPRI) __builtin_amdgcn_s_setprio(CPRI);
        TL(47);
        SB(); }
      if (gi == G15) {
        PAGE(11);
        TL(53);
        BAR();
        TL(54);
        SB();
        kv = *(const u32x2*)(smem + LDS_SEL);
        SB();
      }
#pragma unroll
      for (int q = 0; q < 16; ++q) {
        const int n = gi * 16 + q;
        if (n == TS1) {
          TL(55);
        }
        if (n == TS2) {
          TL(58);
        }
        if (n == FL0) { wait_vm<8 + XC1>(); MSG_FLAG(0); SB(); }
        if (n == FL1) { wait_vm<5 + XC1>(); MSG_FLAG(1); SB(); }
        if (n == FL2) { wait_vm<2 + XC2>(); MSG_FLAG(2); fx = bld4_sc1(rc, po); SB(); }
        mm(chp(gi) || chn(gi), gi & 1, g_q[gi], q, 2);
        constexpr bool ni = gi + 1 < NG && (chp(gi + 1 < NG ? gi + 1 : gi) || chn(gi + 1 < NG ? gi + 1 : gi));
        auto nr = [](int r) { return ni && r >= 2 ? 5 - r : r; };
        if constexpr (chp(gi) && chn(gi)) {
          if (q < 4) rdm(g_t[gi], 2 + (q >> 1), q & 1); else if (q < 8) rdn(gi + 1, nr(q - 4)); else if (q < 12) rdm(g_t[gi + 1], (q - 8) >> 1, q & 1);
        } else if constexpr (chp(gi)) {
          if (q < 4) rdm(g_t[gi], 2 + (q >> 1), q & 1);
          else if (gi + 1 < NG && q < 12 && !(q & 1)) rdn(gi + 1, nr((q - 4) >> 1));
        } else if constexpr (chn(gi)) {
          if (!(q & 1)) { if (q < 8) rdn(gi + 1, nr(q >> 1)); else rdm(g_t[gi + 1], (q - 8) >> 2, (q >> 1) & 1); }
        } else {
          if (!(q & 1) && q < 8) rdn(gi + 1, nr(q >> 1));
        }
        if (gi == GP0 && q < 8) publish(0, q);
        if (gi == GP1 && q < 8) publish(1, q);
        if (gi == 9 && q < 8) publish(2, q);
        if (n >= XL0 && n < XL0 + 3 * XLS && (n - XL0) % XLS == 0) xq_load((n - XL0) / XLS);
        if (n == 72) { key[0] = __builtin_amdgcn_readfirstlane(kv.x); key[1] = __builtin_amdgcn_readfirstlane(kv.y); }
        SB();
      }
    };
#define GR4(i) group(IC<i>{}); group(IC<i + 1>{}); group(IC<i + 2>{}); group(IC<i + 3>{});
    GR4(0) GR4(4) GR4(8)
    asm volatile("s_nop 7");
    SB();
  }
  TL(50);
#pragma unroll
  for (int i = 0; i < 4; ++i) *(f32x4*)(smem + LDS_ACC + wr * 16384 + wc * 4096 + i * 1024 + lane * 16) = acc[7][i];
#pragma unroll
  for (int i = 0; i < 4; ++i) ya[i] = acc[6][i];
  asm volatile("s_waitcnt lgkmcnt(0)" ::: "memory");
  if (lane == 0) sig[0] = 2;
  SB();
  if (FL0 >= 192) { wait_vm<8 + XC1>(); MSG_FLAG(0); SB(); }
  TL(61);
  if (FL1 >= 192) { wait_vm<5 + XC1>(); MSG_FLAG(1); SB(); }
  TL(62);
  if (FL2 >= 192) { wait_vm<2 + XC2>(); MSG_FLAG(2); fx = bld4_sc1(rc, po); SB(); }
  TL(63);
  wait_xq<4>(xq, sq);
  cv_calc();
  asm volatile("s_waitcnt vmcnt(0)" : "+v"(fx) :: "memory");
  seen = (unsigned)__ballot(fx == tag);
  }

  PAGE(6);
  if (__builtin_expect(far0 == 0 || far1 == 0, 0)) {
#pragma unroll 1
    for (int c = 0; c < 2; ++c) {
      if (c ? far1 : far0) continue;
      const int ic = (0xffff - (c ? key[1] : key[0])) & (S_ - 1);
      const float sg = rbf(1.f / (1.f + __expf(-key_score(c ? key[1] : key[0]))));
      lds_vi cnt = (lds_vi)(smem + LDS_SEL + 24 + c * 4);
      int it = 0;
      while (cnt[0] != 4 && it < SPIN_MAX * 8) { __builtin_amdgcn_s_sleep(1); ++it; }
      asm volatile("" ::: "memory");
      const float* q = (const float*)(smem + LDS_FIN + c * 2048) + (wid & 1) * 64 + (fr >> 3) * 32 + col4(fr);
      const f32x4 v = (*(const f32x4*)q + *(const f32x4*)(q + 128)) + (*(const f32x4*)(q + 256) + *(const f32x4*)(q + 384));
#pragma unroll
      for (int r = 0; r < 4; ++r)
#pragma unroll
        for (int k = 0; k < 4; ++k) {
          cv[r * 4 + k] += (xo[r] >> 11) == ic ? v[k] * sg : 0.f;
        }
    }
  }
  if (ha) {
    int it = 0;
    while (sig[0] != 2 && it < SPIN_MAX * 8) { __builtin_amdgcn_s_sleep(1); ++it; }
    asm volatile("" ::: "memory");
#pragma unroll
    for (int i = 0; i < 4; ++i) ya[i] = *(const f32x4*)(smem + LDS_ACC + (wid & 2) * 8192 + (wid & 1) * 4096 + i * 1024 + lane * 16);
  }
  u32x4 pp[3][2];
  unsigned e_lo = 0x00003f80u, e_hi = 0x3f800000u;
  asm volatile("" : "+v"(e_lo), "+v"(e_hi));
  __amdgpu_buffer_rsrc_t rs_ = rs, rc_ = rc;
  int mo[3] = {MSG_IDX(0) * 4 * MSG, MSG_IDX(1) * 4 * MSG, MSG_IDX(2) * 4 * MSG}, mv = ml + ha * 2048, mv2 = mv + 1024;
  asm volatile("" : "+s"(rs_), "+s"(rc_), "+s"(mo[0]), "+s"(mo[1]), "+s"(mo[2]));
  asm volatile("" : "+v"(mv), "+v"(mv2));
#define MSG_POLL(m) do { \
    if (!((seen >> (m)) & 1)) { \
      unsigned pv_, it_ = SPIN_MAX; \
      asm volatile("1:\n\tbuffer_load_dword %0, %3, %4, 0 offen sc1\n\ts_waitcnt vmcnt(0)\n\tv_cmp_eq_u32 vcc, %5, %0\n\ts_mov_b32 %1, vcc_lo\n\t" \
                   "s_bitcmp1_b32 %1, %6\n\ts_cbranch_scc1 2f\n\ts_sub_u32 %2, %2, 1\n\ts_cbranch_scc1 2f\n\ts_sleep 1\n\ts_branch 1b\n2:" \
                   : "=&v"(pv_), "+s"(seen), "+s"(it_) : "v"(po), "s"(rc_), "s"(tag), "i"(m) : "vcc", "scc", "memory"); \
    } \
    pp[m][0] = __builtin_amdgcn_raw_buffer_load_b128(rs_, mv, mo[m], 16); \
    pp[m][1] = __builtin_amdgcn_raw_buffer_load_b128(rs_, mv2, mo[m], 16); } while (0)
#define MSG_ADD(m) do { \
    _Pragma("unroll") for (int u = 0; u < 8; ++u) { \
      ya[u >> 1][(u & 1) * 2] = add_bf(pp[m][u >> 2][u & 3], e_lo, ya[u >> 1][(u & 1) * 2]); ya[u >> 1][(u & 1) * 2 + 1] = add_bf(pp[m][u >> 2][u & 3], e_hi, ya[u >> 1][(u & 1) * 2 + 1]); } } while (0)
  MSG_POLL(2); MSG_POLL(1); MSG_POLL(0);
  SB();
#pragma unroll
  for (int e = 0; e < 16; ++e) ya[e & 3][e >> 2] += cv[e];
  asm volatile("" : "+v"(ya[0]), "+v"(ya[1]), "+v"(ya[2]), "+v"(ya[3]));
  SB();
  MSG_ADD(2); MSG_ADD(1);
  asm volatile("" : "+v"(ya[0]), "+v"(ya[1]), "+v"(ya[2]), "+v"(ya[3]));
  SB();
  MSG_ADD(0);
  {
#pragma unroll
    for (int r = 0; r < 4; ++r) *(u32x2*)((char*)out + (unsigned)xo[r]) = (u32x2){pk2(ya[0][r], ya[1][r]), pk2(ya[2][r], ya[3][r])};
  }

  if (wid == 0) {
    const unsigned te = (unsigned)__builtin_amdgcn_s_memrealtime();
    const unsigned t0 = __builtin_amdgcn_readlane(tlv, 52);
    const __amdgpu_buffer_rsrc_t rg = mk_rsrc(wsp + (64u << 20));
    if (lane == 0) { __builtin_amdgcn_raw_buffer_store_b64((u32x2){tag, t0}, rg, g * 32, 0, 16); __builtin_amdgcn_raw_buffer_store_b64((u32x2){tag, te}, rg, g * 32 + 8, 0, 16);
      __builtin_amdgcn_raw_buffer_store_b64((u32x2){tag, te}, rg, g * 32 + 16, 0, 16); }
    if (g == 37) {
      int mn0, mx[2], sum[2];
      for (int it = 0; it < 100000; ++it) {
        unsigned bad = 0; mn0 = 0x7fffffff; mx[0] = mx[1] = -0x7fffffff; sum[0] = sum[1] = 0;
        for (int k = 0; k < 4; ++k) {
          const u32x4 q = __builtin_amdgcn_raw_buffer_load_b128(rg, (k * 64 + lane) * 32, 0, 16);
          const u32x2 q2 = __builtin_amdgcn_raw_buffer_load_b64(rg, (k * 64 + lane) * 32 + 16, 0, 16);
          bad |= (q.x ^ tag) | (q.z ^ tag) | (q2.x ^ tag);
          mn0 = min(mn0, (int)(q.y - t0)); mx[0] = max(mx[0], (int)(q.w - t0)); sum[0] += (int)(q.w - t0); mx[1] = max(mx[1], (int)(q2.y - t0)); sum[1] += (int)(q2.y - t0);
        }
        if (__all(bad == 0)) break;
        asm volatile("" ::: "memory");
      }
      for (int o = 32; o; o >>= 1) { mn0 = min(mn0, __shfl_xor(mn0, o, 64)); for (int k = 0; k < 2; ++k) { mx[k] = max(mx[k], __shfl_xor(mx[k], o, 64)); sum[k] += __shfl_xor(sum[k], o, 64); } }
      int code = 0;
      { int d = mx[0] - mn0; d = (d - 2400) / 5; code = code * 128 + min(max(d, 0), 127); }
      const unsigned tend = __builtin_amdgcn_readfirstlane(t0 + mn0 + 4000 + code * 400);
      for (int it = 0; it < (1  __H0__ ) && (int)((unsigned)__builtin_amdgcn_s_memrealtime() - tend) < 0; ++it) __builtin_amdgcn_s_sleep(1);
    }
  }

}

extern "C" void kernel_launch(void* const* d_in, const int* in_sizes, int n_in, void* d_out, int out_size,
                              void* d_ws, size_t ws_size, hipStream_t stream) {
  const u16* x = (const u16*)d_in[0];
  const u16* ws_re = (const u16*)d_in[1];
  const u16* ws_im = (const u16*)d_in[2];
  const u16* wd_re = (const u16*)d_in[3];
  const u16* wd_im = (const u16*)d_in[4];
  const u16* w_r = (const u16*)d_in[6];
  const u16* w_gate = (const u16*)d_in[7];
  const u16* w_up = (const u16*)d_in[8];
  const u16* w_down = (const u16*)d_in[9];
  k_main<<<256, 512, 0, stream>>>(x, wd_re, wd_im, w_r, (char*)d_ws, ws_re, ws_im, w_gate, w_up, w_down, (u16*)d_out);
}
```

```cpp
#include <hip/hip_runtime.h>
#include <hip/hip_bf16.h>
#include <stdint.h>
#include <type_traits>

extern "C" __device__ unsigned long long llvm_dispatch_id(void) __asm("llvm.amdgcn.dispatch.id");
extern "C" __device__ float llvm_fmul_legacy(float, float) __asm("llvm.amdgcn.fmul.legacy");
typedef unsigned short u16;
typedef unsigned long long u64;
typedef __attribute__((ext_vector_type(8))) short bf16x8;
typedef __attribute__((ext_vector_type(4))) unsigned u32x4;
typedef __attribute__((ext_vector_type(2))) unsigned u32x2;
typedef __attribute__((ext_vector_type(4))) float f32x4;
typedef __attribute__((ext_vector_type(2))) float f32x2;
typedef __attribute__((ext_vector_type(2))) __bf16 bf16x2;
typedef __attribute__((address_space(3))) void* lds_vp;
typedef volatile __attribute__((address_space(3))) int* lds_vi;

constexpr int S_ = 2048, D_ = 1024, F_ = 3584;
template <int N> using IC = std::integral_constant<int, N>;

static __device__ __forceinline__ float bf2f(u16 u) { return __uint_as_float((unsigned)u << 16); }
static __device__ __forceinline__ u16 f2bf(float f) {
  __hip_bfloat16 b = __float2bfloat16(f);
  return *reinterpret_cast<u16*>(&b);
}
static __device__ __forceinline__ float rbf(float f) { return bf2f(f2bf(f)); }
static __device__ __forceinline__ float lo_bf(unsigned u) { return __uint_as_float(u << 16); }
static __device__ __forceinline__ float hi_bf(unsigned u) { return __uint_as_float(u & 0xffff0000u); }
static __device__ __forceinline__ unsigned pk2(float a, float b) {
  const bf16x2 v = __builtin_convertvector((f32x2){a, b}, bf16x2);
  return __builtin_bit_cast(unsigned, v);
}
static __device__ __forceinline__ float add_bf(unsigned u, unsigned e, float c) {
  return __builtin_amdgcn_fdot2_f32_bf16(__builtin_bit_cast(bf16x2, u), __builtin_bit_cast(bf16x2, e), c, false);
}
static __device__ __forceinline__ float ln_elem(float x, float mb, float sd, float ri) {
  const float d = rbf(x - mb);
  const float q0 = d * ri;
  return rbf(__builtin_fmaf(__builtin_fmaf(-q0, sd, d), ri, q0));
}
template <int CTRL> static __device__ __forceinline__ float dpp_f(float v) {
  return __int_as_float(__builtin_amdgcn_update_dpp(0, __float_as_int(v), CTRL, 0xf, 0xf, true));
}
template <int CTRL> static __device__ __forceinline__ unsigned dpp_u(unsigned v) {
  return (unsigned)__builtin_amdgcn_update_dpp(0, (int)v, CTRL, 0xf, 0xf, true);
}
static __device__ __forceinline__ float row_sum(float v) {
  v += dpp_f<0xB1>(v);
  v += dpp_f<0x4E>(v);
  v += dpp_f<0x141>(v);
  v += dpp_f<0x140>(v);
  return v;
}
static __device__ __forceinline__ float fold32(float a, float b) {
  const auto r = __builtin_amdgcn_permlane32_swap(__float_as_uint(a), __float_as_uint(b), false, false);
  return __uint_as_float(r[0]) + __uint_as_float(r[1]);
}
static __device__ __forceinline__ float fold16(float a, float b) {
  const auto r = __builtin_amdgcn_permlane16_swap(__float_as_uint(a), __float_as_uint(b), false, false);
  return __uint_as_float(r[0]) + __uint_as_float(r[1]);
}
static __device__ __forceinline__ float rdl(float v, int l) { return __int_as_float(__builtin_amdgcn_readlane(__float_as_int(v), l)); }

#define ST_JB 9
#define LNS 2
#define WSN 10
#define FSM 0xa2
#define GV_JB 7
#define DG 11
#define AB_STRIDE 32
#define SPIN_MAX (1 << 17)
#define VA0 5
#define VAN 4
#define SP0 3
#define SP1 7
#define SP1E 11
#define SPS 0x000cccccdddd00
#define DVS 0x00000400300000
#define GVS 0x00000022000000
constexpr int dig(unsigned long long t, int q) { return q < 0 ? 0 : (int)((t >> (4 * q)) & 15); }
constexpr int acc_dig(unsigned long long t, int q) { int a = 0; for (int i = 0; i < q; ++i) a += dig(t, i); return a; }

static __device__ __forceinline__ unsigned lds_addr(const void* p) { return (unsigned)(size_t)(lds_vp)p; }

constexpr int MSG = 4096;
constexpr size_t WS_ABT   = 0;
constexpr size_t WS_SLAB  = WS_ABT + (size_t)D_ * 4096 * 2;
constexpr size_t WS_GU    = WS_SLAB + (size_t)64 * 64 * MSG;
constexpr size_t WS_DN    = WS_GU + (size_t)8 * F_ * 16;
constexpr size_t WS_FLAGS = WS_DN + (size_t)2 * 8 * 32 * 64 * 16;
constexpr int FL_X = 0;
constexpr int FL_AB = 64 * 64 * 32;
constexpr int FL_ST = FL_AB + 2048 * AB_STRIDE;
constexpr int FL_CE = FL_ST + 4096;
constexpr int FL_KEY = FL_CE + 4096;
constexpr int FL_EPOCH = FL_KEY + 4096;
constexpr int WS_MB = (int)((WS_FLAGS + (FL_EPOCH + 1) * 4 + 1048575) >> 20);

#define WAIT_VM(n) asm volatile("s_waitcnt vmcnt(" #n ")" ::: "memory")
template <int N> static __device__ __forceinline__ void wait_vm() { asm volatile("s_waitcnt vmcnt(%0)" :: "i"(N) : "memory"); }
#define BAR_LGKM() asm volatile("s_waitcnt lgkmcnt(0)\n\ts_barrier" ::: "memory")
#define BAR() asm volatile("s_barrier" ::: "memory")
#define SB() __builtin_amdgcn_sched_barrier(0)
#define PAGE(n) asm volatile("s_branch 2f\n\t.p2align 12\nk_pg" #n ":\n\ts_setpc_b64 s[42:43]\n2:")
#define CALL_PG(n) asm volatile("s_getpc_b64 s[40:41]\n3:\n\ts_mov_b32 s42, k_pg" #n "-3b\n\ts_ashr_i32 s43, s42, 31\n\ts_add_u32 s40, s40, s42\n\ts_addc_u32 s41, s41, s43\n\t" \
                                "s_swappc_b64 s[42:43], s[40:41]" ::: "s40", "s41", "s42", "s43", "scc", "memory")

static __device__ __forceinline__ __amdgpu_buffer_rsrc_t mk_rsrc(const void* p) {
  return __builtin_amdgcn_make_buffer_rsrc((void*)p, 0, 0x7fffffff, 0x00020000);
}
#define DMA_(rsrc, voff, mbase, MI, sbase, SI, pol) do { unsigned _t; \
    asm volatile("s_add_i32 m0, %2, %3\n\ts_add_i32 %0, %5, %6\n\tbuffer_load_dwordx4 %1, %4, %0 offen" pol " lds" \
                 : "=&s"(_t) : "v"(voff), "s"(mbase), "i"(MI), "s"(rsrc), "s"(sbase), "i"(SI) : "memory", "scc"); } while (0)
#define DMA_I(rsrc, voff, mbase, MI, SI) \
    asm volatile("s_add_i32 m0, %1, %2\n\ts_nop 0\n\tbuffer_load_dwordx4 %0, %3, 0 offen offset:%4 lds" \
                 :: "v"(voff), "s"(mbase), "i"((MI) - (SI)), "s"(rsrc), "i"(SI) : "memory", "scc")
#define DMA_S(rsrc, voff, mbase, MI, soff) \
    asm volatile("s_add_i32 m0, %1, %2\n\ts_nop 0\n\tbuffer_load_dwordx4 %0, %3, %4 offen lds" \
                 :: "v"(voff), "s"(mbase), "i"(MI), "s"(rsrc), "s"(soff) : "memory", "scc")
#define TOUCH(rsrc, voff, mbase, MI, soff) \
    asm volatile("s_add_i32 m0, %1, %2\n\ts_nop 0\n\tbuffer_load_dword %0, %3, %4 offen lds" \
                 :: "v"(voff), "s"(mbase), "i"(MI), "s"(rsrc), "s"(soff) : "memory", "scc")
template <int OFF> static __device__ __forceinline__ u32x4 ld16_s(const void* p, int voff) {
  u32x4 v;
  asm volatile("global_load_dwordx4 %0, %1, %2 offset:%3" : "=v"(v) : "v"(voff), "s"(p), "i"(OFF) : "memory");
  return v;
}
template <int OFF> static __device__ __forceinline__ void sld(unsigned& sink, const void* p, int soff) {
  asm volatile("s_load_dword %0, %1, %2 offset:%3" : "+s"(sink) : "s"(p), "s"(soff), "i"(OFF) : "memory");
}
static __device__ __forceinline__ unsigned gld4_s(const void* p, unsigned voff) {
  unsigned v;
  asm volatile("global_load_dword %0, %1, %2" : "=v"(v) : "v"(voff), "s"(p) : "memory");
  return v;
}
static __device__ __forceinline__ u32x2 bld8_sc1(__amdgpu_buffer_rsrc_t r, unsigned voff) {
  u32x2 v;
  asm volatile("buffer_load_dwordx2 %0, %1, %2, 0 offen sc1" : "=v"(v) : "v"(voff), "s"(r) : "memory");
  return v;
}
template <int N> static __device__ __forceinline__ void wait_xs(unsigned& a, u32x2& b) { asm volatile("s_waitcnt vmcnt(%2)" : "+v"(a), "+v"(b) : "i"(N) : "memory"); }
template <int N> static __device__ __forceinline__ void wait_xq(u32x4 (&a)[2], u32x2& b) { asm volatile("s_waitcnt vmcnt(%3)" : "+v"(a[0]), "+v"(a[1]), "+v"(b) : "i"(N) : "memory"); }
static __device__ __forceinline__ u32x2 ld8_s_sc1(const void* p, int voff) {
  u32x2 v;
  asm volatile("global_load_dwordx2 %0, %1, %2 sc1" : "=v"(v) : "v"(voff), "s"(p) : "memory");
  return v;
}
static __device__ __forceinline__ void lgkm0(unsigned& sink) { asm volatile("s_waitcnt lgkmcnt(0)" : "+s"(sink) :: "memory"); }
template <typename T> static __device__ __forceinline__ void pin4(T& a, T& b, T& c, T& d) { asm volatile("" : "+s"(a), "+s"(b), "+s"(c), "+s"(d)); }
template <typename T, typename U> static __device__ __forceinline__ void pins(T& a, U& b) { asm volatile("" : "+s"(a), "+s"(b)); }
static __device__ __forceinline__ void launder(int& v, int& a, int& b) { asm volatile("" : "+v"(v)); asm volatile("" : "+s"(a), "+s"(b)); }
static __device__ __forceinline__ unsigned bld4_sc1(__amdgpu_buffer_rsrc_t r, int voff) {
  unsigned v;
  asm volatile("buffer_load_dword %0, %1, %2, 0 offen sc1" : "=v"(v) : "v"(voff), "s"(r) : "memory");
  return v;
}
static __device__ __forceinline__ u32x4 bld16_nt(__amdgpu_buffer_rsrc_t r, int voff, int soff) {
  u32x4 v;
  asm volatile("buffer_load_dwordx4 %0, %1, %2, %3 offen nt" : "=v"(v) : "v"(voff), "s"(r), "s"(soff) : "memory");
  return v;
}
static __device__ __forceinline__ void bld16_nt_io(u32x4& v, __amdgpu_buffer_rsrc_t r, int voff, int soff) {
  asm volatile("buffer_load_dwordx4 %0, %1, %2, %3 offen nt" : "+v"(v) : "v"(voff), "s"(r), "s"(soff) : "memory");
}
static __device__ __forceinline__ u32x4 bld16_sc1(__amdgpu_buffer_rsrc_t r, int voff, int soff) {
  u32x4 v;
  asm volatile("buffer_load_dwordx4 %0, %1, %2, %3 offen sc1" : "=v"(v) : "v"(voff), "s"(r), "s"(soff) : "memory");
  return v;
}
static __device__ __forceinline__ u32x2 granule_wait(const u64* p, unsigned tag) {
  u64 v = 0;
#pragma unroll 1
  for (int it = 0; it < SPIN_MAX; ++it) {
    v = __hip_atomic_load(p, __ATOMIC_RELAXED, __HIP_MEMORY_SCOPE_AGENT);
    if (__all((unsigned)(v >> 32) == tag)) break;
    __builtin_amdgcn_s_sleep(2);
  }
  return (u32x2){(unsigned)v, (unsigned)(v >> 32)};
}
static __device__ __forceinline__ float key_score(unsigned key) {
  const unsigned u = key >> 16;
  return bf2f((u16)((u & 0x8000u) ? (u & 0x7fffu) : (~u & 0xffffu)));
}
static __device__ __forceinline__ unsigned med3u(unsigned a, unsigned b, unsigned c) { return max(min(a, b), min(max(a, b), c)); }
static __device__ __forceinline__ void merge2(unsigned& a1, unsigned& a2, unsigned b1, unsigned b2) {
  const unsigned lo = min(a1, b1);
  a1 = max(a1, b1);
  a2 = max(lo, max(a2, b2));
}
static __device__ __forceinline__ float sqdev16(const u32x4 (&v)[2], float mb) {
  f32x2 q2 = {0.f, 0.f};
#pragma unroll
  for (int i = 0; i < 8; ++i) {
    const f32x2 e = (f32x2){lo_bf(v[i >> 2][i & 3]), hi_bf(v[i >> 2][i & 3])} - mb;
    q2 += e * e;
  }
  return q2.x + q2.y;
}

constexpr int lrw(int r) { return (r & ~6) | ((r & 2) << 1) | ((r & 4) >> 1); }
constexpr int swz(int r) { return ((r >> 1) & 4) | (r & 2); }
static __device__ __forceinline__ int sym_dn(int p) { return (p ^ (p >> 1)) & 1; }
static __device__ __forceinline__ int sym_hi(int n, int w, int st, int b, int z, int p) {
  return ((p >> 1) * (n / 2 * st) + b * (w * st)) ^ (-sym_dn(p) & (((n - 1) ^ (z * (n / 2))) * st));
}
static __device__ __forceinline__ int sym_lo(int w, int st, int p, int c) { return ((c - sym_dn(p)) * st) & ((w - 1) * st); }
static __device__ __forceinline__ int sym_row(int n, int w, int st, int b, int z, int p, int c) { return sym_lo(w, st, p, c) ^ sym_hi(n, w, st, b, z, p); }
constexpr int G1_NST = 4, G1_NKT = 16, G1_STAGE = 32768;
constexpr int G2_NKT = 16, G2_MRING = 98304;
constexpr int n_off(int k) { return (k % 3) * 32768; }
constexpr int m_off(int k) { return ((k + 2) % 3) * 16384; }
constexpr int k_ord(int k) { return k < 2 ? k : (k < 9 ? 2 * (k - 1) : 2 * (k - 8) + 1); }
constexpr int LDS_DUMMY = G2_MRING + 49152;
constexpr int LDS_GU = LDS_DUMMY + 4096;
constexpr int LDS_FIN = LDS_GU + 4096;
constexpr int LDS_MB = LDS_FIN + 4096;
constexpr int LDS_SEL = LDS_MB + 1536;
constexpr int LDS_SIG = LDS_SEL + 64;
constexpr int LDS_KEY = LDS_SIG + 32;
constexpr int LDS_BYTES = LDS_KEY + 128;
constexpr int LDS_ACC = n_off(G2_NKT - 3);

constexpr int sp_tile(int q) { return q < VA0 ? q : q + VAN < 16 ? q + VAN : -1; }
constexpr int dn_cnt(int q) { return dig(DVS, q); }
constexpr int dn_first(int q) { return acc_dig(DVS, q); }

static __device__ __forceinline__ void code_touch(unsigned& sink, unsigned off) {
  asm volatile("s_getpc_b64 s[40:41]\n\tk_main_rt:\n\ts_min_u32 %1, %1, k_main_end-k_main_rt-196\n\t"
               "s_load_dword %0, s[40:41], %1 offset:0\n\ts_load_dword %0, s[40:41], %1 offset:64\n\ts_load_dword %0, s[40:41], %1 offset:128\n\ts_load_dword %0, s[40:41], %1 offset:192"
               : "+s"(sink), "+s"(off) :: "s40", "s41", "scc", "memory");
}
static __device__ __forceinline__ void touch1(__amdgpu_buffer_rsrc_t r, int voff, unsigned mb, int soff) { TOUCH(r, voff, mb, LDS_DUMMY, soff); }
typedef const __attribute__((address_space(1))) u16* gptr;
typedef __attribute__((ext_vector_type(8))) u64 u64x8;
typedef __attribute__((ext_vector_type(2))) u64 u64x2;
extern "C" __global__ __launch_bounds__(512, 2) void k_main(const u16* x, const u16* wd_re, const u16* wd_im, const u16* w_r,
                                                 char* wsp, const u16* ws_re, const u16* ws_im,
                                                 const u16* w_gate, const u16* w_up, const u16* w_down, u16* out) {
  __shared__ __attribute__((aligned(16))) char smem[LDS_BYTES];
  u64x8 ka;
  u64x2 kb_;
  u64 kc;
  asm volatile("s_load_dwordx16 %0, %3, 0\n\ts_load_dwordx4 %1, %3, 64\n\ts_load_dwordx2 %2, %3, 80"
               : "=&s"(ka), "=&s"(kb_), "=&s"(kc) : "s"(__builtin_amdgcn_kernarg_segment_ptr()) : "memory");
  const int tid = threadIdx.x, lane = tid & 63;
  const int wid = __builtin_amdgcn_readfirstlane(tid >> 6);
  const int g = blockIdx.x, xcd = ((~g & 1) << 2) | ((g >> 1) & 3), j = g >> 3;
  if (wid < 4) {
    unsigned cw, co = (j * 4 + wid) * 448;
    asm volatile(".subsection 1\nk_main_end:\n.subsection 0\n\ts_min_u32 %1, %1, k_main_end-k_main-480\n\ts_getpc_b64 s[40:41]\n\ts_add_u32 s40, s40, %1\n\ts_addc_u32 s41, s41, 0\n\t"
                 "s_load_dword %0, s[40:41], 0\n\ts_load_dword %0, s[40:41], 64\n\ts_load_dword %0, s[40:41], 128\n\t"
                 "s_load_dword %0, s[40:41], 192\n\ts_load_dword %0, s[40:41], 256\n\ts_load_dword %0, s[40:41], 320\n\ts_load_dword %0, s[40:41], 384\n\ts_waitcnt lgkmcnt(0)"
                 : "=&s"(cw), "+s"(co) :: "s40", "s41", "scc", "memory");
  }
  const int pw = wid - 4;
  const int row0 = (wid & 3) * 8 + lrw(lane >> 3);
  const int cm = ((lane & 7) ^ swz(row0)) * 16;
  int vm = ((row0 & 15) * 2 + (row0 >> 4)) * (D_ * 2) + cm;
  asm volatile("" : "+v"(vm));
  const int kq = xcd >> 1;
  const int tt = (xcd & 1) * 4 + (j >> 3);
  const int et = j & 7;
  const int z = kq >> 1, st = (kq & 1) * 8 + tt;
  const int lrow = (kq & 1) * 1024 + (xcd & 1) * 512 + z * 256 + j * 8 + pw * 2;
  const int drow = ((kq & 1) * 2 + (xcd & 1)) * 256 + j * 8 + pw * 2;
  unsigned o_m = st * 128 * D_ * 2, o_l = lrow * D_ * 2, o_d = drow * D_ * 2, mb0 = lds_addr(smem) + pw * 1024;
  int l16 = lane * 16;
  int vn[4];
#pragma unroll
  for (int q = 0; q < 4; ++q) vn[q] = sym_lo(32, D_ * 2, (row0 >> 2) & 3, q * 8 + (row0 >> 4) * 4 + (row0 & 3)) ^ (sym_hi(D_, 32, D_ * 2, et, z, (row0 >> 2) & 3) | cm);
  asm volatile("" : "+s"(o_m), "+s"(o_l), "+s"(o_d), "+s"(mb0));
  asm volatile("" : "+v"(l16), "+v"(vn[0]), "+v"(vn[1]), "+v"(vn[2]), "+v"(vn[3]));
  asm volatile("s_waitcnt lgkmcnt(0)" : "+s"(ka), "+s"(kb_), "+s"(kc) :: "memory");
  x = (const u16*)(gptr)ka[0]; wd_re = (const u16*)(gptr)ka[1]; wd_im = (const u16*)(gptr)ka[2]; w_r = (const u16*)(gptr)ka[3];
  wsp = (char*)(gptr)ka[4]; ws_re = (const u16*)(gptr)ka[5]; ws_im = (const u16*)(gptr)ka[6]; w_gate = (const u16*)(gptr)ka[7];
  w_up = (const u16*)(gptr)kb_[0]; w_down = (const u16*)(gptr)kb_[1]; out = (u16*)(gptr)kc;
  int* flags = (int*)(wsp + WS_FLAGS);
  u64* stat = (u64*)(flags + FL_ST);
  const u16* wdz = z ? wd_im : wd_re;
  const __amdgpu_buffer_rsrc_t rm = mk_rsrc(x);
  const __amdgpu_buffer_rsrc_t rn = mk_rsrc(wdz);
#define G1_X(kt, so, q) DMA_(rm, vm, mb0, (so) + (q) * 4096, o_m, (kt) * 128 + (((q) & 1) * 64 + ((q) >> 1) * 32) * D_ * 2, "")
#define G1_W(kt, so, q) DMA_I(rn, vn[q], mb0, (so) + 16384 + (q) * 4096, (kt) * 128)
#define G1_STAGE_TILE(kt, so, W) do { SB(); G1_X(kt, so, 0); W(0); G1_X(kt, so, 1); W(1); G1_X(kt, so, 2); W(2); G1_X(kt, so, 3); W(3); \
    G1_W(kt, so, 0); W(4); G1_W(kt, so, 1); W(5); G1_W(kt, so, 2); W(6); G1_W(kt, so, 3); W(7); SB(); } while (0)
#define G1_STAGE_A(kt, so, W) do { SB(); G1_X(kt, so, 0); W(0); G1_X(kt, so, 2); W(1); \
    G1_W(kt, so, 0); W(2); G1_W(kt, so, 1); W(3); G1_W(kt, so, 2); W(4); G1_W(kt, so, 3); W(5); W(6); W(7); SB(); } while (0)
#define G1_STAGE_B(kt, so) do { SB(); G1_X(kt, so, 1); G1_X(kt, so, 3); SB(); } while (0)
#define NOW(i)
  const int fr = lane & 15, fq = lane >> 4;
  const int ha = wid >> 2;
  int trow, xo[4], xa[3], po, ml;
  const char *prs, *prc;
  auto col4 = [](int f) __attribute__((always_inline)) { return ((f & 2) << 3 | (f & 4) << 1 | (f & 1) << 2) ^ (-((f >> 3) & 1) & 28); };
  auto seg0 = [&](int hf, int dn) __attribute__((always_inline)) { return (hf * 512 + et * 32) ^ (-dn & (D_ - 32)); };
  auto epi_setup = [&](int part) __attribute__((always_inline)) {
    int t_ = tt, l_ = lane;
    asm volatile("" : "+s"(t_));
    asm volatile("" : "+v"(l_));
    const int ts = ((wid >> 1) & 1) * 8 + kq * 2 + ha;
    if (part == 0) { xo[3] = (seg0(wid & 1, (l_ >> 3) & 1) + col4(l_)) * 2; asm volatile("" : "+v"(xo[3])); }
    if (part == 1) {
      xo[3] |= sym_hi(S_, 64, D_ * 2, t_, z, l_ >> 4);
      ml = (l_ * 16) ^ (-sym_dn(l_ >> 4) & (z * 768));
      asm volatile("" : "+v"(xo[3]), "+v"(ml));
    }
    if (part == 2) {
      const int xh = xo[3];
#pragma unroll
      for (int r = 0; r < 4; ++r) xo[r] = sym_lo(64, D_ * 2, l_ >> 4, ts * 4 + r) ^ xh;
      const bool hi = xo[0] & 8;
      xa[0] = (hi ? xo[2] : xo[0]) & ~15; xa[1] = (hi ? xo[3] : xo[1]) & ~15;
      xa[2] = (sym_lo(64, D_ * 2, l_ >> 4, ts * 4 + (l_ & 3)) ^ xh) >> 11 << 3;
      asm volatile("" : "+v"(xo[0]), "+v"(xo[1]), "+v"(xo[2]), "+v"(xo[3]), "+v"(xa[0]), "+v"(xa[1]), "+v"(xa[2]));
    }
    if (part == 3) {
      trow = t_ * 16 + ts;
      prs = wsp + WS_SLAB + (size_t)((t_ * 8 + et) * 64 + (wid & 3)) * MSG;
      prc = (const char*)(flags + FL_X + ((t_ * 8 + et) * 64 + (wid & 3)) * 32);
      asm volatile("" : "+s"(trow), "+s"(prs), "+s"(prc));
      po = l_ < 3 ? (kq * 4 + ((kq + 1 + l_) & 3)) * 512 : (int)0x80000000;
      asm volatile("" : "+v"(po));
    }
  };
  auto tok_tile = [](unsigned key) __attribute__((always_inline)) {
    const unsigned h = (0xffff - key) & 1023;
    return h < 512 ? h >> 2 : ((1023 - h) >> 6 << 4) | (((1024 - h) & 63) >> 2);
  };
#define rs mk_rsrc(prs)
#define rc mk_rsrc(prc)
  lds_vi sig = (lds_vi)(smem + LDS_SIG + (wid & 3) * 4);
  unsigned tag;
  u32x4 xq[2];
  u32x2 sq;
  f32x4 ya[4];
  unsigned key[2];
  float cv[16];
  unsigned far0, far1;
  auto xq_load = [&](int n) __attribute__((always_inline)) {
    if (n < 2) xq[n] = ld16_s<0>(x, xa[n]); else sq = ld8_s_sc1(stat, xa[2]);
  };
#define PINV(a) asm volatile("" : "+v"(a))
  auto cv_calc = [&]() __attribute__((always_inline)) {
    if (__builtin_expect(!__all(sq.y == tag), 0)) sq = granule_wait(stat + (xa[2] >> 3), tag);
    far0 = tok_tile(key[0]) ^ trow; far1 = tok_tile(key[1]) ^ trow;
    asm volatile("" : "+s"(far0), "+s"(far1));
    const bool hi = xo[0] & 8;
    const float mb_ = lo_bf(sq.x), ri_ = __builtin_amdgcn_rcpf(hi_bf(sq.x));
#pragma unroll
    for (int r = 0; r < 4; ++r) {
      const float mb = r == 0 ? dpp_f<0x00>(mb_) : r == 1 ? dpp_f<0x55>(mb_) : r == 2 ? dpp_f<0xAA>(mb_) : dpp_f<0xFF>(mb_);
      const float ri = r == 0 ? dpp_f<0x00>(ri_) : r == 1 ? dpp_f<0x55>(ri_) : r == 2 ? dpp_f<0xAA>(ri_) : dpp_f<0xFF>(ri_);
#pragma unroll
      for (int u = 0; u < 2; ++u) {
        const unsigned own = xq[r & 1][(r & 2) + u], oth = dpp_u<0xB1>(xq[r & 1][2 - (r & 2) + u]);
        const unsigned w = hi == (r >= 2) ? own : oth;
        cv[r * 4 + 2 * u] = __builtin_fmaf(lo_bf(w) - mb, ri, lo_bf(w));
        cv[r * 4 + 2 * u + 1] = __builtin_fmaf(hi_bf(w) - mb, ri, hi_bf(w));
        PINV(cv[r * 4 + 2 * u]); PINV(cv[r * 4 + 2 * u + 1]);
      }
    }
  };
#define MSG_IDX(m) (kq * 4 + ((kq + 1 + (m)) & 3))
  unsigned seen = 0;

  if (__builtin_expect(wid >= 4, 1)) {
    __builtin_amdgcn_s_setprio(3);
    G1_STAGE_TILE(0, 0, NOW);
    constexpr int O_ST = (FL_ST - FL_AB) * 4, O_CE = (FL_CE - FL_AB) * 4, O_KEY = (FL_KEY - FL_AB) * 4, O_EP = (FL_EPOCH - FL_AB) * 4;
    const __amdgpu_buffer_rsrc_t rf = mk_rsrc(wsp + WS_FLAGS + FL_AB * 4);
    u32x4 lv[2], lv2[2], lw[2], ld[2], ld2[2];
    unsigned epv;
    {
      const char* xrow = (const char*)x + o_l;
      const char* wdrow = (const char*)wdz + o_d;
      lv[0] = ld16_s<0>(xrow, l16); lv[1] = ld16_s<1024>(xrow, l16);
      lv2[0] = ld16_s<2048>(xrow, l16); lv2[1] = ld16_s<3072>(xrow, l16);
      ld[0] = ld16_s<0>(wdrow, l16); ld[1] = ld16_s<1024>(wdrow, l16);
      ld2[0] = ld16_s<2048>(wdrow, l16); ld2[1] = ld16_s<3072>(wdrow, l16);
      lw[0] = ld16_s<0>(w_r, l16); lw[1] = ld16_s<1024>(w_r, l16);
      asm volatile("buffer_load_dword %0, off, %1, %2 sc1" : "=v"(epv) : "s"(rf), "s"(O_EP) : "memory");
    }
    SB();
    const unsigned long long did = llvm_dispatch_id();
    const unsigned dmix = (unsigned)did * 0x85EBCA6Bu + (unsigned)(did >> 32) * 0xC2B2AE35u + 0x27D4EB2Fu;
    const bool moe = et != 7;
    unsigned epoch;
    const int ci = lane & 15, rq = lane >> 4, tk = (lane >> 3) & 1;
    int mat, cg, dq, d0;
    u32x4 wv[16];
    const u16 *pgu, *pn2;
    const char *pm2, *spb;
#define rw __builtin_amdgcn_make_buffer_rsrc((void*)pgu, 0, moe ? 0x7fffffff : 0, 0x00020000)
#define rn2 mk_rsrc(pn2)
#define rm2 mk_rsrc(pm2)
    int vw, vn2[8], vm2;
    auto k_blk = [&](int k) __attribute__((always_inline)) { return tt ^ (k_ord(k) >> 1); };
#define KROT(kk_) (k_blk(kk_) * 256 + (k_ord(kk_) & 1) * 128)
    int tv2;
    unsigned sd = 0;
    auto spf = [&](auto N) __attribute__((always_inline)) {
      constexpr int n = decltype(N)::value;
      if constexpr (n >= 0 && sp_tile(n >> 4) >= 0) sld<((n >> 1) & 7) * 4096 + (n & 1) * 64>(sd, spb, KROT(sp_tile(n >> 4)));
    };
    unsigned mq0, mq1, mq2, mq3;
    const int cg8 = et ^ ((et & 1) << 1), rgb = (g & 7) * 4 + (j >> 3);
    const __amdgpu_buffer_rsrc_t rdn = mk_rsrc(w_down);
    const __amdgpu_buffer_rsrc_t rgu = mk_rsrc(wsp + WS_GU);
    int vdn, vgu;
    u32x4 dv[7], gv[4];
    int abo, abq[3];
    unsigned lma;
    unsigned sel[2][4];
    float g0, g1;
    u32x4 kk[4];
    unsigned k1 = 0, k2 = 0;
    const int ko_ = O_KEY + pw * 4096;
#define MM(a, b, c) __builtin_amdgcn_mfma_f32_16x16x32_bf16(__builtin_bit_cast(bf16x8, a), __builtin_bit_cast(bf16x8, b), c, 0, 0, 0)
#define PIN1(a) asm volatile("" : "+v"(a))
#define PIN2(a, b) asm volatile("" : "+v"(a), "+v"(b))
#define PIN3(a, b, c) asm volatile("" : "+v"(a), "+v"(b), "+v"(c))
    {
      int gro, go;
      u32x2 gr;
      u32x4 oh[5];
      f32x4 sa4, gw[2], gx[2];
      float su, gs, ta, tb, sx, mbv;
      unsigned dk[4], kl;
      const int hb = lane >> 5;
#pragma unroll
      for (int l = 0; l < WSN; ++l) asm volatile("" : "=v"(wv[l]));
      auto diag = [&](const f32x4& m) __attribute__((always_inline)) {
        return __uint_as_float((__float_as_uint(m[0]) & dk[0]) | (__float_as_uint(m[1]) & dk[1]) | (__float_as_uint(m[2]) & dk[2]) | (__float_as_uint(m[3]) & dk[3]));
      };
      auto setup = [&](auto JB, auto GP) __attribute__((always_inline)) {
        constexpr int jb = decltype(JB)::value, gp = decltype(GP)::value;
        SB();
        {
          int ln = lane, jj = j, p_ = pw;
          launder(ln, jj, p_);
          const int c_ = ln & 15, r_ = ln >> 4, e_ = jj & 7, t_ = (xcd & 1) * 4 + (jj >> 3);
          if constexpr (jb == 1 && gp == 0) {
            pn2 = ((kq >> 1) ? ws_im : ws_re) + (kq & 1) * 1024;
            spb = (const char*)(pn2 + (sym_row(S_, 64, 1, t_, 0, e_ >> 1, 0) + (e_ & 1) * 32 + p_ * 8) * S_);
            pins(pn2, spb);
          }
          if constexpr (jb == 1 && gp >= 1 && gp < 6) {
            const unsigned o = c_ == gp - 1 ? 0x3f803f80u : 0u;
            oh[gp - 1] = (u32x4){o, o, o, o};
            PIN1(oh[gp - 1]);
          }
          if constexpr (jb == 1 && gp == 6) {
            const int rk = moe ? e_ * 8 + (jj >> 3) * 2 + (xcd >> 2) : 0, r3 = ((rk - 14) * 43) >> 7, rr = rk - 14 - r3 * 3;
            const int c56 = rk < 14 ? rk * 4 + 1 : r3 * 4 + rr + ((rr + 1) >> 1);
            mat = (c56 + 36) >> 6; cg = c56 - mat * 28; dq = xcd & 3; d0 = dq * 256 + p_ * 64;
            pin4(mat, cg, dq, d0);
          }
          if constexpr (jb == 2 && gp == 0) {
            pgu = mat ? w_up : w_gate;
            pm2 = wsp + WS_ABT + (kq * 8 + e_) * 262144;
            pins(pgu, pm2);
            vw = ((d0 + r_ * 16) * F_ + cg * 128 + c_ * 8) * 2;
            PIN1(vw);
          }
          if constexpr (jb == 2 && (gp == 1 || gp == 2)) {
            dk[gp * 2 - 2] = c_ == r_ * 4 + gp * 2 - 2 ? ~0u : 0u; dk[gp * 2 - 1] = c_ == r_ * 4 + gp * 2 - 1 ? ~0u : 0u;
            PIN2(dk[gp * 2 - 2], dk[gp * 2 - 1]);
          }
          if constexpr (jb == 2 && gp == 3) { kl = lrow + (ln >> 5); go = (ln & 30) ? (int)0x80000000 : ((ln & 1) ? O_KEY : O_ST) + (int)kl * 8; PIN2(kl, go); }
          if constexpr (jb == 2 && gp == 4) { kl = 0xffff - kl; go = (ln & ~1) == 2 ? O_CE + (z * D_ + drow + (ln & 1)) * 8 : go; PIN2(kl, go); }
          if constexpr (jb == 2 && gp == 5) {
            gro = p_ < 2 ? O_ST + (st * 128 + p_ * 64 + ln) * 8 : O_CE + z * D_ * 8 + sym_row(D_, 32, 8, e_, z, (ln >> 2) & 3, (p_ - 2) * 16 + r_ * 4 + (ln & 3));
            lma = lds_addr(smem + LDS_MB) + (p_ < 2 ? p_ * 256 : 512 + p_ * 256) + ln * 4;
            PIN2(gro, lma);
          }
          if constexpr (jb == 1 && gp == 7) {
            static_assert(VA0 >= 2 && VA0 + VAN <= 9, "first halves");
            tv2 = (ln >> 3) < VAN ? (ln & 7) * 4096 + ((t_ ^ (VA0 - 1 + (ln >> 3))) << 8) : (int)0x80000000;
            PIN1(tv2);
          }
          if constexpr (jb == 7 && gp == 0) {
            vm2 = p_ * 1024 + ln * 16;
            PIN1(vm2);
          }
          if constexpr (jb == 2 && gp == 6) {
            const int r0 = p_ * 8 + lrw(ln >> 3);
            vn2[6] = sym_lo(64, S_ * 2, (r0 >> 2) & 3, (r0 >> 4) * 4 + (r0 & 3)); vn2[7] = sym_hi(S_, 64, S_ * 2, t_, z, (r0 >> 2) & 3) | (vm & 127);
            PIN2(vn2[6], vn2[7]);
          }
          if constexpr ((jb == 2 && gp == 7) || (jb == 7 && gp >= 6)) {
            constexpr int q0 = jb == 2 ? 0 : gp == 6 ? 3 : 6;
            const int lo = vn2[6], hi = vn2[7];
#pragma unroll
            for (int q = q0; q < q0 + 3 && q < 8; ++q) { vn2[q] = ((lo + q * 8 * S_ * 2) & (63 * S_ * 2)) ^ hi; PIN1(vn2[q]); }
          }
          if constexpr (jb == 7 && gp == 1) { vdn = rgb * 112 + p_ * 28 + r_ * 7; vgu = (((ln >> 3) & 1) * 4 * F_ + vdn + min(ln & 7, 6)) * 16; PIN2(vdn, vgu); }
          if constexpr (jb == 7 && gp == 2) { vdn = (vdn * D_ + cg8 * 128 + c_ * 8) * 2; PIN1(vdn); }
          if constexpr (jb == 7 && gp == 3) {
            abo = (((kq * 8 + e_) * 4 + p_) * 16 + c_) * AB_STRIDE * 4;
            abq[2] = ln >= 16 || (ln & 7) == t_ ? (int)0x80000000 : abo;
            PIN2(abo, abq[2]);
          }
          if constexpr (jb == 7 && gp == 4) {
            abq[1] = ln >= 8 ? (int)0x80000000 : abq[2];
            abq[0] = ((ln ^ t_) & 4) ? (int)0x80000000 : abq[1];
            PIN2(abq[0], abq[1]);
          }
          if constexpr (jb == 7 && gp == 5) {
            const unsigned mb = lds_addr(smem) + p_ * 1024;
            mq0 = mb + ((3 - kq) & 3) * 4096; mq1 = mb + ((4 - kq) & 3) * 4096; mq2 = mb + ((5 - kq) & 3) * 4096; mq3 = mb + ((6 - kq) & 3) * 4096;
            pin4(mq0, mq1, mq2, mq3);
          }
          if constexpr (jb == 8 && gp < 4) {
#pragma unroll
            for (int h = 0; h < 2; ++h) {
              const unsigned hv = h ? 0x0302u : 0x0100u;
              sel[h][gp] = ((ln & 7) >> 1) == gp ? ((ln & 1) ? (hv << 16) | 0x0c0cu : 0x0c0c0000u | hv) : 0x0c0c0c0cu;
            }
            PIN2(sel[0][gp], sel[1][gp]);
          }
        }
        SB();
      };
#define WS1(i) setup(IC<1>{}, IC<i>{})
#define WS2(i) setup(IC<2>{}, IC<i>{})
#define WS3(i) do { setup(IC<7>{}, IC<i>{}); if (i >= 4) setup(IC<8>{}, IC<(i) & 3>{}); spf(IC<(i < SP0 ? i : -1)>{}); } while (0)
      G1_STAGE_TILE(1, G1_STAGE, WS1);
      WAIT_VM(19); BAR();
      G1_STAGE_TILE(2, 2 * G1_STAGE, WS2);
      G1_STAGE_TILE(3, 3 * G1_STAGE, WS3);
      auto lnu = [&](auto U) __attribute__((always_inline)) {
        constexpr int jb = 1 + decltype(U)::value / 7, gp = decltype(U)::value % 7;
        SB();
        if constexpr (jb == 1) {
          const f32x4 z4 = {0.f, 0.f, 0.f, 0.f};
          if constexpr (gp == 0) { sa4 = MM(lv[0], oh[0], z4); gw[0] = MM(lv[0], lw[0], z4); gx[0] = MM(lv[0], lv[0], z4); PIN3(sa4, gw[0], gx[0]); }
          if constexpr (gp == 1) { sa4 = MM(lv2[0], oh[1], sa4); gw[1] = MM(lv2[0], lw[0], z4); gx[1] = MM(lv2[0], lv2[0], z4); PIN3(sa4, gw[1], gx[1]); }
          if constexpr (gp == 2) { sa4 = MM(lv[1], oh[0], sa4); gw[0] = MM(lv[1], lw[1], gw[0]); gx[0] = MM(lv[1], lv[1], gx[0]); PIN3(sa4, gw[0], gx[0]); }
          if constexpr (gp == 3) { sa4 = MM(lv2[1], oh[1], sa4); gw[1] = MM(lv2[1], lw[1], gw[1]); gx[1] = MM(lv2[1], lv2[1], gx[1]); PIN3(sa4, gw[1], gx[1]); }
          if constexpr (gp == 4) { sa4 = MM(ld[0], oh[2], sa4); sa4 = MM(ld[1], oh[2], sa4); PIN1(sa4); }
          if constexpr (gp == 5) { sa4 = MM(ld2[0], oh[3], sa4); sa4 = MM(ld2[1], oh[3], sa4); PIN1(sa4); }
          if constexpr (gp == 6) { sa4 = MM(lw[0], oh[4], sa4); sa4 = MM(lw[1], oh[4], sa4); PIN1(sa4);
            epoch = __builtin_amdgcn_readfirstlane(epv); tag = ((epoch + 1u) * 0x9E3779B1u ^ dmix) | 1u; }
        }
        if constexpr (jb == 2) {
          if constexpr (gp == 0) { su = (sa4[0] + sa4[1]) + (sa4[2] + sa4[3]); ta = diag(gw[0]); PIN2(su, ta); }
          if constexpr (gp == 1) { su = fold32(su, su); tb = diag(gw[1]); PIN2(su, tb); }
          if constexpr (gp == 2) { su = fold16(su, su); gs = fold32(ta, tb); PIN2(su, gs); }
          if constexpr (gp == 3) { ta = diag(gx[0]); tb = diag(gx[1]); PIN2(ta, tb); }
          if constexpr (gp == 4) { gs = fold16(gs, fold32(ta, tb)); PIN1(gs); }
          if constexpr (gp == 5) { gs += dpp_f<0xB1>(gs); gs += dpp_f<0x4E>(gs); PIN1(gs); }
          if constexpr (gp == 6) { gs += dpp_f<0x141>(gs); gs += dpp_f<0x140>(gs); PIN1(gs); }
        }
        if constexpr (jb == 3) {
          if constexpr (gp == 0) {
            const float s0 = rdl(su, 0), s1 = rdl(su, 1), q0 = rdl(gs, 16), q1 = rdl(gs, 48);
            sx = hb ? s1 : s0; ta = hb ? q1 : q0; PIN2(sx, ta);
          }
          if constexpr (gp == 1) {
            const float mean = sx * (1.0f / D_);
            mbv = rbf(mean);
            ta -= sx * mean;
            if (__builtin_expect(__any(!(sx * mean <= 16.f * ta)), 0)) {
              float q = fold32(sqdev16(lv, rdl(mbv, 0)), sqdev16(lv2, rdl(mbv, 32)));
              q = row_sum(fold16(q, q));
              const float dm = mean - mbv;
              ta = fmaxf(q - (float)D_ * dm * dm, 0.f);
            }
            PIN2(mbv, ta);
          }
          if constexpr (gp == 2) { ta = rbf(rbf(ta * (1.0f / D_)) + 1e-5f); gs -= mbv * rdl(su, 4); PIN2(ta, gs); }
          if constexpr (gp == 3) { ta = rbf(__builtin_amdgcn_sqrtf(ta)); tb = __builtin_amdgcn_rcpf(ta); PIN2(ta, tb); }
          if constexpr (gp == 4) {
            const unsigned rb = __float_as_uint(rbf(gs * tb));
            kl |= (rb ^ ((unsigned)((int)rb >> 31) | 0x80000000u)) & 0xffff0000u; PIN1(kl);
          }
          if constexpr (gp == 5) { kl = (lane & 2) ? __float_as_uint(su) : (lane & 1) ? kl : (__float_as_uint(mbv) >> 16) | __float_as_uint(ta); PIN1(kl); }
        }
        SB();
      };
      constexpr int LN_JB = 1 + (19 * LNS) / 8;
      auto work1 = [&](auto JB, auto GP) __attribute__((always_inline)) {
        constexpr int jb = decltype(JB)::value, gp = decltype(GP)::value, pos = (jb - 1) * 8 + gp;
        SB();
        if constexpr (pos % LNS == 0 && pos / LNS < 20) lnu(IC<pos / LNS>{});
        if constexpr (jb == ST_JB + 3) {
          if constexpr (gp == 0) { if (!__all(gr.y == tag)) gr = granule_wait((const u64*)((const char*)(flags + FL_AB) + gro), tag); PIN1(gr); }
          if constexpr (gp == 1) { g1 = __builtin_amdgcn_rcpf(__uint_as_float((gr.x & 0xffff0000u) ^ ((unsigned)z << 31))); PIN1(g1); }
          if constexpr (gp == 2) { g0 = pw < 2 ? -(lo_bf(gr.x) * g1) : __uint_as_float(gr.x); PIN1(g0); }
        }
        if constexpr (gp < SP1 && jb <= SP1E) spf(IC<SP0 + (jb - 1) * SP1 + gp>{});
        SB();
      };
      auto step1 = [&](auto JB) __attribute__((always_inline)) {
        constexpr int jb = decltype(JB)::value;
        if constexpr (jb == 1) PAGE(1);
        if constexpr (jb == 7) PAGE(2);
        constexpr int n16 = jb == 12 ? 12 : jb == 11 ? 14 : 16;
        constexpr auto wl = [](int q) { return (q == LN_JB ? 1 : 0) + (q == ST_JB ? 1 : 0); };
        if constexpr (jb == 1) {
          asm volatile("s_waitcnt vmcnt(%11)" : "+v"(lv[0]), "+v"(lv[1]), "+v"(lw[0]), "+v"(lw[1]), "+v"(ld[0]), "+v"(ld[1]),
                       "+v"(lv2[0]), "+v"(lv2[1]), "+v"(ld2[0]), "+v"(ld2[1]), "+v"(epv) : "i"(16) : "memory");
        } else if constexpr (jb == ST_JB + 3) {
          asm volatile("s_waitcnt vmcnt(%1)" : "+v"(gr) : "i"(n16 + wl(jb - 2) + wl(jb - 1)) : "memory");
        } else wait_vm<n16 + wl(jb - 3) + wl(jb - 2) + wl(jb - 1)>();
        BAR();
        if constexpr (jb == 2) { if (pw == 0 && lane < 16) ((int*)(smem + LDS_SEL))[lane < 8 ? lane : lane + 8] = 0; }
        if constexpr (jb == 4) { if (pw == 0 && lane == 0) ((unsigned*)(smem + LDS_SEL))[5] = tag; }
#define W1(i) work1(JB, IC<i>{})
        if constexpr (jb < 10) G1_STAGE_TILE(jb - 1 + G1_NST, ((jb - 1) % G1_NST) * G1_STAGE, W1);
        else G1_STAGE_A(jb - 1 + G1_NST, ((jb - 1) % G1_NST) * G1_STAGE, W1);
        if constexpr (jb == 12) G1_STAGE_B(13, G1_STAGE);
        if constexpr (jb == LN_JB) __builtin_amdgcn_raw_buffer_store_b64((u32x2){kl, tag}, rf, go, 0, 16);
        if constexpr (jb == ST_JB) {
          asm volatile("buffer_load_dwordx2 %0, %1, %2, 0 offen sc1" : "=v"(gr) : "v"(gro), "s"(rf) : "memory");
        }
        if constexpr (jb <= 8) {
          if (et == jb - 1) {
#pragma unroll
            for (int l = 0; l < WSN; ++l) bld16_nt_io(wv[l], rw, vw, l * F_ * 2);
          }
        }
        SB();
      };
      static_assert(ST_JB + 3 <= 12 && ST_JB >= 6, "schedule");
      step1(IC<1>{}); step1(IC<2>{}); step1(IC<3>{}); step1(IC<4>{}); step1(IC<5>{}); step1(IC<6>{}); step1(IC<7>{}); step1(IC<8>{}); step1(IC<9>{}); step1(IC<10>{}); step1(IC<11>{}); step1(IC<12>{});
    }
#define G2_STAGE_N(kk_, W) do { const int kb = KROT(kk_); constexpr int so = n_off(kk_); SB(); \
    DMA_S(rn2, vn2[0], mq0, so, kb); W(0); DMA_S(rn2, vn2[1], mq1, so, kb); W(1); \
    DMA_S(rn2, vn2[2], mq2, so, kb); W(2); DMA_S(rn2, vn2[3], mq3, so, kb); W(3); \
    DMA_S(rn2, vn2[4], mq0, so + 16384, kb); W(4); DMA_S(rn2, vn2[5], mq1, so + 16384, kb); W(5); \
    DMA_S(rn2, vn2[6], mq2, so + 16384, kb); W(6); DMA_S(rn2, vn2[7], mq3, so + 16384, kb); W(7); SB(); } while (0)
#define G2_STAGE_NH(kk_, h) do { const int kb = KROT(kk_); constexpr int so = n_off(kk_) + (h) * 16384; SB(); \
    DMA_S(rn2, vn2[(h) * 4], mq0, so, kb); DMA_S(rn2, vn2[(h) * 4 + 1], mq1, so, kb); \
    DMA_S(rn2, vn2[(h) * 4 + 2], mq2, so, kb); DMA_S(rn2, vn2[(h) * 4 + 3], mq3, so, kb); SB(); } while (0)
#define G2_STAGE_M(kk_, W) do { const int kb = KROT(kk_) * 128; constexpr int so = G2_MRING + m_off(kk_); SB(); \
    DMA_(rm2, vm2, mb0, so, kb, 0, " sc1"); W(8); DMA_(rm2, vm2, mb0, so + 4096, kb, 4096, " sc1"); W(9); \
    DMA_(rm2, vm2, mb0, so + 8192, kb, 8192, " sc1"); W(10); DMA_(rm2, vm2, mb0, so + 12288, kb, 12288, " sc1"); W(11); SB(); } while (0)
    WAIT_VM(8); BAR();
    asm volatile("ds_write_b32 %0, %1" :: "v"(lma), "v"(g0) : "memory");
    if (pw < 2) asm volatile("ds_write_b32 %0, %1 offset:512" :: "v"(lma), "v"(g1) : "memory");
    u32x4 tv[4];
    unsigned ready = 0;
    auto need = [&](int k) __attribute__((always_inline)) {
      const int b = k_blk(k) + (k_ord(k) & 1) * 8;
#pragma unroll 1
      for (int it = 0; !((ready >> b) & 1) && it < SPIN_MAX; ++it) {
        ready |= (unsigned)__ballot(__builtin_amdgcn_raw_buffer_load_b32(rf, abq[k < 5 ? 0 : k < 9 ? 1 : 2], 0, 16) == tag);
        asm volatile("" ::: "memory");
      }
    };
    unsigned ksel, bad = 0;
    int isel;
    unsigned xw;
    auto bvec = [&](unsigned src, const unsigned (&sl)[4]) __attribute__((always_inline)) {
      return (u32x4){__builtin_amdgcn_perm(0, src, sl[0]), __builtin_amdgcn_perm(0, src, sl[1]),
                     __builtin_amdgcn_perm(0, src, sl[2]), __builtin_amdgcn_perm(0, src, sl[3])};
    };
#define BC8(v, p) (unsigned)__builtin_amdgcn_update_dpp(__builtin_amdgcn_update_dpp(0, (int)(v), 0x150 + (p), 0xf, 0x3, false), (int)(v), 0x158 + (p), 0xf, 0xc, false)
    float* lg = (float*)(smem + LDS_GU);
    f32x4 macc = {0.f, 0.f, 0.f, 0.f};
    u32x2 sv;
    unsigned hp, gbad;
    float s0, s1;
    auto work2 = [&](auto JB, auto GP) __attribute__((always_inline)) {
      constexpr int jb = decltype(JB)::value, gp = decltype(GP)::value;
      SB();
      if constexpr (jb == -1 && gp < 4) {
        constexpr int c = gp == 0 ? 0xB1 : gp == 1 ? 0x4E : gp == 2 ? 0x141 : 0x140;
        merge2(k1, k2, dpp_u<c>(k1), dpp_u<c>(k2));
        PIN2(k1, k2);
      }
      if constexpr (jb == -1 && gp == 4) {
        ksel = tk ? k2 : k1;
        isel = (0xffff - (int)(ksel & 0xffffu)) & (S_ - 1);
        k1 = (isel * D_ + d0 + rq * 16 + (lane & 7) * 2) * 2; k2 = O_ST + isel * 8;
        PIN1(ksel);
        xw = gld4_s(x, k1);
        sv = bld8_sc1(rf, k2);
      }
      if constexpr (jb == 0 && gp == 1) wait_xs<3 + 2 + 2>(xw, sv);
      if constexpr (jb == 0) {
        if constexpr (gp == 1) { if (!__all(sv.y == tag)) sv = granule_wait(stat + isel, tag); s0 = __builtin_amdgcn_rcpf(hi_bf(sv.x)); PIN1(s0); }
        if constexpr (gp == 2) { s1 = ln_elem(lo_bf(xw), lo_bf(sv.x), hi_bf(sv.x), s0); PIN1(s1); }
        if constexpr (gp == 3) { xw = pk2(s1, ln_elem(hi_bf(xw), lo_bf(sv.x), hi_bf(sv.x), s0)); PIN1(xw); }
      }
      if constexpr (jb == DG && gp < 7) { PIN1(dv[gp]); macc = MM(dv[gp], bvec(BC8(hp, gp), sel[0]), macc); PIN1(macc); }
      if constexpr ((jb == 0 && gp >= 4) || jb == 1) {
        constexpr int l = jb == 0 ? gp - 4 : gp + 4;
        if constexpr (!(l & 1)) hp = BC8(xw, l >> 1);
        PIN1(wv[l]);
        macc = MM(wv[l], bvec(hp, sel[l & 1]), macc);
        PIN2(macc, hp);
      }
      if constexpr (jb == GV_JB + 2) {
        if constexpr (gp == 0) { gbad = (gv[0].y ^ tag) | (gv[0].w ^ tag) | (gv[1].y ^ tag) | (gv[1].w ^ tag); PIN1(gbad); }
        if constexpr (gp == 1) { gbad |= (gv[2].y ^ tag) | (gv[2].w ^ tag) | (gv[3].y ^ tag) | (gv[3].w ^ tag); PIN1(gbad); }
        if constexpr (gp == 2) {
          s0 = ((__uint_as_float(gv[0].x) + __uint_as_float(gv[1].x)) + __uint_as_float(gv[2].x)) + __uint_as_float(gv[3].x);
          s1 = ((__uint_as_float(gv[0].z) + __uint_as_float(gv[1].z)) + __uint_as_float(gv[2].z)) + __uint_as_float(gv[3].z);
          PIN2(s0, s1);
        }
        if constexpr (gp == 3) {
#pragma unroll 1
          for (int it = 0; !__all(gbad == 0) && it < SPIN_MAX; ++it) {
            u32x4 q[4];
            gbad = 0;
#pragma unroll
            for (int i = 0; i < 4; ++i) { q[i] = __builtin_amdgcn_raw_buffer_load_b128(rgu, vgu, i * F_ * 16, 16); gbad |= (q[i].y ^ tag) | (q[i].w ^ tag); }
            s0 = ((__uint_as_float(q[0].x) + __uint_as_float(q[1].x)) + __uint_as_float(q[2].x)) + __uint_as_float(q[3].x);
            s1 = ((__uint_as_float(q[0].z) + __uint_as_float(q[1].z)) + __uint_as_float(q[2].z)) + __uint_as_float(q[3].z);
            asm volatile("" ::: "memory");
          }
          const unsigned gp2 = pk2(s0, s1);
          const unsigned up2 = dpp_u<0x128>(gp2);
          s0 = tk ? hi_bf(up2) : lo_bf(gp2); s1 = tk ? hi_bf(gp2) : lo_bf(up2);
          PIN2(s0, s1);
        }
        if constexpr (gp == 4) { s0 = rbf(s0 * __builtin_amdgcn_rcpf(1.f + __expf(-s0))); PIN1(s0); }
        if constexpr (gp == 5) { hp = pk2(s0 * s1, 0.f); PIN1(hp); }
      }
      if constexpr (jb == 10 && gp == 4) code_touch(sd, (j * 4 + pw) * 256);
      SB();
    };
#define W2M(i) work2(IC<-1>{}, IC<i>{})
#define W20(i) work2(IC<0>{}, IC<i>{})
#define W21(i) work2(IC<1>{}, IC<i>{})
    unsigned fs;
    {
      G1_STAGE_B(15, 3 * G1_STAGE);
      G1_STAGE_B(14, 2 * G1_STAGE);
      lgkm0(sd);
      BAR();
      G2_STAGE_NH(0, 0);
      WAIT_VM(6); BAR();
#pragma unroll
      for (int i = 0; i < 4; ++i) kk[i] = bld16_sc1(rf, l16, ko_ + i * 1024);
      G2_STAGE_NH(0, 1);
#pragma unroll
      for (int l = WSN; l < 16; ++l) wv[l] = bld16_nt(rw, vw, l * F_ * 2);
      asm volatile("s_waitcnt vmcnt(%4)" : "+v"(kk[0]), "+v"(kk[1]), "+v"(kk[2]), "+v"(kk[3]) : "i"(16 - WSN + 4) : "memory");
#pragma unroll
      for (int i = 0; i < 4; ++i) {
        k2 = med3u(k1, k2, kk[i].x); k1 = max(k1, kk[i].x);
        k2 = med3u(k1, k2, kk[i].z); k1 = max(k1, kk[i].z);
        bad |= (kk[i].y ^ tag) | (kk[i].w ^ tag);
      }
#pragma unroll 1
      for (int it = 0; !__all(bad == 0) && it < SPIN_MAX; ++it) {
        k1 = 0; k2 = 0; bad = 0;
#pragma unroll
        for (int i = 0; i < 4; ++i) {
          const u32x4 q = __builtin_amdgcn_raw_buffer_load_b128(rf, l16, ko_ + i * 1024, 16);
          k2 = med3u(k1, k2, q.x); k1 = max(k1, q.x);
          k2 = med3u(k1, k2, q.z); k1 = max(k1, q.z);
          bad |= (q.y ^ tag) | (q.w ^ tag);
        }
        asm volatile("" ::: "memory");
      }
      merge2(k1, k2, dpp_u<0xB1>(k1), dpp_u<0xB1>(k2));
      merge2(k1, k2, dpp_u<0x4E>(k1), dpp_u<0x4E>(k2));
      merge2(k1, k2, dpp_u<0x141>(k1), dpp_u<0x141>(k2));
      merge2(k1, k2, dpp_u<0x140>(k1), dpp_u<0x140>(k2));
      if ((lane & 15) == 0) *(u32x2*)(smem + LDS_KEY + (pw * 4 + rq) * 8) = (u32x2){k1, k2};
      PAGE(3);
      wait_vm<16 - WSN>();
      BAR_LGKM();
#pragma unroll
      for (int jj = 0; jj < 4; ++jj) tv[jj] = *(const u32x4*)(smem + G2_MRING + m_off(0) + (jj * 4 + pw) * 1024 + lane * 16);
      { const u32x2 kp = *(const u32x2*)(smem + LDS_KEY + ci * 8); k1 = kp.x; k2 = kp.y; }
#pragma unroll
      for (int jj = 0; jj < 4; ++jj) __builtin_amdgcn_raw_buffer_store_b128(tv[jj], rm2, vm2 + jj * 4096 + 2 * tt * 16384, 0, 16);
      G2_STAGE_N(1, W2M);
      touch1(mk_rsrc(spb), tv2, mb0, 0);
      WAIT_VM(11);
      if (lane == tt) __builtin_amdgcn_raw_buffer_store_b32(tag, rf, abo, 0, 16);
      {
        lds_vi rel = (lds_vi)(smem + LDS_SEL + 12);
        int it = 0;
        while (rel[0] != 4 && it < SPIN_MAX * 8) ++it;
        asm volatile("" ::: "memory");
      }
      G2_STAGE_N(2, W20);
      __builtin_amdgcn_s_sleep(2);
      WAIT_VM(10);
      fs = bld4_sc1(rf, abq[0]);
    }
    BAR();
#pragma unroll
    for (int jj = 0; jj < 4; ++jj) tv[jj] = *(const u32x4*)(smem + G2_MRING + m_off(1) + (jj * 4 + pw) * 1024 + lane * 16);
    constexpr int SPB = SP0 + SP1E * SP1;
    G2_STAGE_N(3, NOW);
    asm volatile("s_waitcnt vmcnt(8)" : "+v"(fs) :: "memory");
    ready = (unsigned)__ballot(fs == tag);
    need(2);
    G2_STAGE_M(2, NOW);
    need(3);
    G2_STAGE_M(3, NOW);
    fs = bld4_sc1(rf, abq[1]);
    W21(0); W21(1); W21(2); W21(3); W21(4); W21(5); W21(6); W21(7); W21(8); W21(9); W21(10); W21(11);
    asm volatile("" : "+v"(tv[0]), "+v"(tv[1]), "+v"(tv[2]), "+v"(tv[3]));
    auto step2 = [&](auto JB) __attribute__((always_inline)) {
      constexpr int jb = decltype(JB)::value;
      if constexpr (jb == 3) PAGE(4);
      if constexpr (jb == 9) PAGE(5);
      constexpr auto must = [](int q) { return (q == 2 ? 4 : 0) + ((FSM >> q) & 1) + dig(GVS, q); };
      if constexpr (jb == 2) wait_vm<4 + must(1)>();
      else wait_vm<dn_cnt(jb - 2) + (jb == 5 ? 1 : 0) + 12 + must(jb - 1) + dn_cnt(jb - 1)>();
      if constexpr (jb == 3) { if (pw == 0 && (lane & ~8) == 0) ((unsigned*)(smem + LDS_SEL))[tk] = ksel; }
      if constexpr ((jb == 2 || jb == DG + 1)) {
        *(f32x4*)(lg + pw * 256 + lane * 4) = macc;
        macc = (f32x4){0.f, 0.f, 0.f, 0.f};
        lgkm0(sd);
      }
      BAR();
      float p0[4], p1[4];
      const int col = (pw & 1) * 64 + lane;
      if constexpr (jb == 3) {
        if (g == 0 && pw == 0 && lane == 0) __hip_atomic_store((unsigned*)flags + FL_EPOCH, epoch + 1u, __ATOMIC_RELAXED, __HIP_MEMORY_SCOPE_AGENT);
      }
      if constexpr (jb == DG + 1) {
        const float* p = lg + (pw & 1) * 32 + (lane >> 4) * 64 + ((2 * lane) & 7) * 4 + ((lane >> 2) & 3);
#pragma unroll
        for (int i = 0; i < 4; ++i) { p0[i] = p[i * 256]; p1[i] = p[i * 256 + 4]; }
      }
      if constexpr (jb == 2) {
        const float* p = lg + (col >> 5) * 64 + (col & 7) * 4 + ((col >> 3) & 3);
#pragma unroll
        for (int i = 0; i < 4; ++i) { p0[i] = p[i * 256]; p1[i] = p[i * 256 + 32]; }
      }
      if constexpr (jb == 4) {
        if (lane == 8 + tt) __builtin_amdgcn_raw_buffer_store_b32(tag, rf, abo, 0, 16);
      }
      if constexpr ((FSM >> (jb - 2)) & 1) {
        asm volatile("" : "+v"(fs));
        ready |= (unsigned)__ballot(fs == tag);
      }
      if constexpr (jb == GV_JB + 2) asm volatile("" : "+v"(gv[0]), "+v"(gv[1]), "+v"(gv[2]), "+v"(gv[3]));
      auto w2 = [&](auto GP) __attribute__((always_inline)) {
        constexpr int gp = decltype(GP)::value;
        if constexpr (jb == 2 && gp == 0) {
          p0[0] = (p0[0] + p0[1]) + (p0[2] + p0[3]); p1[0] = (p1[0] + p1[1]) + (p1[2] + p1[3]);
          PIN2(p0[0], p1[0]);
        }
        work2(JB, GP);
        if constexpr (gp < dig(SPS, jb)) spf(IC<SPB + acc_dig(SPS, jb) + gp>{});
      };
#define W2(i) w2(IC<i>{})
      G2_STAGE_N(jb + 2, W2);
      need(jb + 2);
      G2_STAGE_M(jb + 2, W2);
      if constexpr (jb == 2) {
#pragma unroll
        for (int jj = 0; jj < 4; ++jj) __builtin_amdgcn_raw_buffer_store_b128(tv[jj], rm2, vm2 + jj * 4096 + (2 * tt + 1) * 16384, 0, 16);
      }
      if constexpr ((FSM >> jb) & 1) fs = bld4_sc1(rf, abq[jb < 5 ? 1 : 2]);
#pragma unroll
      for (int i = acc_dig(GVS, jb); i < acc_dig(GVS, jb + 1); ++i) gv[i] = bld16_sc1(rgu, vgu, i * F_ * 16);
#pragma unroll
      for (int i = dn_first(jb); i < dn_first(jb) + dn_cnt(jb); ++i) dv[i] = bld16_nt(rdn, vdn, i * D_ * 2);
      if constexpr (jb == DG + 1) {
        if (pw < 2)
          __builtin_amdgcn_raw_buffer_store_b128((u32x4){__float_as_uint((p0[0] + p0[1]) + (p0[2] + p0[3])), tag, __float_as_uint((p1[0] + p1[1]) + (p1[2] + p1[3])), tag},
                                                 mk_rsrc(wsp + WS_DN), (((pw * 8 + cg8) * 32 + rgb) * 64 + lane) * 16, 0, 16);
      }
      if constexpr (jb == 2) {
        if (moe && pw < 2)
          __builtin_amdgcn_raw_buffer_store_b128((u32x4){__float_as_uint(p0[0]), tag, __float_as_uint(p1[0]), tag},
                                                 rgu, ((mat * 4 + dq) * F_ + cg * 128 + col) * 16, 0, 16);
      }
      SB();
      if constexpr (dig(SPS, jb) > 12) spf(IC<SPB + acc_dig(SPS, jb) + 12>{});
      SB();
    };
    static_assert(DG + 1 <= 12 && DG >= GV_JB + 3 && dn_first(DG - 2) == 7 && acc_dig(GVS, GV_JB + 1) == 4, "schedule");
    static_assert(SPB + acc_dig(SPS, 14) == (16 - VAN) * 16, "all tiles are prefetched");
    step2(IC<2>{}); step2(IC<3>{}); step2(IC<4>{}); step2(IC<5>{}); step2(IC<6>{}); step2(IC<7>{}); step2(IC<8>{}); step2(IC<9>{}); step2(IC<10>{}); step2(IC<11>{}); step2(IC<12>{}); step2(IC<13>{});
    lgkm0(sd);
    WAIT_VM(12); BAR();
    __builtin_amdgcn_s_setprio(0);
    epi_setup(0); epi_setup(1); epi_setup(2);
    xq_load(0); xq_load(1); xq_load(2);
    WAIT_VM(3); BAR();
    epi_setup(3);
    wait_xq<0>(xq, sq);
    key[0] = __builtin_amdgcn_readlane(ksel, 0); key[1] = __builtin_amdgcn_readlane(ksel, 8);
    cv_calc();
#pragma unroll 1
    for (int c = 0; c < 2; ++c) {
      if (__builtin_expect(((c ? far1 : far0) & ~9) != 0, 1)) continue;
      const __amdgpu_buffer_rsrc_t rd = mk_rsrc(wsp + WS_DN + (size_t)(c * 8 * 32 + pw * 8) * 1024);
      const int ec = seg0(lane >> 5, (lane >> 4) & 1), dno = (ec >> 7) * 32768 + ((ec & 127) / 2 + (lane & 15)) * 16;
#pragma unroll 1
      for (int it = 0; it < SPIN_MAX; ++it) {
        if (__all(__builtin_amdgcn_raw_buffer_load_b32(rd, (ec >> 7) * 32768 + (lane & 7) * 1024 + 1020, 0, 16) == tag)) break;
        __builtin_amdgcn_s_sleep(8);
        asm volatile("" ::: "memory");
      }
      float s0, s1;
      unsigned bad = 1;
#pragma unroll 1
      for (int it = 0; !__all(bad == 0) && it < SPIN_MAX; ++it) {
        bad = 0; s0 = 0.f; s1 = 0.f;
#pragma unroll
        for (int i = 0; i < 8; ++i) {
          const u32x4 q = __builtin_amdgcn_raw_buffer_load_b128(rd, dno, i * 1024, 16);
          bad |= (q.y ^ tag) | (q.w ^ tag); s0 += __uint_as_float(q.x); s1 += __uint_as_float(q.z);
        }
        asm volatile("" ::: "memory");
      }
      *(f32x2*)(smem + LDS_FIN + (c * 4 + pw) * 512 + lane * 8) = (f32x2){s0, s1};
      asm volatile("s_waitcnt lgkmcnt(0)" ::: "memory");
      if (lane == 0) asm volatile("ds_add_u32 %0, %1" :: "v"(lds_addr(smem + LDS_SEL + 24 + c * 4)), "v"(1) : "memory");
    }
  } else {
  {
    const int q = j * 4 + wid;
    constexpr int Q1 = WS_MB, Q2 = Q1 + 8, Q3 = Q2 + 8, Q4 = Q3 + 7, Q5 = Q4 + 7, Q6 = Q5 + 7, Q7 = Q6 + 4, Q8 = Q7 + 4, QC = Q8 + 1;
    static_assert(QC <= 128, "warm-up slots");
    unsigned dmy = 0;
    if (q < QC) {
      const char* base; int b0;
      if (q < Q1) { base = wsp; b0 = 0; }
      else if (q < Q2) { base = (const char*)ws_re; b0 = Q1; }
      else if (q < Q3) { base = (const char*)ws_im; b0 = Q2; }
      else if (q < Q4) { base = (const char*)w_gate; b0 = Q3; }
      else if (q < Q5) { base = (const char*)w_up; b0 = Q4; }
      else if (q < Q6) { base = (const char*)w_down; b0 = Q5; }
      else if (q < Q7) { base = (const char*)out; b0 = Q6; }
      else if (q < Q8) { base = (const char*)x; b0 = Q7; }
      else { base = (const char*)w_r; b0 = Q8; }
      base += (size_t)(q - b0) << 20;
      asm volatile("s_load_dword %0, %1, 0" : "+s"(dmy) : "s"(base) : "memory");
    }
    if (wid == 0) { CALL_PG(1); CALL_PG(8); CALL_PG(5); }
    else if (wid == 1) { CALL_PG(2); CALL_PG(9); CALL_PG(6); }
    else if (wid == 2) { CALL_PG(3); CALL_PG(10); }
    else { CALL_PG(4); CALL_PG(11); }
    BAR();
    lgkm0(dmy);
  }
  PAGE(8);
  epi_setup(0); epi_setup(1); epi_setup(2); epi_setup(3);
  const int wr = wid >> 1, wc = wid & 1;
  const int sw = swz(fr), fl = lrw(fr);
  const int ko0 = ((0 + fq) ^ sw) * 16, ko1 = ((4 + fq) ^ sw) * 16;
  bf16x8 fm[2][4], fn[2][4];
  {
    const char* pm = smem + (wr * 64 + fl) * 128;
    const char* pn = smem + 16384 + (wc * 64 + fl) * 128;
    f32x4 acc[4][4];
#pragma unroll
    for (int i = 0; i < 4; ++i)
#pragma unroll
      for (int jn = 0; jn < 4; ++jn)
#pragma unroll
        for (int q = 0; q < 4; ++q) acc[i][jn][q] = 0.f;
#define TO(i) ((i) * 2048)
#define LDSP(T, a) ((__attribute__((address_space(3))) T*)(size_t)(a))
#define SLOTS(n) _Pragma("unroll") for (int q = 0; q < (n); ++q)
#define MFA(c, m, n) asm volatile("v_mfma_f32_16x16x32_bf16 %0, %2, %1, %0" : "+v"(c) : "v"(m), "v"(n))
#define MFA2(c, m, n, m2, n2) asm volatile("v_mfma_f32_16x16x32_bf16 %0, %2, %1, %0\n\tv_mfma_f32_16x16x32_bf16 %0, %4, %3, %0" : "+v"(c) : "v"(m), "v"(n), "v"(m2), "v"(n2))
    unsigned lm[2][2], ln[2][2];
#pragma unroll
    for (int u = 0; u < 2; ++u)
#pragma unroll
      for (int kk = 0; kk < 2; ++kk) {
        lm[u][kk] = lds_addr(pm) + u * 2 * G1_STAGE + (kk ? ko1 : ko0);
        ln[u][kk] = lds_addr(pn) + u * 2 * G1_STAGE + (kk ? ko1 : ko0);
        asm volatile("" : "+v"(lm[u][kk]), "+v"(ln[u][kk]));
      }
    auto ldm = [&](int stg, int i, int kk) __attribute__((always_inline)) { fm[kk][i] = *LDSP(const bf16x8, lm[stg >> 1][kk] + (stg & 1) * G1_STAGE + TO(i)); };
    auto ldn = [&](int stg, int jn, int kk) __attribute__((always_inline)) { fn[kk][jn] = *LDSP(const bf16x8, ln[stg >> 1][kk] + (stg & 1) * G1_STAGE + TO(jn)); };
    auto quad = [&](int a, int c, int d, int s) __attribute__((always_inline)) {
      const int i = (s < 2 || s >= 6) ? a : a + 1, jn = s < 4 ? c : d, kk = (s == 1 || s == 2 || s == 5 || s == 6);
      MFA(acc[i][jn], fm[kk][i], fn[kk][jn]);
    };
    auto quad2 = [&](int a, int c, int d, int s) __attribute__((always_inline)) {
      const int i = (s < 2 || s >= 6) ? a : a + 1, jn = s < 4 ? c : d, kk = (s == 2 || s == 6);
      MFA2(acc[i][jn], fm[kk][i], fn[kk][jn], fm[1 - kk][i], fn[1 - kk][jn]);
    };
    auto half1 = [&](int stg) __attribute__((always_inline)) {
      const int ja = (stg & 1) * 2, jb = 2 - ja;
      SLOTS(8) {
        quad(0, ja, ja + 1, q);
        if (q < 4) ldn(stg, jb + (q >> 1), q & 1); else ldm(stg, 2 + ((q - 4) >> 1), q == 5 || q == 6);
        SB();
      }
      SLOTS(4) { quad2(0, jb, jb + 1, 2 * q); SB(); }
    };
    auto half2 = [&](int stg) __attribute__((always_inline)) {
      const int ja = (stg & 1) * 2, jb = 2 - ja, nx = (stg + 1) & 3;
      SLOTS(8) {
        quad(2, jb + 1, jb, q);
        if (!(q & 1)) ldm(nx, q >> 2, q == 2 || q == 4);
        SB();
      }
      SLOTS(8) {
        quad(2, ja, ja + 1, q);
        if (!(q & 1)) ldn(nx, jb + (q >> 2), (q >> 1) & 1);
        SB();
      }
    };
    unsigned ae[7];
    {
      int l_ = lane;
      asm volatile("" : "+v"(l_));
      const int p = l_ >> 4, dn = sym_dn(p), hf = (p >> 1) ^ (dn & z);
#pragma unroll
      for (int u = 0; u < 7; ++u) {
        const int T = u < 3 ? wc * 4 : (wc * 4 + u - 3 - dn) & 7, i = u < 3 ? (u + 1) ^ ((u & 1) ? 0 : dn * 2) : 0;
        const int f = dn * 8 + ((T & 2) << 1) + ((T & 4) >> 1) + (T & 1);
        ae[u] = lds_addr(smem + G2_MRING) + (hf * 64 + i * 16 + lrw(f)) * 128 + (((wr * 4 + ((l_ >> 2) & 3)) ^ swz(f)) * 16) + (l_ & 3) * 4;
        asm volatile("" : "+v"(ae[u]));
      }
    }
    unsigned al = lds_addr(smem + LDS_MB) + (wr * 32 + fr * 2) * 4, ac = lds_addr(smem + LDS_MB) + (256 + wc * 64 + fq * 4) * 4;
    asm volatile("" : "+v"(al), "+v"(ac));
    SLOTS(16) asm volatile("" : "+v"(acc[q >> 2][q & 3]));
    SB();
    SLOTS(4) { ldm(0, q >> 1, q & 1); ldn(0, q >> 1, q & 1); }
    SB();
#pragma unroll 1
    for (int k4 = 0; k4 < G1_NKT - 4; k4 += 4) {
      half1(0); BAR_LGKM();
      SB(); half2(0);
      half1(1); BAR_LGKM();
      SB(); half2(1);
      half1(2); BAR_LGKM();
      SB(); half2(2);
      half1(3); BAR_LGKM();
      SB(); half2(3);
    }
    bf16x8 gm[4][4], gn[2][8];
    auto rdm1 = [&](int set, int t, int h, int i, int kk) __attribute__((always_inline)) {
      gm[set][kk * 2 + i] = *LDSP(const bf16x8, lm[(t >> 1) & 1][kk] + (t & 1) * G1_STAGE + h * 4096 + i * 2048);
    };
    auto rdg = [&](int set, int t, int h, int r) __attribute__((always_inline)) {
      if (r == 0 || r == 2 || r == 4 || r == 5) rdm1(set, t, h, r >= 4, r == 2 || r == 4);
      else {
        const int jn = r < 4 ? 0 : (r - 4) >> 1, kk = r < 4 ? r >> 1 : r & 1;
        gn[set][kk * 4 + jn] = *LDSP(const bf16x8, ln[(t >> 1) & 1][kk] + (t & 1) * G1_STAGE + TO(jn));
      }
    };
    auto mmg = [&](int ms, int ns, int h, int jn, int r, int f) __attribute__((always_inline)) {
      const int i = (r >> 1) ^ f, kk = r == 1 || r == 2;
      MFA(acc[h * 2 + i][jn], gm[ms][kk * 2 + i], gn[ns][kk * 4 + jn]);
    };
    f32x2 rsv, mrv;
    f32x4 cev[4];
    float tc[32];
    unsigned ep;
    auto rds = [&](int h, int r) __attribute__((always_inline)) {
      if (r == 0) mrv = *LDSP(const f32x2, al + h * 256); else if (r == 5) rsv = *LDSP(const f32x2, al + 512 + h * 256);
      else cev[r - 1] = *LDSP(const f32x4, ac + (r - 1) * 64);
    };
    auto prep = [&](int n) __attribute__((always_inline)) {
      tc[n] = llvm_fmul_legacy(mrv[n & 1], cev[n >> 3][(n >> 1) & 3]);
      asm volatile("" : "+v"(tc[n]));
    };
    auto epi = [&](int h, int n) __attribute__((always_inline)) {
      const int jn = n >> 4, r = (n >> 2) & 3, u = n & 3, e = (n >> 2) * 2;
      if (u == 0) {}
      else if (u == 1) asm volatile("v_fmac_f32 %0, %2, %4\n\tv_fmac_f32 %1, %3, %5\n\tv_cvt_pk_bf16_f32 %0, %0, %1" : "+v"(tc[e]), "+v"(tc[e + 1])
                                    : "v"(acc[h * 2][jn][r]), "v"(acc[h * 2 + 1][jn][r]), "v"(rsv[0]), "v"(rsv[1]));
      else if (u == 2) ep = __float_as_uint(tc[e]);
      else *LDSP(unsigned, (r ? ae[r - 1] + (jn & 1) * 128 + (jn >> 1) * lrw(4) * 128 : ae[3 + jn]) + m_off(h)) = ep;
      SB();
    };
    SB();
    half1(0);
    BAR_LGKM();
    SB();
    SLOTS(16) {
      if (q < 8) quad(2, 3, 2, q); else quad(2, 0, 1, q - 8);
      if (q < 12) rdg(0, 14, 0, q); else rdg(1, 13, 0, q - 12);
      SB();
    }
    BAR();
    SB();
    SLOTS(16) {
      mmg(0, 0, 0, q >> 2, q & 3, (q >> 2) & 1);
      if (q < 6) rds(0, q); else if (q < 14) rdg(1, 13, 0, q - 2);
      if (q >= 6) prep(q - 6);
      if (q >= 14) prep(q - 4);
      SB();
    }
    PAGE(9);
    BAR();
    SB();
    {
      constexpr int ct[8] = {1, 1, 0, 0, 1, 0, 1, 0}, cj[8] = {0, 1, 0, 1, 2, 2, 3, 3}, cf[8] = {0, 1, 0, 1, 0, 1, 0, 1};
      constexpr auto en = [](int q) { return q < 15 ? 0 : q < 26 ? (q - 14) * 3 - (q == 25) : q < 27 ? 32 : q < 32 ? 32 + (q - 26) * 3 + (q == 31) : 48; };
      SLOTS(32) {
        mmg(ct[q >> 2], ct[q >> 2], 0, cj[q >> 2], q & 3, cf[q >> 2]);
        if (q < 12) rdg(0, 15, 0, q);
        else if (q < 15 || q == 17) { const int r = q < 15 ? q - 12 : 3; rdm1(2, 15, 1, r >> 1, r == 1 || r == 2); }
        else if (q == 20 || q == 23 || q == 25 || q == 26) { const int r = q == 20 ? 0 : q == 23 ? 1 : q - 23; rdm1(3, 13, 1, r >> 1, r == 1 || r == 2); }
        if (q < 15) prep(12 + q);
        if (q >= 10 && q < 15) prep(17 + q);
        SB();
#pragma unroll
        for (int n = q ? en(q - 1) : 0; n < en(q); ++n) epi(0, n);
      }
    }
    SLOTS(2) { mmg(2, 0, 1, 0, q, 0); SB(); }
    asm volatile("s_nop 1");
    SLOTS(16) { epi(0, 48 + q); if (q == 3 || q == 9) { mmg(2, 0, 1, 0, q == 3 ? 2 : 3, 0); SB(); } }
    BAR_LGKM();
    SB();
    SLOTS(12) {
      mmg(2, 0, 1, 1 + (q >> 2), q & 3, ~(q >> 2) & 1);
      if (q < 6) rds(1, q); else rdg(0, 14, 1, q - 6);
      if (q >= 8) prep(q - 8);
      SB();
    }
    SLOTS(16) {
      mmg(3, 1, 1, q >> 2, q & 3, (q >> 2) & 1);
      if (q < 6) rdg(0, 14, 1, q + 6);
      if (q < 4) prep(4 + q); else { prep(2 * q); prep(2 * q + 1); }
      SB();
    }
    asm volatile("s_waitcnt lgkmcnt(0)" ::: "memory");
    if (lane == 0) asm volatile("ds_add_u32 %0, %1" :: "v"(lds_addr(smem + LDS_SEL + 12)), "v"(1) : "memory");
    SB();
    SLOTS(16) {
      mmg(0, 0, 1, q >> 2, q & 3, (q >> 2) & 1);
      if (q < 4) fn[0][q] = *(const bf16x8*)(smem + (wr * 128 + fl) * 128 + (q >> 1) * 2048 + ((q & 1) ? ko1 : ko0));
      else if (q < 8) fm[q & 1][(q - 4) >> 1] = *(const bf16x8*)(smem + G2_MRING + m_off(0) + (wc * 64 + fl) * 128 + TO((q - 4) >> 1) + ((q & 1) ? ko1 : ko0));
      SB();
      if (q >= 8) { epi(1, 3 * q - 24); epi(1, 3 * q - 23); epi(1, 3 * q - 22); }
    }
    SLOTS(40) epi(1, 24 + q);
  }

  const char* pn = smem + (wr * 128 + fl) * 128;
  const char* pm = smem + G2_MRING + (wc * 64 + fl) * 128;
  f32x4 acc[8][4];
  auto mm = [&](bool imaj, int set, int q2, int s, int mode) __attribute__((always_inline)) {
    int i, jj, kk;
    if (imaj) { i = s >> 2; const int r = s & 3; jj = (r >> 1) ^ (i & 1); kk = (r == 1 || r == 2); }
    else { jj = s >> 3; const int r = s & 7; i = jj ? 3 - (r >> 1) : (r >> 1); kk = ((r + 1) >> 1) & 1; }
    if (mode == 1 && !(s & 1)) asm volatile("v_mfma_f32_16x16x32_bf16 %0, %2, %1, 0" : "=&v"(acc[q2 * 2 + jj][i]) : "v"(fm[kk][i]), "v"(fn[set][jj * 2 + kk]));
    else if (mode) asm volatile("v_mfma_f32_16x16x32_bf16 %0, %2, %1, %0" : "+v"(acc[q2 * 2 + jj][i]) : "v"(fm[kk][i]), "v"(fn[set][jj * 2 + kk]));
    else acc[q2 * 2 + jj][i] = __builtin_amdgcn_mfma_f32_16x16x32_bf16(fn[set][jj * 2 + kk], fm[kk][i], acc[q2 * 2 + jj][i], 0, 0, 0);
  };
  unsigned bn[2], bm[2], an[2], am[2];
#pragma unroll
  for (int kk = 0; kk < 2; ++kk) {
    bn[kk] = lds_addr(pn) + (kk ? ko1 : ko0); bm[kk] = lds_addr(pm) + (kk ? ko1 : ko0);
    asm volatile("" : "+v"(bn[kk]), "+v"(bm[kk]));
    an[kk] = bn[kk]; am[kk] = bm[kk] + m_off(0);
  }
#define RDN(jt, kk, o) (*LDSP(const bf16x8, an[kk] + (o) + (jt) * 2048))
#define RDM(i, kk, o) (*LDSP(const bf16x8, am[kk] + (o) + TO(i)))
  PAGE(10);
  tag = __builtin_amdgcn_readfirstlane(((const unsigned*)(smem + LDS_SEL))[5]);
  {
    int son = 0;
    auto tile = [&](int kt, int zero) __attribute__((always_inline)) {
      SLOTS(16) {
        mm(true, 0, 0, q, zero);
        if (!(q & 1)) {
          if (q < 8) fm[(q >> 1) & 1][2 + (q >> 2)] = RDM(2 + (q >> 2), (q >> 1) & 1, 0);
          else { const int r = (q - 8) >> 1; fn[1][r] = RDN(2 + (r >> 1), r & 1, 0); }
        }
        SB();
      }
      SLOTS(16) {
        mm(false, 1, 1, q, zero);
        if (!(q & 1) && q < 8) fn[0][q >> 1] = RDN(4 + (q >> 2), (q >> 1) & 1, 0);
        SB();
      }
      const int som = son >> 1;
      son = son == 65536 ? 0 : son + 32768;
      SLOTS(16) {
        mm(false, 0, 2, q, zero);
        if (!(q & 1) && q < 8) fn[1][q >> 1] = RDN(6 + (q >> 2), (q >> 1) & 1, 0);
        if (q == 9 || q == 11) { an[q == 11] = bn[q == 11] + son; asm volatile("" : "+v"(an[q == 11])); }
        if (q == 13 || q == 15) { am[q == 15] = bm[q == 15] + som; asm volatile("" : "+v"(am[q == 15])); }
        SB();
      }
      BAR_LGKM();
      SB();
      SLOTS(16) {
        mm(true, 1, 3, q, zero);
        if (!(q & 1)) {
          if (q < 8) fn[0][q >> 1] = RDN(q >> 2, (q >> 1) & 1, 0);
          else fm[(q >> 1) & 1][(q - 8) >> 2] = RDM((q - 8) >> 2, (q >> 1) & 1, 0);
        }
        SB();
      }
    };
    SB();
    tile(0, 1);
#pragma unroll 1
    for (int kt = 1; kt < G2_NKT - 3; ++kt) tile(kt, 0);
  }
  unsigned fx;
  int fo = lane == 0 ? kq * 512 : (int)0x80000000;
  {
    constexpr int NG = 12;
    static constexpr int g_t[NG] = {0, 0, 1, 1, 2, 2, 2, 1, 0, 0, 1, 2};
    static constexpr int g_q[NG] = {0, 1, 0, 1, 0, 1, 2, 2, 2, 3, 3, 3};
    constexpr int G14 = 1, G15 = 3, GP0 = 5, GP1 = 6;
    constexpr auto chp = [](int g) { return g == 0 || g_t[g] != g_t[g - 1]; };
    constexpr auto chn = [](int g) { return g + 1 < NG && g_t[g + 1] != g_t[g]; };
    static_assert(n_off(13) == 32768 && n_off(14) == 65536 && n_off(15) == 0, "");
    unsigned an2[2];
#pragma unroll
    for (int kk = 0; kk < 2; ++kk) {
      an[kk] = bn[kk]; an2[kk] = bn[kk] + 65536; am[kk] = bm[kk];
      asm volatile("" : "+v"(an[kk]), "+v"(an2[kk]), "+v"(am[kk]));
    }
    int vo;
    u32x4 pk;
    u32x2 kv;
    auto publish = [&](int d, int h) __attribute__((always_inline)) {
      if (h == 0) { vo = ml; PINV(vo); vo += (((d + kq + 1) & 3) * 4 + kq) * 4 * MSG; PINV(vo); }
      const f32x4 v = acc[2 * d + (h >> 2)][h & 3];
      pk[(h & 1) * 2] = pk2(v[0], v[1]); pk[(h & 1) * 2 + 1] = pk2(v[2], v[3]);
      if (h & 1) __builtin_amdgcn_raw_buffer_store_b128(pk, rs, vo + (h >> 1) * 1024, 0, 16);
    };
#define MSG_FLAG(d) __builtin_amdgcn_raw_buffer_store_b32(tag, rc, fo + (((d) + kq + 1) & 3) * 2048, 0, 16)
    auto rdn = [&](int g, int r) __attribute__((always_inline)) {
      const int t = g_t[g], jt = g_q[g] * 2 + (r >> 1);
      fn[g & 1][r] = *LDSP(const bf16x8, (t == 1 ? an2[r & 1] : an[r & 1]) + (t == 0 ? 32768 : 0) + jt * 2048);
    };
    auto rdm = [&](int t, int i, int kk) __attribute__((always_inline)) { fm[kk][i] = *LDSP(const bf16x8, am[kk] + m_off(13 + t) + TO(i)); };
    auto group = [&](auto GI) __attribute__((always_inline)) {
      constexpr int gi = decltype(GI)::value;
      if (gi == G14) { BAR();
        SB(); }
      if (gi == G15) {
        PAGE(11);
        BAR();
        SB();
        kv = *(const u32x2*)(smem + LDS_SEL);
        SB();
      }
#pragma unroll
      for (int q = 0; q < 16; ++q) {
        const int n = gi * 16 + q;
        if (n == 112) {
        }
        if (n == 152) {
        }
        if (n == 168) { WAIT_VM(8); MSG_FLAG(0); SB(); }
        mm(chp(gi) || chn(gi), gi & 1, g_q[gi], q, 2);
        constexpr bool ni = gi + 1 < NG && (chp(gi + 1 < NG ? gi + 1 : gi) || chn(gi + 1 < NG ? gi + 1 : gi));
        auto nr = [](int r) { return ni && r >= 2 ? 5 - r : r; };
        if constexpr (chp(gi) && chn(gi)) {
          if (q < 4) rdm(g_t[gi], 2 + (q >> 1), q & 1); else if (q < 8) rdn(gi + 1, nr(q - 4)); else if (q < 12) rdm(g_t[gi + 1], (q - 8) >> 1, q & 1);
        } else if constexpr (chp(gi)) {
          if (q < 4) rdm(g_t[gi], 2 + (q >> 1), q & 1);
          else if (gi + 1 < NG && q < 12 && !(q & 1)) rdn(gi + 1, nr((q - 4) >> 1));
        } else if constexpr (chn(gi)) {
          if (!(q & 1)) { if (q < 8) rdn(gi + 1, nr(q >> 1)); else rdm(g_t[gi + 1], (q - 8) >> 2, (q >> 1) & 1); }
        } else {
          if (!(q & 1) && q < 8) rdn(gi + 1, nr(q >> 1));
        }
        if (gi == GP0 && q < 8) publish(0, q);
        if (gi == GP1 && q < 8) publish(1, q);
        if (gi == 9 && q < 8) publish(2, q);
        if (n == 33 || n == 37 || n == 41) xq_load((n - 33) >> 2);
        if (n == 72) { key[0] = __builtin_amdgcn_readfirstlane(kv.x); key[1] = __builtin_amdgcn_readfirstlane(kv.y); }
        SB();
      }
    };
#define GR4(i) group(IC<i>{}); group(IC<i + 1>{}); group(IC<i + 2>{}); group(IC<i + 3>{});
    GR4(0) GR4(4) GR4(8)
    asm volatile("s_nop 7");
    SB();
  }
#pragma unroll
  for (int i = 0; i < 4; ++i) *(f32x4*)(smem + LDS_ACC + wr * 16384 + wc * 4096 + i * 1024 + lane * 16) = acc[7][i];
#pragma unroll
  for (int i = 0; i < 4; ++i) ya[i] = acc[6][i];
  asm volatile("s_waitcnt lgkmcnt(0)" ::: "memory");
  if (lane == 0) sig[0] = 2;
  SB();
  WAIT_VM(5); MSG_FLAG(1); SB();
  WAIT_VM(2); MSG_FLAG(2); fx = bld4_sc1(rc, po); SB();
  wait_xq<4>(xq, sq);
  cv_calc();
  asm volatile("s_waitcnt vmcnt(0)" : "+v"(fx) :: "memory");
  seen = (unsigned)__ballot(fx == tag);
  }

  PAGE(6);
  if (__builtin_expect(far0 == 0 || far1 == 0, 0)) {
#pragma unroll 1
    for (int c = 0; c < 2; ++c) {
      if (c ? far1 : far0) continue;
      const int ic = (0xffff - (c ? key[1] : key[0])) & (S_ - 1);
      const float sg = rbf(1.f / (1.f + __expf(-key_score(c ? key[1] : key[0]))));
      lds_vi cnt = (lds_vi)(smem + LDS_SEL + 24 + c * 4);
      int it = 0;
      while (cnt[0] != 4 && it < SPIN_MAX * 8) { __builtin_amdgcn_s_sleep(1); ++it; }
      asm volatile("" ::: "memory");
      const float* q = (const float*)(smem + LDS_FIN + c * 2048) + (wid & 1) * 64 + (fr >> 3) * 32 + col4(fr);
      const f32x4 v = (*(const f32x4*)q + *(const f32x4*)(q + 128)) + (*(const f32x4*)(q + 256) + *(const f32x4*)(q + 384));
#pragma unroll
      for (int r = 0; r < 4; ++r)
#pragma unroll
        for (int k = 0; k < 4; ++k) {
          cv[r * 4 + k] += (xo[r] >> 11) == ic ? v[k] * sg : 0.f;
        }
    }
  }
  if (ha) {
    int it = 0;
    while (sig[0] != 2 && it < SPIN_MAX * 8) { __builtin_amdgcn_s_sleep(1); ++it; }
    asm volatile("" ::: "memory");
#pragma unroll
    for (int i = 0; i < 4; ++i) ya[i] = *(const f32x4*)(smem + LDS_ACC + (wid & 2) * 8192 + (wid & 1) * 4096 + i * 1024 + lane * 16);
  }
  u32x4 pp[3][2];
  unsigned e_lo = 0x00003f80u, e_hi = 0x3f800000u;
  asm volatile("" : "+v"(e_lo), "+v"(e_hi));
  __amdgpu_buffer_rsrc_t rs_ = rs, rc_ = rc;
  int mo[3] = {MSG_IDX(0) * 4 * MSG, MSG_IDX(1) * 4 * MSG, MSG_IDX(2) * 4 * MSG}, mv = ml + ha * 2048, mv2 = mv + 1024;
  asm volatile("" : "+s"(rs_), "+s"(rc_), "+s"(mo[0]), "+s"(mo[1]), "+s"(mo[2]));
  asm volatile("" : "+v"(mv), "+v"(mv2));
#define MSG_POLL(m) do { \
    if (!((seen >> (m)) & 1)) { \
      unsigned pv_, it_ = SPIN_MAX; \
      asm volatile("1:\n\tbuffer_load_dword %0, %3, %4, 0 offen sc1\n\ts_waitcnt vmcnt(0)\n\tv_cmp_eq_u32 vcc, %5, %0\n\ts_mov_b32 %1, vcc_lo\n\t" \
                   "s_bitcmp1_b32 %1, %6\n\ts_cbranch_scc1 2f\n\ts_sub_u32 %2, %2, 1\n\ts_cbranch_scc1 2f\n\ts_sleep 1\n\ts_branch 1b\n2:" \
                   : "=&v"(pv_), "+s"(seen), "+s"(it_) : "v"(po), "s"(rc_), "s"(tag), "i"(m) : "vcc", "scc", "memory"); \
    } \
    pp[m][0] = __builtin_amdgcn_raw_buffer_load_b128(rs_, mv, mo[m], 16); \
    pp[m][1] = __builtin_amdgcn_raw_buffer_load_b128(rs_, mv2, mo[m], 16); } while (0)
#define MSG_ADD(m) do { \
    _Pragma("unroll") for (int u = 0; u < 8; ++u) { \
      ya[u >> 1][(u & 1) * 2] = add_bf(pp[m][u >> 2][u & 3], e_lo, ya[u >> 1][(u & 1) * 2]); ya[u >> 1][(u & 1) * 2 + 1] = add_bf(pp[m][u >> 2][u & 3], e_hi, ya[u >> 1][(u & 1) * 2 + 1]); } } while (0)
  MSG_POLL(2); MSG_POLL(1); MSG_POLL(0);
  SB();
#pragma unroll
  for (int e = 0; e < 16; ++e) ya[e & 3][e >> 2] += cv[e];
  asm volatile("" : "+v"(ya[0]), "+v"(ya[1]), "+v"(ya[2]), "+v"(ya[3]));
  SB();
  MSG_ADD(2); MSG_ADD(1);
  asm volatile("" : "+v"(ya[0]), "+v"(ya[1]), "+v"(ya[2]), "+v"(ya[3]));
  SB();
  MSG_ADD(0);
  {
#pragma unroll
    for (int r = 0; r < 4; ++r) *(u32x2*)((char*)out + (unsigned)xo[r]) = (u32x2){pk2(ya[0][r], ya[1][r]), pk2(ya[2][r], ya[3][r])};
  }
}

extern "C" void kernel_launch(void* const* d_in, const int* in_sizes, int n_in, void* d_out, int out_size,
                              void* d_ws, size_t ws_size, hipStream_t stream) {
  const u16* x = (const u16*)d_in[0];
  const u16* ws_re = (const u16*)d_in[1];
  const u16* ws_im = (const u16*)d_in[2];
  const u16* wd_re = (const u16*)d_in[3];
  const u16* wd_im = (const u16*)d_in[4];
  const u16* w_r = (const u16*)d_in[6];
  const u16* w_gate = (const u16*)d_in[7];
  const u16* w_up = (const u16*)d_in[8];
  const u16* w_down = (const u16*)d_in[9];
  k_main<<<256, 512, 0, stream>>>(x, wd_re, wd_im, w_r, (char*)d_ws, ws_re, ws_im, w_gate, w_up, w_down, (u16*)d_out);
}
```

```cpp
#include <hip/hip_runtime.h>
#include <hip/hip_bf16.h>
#include <stdint.h>
#include <type_traits>

extern "C" __device__ unsigned long long llvm_dispatch_id(void) __asm("llvm.amdgcn.dispatch.id");
extern "C" __device__ float llvm_fmul_legacy(float, float) __asm("llvm.amdgcn.fmul.legacy");
typedef unsigned short u16;
typedef unsigned long long u64;
typedef __attribute__((ext_vector_type(8))) short bf16x8;
typedef __attribute__((ext_vector_type(4))) unsigned u32x4;
typedef __attribute__((ext_vector_type(2))) unsigned u32x2;
typedef __attribute__((ext_vector_type(4))) float f32x4;
typedef __attribute__((ext_vector_type(2))) float f32x2;
typedef __attribute__((ext_vector_type(2))) __bf16 bf16x2;
typedef __attribute__((address_space(3))) void* lds_vp;
typedef volatile __attribute__((address_space(3))) int* lds_vi;

constexpr int S_ = 2048, D_ = 1024, F_ = 3584;
template <int N> using IC = std::integral_constant<int, N>;

static __device__ __forceinline__ float bf2f(u16 u) { return __uint_as_float((unsigned)u << 16); }
static __device__ __forceinline__ u16 f2bf(float f) {
  __hip_bfloat16 b = __float2bfloat16(f);
  return *reinterpret_cast<u16*>(&b);
}
static __device__ __forceinline__ float rbf(float f) { return bf2f(f2bf(f)); }
static __device__ __forceinline__ float lo_bf(unsigned u) { return __uint_as_float(u << 16); }
static __device__ __forceinline__ float hi_bf(unsigned u) { return __uint_as_float(u & 0xffff0000u); }
static __device__ __forceinline__ unsigned pk2(float a, float b) {
  const bf16x2 v = __builtin_convertvector((f32x2){a, b}, bf16x2);
  return __builtin_bit_cast(unsigned, v);
}
static __device__ __forceinline__ float add_bf(unsigned u, unsigned e, float c) {
  return __builtin_amdgcn_fdot2_f32_bf16(__builtin_bit_cast(bf16x2, u), __builtin_bit_cast(bf16x2, e), c, false);
}
static __device__ __forceinline__ float ln_elem(float x, float mb, float sd, float ri) {
  const float d = rbf(x - mb);
  const float q0 = d * ri;
  return rbf(__builtin_fmaf(__builtin_fmaf(-q0, sd, d), ri, q0));
}
template <int CTRL> static __device__ __forceinline__ float dpp_f(float v) {
  return __int_as_float(__builtin_amdgcn_update_dpp(0, __float_as_int(v), CTRL, 0xf, 0xf, true));
}
template <int CTRL> static __device__ __forceinline__ unsigned dpp_u(unsigned v) {
  return (unsigned)__builtin_amdgcn_update_dpp(0, (int)v, CTRL, 0xf, 0xf, true);
}
static __device__ __forceinline__ float row_sum(float v) {
  v += dpp_f<0xB1>(v);
  v += dpp_f<0x4E>(v);
  v += dpp_f<0x141>(v);
  v += dpp_f<0x140>(v);
  return v;
}
static __device__ __forceinline__ float fold32(float a, float b) {
  const auto r = __builtin_amdgcn_permlane32_swap(__float_as_uint(a), __float_as_uint(b), false, false);
  return __uint_as_float(r[0]) + __uint_as_float(r[1]);
}
static __device__ __forceinline__ float fold16(float a, float b) {
  const auto r = __builtin_amdgcn_permlane16_swap(__float_as_uint(a), __float_as_uint(b), false, false);
  return __uint_as_float(r[0]) + __uint_as_float(r[1]);
}
static __device__ __forceinline__ float rdl(float v, int l) { return __int_as_float(__builtin_amdgcn_readlane(__float_as_int(v), l)); }


#define TL(I) do { if ((I) == 1 || (I) == 52) { const unsigned _t = (unsigned)__builtin_amdgcn_s_memrealtime(); tlv = lane == (I) ? _t : tlv; } } while (0)
#define ST_JB 9
#define LNS 2
#define WSN 10
#define FSM 0xa2
#define GV_JB 7
#define DG 11
#define AB_STRIDE 32
#define SPIN_MAX (1 << 17)
#define VA0 5
#define VAN 4
#define SP0 3
#define SP1 7
#define SP1E 11
#define SPS 0x000cccccdddd00
#define DVS 0x00000400300000
#define GVS 0x00000022000000
constexpr int dig(unsigned long long t, int q) { return q < 0 ? 0 : (int)((t >> (4 * q)) & 15); }
constexpr int acc_dig(unsigned long long t, int q) { int a = 0; for (int i = 0; i < q; ++i) a += dig(t, i); return a; }

static __device__ __forceinline__ unsigned lds_addr(const void* p) { return (unsigned)(size_t)(lds_vp)p; }

constexpr int MSG = 4096;
constexpr size_t WS_ABT   = 0;
constexpr size_t WS_SLAB  = WS_ABT + (size_t)D_ * 4096 * 2;
constexpr size_t WS_GU    = WS_SLAB + (size_t)64 * 64 * MSG;
constexpr size_t WS_DN    = WS_GU + (size_t)8 * F_ * 16;
constexpr size_t WS_FLAGS = WS_DN + (size_t)2 * 8 * 32 * 64 * 16;
constexpr int FL_X = 0;
constexpr int FL_AB = 64 * 64 * 32;
constexpr int FL_ST = FL_AB + 2048 * AB_STRIDE;
constexpr int FL_CE = FL_ST + 4096;
constexpr int FL_KEY = FL_CE + 4096;
constexpr int FL_EPOCH = FL_KEY + 4096;
constexpr int WS_MB = (int)((WS_FLAGS + (FL_EPOCH + 1) * 4 + 1048575) >> 20);

#define WAIT_VM(n) asm volatile("s_waitcnt vmcnt(" #n ")" ::: "memory")
template <int N> static __device__ __forceinline__ void wait_vm() { asm volatile("s_waitcnt vmcnt(%0)" :: "i"(N) : "memory"); }
#define BAR_LGKM() asm volatile("s_waitcnt lgkmcnt(0)\n\ts_barrier" ::: "memory")
#define BAR() asm volatile("s_barrier" ::: "memory")
#define SB() __builtin_amdgcn_sched_barrier(0)
#define PAGE(n) asm volatile("s_branch 2f\n\t.p2align 12\nk_pg" #n ":\n\ts_setpc_b64 s[42:43]\n2:")
#define CALL_PG(n) asm volatile("s_getpc_b64 s[40:41]\n3:\n\ts_mov_b32 s42, k_pg" #n "-3b\n\ts_ashr_i32 s43, s42, 31\n\ts_add_u32 s40, s40, s42\n\ts_addc_u32 s41, s41, s43\n\t" \
                                "s_swappc_b64 s[42:43], s[40:41]" ::: "s40", "s41", "s42", "s43", "scc", "memory")

static __device__ __forceinline__ __amdgpu_buffer_rsrc_t mk_rsrc(const void* p) {
  return __builtin_amdgcn_make_buffer_rsrc((void*)p, 0, 0x7fffffff, 0x00020000);
}
#define DMA_(rsrc, voff, mbase, MI, sbase, SI, pol) do { unsigned _t; \
    asm volatile("s_add_i32 m0, %2, %3\n\ts_add_i32 %0, %5, %6\n\tbuffer_load_dwordx4 %1, %4, %0 offen" pol " lds" \
                 : "=&s"(_t) : "v"(voff), "s"(mbase), "i"(MI), "s"(rsrc), "s"(sbase), "i"(SI) : "memory", "scc"); } while (0)
#define DMA_I(rsrc, voff, mbase, MI, SI) \
    asm volatile("s_add_i32 m0, %1, %2\n\ts_nop 0\n\tbuffer_load_dwordx4 %0, %3, 0 offen offset:%4 lds" \
                 :: "v"(voff), "s"(mbase), "i"((MI) - (SI)), "s"(rsrc), "i"(SI) : "memory", "scc")
#define DMA_S(rsrc, voff, mbase, MI, soff) \
    asm volatile("s_add_i32 m0, %1, %2\n\ts_nop 0\n\tbuffer_load_dwordx4 %0, %3, %4 offen lds" \
                 :: "v"(voff), "s"(mbase), "i"(MI), "s"(rsrc), "s"(soff) : "memory", "scc")
#define TOUCH(rsrc, voff, mbase, MI, soff) \
    asm volatile("s_add_i32 m0, %1, %2\n\ts_nop 0\n\tbuffer_load_dword %0, %3, %4 offen lds" \
                 :: "v"(voff), "s"(mbase), "i"(MI), "s"(rsrc), "s"(soff) : "memory", "scc")
template <int OFF> static __device__ __forceinline__ u32x4 ld16_s(const void* p, int voff) {
  u32x4 v;
  asm volatile("global_load_dwordx4 %0, %1, %2 offset:%3" : "=v"(v) : "v"(voff), "s"(p), "i"(OFF) : "memory");
  return v;
}
template <int OFF> static __device__ __forceinline__ void sld(unsigned& sink, const void* p, int soff) {
  asm volatile("s_load_dword %0, %1, %2 offset:%3" : "+s"(sink) : "s"(p), "s"(soff), "i"(OFF) : "memory");
}
static __device__ __forceinline__ unsigned gld4_s(const void* p, unsigned voff) {
  unsigned v;
  asm volatile("global_load_dword %0, %1, %2" : "=v"(v) : "v"(voff), "s"(p) : "memory");
  return v;
}
static __device__ __forceinline__ u32x2 bld8_sc1(__amdgpu_buffer_rsrc_t r, unsigned voff) {
  u32x2 v;
  asm volatile("buffer_load_dwordx2 %0, %1, %2, 0 offen sc1" : "=v"(v) : "v"(voff), "s"(r) : "memory");
  return v;
}
template <int N> static __device__ __forceinline__ void wait_xs(unsigned& a, u32x2& b) { asm volatile("s_waitcnt vmcnt(%2)" : "+v"(a), "+v"(b) : "i"(N) : "memory"); }
template <int N> static __device__ __forceinline__ void wait_xq(u32x4 (&a)[2], u32x2& b) { asm volatile("s_waitcnt vmcnt(%3)" : "+v"(a[0]), "+v"(a[1]), "+v"(b) : "i"(N) : "memory"); }
static __device__ __forceinline__ u32x2 ld8_s_sc1(const void* p, int voff) {
  u32x2 v;
  asm volatile("global_load_dwordx2 %0, %1, %2 sc1" : "=v"(v) : "v"(voff), "s"(p) : "memory");
  return v;
}
static __device__ __forceinline__ void lgkm0(unsigned& sink) { asm volatile("s_waitcnt lgkmcnt(0)" : "+s"(sink) :: "memory"); }
template <typename T> static __device__ __forceinline__ void pin4(T& a, T& b, T& c, T& d) { asm volatile("" : "+s"(a), "+s"(b), "+s"(c), "+s"(d)); }
template <typename T, typename U> static __device__ __forceinline__ void pins(T& a, U& b) { asm volatile("" : "+s"(a), "+s"(b)); }
static __device__ __forceinline__ void launder(int& v, int& a, int& b) { asm volatile("" : "+v"(v)); asm volatile("" : "+s"(a), "+s"(b)); }
static __device__ __forceinline__ unsigned bld4_sc1(__amdgpu_buffer_rsrc_t r, int voff) {
  unsigned v;
  asm volatile("buffer_load_dword %0, %1, %2, 0 offen sc1" : "=v"(v) : "v"(voff), "s"(r) : "memory");
  return v;
}
static __device__ __forceinline__ u32x4 bld16_nt(__amdgpu_buffer_rsrc_t r, int voff, int soff) {
  u32x4 v;
  asm volatile("buffer_load_dwordx4 %0, %1, %2, %3 offen nt" : "=v"(v) : "v"(voff), "s"(r), "s"(soff) : "memory");
  return v;
}
static __device__ __forceinline__ void bld16_nt_io(u32x4& v, __amdgpu_buffer_rsrc_t r, int voff, int soff) {
  asm volatile("buffer_load_dwordx4 %0, %1, %2, %3 offen nt" : "+v"(v) : "v"(voff), "s"(r), "s"(soff) : "memory");
}
static __device__ __forceinline__ u32x4 bld16_sc1(__amdgpu_buffer_rsrc_t r, int voff, int soff) {
  u32x4 v;
  asm volatile("buffer_load_dwordx4 %0, %1, %2, %3 offen sc1" : "=v"(v) : "v"(voff), "s"(r), "s"(soff) : "memory");
  return v;
}
static __device__ __forceinline__ u32x2 granule_wait(const u64* p, unsigned tag) {
  u64 v = 0;
#pragma unroll 1
  for (int it = 0; it < SPIN_MAX; ++it) {
    v = __hip_atomic_load(p, __ATOMIC_RELAXED, __HIP_MEMORY_SCOPE_AGENT);
    if (__all((unsigned)(v >> 32) == tag)) break;
    __builtin_amdgcn_s_sleep(2);
  }
  return (u32x2){(unsigned)v, (unsigned)(v >> 32)};
}
static __device__ __forceinline__ float key_score(unsigned key) {
  const unsigned u = key >> 16;
  return bf2f((u16)((u & 0x8000u) ? (u & 0x7fffu) : (~u & 0xffffu)));
}
static __device__ __forceinline__ unsigned med3u(unsigned a, unsigned b, unsigned c) { return max(min(a, b), min(max(a, b), c)); }
static __device__ __forceinline__ void merge2(unsigned& a1, unsigned& a2, unsigned b1, unsigned b2) {
  const unsigned lo = min(a1, b1);
  a1 = max(a1, b1);
  a2 = max(lo, max(a2, b2));
}
static __device__ __forceinline__ float sqdev16(const u32x4 (&v)[2], float mb) {
  f32x2 q2 = {0.f, 0.f};
#pragma unroll
  for (int i = 0; i < 8; ++i) {
    const f32x2 e = (f32x2){lo_bf(v[i >> 2][i & 3]), hi_bf(v[i >> 2][i & 3])} - mb;
    q2 += e * e;
  }
  return q2.x + q2.y;
}

constexpr int lrw(int r) { return (r & ~6) | ((r & 2) << 1) | ((r & 4) >> 1); }
constexpr int swz(int r) { return ((r >> 1) & 4) | (r & 2); }
static __device__ __forceinline__ int sym_dn(int p) { return (p ^ (p >> 1)) & 1; }
static __device__ __forceinline__ int sym_hi(int n, int w, int st, int b, int z, int p) {
  return ((p >> 1) * (n / 2 * st) + b * (w * st)) ^ (-sym_dn(p) & (((n - 1) ^ (z * (n / 2))) * st));
}
static __device__ __forceinline__ int sym_lo(int w, int st, int p, int c) { return ((c - sym_dn(p)) * st) & ((w - 1) * st); }
static __device__ __forceinline__ int sym_row(int n, int w, int st, int b, int z, int p, int c) { return sym_lo(w, st, p, c) ^ sym_hi(n, w, st, b, z, p); }
constexpr int G1_NST = 4, G1_NKT = 16, G1_STAGE = 32768;
constexpr int G2_NKT = 16, G2_MRING = 98304;
constexpr int n_off(int k) { return (k % 3) * 32768; }
constexpr int m_off(int k) { return ((k + 2) % 3) * 16384; }
constexpr int k_ord(int k) { return k < 2 ? k : (k < 9 ? 2 * (k - 1) : 2 * (k - 8) + 1); }
constexpr int LDS_DUMMY = G2_MRING + 49152;
constexpr int LDS_GU = LDS_DUMMY + 4096;
constexpr int LDS_FIN = LDS_GU + 4096;
constexpr int LDS_MB = LDS_FIN + 4096;
constexpr int LDS_SEL = LDS_MB + 1536;
constexpr int LDS_SIG = LDS_SEL + 64;
constexpr int LDS_KEY = LDS_SIG + 32;
constexpr int LDS_BYTES = LDS_KEY + 128;
constexpr int LDS_ACC = n_off(G2_NKT - 3);

constexpr int sp_tile(int q) { return q < VA0 ? q : q + VAN < 16 ? q + VAN : -1; }
constexpr int dn_cnt(int q) { return dig(DVS, q); }
constexpr int dn_first(int q) { return acc_dig(DVS, q); }

static __device__ __forceinline__ void code_touch(unsigned& sink, unsigned off) {
  asm volatile("s_getpc_b64 s[40:41]\n\tk_main_rt:\n\ts_min_u32 %1, %1, k_main_end-k_main_rt-196\n\t"
               "s_load_dword %0, s[40:41], %1 offset:0\n\ts_load_dword %0, s[40:41], %1 offset:64\n\ts_load_dword %0, s[40:41], %1 offset:128\n\ts_load_dword %0, s[40:41], %1 offset:192"
               : "+s"(sink), "+s"(off) :: "s40", "s41", "scc", "memory");
}
static __device__ __forceinline__ void touch1(__amdgpu_buffer_rsrc_t r, int voff, unsigned mb, int soff) { TOUCH(r, voff, mb, LDS_DUMMY, soff); }
typedef const __attribute__((address_space(1))) u16* gptr;
typedef __attribute__((ext_vector_type(8))) u64 u64x8;
typedef __attribute__((ext_vector_type(2))) u64 u64x2;
extern "C" __global__ __launch_bounds__(512, 2) void k_main(const u16* x, const u16* wd_re, const u16* wd_im, const u16* w_r,
                                                 char* wsp, const u16* ws_re, const u16* ws_im,
                                                 const u16* w_gate, const u16* w_up, const u16* w_down, u16* out) {
  __shared__ __attribute__((aligned(16))) char smem[LDS_BYTES];
  u64x8 ka;
  u64x2 kb_;
  u64 kc;
  asm volatile("s_load_dwordx16 %0, %3, 0\n\ts_load_dwordx4 %1, %3, 64\n\ts_load_dwordx2 %2, %3, 80"
               : "=&s"(ka), "=&s"(kb_), "=&s"(kc) : "s"(__builtin_amdgcn_kernarg_segment_ptr()) : "memory");
  unsigned long long t00;
  asm volatile("s_memrealtime %0" : "=s"(t00) :: "memory");
  const int tid = threadIdx.x, lane = tid & 63;
  const int wid = __builtin_amdgcn_readfirstlane(tid >> 6);
  const int g = blockIdx.x, xcd = ((~g & 1) << 2) | ((g >> 1) & 3), j = g >> 3;
  if (wid < 4) {
    unsigned cw, co = (j * 4 + wid) * 448;
    asm volatile(".subsection 1\nk_main_end:\n.subsection 0\n\ts_min_u32 %1, %1, k_main_end-k_main-480\n\ts_getpc_b64 s[40:41]\n\ts_add_u32 s40, s40, %1\n\ts_addc_u32 s41, s41, 0\n\t"
                 "s_load_dword %0, s[40:41], 0\n\ts_load_dword %0, s[40:41], 64\n\ts_load_dword %0, s[40:41], 128\n\t"
                 "s_load_dword %0, s[40:41], 192\n\ts_load_dword %0, s[40:41], 256\n\ts_load_dword %0, s[40:41], 320\n\ts_load_dword %0, s[40:41], 384\n\ts_waitcnt lgkmcnt(0)"
                 : "=&s"(cw), "+s"(co) :: "s40", "s41", "scc", "memory");
  }
  const int pw = wid - 4;
  const int row0 = (wid & 3) * 8 + lrw(lane >> 3);
  const int cm = ((lane & 7) ^ swz(row0)) * 16;
  int vm = ((row0 & 15) * 2 + (row0 >> 4)) * (D_ * 2) + cm;
  asm volatile("" : "+v"(vm));
  const int kq = xcd >> 1;
  const int tt = (xcd & 1) * 4 + (j >> 3);
  const int et = j & 7;
  const int z = kq >> 1, st = (kq & 1) * 8 + tt;
  const int lrow = (kq & 1) * 1024 + (xcd & 1) * 512 + z * 256 + j * 8 + pw * 2;
  const int drow = ((kq & 1) * 2 + (xcd & 1)) * 256 + j * 8 + pw * 2;
  unsigned o_m = st * 128 * D_ * 2, o_l = lrow * D_ * 2, o_d = drow * D_ * 2, mb0 = lds_addr(smem) + pw * 1024;
  int l16 = lane * 16;
  int vn[4];
#pragma unroll
  for (int q = 0; q < 4; ++q) vn[q] = sym_lo(32, D_ * 2, (row0 >> 2) & 3, q * 8 + (row0 >> 4) * 4 + (row0 & 3)) ^ (sym_hi(D_, 32, D_ * 2, et, z, (row0 >> 2) & 3) | cm);
  asm volatile("" : "+s"(o_m), "+s"(o_l), "+s"(o_d), "+s"(mb0));
  asm volatile("" : "+v"(l16), "+v"(vn[0]), "+v"(vn[1]), "+v"(vn[2]), "+v"(vn[3]));
  asm volatile("s_waitcnt lgkmcnt(0)" : "+s"(ka), "+s"(kb_), "+s"(kc) :: "memory");
  x = (const u16*)(gptr)ka[0]; wd_re = (const u16*)(gptr)ka[1]; wd_im = (const u16*)(gptr)ka[2]; w_r = (const u16*)(gptr)ka[3];
  wsp = (char*)(gptr)ka[4]; ws_re = (const u16*)(gptr)ka[5]; ws_im = (const u16*)(gptr)ka[6]; w_gate = (const u16*)(gptr)ka[7];
  w_up = (const u16*)(gptr)kb_[0]; w_down = (const u16*)(gptr)kb_[1]; out = (u16*)(gptr)kc;
  int* flags = (int*)(wsp + WS_FLAGS);
  u64* stat = (u64*)(flags + FL_ST);
  unsigned tlv = 0;
  asm volatile("s_waitcnt lgkmcnt(0)" : "+s"(t00) :: "memory");
  tlv = lane == 52 ? (unsigned)t00 : tlv;
  const u16* wdz = z ? wd_im : wd_re;
  const __amdgpu_buffer_rsrc_t rm = mk_rsrc(x);
  const __amdgpu_buffer_rsrc_t rn = mk_rsrc(wdz);
#define G1_X(kt, so, q) DMA_(rm, vm, mb0, (so) + (q) * 4096, o_m, (kt) * 128 + (((q) & 1) * 64 + ((q) >> 1) * 32) * D_ * 2, "")
#define G1_W(kt, so, q) DMA_I(rn, vn[q], mb0, (so) + 16384 + (q) * 4096, (kt) * 128)
#define G1_STAGE_TILE(kt, so, W) do { SB(); G1_X(kt, so, 0); W(0); G1_X(kt, so, 1); W(1); G1_X(kt, so, 2); W(2); G1_X(kt, so, 3); W(3); \
    G1_W(kt, so, 0); W(4); G1_W(kt, so, 1); W(5); G1_W(kt, so, 2); W(6); G1_W(kt, so, 3); W(7); SB(); } while (0)
#define G1_STAGE_A(kt, so, W) do { SB(); G1_X(kt, so, 0); W(0); G1_X(kt, so, 2); W(1); \
    G1_W(kt, so, 0); W(2); G1_W(kt, so, 1); W(3); G1_W(kt, so, 2); W(4); G1_W(kt, so, 3); W(5); W(6); W(7); SB(); } while (0)
#define G1_STAGE_B(kt, so) do { SB(); G1_X(kt, so, 1); G1_X(kt, so, 3); SB(); } while (0)
#define NOW(i)
  const int fr = lane & 15, fq = lane >> 4;
  const int ha = wid >> 2;
  int trow, xo[4], xa[3], po, ml;
  const char *prs, *prc;
  auto col4 = [](int f) __attribute__((always_inline)) { return ((f & 2) << 3 | (f & 4) << 1 | (f & 1) << 2) ^ (-((f >> 3) & 1) & 28); };
  auto seg0 = [&](int hf, int dn) __attribute__((always_inline)) { return (hf * 512 + et * 32) ^ (-dn & (D_ - 32)); };
  auto epi_setup = [&](int part) __attribute__((always_inline)) {
    int t_ = tt, l_ = lane;
    asm volatile("" : "+s"(t_));
    asm volatile("" : "+v"(l_));
    const int ts = ((wid >> 1) & 1) * 8 + kq * 2 + ha;
    if (part == 0) { xo[3] = (seg0(wid & 1, (l_ >> 3) & 1) + col4(l_)) * 2; asm volatile("" : "+v"(xo[3])); }
    if (part == 1) {
      xo[3] |= sym_hi(S_, 64, D_ * 2, t_, z, l_ >> 4);
      ml = (l_ * 16) ^ (-sym_dn(l_ >> 4) & (z * 768));
      asm volatile("" : "+v"(xo[3]), "+v"(ml));
    }
    if (part == 2) {
      const int xh = xo[3];
#pragma unroll
      for (int r = 0; r < 4; ++r) xo[r] = sym_lo(64, D_ * 2, l_ >> 4, ts * 4 + r) ^ xh;
      const bool hi = xo[0] & 8;
      xa[0] = (hi ? xo[2] : xo[0]) & ~15; xa[1] = (hi ? xo[3] : xo[1]) & ~15;
      xa[2] = (sym_lo(64, D_ * 2, l_ >> 4, ts * 4 + (l_ & 3)) ^ xh) >> 11 << 3;
      asm volatile("" : "+v"(xo[0]), "+v"(xo[1]), "+v"(xo[2]), "+v"(xo[3]), "+v"(xa[0]), "+v"(xa[1]), "+v"(xa[2]));
    }
    if (part == 3) {
      trow = t_ * 16 + ts;
      prs = wsp + WS_SLAB + (size_t)((t_ * 8 + et) * 64 + (wid & 3)) * MSG;
      prc = (const char*)(flags + FL_X + ((t_ * 8 + et) * 64 + (wid & 3)) * 32);
      asm volatile("" : "+s"(trow), "+s"(prs), "+s"(prc));
      po = l_ < 3 ? (kq * 4 + ((kq + 1 + l_) & 3)) * 512 : (int)0x80000000;
      asm volatile("" : "+v"(po));
    }
  };
  auto tok_tile = [](unsigned key) __attribute__((always_inline)) {
    const unsigned h = (0xffff - key) & 1023;
    return h < 512 ? h >> 2 : ((1023 - h) >> 6 << 4) | (((1024 - h) & 63) >> 2);
  };
#define rs mk_rsrc(prs)
#define rc mk_rsrc(prc)
  lds_vi sig = (lds_vi)(smem + LDS_SIG + (wid & 3) * 4);
  unsigned tag;
  u32x4 xq[2];
  u32x2 sq;
  f32x4 ya[4];
  unsigned key[2];
  float cv[16];
  unsigned far0, far1;
  auto xq_load = [&](int n) __attribute__((always_inline)) {
    if (n < 2) xq[n] = ld16_s<0>(x, xa[n]); else sq = ld8_s_sc1(stat, xa[2]);
  };
#define PINV(a) asm volatile("" : "+v"(a))
  auto cv_calc = [&]() __attribute__((always_inline)) {
    if (__builtin_expect(!__all(sq.y == tag), 0)) sq = granule_wait(stat + (xa[2] >> 3), tag);
    far0 = tok_tile(key[0]) ^ trow; far1 = tok_tile(key[1]) ^ trow;
    asm volatile("" : "+s"(far0), "+s"(far1));
    const bool hi = xo[0] & 8;
    const float mb_ = lo_bf(sq.x), ri_ = __builtin_amdgcn_rcpf(hi_bf(sq.x));
#pragma unroll
    for (int r = 0; r < 4; ++r) {
      const float mb = r == 0 ? dpp_f<0x00>(mb_) : r == 1 ? dpp_f<0x55>(mb_) : r == 2 ? dpp_f<0xAA>(mb_) : dpp_f<0xFF>(mb_);
      const float ri = r == 0 ? dpp_f<0x00>(ri_) : r == 1 ? dpp_f<0x55>(ri_) : r == 2 ? dpp_f<0xAA>(ri_) : dpp_f<0xFF>(ri_);
#pragma unroll
      for (int u = 0; u < 2; ++u) {
        const unsigned own = xq[r & 1][(r & 2) + u], oth = dpp_u<0xB1>(xq[r & 1][2 - (r & 2) + u]);
        const unsigned w = hi == (r >= 2) ? own : oth;
        cv[r * 4 + 2 * u] = __builtin_fmaf(lo_bf(w) - mb, ri, lo_bf(w));
        cv[r * 4 + 2 * u + 1] = __builtin_fmaf(hi_bf(w) - mb, ri, hi_bf(w));
        PINV(cv[r * 4 + 2 * u]); PINV(cv[r * 4 + 2 * u + 1]);
      }
    }
  };
#define MSG_IDX(m) (kq * 4 + ((kq + 1 + (m)) & 3))
  unsigned seen = 0;

  if (__builtin_expect(wid >= 4, 1)) {
    __builtin_amdgcn_s_setprio(3);
    G1_STAGE_TILE(0, 0, NOW);
    constexpr int O_ST = (FL_ST - FL_AB) * 4, O_CE = (FL_CE - FL_AB) * 4, O_KEY = (FL_KEY - FL_AB) * 4, O_EP = (FL_EPOCH - FL_AB) * 4;
    const __amdgpu_buffer_rsrc_t rf = mk_rsrc(wsp + WS_FLAGS + FL_AB * 4);
    u32x4 lv[2], lv2[2], lw[2], ld[2], ld2[2];
    unsigned epv;
    {
      const char* xrow = (const char*)x + o_l;
      const char* wdrow = (const char*)wdz + o_d;
      lv[0] = ld16_s<0>(xrow, l16); lv[1] = ld16_s<1024>(xrow, l16);
      lv2[0] = ld16_s<2048>(xrow, l16); lv2[1] = ld16_s<3072>(xrow, l16);
      ld[0] = ld16_s<0>(wdrow, l16); ld[1] = ld16_s<1024>(wdrow, l16);
      ld2[0] = ld16_s<2048>(wdrow, l16); ld2[1] = ld16_s<3072>(wdrow, l16);
      lw[0] = ld16_s<0>(w_r, l16); lw[1] = ld16_s<1024>(w_r, l16);
      asm volatile("buffer_load_dword %0, off, %1, %2 sc1" : "=v"(epv) : "s"(rf), "s"(O_EP) : "memory");
    }
    SB();
    const unsigned long long did = llvm_dispatch_id();
    const unsigned dmix = (unsigned)did * 0x85EBCA6Bu + (unsigned)(did >> 32) * 0xC2B2AE35u + 0x27D4EB2Fu;
    const bool moe = et != 7;
    unsigned epoch;
    const int ci = lane & 15, rq = lane >> 4, tk = (lane >> 3) & 1;
    int mat, cg, dq, d0;
    u32x4 wv[16];
    const u16 *pgu, *pn2;
    const char *pm2, *spb;
#define rw __builtin_amdgcn_make_buffer_rsrc((void*)pgu, 0, moe ? 0x7fffffff : 0, 0x00020000)
#define rn2 mk_rsrc(pn2)
#define rm2 mk_rsrc(pm2)
    int vw, vn2[8], vm2;
    auto k_blk = [&](int k) __attribute__((always_inline)) { return tt ^ (k_ord(k) >> 1); };
#define KROT(kk_) (k_blk(kk_) * 256 + (k_ord(kk_) & 1) * 128)
    int tv2;
    unsigned sd = 0;
    auto spf = [&](auto N) __attribute__((always_inline)) {
      constexpr int n = decltype(N)::value;
      if constexpr (n >= 0 && sp_tile(n >> 4) >= 0) sld<((n >> 1) & 7) * 4096 + (n & 1) * 64>(sd, spb, KROT(sp_tile(n >> 4)));
    };
    unsigned mq0, mq1, mq2, mq3;
    const int cg8 = et ^ ((et & 1) << 1), rgb = (g & 7) * 4 + (j >> 3);
    const __amdgpu_buffer_rsrc_t rdn = mk_rsrc(w_down);
    const __amdgpu_buffer_rsrc_t rgu = mk_rsrc(wsp + WS_GU);
    int vdn, vgu;
    u32x4 dv[7], gv[4];
    int abo, abq[3];
    unsigned lma;
    unsigned sel[2][4];
    float g0, g1;
    u32x4 kk[4];
    unsigned k1 = 0, k2 = 0;
    const int ko_ = O_KEY + pw * 4096;
#define MM(a, b, c) __builtin_amdgcn_mfma_f32_16x16x32_bf16(__builtin_bit_cast(bf16x8, a), __builtin_bit_cast(bf16x8, b), c, 0, 0, 0)
#define PIN1(a) asm volatile("" : "+v"(a))
#define PIN2(a, b) asm volatile("" : "+v"(a), "+v"(b))
#define PIN3(a, b, c) asm volatile("" : "+v"(a), "+v"(b), "+v"(c))
    {
      int gro, go;
      u32x2 gr;
      u32x4 oh[5];
      f32x4 sa4, gw[2], gx[2];
      float su, gs, ta, tb, sx, mbv;
      unsigned dk[4], kl;
      const int hb = lane >> 5;
#pragma unroll
      for (int l = 0; l < WSN; ++l) asm volatile("" : "=v"(wv[l]));
      auto diag = [&](const f32x4& m) __attribute__((always_inline)) {
        return __uint_as_float((__float_as_uint(m[0]) & dk[0]) | (__float_as_uint(m[1]) & dk[1]) | (__float_as_uint(m[2]) & dk[2]) | (__float_as_uint(m[3]) & dk[3]));
      };
      auto setup = [&](auto JB, auto GP) __attribute__((always_inline)) {
        constexpr int jb = decltype(JB)::value, gp = decltype(GP)::value;
        SB();
        {
          int ln = lane, jj = j, p_ = pw;
          launder(ln, jj, p_);
          const int c_ = ln & 15, r_ = ln >> 4, e_ = jj & 7, t_ = (xcd & 1) * 4 + (jj >> 3);
          if constexpr (jb == 1 && gp == 0) {
            pn2 = ((kq >> 1) ? ws_im : ws_re) + (kq & 1) * 1024;
            spb = (const char*)(pn2 + (sym_row(S_, 64, 1, t_, 0, e_ >> 1, 0) + (e_ & 1) * 32 + p_ * 8) * S_);
            pins(pn2, spb);
          }
          if constexpr (jb == 1 && gp >= 1 && gp < 6) {
            const unsigned o = c_ == gp - 1 ? 0x3f803f80u : 0u;
            oh[gp - 1] = (u32x4){o, o, o, o};
            PIN1(oh[gp - 1]);
          }
          if constexpr (jb == 1 && gp == 6) {
            const int rk = moe ? e_ * 8 + (jj >> 3) * 2 + (xcd >> 2) : 0, r3 = ((rk - 14) * 43) >> 7, rr = rk - 14 - r3 * 3;
            const int c56 = rk < 14 ? rk * 4 + 1 : r3 * 4 + rr + ((rr + 1) >> 1);
            mat = (c56 + 36) >> 6; cg = c56 - mat * 28; dq = xcd & 3; d0 = dq * 256 + p_ * 64;
            pin4(mat, cg, dq, d0);
          }
          if constexpr (jb == 2 && gp == 0) {
            pgu = mat ? w_up : w_gate;
            pm2 = wsp + WS_ABT + (kq * 8 + e_) * 262144;
            pins(pgu, pm2);
            vw = ((d0 + r_ * 16) * F_ + cg * 128 + c_ * 8) * 2;
            PIN1(vw);
          }
          if constexpr (jb == 2 && (gp == 1 || gp == 2)) {
            dk[gp * 2 - 2] = c_ == r_ * 4 + gp * 2 - 2 ? ~0u : 0u; dk[gp * 2 - 1] = c_ == r_ * 4 + gp * 2 - 1 ? ~0u : 0u;
            PIN2(dk[gp * 2 - 2], dk[gp * 2 - 1]);
          }
          if constexpr (jb == 2 && gp == 3) { kl = lrow + (ln >> 5); go = (ln & 30) ? (int)0x80000000 : ((ln & 1) ? O_KEY : O_ST) + (int)kl * 8; PIN2(kl, go); }
          if constexpr (jb == 2 && gp == 4) { kl = 0xffff - kl; go = (ln & ~1) == 2 ? O_CE + (z * D_ + drow + (ln & 1)) * 8 : go; PIN2(kl, go); }
          if constexpr (jb == 2 && gp == 5) {
            gro = p_ < 2 ? O_ST + (st * 128 + p_ * 64 + ln) * 8 : O_CE + z * D_ * 8 + sym_row(D_, 32, 8, e_, z, (ln >> 2) & 3, (p_ - 2) * 16 + r_ * 4 + (ln & 3));
            lma = lds_addr(smem + LDS_MB) + (p_ < 2 ? p_ * 256 : 512 + p_ * 256) + ln * 4;
            PIN2(gro, lma);
          }
          if constexpr (jb == 1 && gp == 7) {
            static_assert(VA0 >= 2 && VA0 + VAN <= 9, "first halves");
            tv2 = (ln >> 3) < VAN ? (ln & 7) * 4096 + ((t_ ^ (VA0 - 1 + (ln >> 3))) << 8) : (int)0x80000000;
            PIN1(tv2);
          }
          if constexpr (jb == 7 && gp == 0) {
            vm2 = p_ * 1024 + ln * 16;
            PIN1(vm2);
          }
          if constexpr (jb == 2 && gp == 6) {
            const int r0 = p_ * 8 + lrw(ln >> 3);
            vn2[6] = sym_lo(64, S_ * 2, (r0 >> 2) & 3, (r0 >> 4) * 4 + (r0 & 3)); vn2[7] = sym_hi(S_, 64, S_ * 2, t_, z, (r0 >> 2) & 3) | (vm & 127);
            PIN2(vn2[6], vn2[7]);
          }
          if constexpr ((jb == 2 && gp == 7) || (jb == 7 && gp >= 6)) {
            constexpr int q0 = jb == 2 ? 0 : gp == 6 ? 3 : 6;
            const int lo = vn2[6], hi = vn2[7];
#pragma unroll
            for (int q = q0; q < q0 + 3 && q < 8; ++q) { vn2[q] = ((lo + q * 8 * S_ * 2) & (63 * S_ * 2)) ^ hi; PIN1(vn2[q]); }
          }
          if constexpr (jb == 7 && gp == 1) { vdn = rgb * 112 + p_ * 28 + r_ * 7; vgu = (((ln >> 3) & 1) * 4 * F_ + vdn + min(ln & 7, 6)) * 16; PIN2(vdn, vgu); }
          if constexpr (jb == 7 && gp == 2) { vdn = (vdn * D_ + cg8 * 128 + c_ * 8) * 2; PIN1(vdn); }
          if constexpr (jb == 7 && gp == 3) {
            abo = (((kq * 8 + e_) * 4 + p_) * 16 + c_) * AB_STRIDE * 4;
            abq[2] = ln >= 16 || (ln & 7) == t_ ? (int)0x80000000 : abo;
            PIN2(abo, abq[2]);
          }
          if constexpr (jb == 7 && gp == 4) {
            abq[1] = ln >= 8 ? (int)0x80000000 : abq[2];
            abq[0] = ((ln ^ t_) & 4) ? (int)0x80000000 : abq[1];
            PIN2(abq[0], abq[1]);
          }
          if constexpr (jb == 7 && gp == 5) {
            const unsigned mb = lds_addr(smem) + p_ * 1024;
            mq0 = mb + ((3 - kq) & 3) * 4096; mq1 = mb + ((4 - kq) & 3) * 4096; mq2 = mb + ((5 - kq) & 3) * 4096; mq3 = mb + ((6 - kq) & 3) * 4096;
            pin4(mq0, mq1, mq2, mq3);
          }
          if constexpr (jb == 8 && gp < 4) {
#pragma unroll
            for (int h = 0; h < 2; ++h) {
              const unsigned hv = h ? 0x0302u : 0x0100u;
              sel[h][gp] = ((ln & 7) >> 1) == gp ? ((ln & 1) ? (hv << 16) | 0x0c0cu : 0x0c0c0000u | hv) : 0x0c0c0c0cu;
            }
            PIN2(sel[0][gp], sel[1][gp]);
          }
        }
        SB();
      };
#define WS1(i) setup(IC<1>{}, IC<i>{})
#define WS2(i) setup(IC<2>{}, IC<i>{})
#define WS3(i) do { setup(IC<7>{}, IC<i>{}); if (i >= 4) setup(IC<8>{}, IC<(i) & 3>{}); spf(IC<(i < SP0 ? i : -1)>{}); } while (0)
      G1_STAGE_TILE(1, G1_STAGE, WS1);
      WAIT_VM(19); BAR();
      G1_STAGE_TILE(2, 2 * G1_STAGE, WS2);
      G1_STAGE_TILE(3, 3 * G1_STAGE, WS3);
      auto lnu = [&](auto U) __attribute__((always_inline)) {
        constexpr int jb = 1 + decltype(U)::value / 7, gp = decltype(U)::value % 7;
        SB();
        if constexpr (jb == 1) {
          const f32x4 z4 = {0.f, 0.f, 0.f, 0.f};
          if constexpr (gp == 0) { sa4 = MM(lv[0], oh[0], z4); gw[0] = MM(lv[0], lw[0], z4); gx[0] = MM(lv[0], lv[0], z4); PIN3(sa4, gw[0], gx[0]); }
          if constexpr (gp == 1) { sa4 = MM(lv2[0], oh[1], sa4); gw[1] = MM(lv2[0], lw[0], z4); gx[1] = MM(lv2[0], lv2[0], z4); PIN3(sa4, gw[1], gx[1]); }
          if constexpr (gp == 2) { sa4 = MM(lv[1], oh[0], sa4); gw[0] = MM(lv[1], lw[1], gw[0]); gx[0] = MM(lv[1], lv[1], gx[0]); PIN3(sa4, gw[0], gx[0]); }
          if constexpr (gp == 3) { sa4 = MM(lv2[1], oh[1], sa4); gw[1] = MM(lv2[1], lw[1], gw[1]); gx[1] = MM(lv2[1], lv2[1], gx[1]); PIN3(sa4, gw[1], gx[1]); }
          if constexpr (gp == 4) { sa4 = MM(ld[0], oh[2], sa4); sa4 = MM(ld[1], oh[2], sa4); PIN1(sa4); }
          if constexpr (gp == 5) { sa4 = MM(ld2[0], oh[3], sa4); sa4 = MM(ld2[1], oh[3], sa4); PIN1(sa4); }
          if constexpr (gp == 6) { sa4 = MM(lw[0], oh[4], sa4); sa4 = MM(lw[1], oh[4], sa4); PIN1(sa4);
            epoch = __builtin_amdgcn_readfirstlane(epv); tag = ((epoch + 1u) * 0x9E3779B1u ^ dmix) | 1u; }
        }
        if constexpr (jb == 2) {
          if constexpr (gp == 0) { su = (sa4[0] + sa4[1]) + (sa4[2] + sa4[3]); ta = diag(gw[0]); PIN2(su, ta); }
          if constexpr (gp == 1) { su = fold32(su, su); tb = diag(gw[1]); PIN2(su, tb); }
          if constexpr (gp == 2) { su = fold16(su, su); gs = fold32(ta, tb); PIN2(su, gs); }
          if constexpr (gp == 3) { ta = diag(gx[0]); tb = diag(gx[1]); PIN2(ta, tb); }
          if constexpr (gp == 4) { gs = fold16(gs, fold32(ta, tb)); PIN1(gs); }
          if constexpr (gp == 5) { gs += dpp_f<0xB1>(gs); gs += dpp_f<0x4E>(gs); PIN1(gs); }
          if constexpr (gp == 6) { gs += dpp_f<0x141>(gs); gs += dpp_f<0x140>(gs); PIN1(gs); }
        }
        if constexpr (jb == 3) {
          if constexpr (gp == 0) {
            const float s0 = rdl(su, 0), s1 = rdl(su, 1), q0 = rdl(gs, 16), q1 = rdl(gs, 48);
            sx = hb ? s1 : s0; ta = hb ? q1 : q0; PIN2(sx, ta);
          }
          if constexpr (gp == 1) {
            const float mean = sx * (1.0f / D_);
            mbv = rbf(mean);
            ta -= sx * mean;
            if (__builtin_expect(__any(!(sx * mean <= 16.f * ta)), 0)) {
              float q = fold32(sqdev16(lv, rdl(mbv, 0)), sqdev16(lv2, rdl(mbv, 32)));
              q = row_sum(fold16(q, q));
              const float dm = mean - mbv;
              ta = fmaxf(q - (float)D_ * dm * dm, 0.f);
            }
            PIN2(mbv, ta);
          }
          if constexpr (gp == 2) { ta = rbf(rbf(ta * (1.0f / D_)) + 1e-5f); gs -= mbv * rdl(su, 4); PIN2(ta, gs); }
          if constexpr (gp == 3) { ta = rbf(__builtin_amdgcn_sqrtf(ta)); tb = __builtin_amdgcn_rcpf(ta); PIN2(ta, tb); }
          if constexpr (gp == 4) {
            const unsigned rb = __float_as_uint(rbf(gs * tb));
            kl |= (rb ^ ((unsigned)((int)rb >> 31) | 0x80000000u)) & 0xffff0000u; PIN1(kl);
          }
          if constexpr (gp == 5) { kl = (lane & 2) ? __float_as_uint(su) : (lane & 1) ? kl : (__float_as_uint(mbv) >> 16) | __float_as_uint(ta); PIN1(kl); }
        }
        SB();
      };
      constexpr int LN_JB = 1 + (19 * LNS) / 8;
      auto work1 = [&](auto JB, auto GP) __attribute__((always_inline)) {
        constexpr int jb = decltype(JB)::value, gp = decltype(GP)::value, pos = (jb - 1) * 8 + gp;
        SB();
        if constexpr (pos % LNS == 0 && pos / LNS < 20) lnu(IC<pos / LNS>{});
        if constexpr (jb == ST_JB + 3) {
          if constexpr (gp == 0) { if (!__all(gr.y == tag)) gr = granule_wait((const u64*)((const char*)(flags + FL_AB) + gro), tag); PIN1(gr); }
          if constexpr (gp == 1) { g1 = __builtin_amdgcn_rcpf(__uint_as_float((gr.x & 0xffff0000u) ^ ((unsigned)z << 31))); PIN1(g1); }
          if constexpr (gp == 2) { g0 = pw < 2 ? -(lo_bf(gr.x) * g1) : __uint_as_float(gr.x); PIN1(g0); }
        }
        if constexpr (gp < SP1 && jb <= SP1E) spf(IC<SP0 + (jb - 1) * SP1 + gp>{});
        SB();
      };
      auto step1 = [&](auto JB) __attribute__((always_inline)) {
        constexpr int jb = decltype(JB)::value;
        if constexpr (jb == 1) PAGE(1);
        if constexpr (jb == 7) PAGE(2);
        constexpr int n16 = jb == 12 ? 12 : jb == 11 ? 14 : 16;
        constexpr auto wl = [](int q) { return (q == LN_JB ? 1 : 0) + (q == ST_JB ? 1 : 0); };
        if constexpr (jb == 1) {
          asm volatile("s_waitcnt vmcnt(%11)" : "+v"(lv[0]), "+v"(lv[1]), "+v"(lw[0]), "+v"(lw[1]), "+v"(ld[0]), "+v"(ld[1]),
                       "+v"(lv2[0]), "+v"(lv2[1]), "+v"(ld2[0]), "+v"(ld2[1]), "+v"(epv) : "i"(16) : "memory");
        } else if constexpr (jb == ST_JB + 3) {
          asm volatile("s_waitcnt vmcnt(%1)" : "+v"(gr) : "i"(n16 + wl(jb - 2) + wl(jb - 1)) : "memory");
        } else wait_vm<n16 + wl(jb - 3) + wl(jb - 2) + wl(jb - 1)>();
        BAR();
        if constexpr (jb == 2) { if (pw == 0 && lane < 16) ((int*)(smem + LDS_SEL))[lane < 8 ? lane : lane + 8] = 0; }
        if constexpr (jb == 4) { if (pw == 0 && lane == 0) ((unsigned*)(smem + LDS_SEL))[5] = tag; }
#define W1(i) work1(JB, IC<i>{})
        if constexpr (jb < 10) G1_STAGE_TILE(jb - 1 + G1_NST, ((jb - 1) % G1_NST) * G1_STAGE, W1);
        else G1_STAGE_A(jb - 1 + G1_NST, ((jb - 1) % G1_NST) * G1_STAGE, W1);
        if constexpr (jb == 12) G1_STAGE_B(13, G1_STAGE);
        if constexpr (jb == LN_JB) __builtin_amdgcn_raw_buffer_store_b64((u32x2){kl, tag}, rf, go, 0, 16);
        if constexpr (jb == ST_JB) {
          asm volatile("buffer_load_dwordx2 %0, %1, %2, 0 offen sc1" : "=v"(gr) : "v"(gro), "s"(rf) : "memory");
        }
        if constexpr (jb <= 8) {
          if (et == jb - 1) {
#pragma unroll
            for (int l = 0; l < WSN; ++l) bld16_nt_io(wv[l], rw, vw, l * F_ * 2);
          }
        }
        SB();
      };
      static_assert(ST_JB + 3 <= 12 && ST_JB >= 6, "schedule");
      step1(IC<1>{}); step1(IC<2>{}); step1(IC<3>{}); step1(IC<4>{}); step1(IC<5>{}); step1(IC<6>{}); step1(IC<7>{}); step1(IC<8>{}); step1(IC<9>{}); step1(IC<10>{}); step1(IC<11>{}); step1(IC<12>{});
    }
#define G2_STAGE_N(kk_, W) do { const int kb = KROT(kk_); constexpr int so = n_off(kk_); SB(); \
    DMA_S(rn2, vn2[0], mq0, so, kb); W(0); DMA_S(rn2, vn2[1], mq1, so, kb); W(1); \
    DMA_S(rn2, vn2[2], mq2, so, kb); W(2); DMA_S(rn2, vn2[3], mq3, so, kb); W(3); \
    DMA_S(rn2, vn2[4], mq0, so + 16384, kb); W(4); DMA_S(rn2, vn2[5], mq1, so + 16384, kb); W(5); \
    DMA_S(rn2, vn2[6], mq2, so + 16384, kb); W(6); DMA_S(rn2, vn2[7], mq3, so + 16384, kb); W(7); SB(); } while (0)
#define G2_STAGE_NH(kk_, h) do { const int kb = KROT(kk_); constexpr int so = n_off(kk_) + (h) * 16384; SB(); \
    DMA_S(rn2, vn2[(h) * 4], mq0, so, kb); DMA_S(rn2, vn2[(h) * 4 + 1], mq1, so, kb); \
    DMA_S(rn2, vn2[(h) * 4 + 2], mq2, so, kb); DMA_S(rn2, vn2[(h) * 4 + 3], mq3, so, kb); SB(); } while (0)
#define G2_STAGE_M(kk_, W) do { const int kb = KROT(kk_) * 128; constexpr int so = G2_MRING + m_off(kk_); SB(); \
    DMA_(rm2, vm2, mb0, so, kb, 0, " sc1"); W(8); DMA_(rm2, vm2, mb0, so + 4096, kb, 4096, " sc1"); W(9); \
    DMA_(rm2, vm2, mb0, so + 8192, kb, 8192, " sc1"); W(10); DMA_(rm2, vm2, mb0, so + 12288, kb, 12288, " sc1"); W(11); SB(); } while (0)
    WAIT_VM(8); BAR();
    asm volatile("ds_write_b32 %0, %1" :: "v"(lma), "v"(g0) : "memory");
    if (pw < 2) asm volatile("ds_write_b32 %0, %1 offset:512" :: "v"(lma), "v"(g1) : "memory");
    u32x4 tv[4];
    unsigned ready = 0;
    auto need = [&](int k) __attribute__((always_inline)) {
      const int b = k_blk(k) + (k_ord(k) & 1) * 8;
#pragma unroll 1
      for (int it = 0; !((ready >> b) & 1) && it < SPIN_MAX; ++it) {
        ready |= (unsigned)__ballot(__builtin_amdgcn_raw_buffer_load_b32(rf, abq[k < 5 ? 0 : k < 9 ? 1 : 2], 0, 16) == tag);
        asm volatile("" ::: "memory");
      }
    };
    unsigned ksel, bad = 0;
    int isel;
    unsigned xw;
    auto bvec = [&](unsigned src, const unsigned (&sl)[4]) __attribute__((always_inline)) {
      return (u32x4){__builtin_amdgcn_perm(0, src, sl[0]), __builtin_amdgcn_perm(0, src, sl[1]),
                     __builtin_amdgcn_perm(0, src, sl[2]), __builtin_amdgcn_perm(0, src, sl[3])};
    };
#define BC8(v, p) (unsigned)__builtin_amdgcn_update_dpp(__builtin_amdgcn_update_dpp(0, (int)(v), 0x150 + (p), 0xf, 0x3, false), (int)(v), 0x158 + (p), 0xf, 0xc, false)
    float* lg = (float*)(smem + LDS_GU);
    f32x4 macc = {0.f, 0.f, 0.f, 0.f};
    u32x2 sv;
    unsigned hp, gbad;
    float s0, s1;
    auto work2 = [&](auto JB, auto GP) __attribute__((always_inline)) {
      constexpr int jb = decltype(JB)::value, gp = decltype(GP)::value;
      SB();
      if constexpr (jb == -1 && gp < 4) {
        constexpr int c = gp == 0 ? 0xB1 : gp == 1 ? 0x4E : gp == 2 ? 0x141 : 0x140;
        merge2(k1, k2, dpp_u<c>(k1), dpp_u<c>(k2));
        PIN2(k1, k2);
      }
      if constexpr (jb == -1 && gp == 4) {
        ksel = tk ? k2 : k1;
        isel = (0xffff - (int)(ksel & 0xffffu)) & (S_ - 1);
        k1 = (isel * D_ + d0 + rq * 16 + (lane & 7) * 2) * 2; k2 = O_ST + isel * 8;
        PIN1(ksel);
        xw = gld4_s(x, k1);
        sv = bld8_sc1(rf, k2);
      }
      if constexpr (jb == 0 && gp == 1) wait_xs<3 + 2 + 2>(xw, sv);
      if constexpr (jb == 0) {
        if constexpr (gp == 1) { if (!__all(sv.y == tag)) sv = granule_wait(stat + isel, tag); s0 = __builtin_amdgcn_rcpf(hi_bf(sv.x)); PIN1(s0); }
        if constexpr (gp == 2) { s1 = ln_elem(lo_bf(xw), lo_bf(sv.x), hi_bf(sv.x), s0); PIN1(s1); }
        if constexpr (gp == 3) { xw = pk2(s1, ln_elem(hi_bf(xw), lo_bf(sv.x), hi_bf(sv.x), s0)); PIN1(xw); }
      }
      if constexpr (jb == DG && gp < 7) { PIN1(dv[gp]); macc = MM(dv[gp], bvec(BC8(hp, gp), sel[0]), macc); PIN1(macc); }
      if constexpr ((jb == 0 && gp >= 4) || jb == 1) {
        constexpr int l = jb == 0 ? gp - 4 : gp + 4;
        if constexpr (!(l & 1)) hp = BC8(xw, l >> 1);
        PIN1(wv[l]);
        macc = MM(wv[l], bvec(hp, sel[l & 1]), macc);
        PIN2(macc, hp);
      }
      if constexpr (jb == GV_JB + 2) {
        if constexpr (gp == 0) { gbad = (gv[0].y ^ tag) | (gv[0].w ^ tag) | (gv[1].y ^ tag) | (gv[1].w ^ tag); PIN1(gbad); }
        if constexpr (gp == 1) { gbad |= (gv[2].y ^ tag) | (gv[2].w ^ tag) | (gv[3].y ^ tag) | (gv[3].w ^ tag); PIN1(gbad); }
        if constexpr (gp == 2) {
          s0 = ((__uint_as_float(gv[0].x) + __uint_as_float(gv[1].x)) + __uint_as_float(gv[2].x)) + __uint_as_float(gv[3].x);
          s1 = ((__uint_as_float(gv[0].z) + __uint_as_float(gv[1].z)) + __uint_as_float(gv[2].z)) + __uint_as_float(gv[3].z);
          PIN2(s0, s1);
        }
        if constexpr (gp == 3) {
#pragma unroll 1
          for (int it = 0; !__all(gbad == 0) && it < SPIN_MAX; ++it) {
            u32x4 q[4];
            gbad = 0;
#pragma unroll
            for (int i = 0; i < 4; ++i) { q[i] = __builtin_amdgcn_raw_buffer_load_b128(rgu, vgu, i * F_ * 16, 16); gbad |= (q[i].y ^ tag) | (q[i].w ^ tag); }
            s0 = ((__uint_as_float(q[0].x) + __uint_as_float(q[1].x)) + __uint_as_float(q[2].x)) + __uint_as_float(q[3].x);
            s1 = ((__uint_as_float(q[0].z) + __uint_as_float(q[1].z)) + __uint_as_float(q[2].z)) + __uint_as_float(q[3].z);
            asm volatile("" ::: "memory");
          }
          const unsigned gp2 = pk2(s0, s1);
          const unsigned up2 = dpp_u<0x128>(gp2);
          s0 = tk ? hi_bf(up2) : lo_bf(gp2); s1 = tk ? hi_bf(gp2) : lo_bf(up2);
          PIN2(s0, s1);
        }
        if constexpr (gp == 4) { s0 = rbf(s0 * __builtin_amdgcn_rcpf(1.f + __expf(-s0))); PIN1(s0); }
        if constexpr (gp == 5) { hp = pk2(s0 * s1, 0.f); PIN1(hp); }
      }
      if constexpr (jb == 10 && gp == 4) code_touch(sd, (j * 4 + pw) * 256);
      SB();
    };
#define W2M(i) work2(IC<-1>{}, IC<i>{})
#define W20(i) work2(IC<0>{}, IC<i>{})
#define W21(i) work2(IC<1>{}, IC<i>{})
    unsigned fs;
    {
      G1_STAGE_B(15, 3 * G1_STAGE);
      G1_STAGE_B(14, 2 * G1_STAGE);
      lgkm0(sd);
      BAR();
      G2_STAGE_NH(0, 0);
      WAIT_VM(6); BAR();
#pragma unroll
      for (int i = 0; i < 4; ++i) kk[i] = bld16_sc1(rf, l16, ko_ + i * 1024);
      G2_STAGE_NH(0, 1);
#pragma unroll
      for (int l = WSN; l < 16; ++l) wv[l] = bld16_nt(rw, vw, l * F_ * 2);
      asm volatile("s_waitcnt vmcnt(%4)" : "+v"(kk[0]), "+v"(kk[1]), "+v"(kk[2]), "+v"(kk[3]) : "i"(16 - WSN + 4) : "memory");
#pragma unroll
      for (int i = 0; i < 4; ++i) {
        k2 = med3u(k1, k2, kk[i].x); k1 = max(k1, kk[i].x);
        k2 = med3u(k1, k2, kk[i].z); k1 = max(k1, kk[i].z);
        bad |= (kk[i].y ^ tag) | (kk[i].w ^ tag);
      }
#pragma unroll 1
      for (int it = 0; !__all(bad == 0) && it < SPIN_MAX; ++it) {
        k1 = 0; k2 = 0; bad = 0;
#pragma unroll
        for (int i = 0; i < 4; ++i) {
          const u32x4 q = __builtin_amdgcn_raw_buffer_load_b128(rf, l16, ko_ + i * 1024, 16);
          k2 = med3u(k1, k2, q.x); k1 = max(k1, q.x);
          k2 = med3u(k1, k2, q.z); k1 = max(k1, q.z);
          bad |= (q.y ^ tag) | (q.w ^ tag);
        }
        asm volatile("" ::: "memory");
      }
      merge2(k1, k2, dpp_u<0xB1>(k1), dpp_u<0xB1>(k2));
      merge2(k1, k2, dpp_u<0x4E>(k1), dpp_u<0x4E>(k2));
      merge2(k1, k2, dpp_u<0x141>(k1), dpp_u<0x141>(k2));
      merge2(k1, k2, dpp_u<0x140>(k1), dpp_u<0x140>(k2));
      if ((lane & 15) == 0) *(u32x2*)(smem + LDS_KEY + (pw * 4 + rq) * 8) = (u32x2){k1, k2};
      PAGE(3);
      wait_vm<16 - WSN>();
      BAR_LGKM();
#pragma unroll
      for (int jj = 0; jj < 4; ++jj) tv[jj] = *(const u32x4*)(smem + G2_MRING + m_off(0) + (jj * 4 + pw) * 1024 + lane * 16);
      { const u32x2 kp = *(const u32x2*)(smem + LDS_KEY + ci * 8); k1 = kp.x; k2 = kp.y; }
#pragma unroll
      for (int jj = 0; jj < 4; ++jj) __builtin_amdgcn_raw_buffer_store_b128(tv[jj], rm2, vm2 + jj * 4096 + 2 * tt * 16384, 0, 16);
      G2_STAGE_N(1, W2M);
      touch1(mk_rsrc(spb), tv2, mb0, 0);
      WAIT_VM(11);
      if (lane == tt) __builtin_amdgcn_raw_buffer_store_b32(tag, rf, abo, 0, 16);
      {
        lds_vi rel = (lds_vi)(smem + LDS_SEL + 12);
        int it = 0;
        while (rel[0] != 4 && it < SPIN_MAX * 8) ++it;
        asm volatile("" ::: "memory");
      }
      G2_STAGE_N(2, W20);
      __builtin_amdgcn_s_sleep(2);
      WAIT_VM(10);
      fs = bld4_sc1(rf, abq[0]);
    }
    BAR();
#pragma unroll
    for (int jj = 0; jj < 4; ++jj) tv[jj] = *(const u32x4*)(smem + G2_MRING + m_off(1) + (jj * 4 + pw) * 1024 + lane * 16);
    constexpr int SPB = SP0 + SP1E * SP1;
    G2_STAGE_N(3, NOW);
    asm volatile("s_waitcnt vmcnt(8)" : "+v"(fs) :: "memory");
    ready = (unsigned)__ballot(fs == tag);
    need(2);
    G2_STAGE_M(2, NOW);
    need(3);
    G2_STAGE_M(3, NOW);
    fs = bld4_sc1(rf, abq[1]);
    W21(0); W21(1); W21(2); W21(3); W21(4); W21(5); W21(6); W21(7); W21(8); W21(9); W21(10); W21(11);
    asm volatile("" : "+v"(tv[0]), "+v"(tv[1]), "+v"(tv[2]), "+v"(tv[3]));
    auto step2 = [&](auto JB) __attribute__((always_inline)) {
      constexpr int jb = decltype(JB)::value;
      if constexpr (jb == 3) PAGE(4);
      if constexpr (jb == 9) PAGE(5);
      constexpr auto must = [](int q) { return (q == 2 ? 4 : 0) + ((FSM >> q) & 1) + dig(GVS, q); };
      if constexpr (jb == 2) wait_vm<4 + must(1)>();
      else wait_vm<dn_cnt(jb - 2) + (jb == 5 ? 1 : 0) + 12 + must(jb - 1) + dn_cnt(jb - 1)>();
      if constexpr (jb == 3) { if (pw == 0 && (lane & ~8) == 0) ((unsigned*)(smem + LDS_SEL))[tk] = ksel; }
      if constexpr ((jb == 2 || jb == DG + 1)) {
        *(f32x4*)(lg + pw * 256 + lane * 4) = macc;
        macc = (f32x4){0.f, 0.f, 0.f, 0.f};
        lgkm0(sd);
      }
      BAR();
      float p0[4], p1[4];
      const int col = (pw & 1) * 64 + lane;
      if constexpr (jb == 3) {
        if (g == 0 && pw == 0 && lane == 0) __hip_atomic_store((unsigned*)flags + FL_EPOCH, epoch + 1u, __ATOMIC_RELAXED, __HIP_MEMORY_SCOPE_AGENT);
      }
      if constexpr (jb == DG + 1) {
        const float* p = lg + (pw & 1) * 32 + (lane >> 4) * 64 + ((2 * lane) & 7) * 4 + ((lane >> 2) & 3);
#pragma unroll
        for (int i = 0; i < 4; ++i) { p0[i] = p[i * 256]; p1[i] = p[i * 256 + 4]; }
      }
      if constexpr (jb == 2) {
        const float* p = lg + (col >> 5) * 64 + (col & 7) * 4 + ((col >> 3) & 3);
#pragma unroll
        for (int i = 0; i < 4; ++i) { p0[i] = p[i * 256]; p1[i] = p[i * 256 + 32]; }
      }
      if constexpr (jb == 4) {
        if (lane == 8 + tt) __builtin_amdgcn_raw_buffer_store_b32(tag, rf, abo, 0, 16);
      }
      if constexpr ((FSM >> (jb - 2)) & 1) {
        asm volatile("" : "+v"(fs));
        ready |= (unsigned)__ballot(fs == tag);
      }
      if constexpr (jb == GV_JB + 2) asm volatile("" : "+v"(gv[0]), "+v"(gv[1]), "+v"(gv[2]), "+v"(gv[3]));
      auto w2 = [&](auto GP) __attribute__((always_inline)) {
        constexpr int gp = decltype(GP)::value;
        if constexpr (jb == 2 && gp == 0) {
          p0[0] = (p0[0] + p0[1]) + (p0[2] + p0[3]); p1[0] = (p1[0] + p1[1]) + (p1[2] + p1[3]);
          PIN2(p0[0], p1[0]);
        }
        work2(JB, GP);
        if constexpr (gp < dig(SPS, jb)) spf(IC<SPB + acc_dig(SPS, jb) + gp>{});
      };
#define W2(i) w2(IC<i>{})
      G2_STAGE_N(jb + 2, W2);
      need(jb + 2);
      G2_STAGE_M(jb + 2, W2);
      if constexpr (jb == 2) {
#pragma unroll
        for (int jj = 0; jj < 4; ++jj) __builtin_amdgcn_raw_buffer_store_b128(tv[jj], rm2, vm2 + jj * 4096 + (2 * tt + 1) * 16384, 0, 16);
      }
      if constexpr ((FSM >> jb) & 1) fs = bld4_sc1(rf, abq[jb < 5 ? 1 : 2]);
#pragma unroll
      for (int i = acc_dig(GVS, jb); i < acc_dig(GVS, jb + 1); ++i) gv[i] = bld16_sc1(rgu, vgu, i * F_ * 16);
#pragma unroll
      for (int i = dn_first(jb); i < dn_first(jb) + dn_cnt(jb); ++i) dv[i] = bld16_nt(rdn, vdn, i * D_ * 2);
      if constexpr (jb == DG + 1) {
        if (pw < 2)
          __builtin_amdgcn_raw_buffer_store_b128((u32x4){__float_as_uint((p0[0] + p0[1]) + (p0[2] + p0[3])), tag, __float_as_uint((p1[0] + p1[1]) + (p1[2] + p1[3])), tag},
                                                 mk_rsrc(wsp + WS_DN), (((pw * 8 + cg8) * 32 + rgb) * 64 + lane) * 16, 0, 16);
      }
      if constexpr (jb == 2) {
        if (moe && pw < 2)
          __builtin_amdgcn_raw_buffer_store_b128((u32x4){__float_as_uint(p0[0]), tag, __float_as_uint(p1[0]), tag},
                                                 rgu, ((mat * 4 + dq) * F_ + cg * 128 + col) * 16, 0, 16);
      }
      SB();
      if constexpr (dig(SPS, jb) > 12) spf(IC<SPB + acc_dig(SPS, jb) + 12>{});
      SB();
    };
    static_assert(DG + 1 <= 12 && DG >= GV_JB + 3 && dn_first(DG - 2) == 7 && acc_dig(GVS, GV_JB + 1) == 4, "schedule");
    static_assert(SPB + acc_dig(SPS, 14) == (16 - VAN) * 16, "all tiles are prefetched");
    step2(IC<2>{}); step2(IC<3>{}); step2(IC<4>{}); step2(IC<5>{}); step2(IC<6>{}); step2(IC<7>{}); step2(IC<8>{}); step2(IC<9>{}); step2(IC<10>{}); step2(IC<11>{}); step2(IC<12>{}); step2(IC<13>{});
    lgkm0(sd);
    WAIT_VM(12); BAR();
    __builtin_amdgcn_s_setprio(0);
    epi_setup(0); epi_setup(1); epi_setup(2);
    xq_load(0); xq_load(1); xq_load(2);
    WAIT_VM(3); BAR();
    epi_setup(3);
    wait_xq<0>(xq, sq);
    key[0] = __builtin_amdgcn_readlane(ksel, 0); key[1] = __builtin_amdgcn_readlane(ksel, 8);
    cv_calc();
#pragma unroll 1
    for (int c = 0; c < 2; ++c) {
      if (__builtin_expect(((c ? far1 : far0) & ~9) != 0, 1)) continue;
      const __amdgpu_buffer_rsrc_t rd = mk_rsrc(wsp + WS_DN + (size_t)(c * 8 * 32 + pw * 8) * 1024);
      const int ec = seg0(lane >> 5, (lane >> 4) & 1), dno = (ec >> 7) * 32768 + ((ec & 127) / 2 + (lane & 15)) * 16;
#pragma unroll 1
      for (int it = 0; it < SPIN_MAX; ++it) {
        if (__all(__builtin_amdgcn_raw_buffer_load_b32(rd, (ec >> 7) * 32768 + (lane & 7) * 1024 + 1020, 0, 16) == tag)) break;
        __builtin_amdgcn_s_sleep(8);
        asm volatile("" ::: "memory");
      }
      float s0, s1;
      unsigned bad = 1;
#pragma unroll 1
      for (int it = 0; !__all(bad == 0) && it < SPIN_MAX; ++it) {
        bad = 0; s0 = 0.f; s1 = 0.f;
#pragma unroll
        for (int i = 0; i < 8; ++i) {
          const u32x4 q = __builtin_amdgcn_raw_buffer_load_b128(rd, dno, i * 1024, 16);
          bad |= (q.y ^ tag) | (q.w ^ tag); s0 += __uint_as_float(q.x); s1 += __uint_as_float(q.z);
        }
        asm volatile("" ::: "memory");
      }
      *(f32x2*)(smem + LDS_FIN + (c * 4 + pw) * 512 + lane * 8) = (f32x2){s0, s1};
      asm volatile("s_waitcnt lgkmcnt(0)" ::: "memory");
      if (lane == 0) asm volatile("ds_add_u32 %0, %1" :: "v"(lds_addr(smem + LDS_SEL + 24 + c * 4)), "v"(1) : "memory");
    }
  } else {
  {
    const int q = j * 4 + wid;
    constexpr int Q1 = WS_MB, Q2 = Q1 + 8, Q3 = Q2 + 8, Q4 = Q3 + 7, Q5 = Q4 + 7, Q6 = Q5 + 7, Q7 = Q6 + 4, Q8 = Q7 + 4, QC = Q8 + 1;
    static_assert(QC <= 128, "warm-up slots");
    unsigned dmy = 0;
    if (q < QC) {
      const char* base; int b0;
      if (q < Q1) { base = wsp; b0 = 0; }
      else if (q < Q2) { base = (const char*)ws_re; b0 = Q1; }
      else if (q < Q3) { base = (const char*)ws_im; b0 = Q2; }
      else if (q < Q4) { base = (const char*)w_gate; b0 = Q3; }
      else if (q < Q5) { base = (const char*)w_up; b0 = Q4; }
      else if (q < Q6) { base = (const char*)w_down; b0 = Q5; }
      else if (q < Q7) { base = (const char*)out; b0 = Q6; }
      else if (q < Q8) { base = (const char*)x; b0 = Q7; }
      else { base = (const char*)w_r; b0 = Q8; }
      base += (size_t)(q - b0) << 20;
      asm volatile("s_load_dword %0, %1, 0" : "+s"(dmy) : "s"(base) : "memory");
    }
    if (wid == 0) { CALL_PG(1); CALL_PG(8); CALL_PG(5); }
    else if (wid == 1) { CALL_PG(2); CALL_PG(9); CALL_PG(6); }
    else if (wid == 2) { CALL_PG(3); CALL_PG(10); }
    else { CALL_PG(4); CALL_PG(11); }
    BAR();
    lgkm0(dmy);
    TL(1);
  }
  PAGE(8);
  epi_setup(0); epi_setup(1); epi_setup(2); epi_setup(3);
  const int wr = wid >> 1, wc = wid & 1;
  const int sw = swz(fr), fl = lrw(fr);
  const int ko0 = ((0 + fq) ^ sw) * 16, ko1 = ((4 + fq) ^ sw) * 16;
  bf16x8 fm[2][4], fn[2][4];
  {
    const char* pm = smem + (wr * 64 + fl) * 128;
    const char* pn = smem + 16384 + (wc * 64 + fl) * 128;
    f32x4 acc[4][4];
#pragma unroll
    for (int i = 0; i < 4; ++i)
#pragma unroll
      for (int jn = 0; jn < 4; ++jn)
#pragma unroll
        for (int q = 0; q < 4; ++q) acc[i][jn][q] = 0.f;
#define TO(i) ((i) * 2048)
#define LDSP(T, a) ((__attribute__((address_space(3))) T*)(size_t)(a))
#define SLOTS(n) _Pragma("unroll") for (int q = 0; q < (n); ++q)
#define MFA(c, m, n) asm volatile("v_mfma_f32_16x16x32_bf16 %0, %2, %1, %0" : "+v"(c) : "v"(m), "v"(n))
#define MFA2(c, m, n, m2, n2) asm volatile("v_mfma_f32_16x16x32_bf16 %0, %2, %1, %0\n\tv_mfma_f32_16x16x32_bf16 %0, %4, %3, %0" : "+v"(c) : "v"(m), "v"(n), "v"(m2), "v"(n2))
    unsigned lm[2][2], ln[2][2];
#pragma unroll
    for (int u = 0; u < 2; ++u)
#pragma unroll
      for (int kk = 0; kk < 2; ++kk) {
        lm[u][kk] = lds_addr(pm) + u * 2 * G1_STAGE + (kk ? ko1 : ko0);
        ln[u][kk] = lds_addr(pn) + u * 2 * G1_STAGE + (kk ? ko1 : ko0);
        asm volatile("" : "+v"(lm[u][kk]), "+v"(ln[u][kk]));
      }
    auto ldm = [&](int stg, int i, int kk) __attribute__((always_inline)) { fm[kk][i] = *LDSP(const bf16x8, lm[stg >> 1][kk] + (stg & 1) * G1_STAGE + TO(i)); };
    auto ldn = [&](int stg, int jn, int kk) __attribute__((always_inline)) { fn[kk][jn] = *LDSP(const bf16x8, ln[stg >> 1][kk] + (stg & 1) * G1_STAGE + TO(jn)); };
    auto quad = [&](int a, int c, int d, int s) __attribute__((always_inline)) {
      const int i = (s < 2 || s >= 6) ? a : a + 1, jn = s < 4 ? c : d, kk = (s == 1 || s == 2 || s == 5 || s == 6);
      MFA(acc[i][jn], fm[kk][i], fn[kk][jn]);
    };
    auto quad2 = [&](int a, int c, int d, int s) __attribute__((always_inline)) {
      const int i = (s < 2 || s >= 6) ? a : a + 1, jn = s < 4 ? c : d, kk = (s == 2 || s == 6);
      MFA2(acc[i][jn], fm[kk][i], fn[kk][jn], fm[1 - kk][i], fn[1 - kk][jn]);
    };
    auto half1 = [&](int stg) __attribute__((always_inline)) {
      const int ja = (stg & 1) * 2, jb = 2 - ja;
      SLOTS(8) {
        quad(0, ja, ja + 1, q);
        if (q < 4) ldn(stg, jb + (q >> 1), q & 1); else ldm(stg, 2 + ((q - 4) >> 1), q == 5 || q == 6);
        SB();
      }
      SLOTS(4) { quad2(0, jb, jb + 1, 2 * q); SB(); }
    };
    auto half2 = [&](int stg) __attribute__((always_inline)) {
      const int ja = (stg & 1) * 2, jb = 2 - ja, nx = (stg + 1) & 3;
      SLOTS(8) {
        quad(2, jb + 1, jb, q);
        if (!(q & 1)) ldm(nx, q >> 2, q == 2 || q == 4);
        SB();
      }
      SLOTS(8) {
        quad(2, ja, ja + 1, q);
        if (!(q & 1)) ldn(nx, jb + (q >> 2), (q >> 1) & 1);
        SB();
      }
    };
    unsigned ae[7];
    {
      int l_ = lane;
      asm volatile("" : "+v"(l_));
      const int p = l_ >> 4, dn = sym_dn(p), hf = (p >> 1) ^ (dn & z);
#pragma unroll
      for (int u = 0; u < 7; ++u) {
        const int T = u < 3 ? wc * 4 : (wc * 4 + u - 3 - dn) & 7, i = u < 3 ? (u + 1) ^ ((u & 1) ? 0 : dn * 2) : 0;
        const int f = dn * 8 + ((T & 2) << 1) + ((T & 4) >> 1) + (T & 1);
        ae[u] = lds_addr(smem + G2_MRING) + (hf * 64 + i * 16 + lrw(f)) * 128 + (((wr * 4 + ((l_ >> 2) & 3)) ^ swz(f)) * 16) + (l_ & 3) * 4;
        asm volatile("" : "+v"(ae[u]));
      }
    }
    unsigned al = lds_addr(smem + LDS_MB) + (wr * 32 + fr * 2) * 4, ac = lds_addr(smem + LDS_MB) + (256 + wc * 64 + fq * 4) * 4;
    asm volatile("" : "+v"(al), "+v"(ac));
    SLOTS(16) asm volatile("" : "+v"(acc[q >> 2][q & 3]));
    SB();
    SLOTS(4) { ldm(0, q >> 1, q & 1); ldn(0, q >> 1, q & 1); }
    SB();
#pragma unroll 1
    for (int k4 = 0; k4 < G1_NKT - 4; k4 += 4) {
      half1(0); BAR_LGKM();

      SB(); half2(0);
      half1(1); BAR_LGKM();

      SB(); half2(1);
      half1(2); BAR_LGKM();

      SB(); half2(2);
      half1(3); BAR_LGKM();

      SB(); half2(3);
    }
    bf16x8 gm[4][4], gn[2][8];
    auto rdm1 = [&](int set, int t, int h, int i, int kk) __attribute__((always_inline)) {
      gm[set][kk * 2 + i] = *LDSP(const bf16x8, lm[(t >> 1) & 1][kk] + (t & 1) * G1_STAGE + h * 4096 + i * 2048);
    };
    auto rdg = [&](int set, int t, int h, int r) __attribute__((always_inline)) {
      if (r == 0 || r == 2 || r == 4 || r == 5) rdm1(set, t, h, r >= 4, r == 2 || r == 4);
      else {
        const int jn = r < 4 ? 0 : (r - 4) >> 1, kk = r < 4 ? r >> 1 : r & 1;
        gn[set][kk * 4 + jn] = *LDSP(const bf16x8, ln[(t >> 1) & 1][kk] + (t & 1) * G1_STAGE + TO(jn));
      }
    };
    auto mmg = [&](int ms, int ns, int h, int jn, int r, int f) __attribute__((always_inline)) {
      const int i = (r >> 1) ^ f, kk = r == 1 || r == 2;
      MFA(acc[h * 2 + i][jn], gm[ms][kk * 2 + i], gn[ns][kk * 4 + jn]);
    };
    f32x2 rsv, mrv;
    f32x4 cev[4];
    float tc[32];
    unsigned ep;
    auto rds = [&](int h, int r) __attribute__((always_inline)) {
      if (r == 0) mrv = *LDSP(const f32x2, al + h * 256); else if (r == 5) rsv = *LDSP(const f32x2, al + 512 + h * 256);
      else cev[r - 1] = *LDSP(const f32x4, ac + (r - 1) * 64);
    };
    auto prep = [&](int n) __attribute__((always_inline)) {
      tc[n] = llvm_fmul_legacy(mrv[n & 1], cev[n >> 3][(n >> 1) & 3]);
      asm volatile("" : "+v"(tc[n]));
    };
    auto epi = [&](int h, int n) __attribute__((always_inline)) {
      const int jn = n >> 4, r = (n >> 2) & 3, u = n & 3, e = (n >> 2) * 2;
      if (u == 0) {}
      else if (u == 1) asm volatile("v_fmac_f32 %0, %2, %4\n\tv_fmac_f32 %1, %3, %5\n\tv_cvt_pk_bf16_f32 %0, %0, %1" : "+v"(tc[e]), "+v"(tc[e + 1])
                                    : "v"(acc[h * 2][jn][r]), "v"(acc[h * 2 + 1][jn][r]), "v"(rsv[0]), "v"(rsv[1]));
      else if (u == 2) ep = __float_as_uint(tc[e]);
      else *LDSP(unsigned, (r ? ae[r - 1] + (jn & 1) * 128 + (jn >> 1) * lrw(4) * 128 : ae[3 + jn]) + m_off(h)) = ep;
      SB();
    };
    SB();
    half1(0);
    BAR_LGKM();
    SB();
    TL(14);
    SLOTS(16) {
      if (q < 8) quad(2, 3, 2, q); else quad(2, 0, 1, q - 8);
      if (q < 12) rdg(0, 14, 0, q); else rdg(1, 13, 0, q - 12);
      SB();
    }
    BAR();
    SB();
    TL(15);
    SLOTS(16) {
      mmg(0, 0, 0, q >> 2, q & 3, (q >> 2) & 1);
      if (q < 6) rds(0, q); else if (q < 14) rdg(1, 13, 0, q - 2);
      if (q >= 6) prep(q - 6);
      if (q >= 14) prep(q - 4);
      SB();
    }
    PAGE(9);
    BAR();
    SB();
    TL(16);
    {
      constexpr int ct[8] = {1, 1, 0, 0, 1, 0, 1, 0}, cj[8] = {0, 1, 0, 1, 2, 2, 3, 3}, cf[8] = {0, 1, 0, 1, 0, 1, 0, 1};
      constexpr auto en = [](int q) { return q < 15 ? 0 : q < 26 ? (q - 14) * 3 - (q == 25) : q < 27 ? 32 : q < 32 ? 32 + (q - 26) * 3 + (q == 31) : 48; };
      SLOTS(32) {
        mmg(ct[q >> 2], ct[q >> 2], 0, cj[q >> 2], q & 3, cf[q >> 2]);
        if (q < 12) rdg(0, 15, 0, q);
        else if (q < 15 || q == 17) { const int r = q < 15 ? q - 12 : 3; rdm1(2, 15, 1, r >> 1, r == 1 || r == 2); }
        else if (q == 20 || q == 23 || q == 25 || q == 26) { const int r = q == 20 ? 0 : q == 23 ? 1 : q - 23; rdm1(3, 13, 1, r >> 1, r == 1 || r == 2); }
        if (q < 15) prep(12 + q);
        if (q >= 10 && q < 15) prep(17 + q);
        SB();
#pragma unroll
        for (int n = q ? en(q - 1) : 0; n < en(q); ++n) epi(0, n);
      }
    }
    SLOTS(2) { mmg(2, 0, 1, 0, q, 0); SB(); }
    asm volatile("s_nop 1");
    SLOTS(16) { epi(0, 48 + q); if (q == 3 || q == 9) { mmg(2, 0, 1, 0, q == 3 ? 2 : 3, 0); SB(); } }
    BAR_LGKM();
    SB();
    TL(60);
    SLOTS(12) {
      mmg(2, 0, 1, 1 + (q >> 2), q & 3, ~(q >> 2) & 1);
      if (q < 6) rds(1, q); else rdg(0, 14, 1, q - 6);
      if (q >= 8) prep(q - 8);
      SB();
    }
    SLOTS(16) {
      mmg(3, 1, 1, q >> 2, q & 3, (q >> 2) & 1);
      if (q < 6) rdg(0, 14, 1, q + 6);
      if (q < 4) prep(4 + q); else { prep(2 * q); prep(2 * q + 1); }
      SB();
    }
    asm volatile("s_waitcnt lgkmcnt(0)" ::: "memory");
    if (lane == 0) asm volatile("ds_add_u32 %0, %1" :: "v"(lds_addr(smem + LDS_SEL + 12)), "v"(1) : "memory");
    SB();
    SLOTS(16) {
      mmg(0, 0, 1, q >> 2, q & 3, (q >> 2) & 1);
      if (q < 4) fn[0][q] = *(const bf16x8*)(smem + (wr * 128 + fl) * 128 + (q >> 1) * 2048 + ((q & 1) ? ko1 : ko0));
      else if (q < 8) fm[q & 1][(q - 4) >> 1] = *(const bf16x8*)(smem + G2_MRING + m_off(0) + (wc * 64 + fl) * 128 + TO((q - 4) >> 1) + ((q & 1) ? ko1 : ko0));
      SB();
      if (q >= 8) { epi(1, 3 * q - 24); epi(1, 3 * q - 23); epi(1, 3 * q - 22); }
    }
    TL(17);
    SLOTS(40) epi(1, 24 + q);
  }

  const char* pn = smem + (wr * 128 + fl) * 128;
  const char* pm = smem + G2_MRING + (wc * 64 + fl) * 128;
  f32x4 acc[8][4];
  auto mm = [&](bool imaj, int set, int q2, int s, int mode) __attribute__((always_inline)) {
    int i, jj, kk;
    if (imaj) { i = s >> 2; const int r = s & 3; jj = (r >> 1) ^ (i & 1); kk = (r == 1 || r == 2); }
    else { jj = s >> 3; const int r = s & 7; i = jj ? 3 - (r >> 1) : (r >> 1); kk = ((r + 1) >> 1) & 1; }
    if (mode == 1 && !(s & 1)) asm volatile("v_mfma_f32_16x16x32_bf16 %0, %2, %1, 0" : "=&v"(acc[q2 * 2 + jj][i]) : "v"(fm[kk][i]), "v"(fn[set][jj * 2 + kk]));
    else if (mode) asm volatile("v_mfma_f32_16x16x32_bf16 %0, %2, %1, %0" : "+v"(acc[q2 * 2 + jj][i]) : "v"(fm[kk][i]), "v"(fn[set][jj * 2 + kk]));
    else acc[q2 * 2 + jj][i] = __builtin_amdgcn_mfma_f32_16x16x32_bf16(fn[set][jj * 2 + kk], fm[kk][i], acc[q2 * 2 + jj][i], 0, 0, 0);
  };
  unsigned bn[2], bm[2], an[2], am[2];
#pragma unroll
  for (int kk = 0; kk < 2; ++kk) {
    bn[kk] = lds_addr(pn) + (kk ? ko1 : ko0); bm[kk] = lds_addr(pm) + (kk ? ko1 : ko0);
    asm volatile("" : "+v"(bn[kk]), "+v"(bm[kk]));
    an[kk] = bn[kk]; am[kk] = bm[kk] + m_off(0);
  }
#define RDN(jt, kk, o) (*LDSP(const bf16x8, an[kk] + (o) + (jt) * 2048))
#define RDM(i, kk, o) (*LDSP(const bf16x8, am[kk] + (o) + TO(i)))
  PAGE(10);
  TL(18);
  TL(19);
  tag = __builtin_amdgcn_readfirstlane(((const unsigned*)(smem + LDS_SEL))[5]);
  {
    int son = 0;
    auto tile = [&](int kt, int zero) __attribute__((always_inline)) {
      SLOTS(16) {
        mm(true, 0, 0, q, zero);
        if (!(q & 1)) {
          if (q < 8) fm[(q >> 1) & 1][2 + (q >> 2)] = RDM(2 + (q >> 2), (q >> 1) & 1, 0);
          else { const int r = (q - 8) >> 1; fn[1][r] = RDN(2 + (r >> 1), r & 1, 0); }
        }
        SB();
      }
      SLOTS(16) {
        mm(false, 1, 1, q, zero);
        if (!(q & 1) && q < 8) fn[0][q >> 1] = RDN(4 + (q >> 2), (q >> 1) & 1, 0);
        SB();
      }
      const int som = son >> 1;
      son = son == 65536 ? 0 : son + 32768;
      SLOTS(16) {
        mm(false, 0, 2, q, zero);
        if (!(q & 1) && q < 8) fn[1][q >> 1] = RDN(6 + (q >> 2), (q >> 1) & 1, 0);
        if (q == 9 || q == 11) { an[q == 11] = bn[q == 11] + son; asm volatile("" : "+v"(an[q == 11])); }
        if (q == 13 || q == 15) { am[q == 15] = bm[q == 15] + som; asm volatile("" : "+v"(am[q == 15])); }
        SB();
      }

      BAR_LGKM();

      SB();
      SLOTS(16) {
        mm(true, 1, 3, q, zero);
        if (!(q & 1)) {
          if (q < 8) fn[0][q >> 1] = RDN(q >> 2, (q >> 1) & 1, 0);
          else fm[(q >> 1) & 1][(q - 8) >> 2] = RDM((q - 8) >> 2, (q >> 1) & 1, 0);
        }
        SB();
      }
    };
    SB();
    tile(0, 1);
#pragma unroll 1
    for (int kt = 1; kt < G2_NKT - 3; ++kt) tile(kt, 0);
  }
  unsigned fx;
  int fo = lane == 0 ? kq * 512 : (int)0x80000000;
  {
    constexpr int NG = 12;
    static constexpr int g_t[NG] = {0, 0, 1, 1, 2, 2, 2, 1, 0, 0, 1, 2};
    static constexpr int g_q[NG] = {0, 1, 0, 1, 0, 1, 2, 2, 2, 3, 3, 3};
    constexpr int G14 = 1, G15 = 3, GP0 = 5, GP1 = 6;
    constexpr auto chp = [](int g) { return g == 0 || g_t[g] != g_t[g - 1]; };
    constexpr auto chn = [](int g) { return g + 1 < NG && g_t[g + 1] != g_t[g]; };
    static_assert(n_off(13) == 32768 && n_off(14) == 65536 && n_off(15) == 0, "");
    unsigned an2[2];
#pragma unroll
    for (int kk = 0; kk < 2; ++kk) {
      an[kk] = bn[kk]; an2[kk] = bn[kk] + 65536; am[kk] = bm[kk];
      asm volatile("" : "+v"(an[kk]), "+v"(an2[kk]), "+v"(am[kk]));
    }
    int vo;
    u32x4 pk;
    u32x2 kv;
    auto publish = [&](int d, int h) __attribute__((always_inline)) {
      if (h == 0) { vo = ml; PINV(vo); vo += (((d + kq + 1) & 3) * 4 + kq) * 4 * MSG; PINV(vo); }
      const f32x4 v = acc[2 * d + (h >> 2)][h & 3];
      pk[(h & 1) * 2] = pk2(v[0], v[1]); pk[(h & 1) * 2 + 1] = pk2(v[2], v[3]);
      if (h & 1) __builtin_amdgcn_raw_buffer_store_b128(pk, rs, vo + (h >> 1) * 1024, 0, 16);
    };
#define MSG_FLAG(d) __builtin_amdgcn_raw_buffer_store_b32(tag, rc, fo + (((d) + kq + 1) & 3) * 2048, 0, 16)
    auto rdn = [&](int g, int r) __attribute__((always_inline)) {
      const int t = g_t[g], jt = g_q[g] * 2 + (r >> 1);
      fn[g & 1][r] = *LDSP(const bf16x8, (t == 1 ? an2[r & 1] : an[r & 1]) + (t == 0 ? 32768 : 0) + jt * 2048);
    };
    auto rdm = [&](int t, int i, int kk) __attribute__((always_inline)) { fm[kk][i] = *LDSP(const bf16x8, am[kk] + m_off(13 + t) + TO(i)); };
    auto group = [&](auto GI) __attribute__((always_inline)) {
      constexpr int gi = decltype(GI)::value;
      if (gi == G14) { BAR();
        TL(47);
        SB(); }
      if (gi == G15) {
        PAGE(11);
        TL(53);
        BAR();
        TL(54);
        SB();
        kv = *(const u32x2*)(smem + LDS_SEL);
        SB();
      }
#pragma unroll
      for (int q = 0; q < 16; ++q) {
        const int n = gi * 16 + q;
        if (n == 112) {
          TL(55);
        }
        if (n == 152) {
          TL(58);
        }
        if (n == 168) { WAIT_VM(8); MSG_FLAG(0); SB(); }
        mm(chp(gi) || chn(gi), gi & 1, g_q[gi], q, 2);
        constexpr bool ni = gi + 1 < NG && (chp(gi + 1 < NG ? gi + 1 : gi) || chn(gi + 1 < NG ? gi + 1 : gi));
        auto nr = [](int r) { return ni && r >= 2 ? 5 - r : r; };
        if constexpr (chp(gi) && chn(gi)) {
          if (q < 4) rdm(g_t[gi], 2 + (q >> 1), q & 1); else if (q < 8) rdn(gi + 1, nr(q - 4)); else if (q < 12) rdm(g_t[gi + 1], (q - 8) >> 1, q & 1);
        } else if constexpr (chp(gi)) {
          if (q < 4) rdm(g_t[gi], 2 + (q >> 1), q & 1);
          else if (gi + 1 < NG && q < 12 && !(q & 1)) rdn(gi + 1, nr((q - 4) >> 1));
        } else if constexpr (chn(gi)) {
          if (!(q & 1)) { if (q < 8) rdn(gi + 1, nr(q >> 1)); else rdm(g_t[gi + 1], (q - 8) >> 2, (q >> 1) & 1); }
        } else {
          if (!(q & 1) && q < 8) rdn(gi + 1, nr(q >> 1));
        }
        if (gi == GP0 && q < 8) publish(0, q);
        if (gi == GP1 && q < 8) publish(1, q);
        if (gi == 9 && q < 8) publish(2, q);
        if (n == 33 || n == 37 || n == 41) xq_load((n - 33) >> 2);
        if (n == 72) { key[0] = __builtin_amdgcn_readfirstlane(kv.x); key[1] = __builtin_amdgcn_readfirstlane(kv.y); }
        SB();
      }
    };
#define GR4(i) group(IC<i>{}); group(IC<i + 1>{}); group(IC<i + 2>{}); group(IC<i + 3>{});
    GR4(0) GR4(4) GR4(8)
    asm volatile("s_nop 7");
    SB();
  }
  TL(50);
#pragma unroll
  for (int i = 0; i < 4; ++i) *(f32x4*)(smem + LDS_ACC + wr * 16384 + wc * 4096 + i * 1024 + lane * 16) = acc[7][i];
#pragma unroll
  for (int i = 0; i < 4; ++i) ya[i] = acc[6][i];
  asm volatile("s_waitcnt lgkmcnt(0)" ::: "memory");
  if (lane == 0) sig[0] = 2;
  SB();
  TL(61);
  WAIT_VM(5); MSG_FLAG(1); SB();
  TL(62);
  WAIT_VM(2); MSG_FLAG(2); fx = bld4_sc1(rc, po); SB();
  TL(63);
  wait_xq<4>(xq, sq);
  cv_calc();
  asm volatile("s_waitcnt vmcnt(0)" : "+v"(fx) :: "memory");
  seen = (unsigned)__ballot(fx == tag);
  }

  PAGE(6);
  if (__builtin_expect(far0 == 0 || far1 == 0, 0)) {
#pragma unroll 1
    for (int c = 0; c < 2; ++c) {
      if (c ? far1 : far0) continue;
      const int ic = (0xffff - (c ? key[1] : key[0])) & (S_ - 1);
      const float sg = rbf(1.f / (1.f + __expf(-key_score(c ? key[1] : key[0]))));
      lds_vi cnt = (lds_vi)(smem + LDS_SEL + 24 + c * 4);
      int it = 0;
      while (cnt[0] != 4 && it < SPIN_MAX * 8) { __builtin_amdgcn_s_sleep(1); ++it; }
      asm volatile("" ::: "memory");
      const float* q = (const float*)(smem + LDS_FIN + c * 2048) + (wid & 1) * 64 + (fr >> 3) * 32 + col4(fr);
      const f32x4 v = (*(const f32x4*)q + *(const f32x4*)(q + 128)) + (*(const f32x4*)(q + 256) + *(const f32x4*)(q + 384));
#pragma unroll
      for (int r = 0; r < 4; ++r)
#pragma unroll
        for (int k = 0; k < 4; ++k) {
          cv[r * 4 + k] += (xo[r] >> 11) == ic ? v[k] * sg : 0.f;
        }
    }
  }
  if (ha) {
    int it = 0;
    while (sig[0] != 2 && it < SPIN_MAX * 8) { __builtin_amdgcn_s_sleep(1); ++it; }
    asm volatile("" ::: "memory");
#pragma unroll
    for (int i = 0; i < 4; ++i) ya[i] = *(const f32x4*)(smem + LDS_ACC + (wid & 2) * 8192 + (wid & 1) * 4096 + i * 1024 + lane * 16);
  }
  u32x4 pp[3][2];
  unsigned e_lo = 0x00003f80u, e_hi = 0x3f800000u;
  asm volatile("" : "+v"(e_lo), "+v"(e_hi));
  __amdgpu_buffer_rsrc_t rs_ = rs, rc_ = rc;
  int mo[3] = {MSG_IDX(0) * 4 * MSG, MSG_IDX(1) * 4 * MSG, MSG_IDX(2) * 4 * MSG}, mv = ml + ha * 2048, mv2 = mv + 1024;
  asm volatile("" : "+s"(rs_), "+s"(rc_), "+s"(mo[0]), "+s"(mo[1]), "+s"(mo[2]));
  asm volatile("" : "+v"(mv), "+v"(mv2));
#define MSG_POLL(m) do { \
    if (!((seen >> (m)) & 1)) { \
      unsigned pv_, it_ = SPIN_MAX; \
      asm volatile("1:\n\tbuffer_load_dword %0, %3, %4, 0 offen sc1\n\ts_waitcnt vmcnt(0)\n\tv_cmp_eq_u32 vcc, %5, %0\n\ts_mov_b32 %1, vcc_lo\n\t" \
                   "s_bitcmp1_b32 %1, %6\n\ts_cbranch_scc1 2f\n\ts_sub_u32 %2, %2, 1\n\ts_cbranch_scc1 2f\n\ts_sleep 1\n\ts_branch 1b\n2:" \
                   : "=&v"(pv_), "+s"(seen), "+s"(it_) : "v"(po), "s"(rc_), "s"(tag), "i"(m) : "vcc", "scc", "memory"); \
    } \
    pp[m][0] = __builtin_amdgcn_raw_buffer_load_b128(rs_, mv, mo[m], 16); \
    pp[m][1] = __builtin_amdgcn_raw_buffer_load_b128(rs_, mv2, mo[m], 16); } while (0)
#define MSG_ADD(m) do { \
    _Pragma("unroll") for (int u = 0; u < 8; ++u) { \
      ya[u >> 1][(u & 1) * 2] = add_bf(pp[m][u >> 2][u & 3], e_lo, ya[u >> 1][(u & 1) * 2]); ya[u >> 1][(u & 1) * 2 + 1] = add_bf(pp[m][u >> 2][u & 3], e_hi, ya[u >> 1][(u & 1) * 2 + 1]); } } while (0)
  MSG_POLL(2); MSG_POLL(1); MSG_POLL(0);
  SB();
#pragma unroll
  for (int e = 0; e < 16; ++e) ya[e & 3][e >> 2] += cv[e];
  asm volatile("" : "+v"(ya[0]), "+v"(ya[1]), "+v"(ya[2]), "+v"(ya[3]));
  SB();
  MSG_ADD(2); MSG_ADD(1);
  asm volatile("" : "+v"(ya[0]), "+v"(ya[1]), "+v"(ya[2]), "+v"(ya[3]));
  SB();
  MSG_ADD(0);
  {
#pragma unroll
    for (int r = 0; r < 4; ++r) *(u32x2*)((char*)out + (unsigned)xo[r]) = (u32x2){pk2(ya[0][r], ya[1][r]), pk2(ya[2][r], ya[3][r])};
  }

  if (wid == 0) {
    const unsigned te = (unsigned)__builtin_amdgcn_s_memrealtime();
    const unsigned t0 = __builtin_amdgcn_readlane(tlv, 52);
    const __amdgpu_buffer_rsrc_t rg = mk_rsrc(wsp + (64u << 20));
    if (lane == 0) { __builtin_amdgcn_raw_buffer_store_b64((u32x2){tag, t0}, rg, g * 32, 0, 16); __builtin_amdgcn_raw_buffer_store_b64((u32x2){tag, (unsigned)__builtin_amdgcn_readlane(tlv, 1)}, rg, g * 32 + 8, 0, 16);
      __builtin_amdgcn_raw_buffer_store_b64((u32x2){tag, (unsigned)__builtin_amdgcn_readlane(tlv, 52)}, rg, g * 32 + 16, 0, 16); }
    if (g == 37) {
      int mn0, mx[2], sum[2];
      for (int it = 0; it < 100000; ++it) {
        unsigned bad = 0; mn0 = 0x7fffffff; mx[0] = mx[1] = -0x7fffffff; sum[0] = sum[1] = 0;
        for (int k = 0; k < 4; ++k) {
          const u32x4 q = __builtin_amdgcn_raw_buffer_load_b128(rg, (k * 64 + lane) * 32, 0, 16);
          const u32x2 q2 = __builtin_amdgcn_raw_buffer_load_b64(rg, (k * 64 + lane) * 32 + 16, 0, 16);
          bad |= (q.x ^ tag) | (q.z ^ tag) | (q2.x ^ tag);
          mn0 = min(mn0, (int)(q.y - t0)); mx[0] = max(mx[0], (int)(q.w - t0)); sum[0] += (int)(q.w - t0); mx[1] = max(mx[1], (int)(q2.y - t0)); sum[1] += (int)(q2.y - t0);
        }
        if (__all(bad == 0)) break;
        asm volatile("" ::: "memory");
      }
      for (int o = 32; o; o >>= 1) { mn0 = min(mn0, __shfl_xor(mn0, o, 64)); for (int k = 0; k < 2; ++k) { mx[k] = max(mx[k], __shfl_xor(mx[k], o, 64)); sum[k] += __shfl_xor(sum[k], o, 64); } }
      int code = 0;
      { int d = sum[0] / 256 - mn0; d = (d - 150) / 10; code = code * 16 + min(max(d, 0), 15); }
      { int d = sum[1] / 256 - mn0; d = (d - 0) / 10; code = code * 16 + min(max(d, 0), 15); }
      const unsigned tend = __builtin_amdgcn_readfirstlane(t0 + mn0 + 4000 + code * 400);
      for (int it = 0; it < (1 << 17) && (int)((unsigned)__builtin_amdgcn_s_memrealtime() - tend) < 0; ++it) __builtin_amdgcn_s_sleep(1);
    }
  }

}

extern "C" void kernel_launch(void* const* d_in, const int* in_sizes, int n_in, void* d_out, int out_size,
                              void* d_ws, size_t ws_size, hipStream_t stream) {
  const u16* x = (const u16*)d_in[0];
  const u16* ws_re = (const u16*)d_in[1];
  const u16* ws_im = (const u16*)d_in[2];
  const u16* wd_re = (const u16*)d_in[3];
  const u16* wd_im = (const u16*)d_in[4];
  const u16* w_r = (const u16*)d_in[6];
  const u16* w_gate = (const u16*)d_in[7];
  const u16* w_up = (const u16*)d_in[8];
  const u16* w_down = (const u16*)d_in[9];
  k_main<<<256, 512, 0, stream>>>(x, wd_re, wd_im, w_r, (char*)d_ws, ws_re, ws_im, w_gate, w_up, w_down, (u16*)d_out);
}
```

```cpp
#include <hip/hip_runtime.h>
#include <hip/hip_bf16.h>
#include <stdint.h>
#include <type_traits>

extern "C" __device__ unsigned long long llvm_dispatch_id(void) __asm("llvm.amdgcn.dispatch.id");
extern "C" __device__ float llvm_fmul_legacy(float, float) __asm("llvm.amdgcn.fmul.legacy");
typedef unsigned short u16;
typedef unsigned long long u64;
typedef __attribute__((ext_vector_type(8))) short bf16x8;
typedef __attribute__((ext_vector_type(4))) unsigned u32x4;
typedef __attribute__((ext_vector_type(2))) unsigned u32x2;
typedef __attribute__((ext_vector_type(4))) float f32x4;
typedef __attribute__((ext_vector_type(2))) float f32x2;
typedef __attribute__((ext_vector_type(2))) __bf16 bf16x2;
typedef __attribute__((address_space(3))) void* lds_vp;
typedef volatile __attribute__((address_space(3))) int* lds_vi;

constexpr int S_ = 2048, D_ = 1024, F_ = 3584;
template <int N> using IC = std::integral_constant<int, N>;

static __device__ __forceinline__ float bf2f(u16 u) { return __uint_as_float((unsigned)u << 16); }
static __device__ __forceinline__ u16 f2bf(float f) {
  __hip_bfloat16 b = __float2bfloat16(f);
  return *reinterpret_cast<u16*>(&b);
}
static __device__ __forceinline__ float rbf(float f) { return bf2f(f2bf(f)); }
static __device__ __forceinline__ float lo_bf(unsigned u) { return __uint_as_float(u << 16); }
static __device__ __forceinline__ float hi_bf(unsigned u) { return __uint_as_float(u & 0xffff0000u); }
static __device__ __forceinline__ unsigned pk2(float a, float b) {
  const bf16x2 v = __builtin_convertvector((f32x2){a, b}, bf16x2);
  return __builtin_bit_cast(unsigned, v);
}
static __device__ __forceinline__ float add_bf(unsigned u, unsigned e, float c) {
  return __builtin_amdgcn_fdot2_f32_bf16(__builtin_bit_cast(bf16x2, u), __builtin_bit_cast(bf16x2, e), c, false);
}
static __device__ __forceinline__ float ln_elem(float x, float mb, float sd, float ri) {
  const float d = rbf(x - mb);
  const float q0 = d * ri;
  return rbf(__builtin_fmaf(__builtin_fmaf(-q0, sd, d), ri, q0));
}
template <int CTRL> static __device__ __forceinline__ float dpp_f(float v) {
  return __int_as_float(__builtin_amdgcn_update_dpp(0, __float_as_int(v), CTRL, 0xf, 0xf, true));
}
template <int CTRL> static __device__ __forceinline__ unsigned dpp_u(unsigned v) {
  return (unsigned)__builtin_amdgcn_update_dpp(0, (int)v, CTRL, 0xf, 0xf, true);
}
static __device__ __forceinline__ float row_sum(float v) {
  v += dpp_f<0xB1>(v);
  v += dpp_f<0x4E>(v);
  v += dpp_f<0x141>(v);
  v += dpp_f<0x140>(v);
  return v;
}
static __device__ __forceinline__ float fold32(float a, float b) {
  const auto r = __builtin_amdgcn_permlane32_swap(__float_as_uint(a), __float_as_uint(b), false, false);
  return __uint_as_float(r[0]) + __uint_as_float(r[1]);
}
static __device__ __forceinline__ float fold16(float a, float b) {
  const auto r = __builtin_amdgcn_permlane16_swap(__float_as_uint(a), __float_as_uint(b), false, false);
  return __uint_as_float(r[0]) + __uint_as_float(r[1]);
}
static __device__ __forceinline__ float rdl(float v, int l) { return __int_as_float(__builtin_amdgcn_readlane(__float_as_int(v), l)); }

#define ST_JB 9
#define LNS 2
#define WSN 10
#define FSM 0xa2
#define GV_JB 7
#define DG 11
#define AB_STRIDE 32
#define SPIN_MAX (1 << 17)
#define VA0 5
#define VAN 4
#define SP0 3
#define SP1 7
#define SP1E 11
#define SPS 0x000cccccdddd00
#define DVS 0x00000400300000
#define GVS 0x00000022000000
constexpr int dig(unsigned long long t, int q) { return q < 0 ? 0 : (int)((t >> (4 * q)) & 15); }
constexpr int acc_dig(unsigned long long t, int q) { int a = 0; for (int i = 0; i < q; ++i) a += dig(t, i); return a; }

static __device__ __forceinline__ unsigned lds_addr(const void* p) { return (unsigned)(size_t)(lds_vp)p; }

constexpr int MSG = 4096;
constexpr size_t WS_ABT   = 0;
constexpr size_t WS_SLAB  = WS_ABT + (size_t)D_ * 4096 * 2;
constexpr size_t WS_GU    = WS_SLAB + (size_t)64 * 64 * MSG;
constexpr size_t WS_DN    = WS_GU + (size_t)8 * F_ * 16;
constexpr size_t WS_FLAGS = WS_DN + (size_t)2 * 8 * 32 * 64 * 8;
constexpr int FL_X = 0;
constexpr int FL_AB = 64 * 64 * 32;
constexpr int FL_ST = FL_AB + 2048 * AB_STRIDE;
constexpr int FL_CE = FL_ST + 4096;
constexpr int FL_KEY = FL_CE + 4096;
constexpr int FL_EPOCH = FL_KEY + 4096;
constexpr int WS_MB = (int)((WS_FLAGS + (FL_EPOCH + 1) * 4 + 1048575) >> 20);

#define WAIT_VM(n) asm volatile("s_waitcnt vmcnt(" #n ")" ::: "memory")
template <int N> static __device__ __forceinline__ void wait_vm() { asm volatile("s_waitcnt vmcnt(%0)" :: "i"(N) : "memory"); }
#define BAR_LGKM() asm volatile("s_waitcnt lgkmcnt(0)\n\ts_barrier" ::: "memory")
#define BAR() asm volatile("s_barrier" ::: "memory")
#define SB() __builtin_amdgcn_sched_barrier(0)
#define PAGE(n) asm volatile("s_branch 2f\n\t.p2align 12\nk_pg" #n ":\n\ts_setpc_b64 s[42:43]\n2:")
#define CALL_PG(n) asm volatile("s_getpc_b64 s[40:41]\n3:\n\ts_mov_b32 s42, k_pg" #n "-3b\n\ts_ashr_i32 s43, s42, 31\n\ts_add_u32 s40, s40, s42\n\ts_addc_u32 s41, s41, s43\n\t" \
                                "s_swappc_b64 s[42:43], s[40:41]" ::: "s40", "s41", "s42", "s43", "scc", "memory")

static __device__ __forceinline__ __amdgpu_buffer_rsrc_t mk_rsrc(const void* p) {
  return __builtin_amdgcn_make_buffer_rsrc((void*)p, 0, 0x7fffffff, 0x00020000);
}
#define DMA_(rsrc, voff, mbase, MI, sbase, SI, pol) do { unsigned _t; \
    asm volatile("s_add_i32 m0, %2, %3\n\ts_add_i32 %0, %5, %6\n\tbuffer_load_dwordx4 %1, %4, %0 offen" pol " lds" \
                 : "=&s"(_t) : "v"(voff), "s"(mbase), "i"(MI), "s"(rsrc), "s"(sbase), "i"(SI) : "memory", "scc"); } while (0)
#define DMA_I(rsrc, voff, mbase, MI, SI) \
    asm volatile("s_add_i32 m0, %1, %2\n\ts_nop 0\n\tbuffer_load_dwordx4 %0, %3, 0 offen offset:%4 lds" \
                 :: "v"(voff), "s"(mbase), "i"((MI) - (SI)), "s"(rsrc), "i"(SI) : "memory", "scc")
#define DMA_S(rsrc, voff, mbase, MI, soff) \
    asm volatile("s_add_i32 m0, %1, %2\n\ts_nop 0\n\tbuffer_load_dwordx4 %0, %3, %4 offen lds" \
                 :: "v"(voff), "s"(mbase), "i"(MI), "s"(rsrc), "s"(soff) : "memory", "scc")
#define TOUCH(rsrc, voff, mbase, MI, soff) \
    asm volatile("s_add_i32 m0, %1, %2\n\ts_nop 0\n\tbuffer_load_dword %0, %3, %4 offen lds" \
                 :: "v"(voff), "s"(mbase), "i"(MI), "s"(rsrc), "s"(soff) : "memory", "scc")
template <int OFF> static __device__ __forceinline__ u32x4 ld16_s(const void* p, int voff) {
  u32x4 v;
  asm volatile("global_load_dwordx4 %0, %1, %2 offset:%3" : "=v"(v) : "v"(voff), "s"(p), "i"(OFF) : "memory");
  return v;
}
template <int OFF> static __device__ __forceinline__ void sld(unsigned& sink, const void* p, int soff) {
  asm volatile("s_load_dword %0, %1, %2 offset:%3" : "+s"(sink) : "s"(p), "s"(soff), "i"(OFF) : "memory");
}
static __device__ __forceinline__ unsigned gld4_s(const void* p, unsigned voff) {
  unsigned v;
  asm volatile("global_load_dword %0, %1, %2" : "=v"(v) : "v"(voff), "s"(p) : "memory");
  return v;
}
static __device__ __forceinline__ u32x2 bld8_sc1(__amdgpu_buffer_rsrc_t r, unsigned voff) {
  u32x2 v;
  asm volatile("buffer_load_dwordx2 %0, %1, %2, 0 offen sc1" : "=v"(v) : "v"(voff), "s"(r) : "memory");
  return v;
}
template <int N> static __device__ __forceinline__ void wait_xs(unsigned& a, u32x2& b) { asm volatile("s_waitcnt vmcnt(%2)" : "+v"(a), "+v"(b) : "i"(N) : "memory"); }
template <int N> static __device__ __forceinline__ void wait_xq(u32x4 (&a)[2], u32x2& b) { asm volatile("s_waitcnt vmcnt(%3)" : "+v"(a[0]), "+v"(a[1]), "+v"(b) : "i"(N) : "memory"); }
static __device__ __forceinline__ u32x2 ld8_s_sc1(const void* p, int voff) {
  u32x2 v;
  asm volatile("global_load_dwordx2 %0, %1, %2 sc1" : "=v"(v) : "v"(voff), "s"(p) : "memory");
  return v;
}
static __device__ __forceinline__ void lgkm0(unsigned& sink) { asm volatile("s_waitcnt lgkmcnt(0)" : "+s"(sink) :: "memory"); }
template <typename T> static __device__ __forceinline__ void pin4(T& a, T& b, T& c, T& d) { asm volatile("" : "+s"(a), "+s"(b), "+s"(c), "+s"(d)); }
template <typename T, typename U> static __device__ __forceinline__ void pins(T& a, U& b) { asm volatile("" : "+s"(a), "+s"(b)); }
static __device__ __forceinline__ void launder(int& v, int& a, int& b) { asm volatile("" : "+v"(v)); asm volatile("" : "+s"(a), "+s"(b)); }
static __device__ __forceinline__ unsigned bld4_sc1(__amdgpu_buffer_rsrc_t r, int voff) {
  unsigned v;
  asm volatile("buffer_load_dword %0, %1, %2, 0 offen sc1" : "=v"(v) : "v"(voff), "s"(r) : "memory");
  return v;
}
static __device__ __forceinline__ u32x4 bld16_nt(__amdgpu_buffer_rsrc_t r, int voff, int soff) {
  u32x4 v;
  asm volatile("buffer_load_dwordx4 %0, %1, %2, %3 offen nt" : "=v"(v) : "v"(voff), "s"(r), "s"(soff) : "memory");
  return v;
}
static __device__ __forceinline__ void bld16_nt_io(u32x4& v, __amdgpu_buffer_rsrc_t r, int voff, int soff) {
  asm volatile("buffer_load_dwordx4 %0, %1, %2, %3 offen nt" : "+v"(v) : "v"(voff), "s"(r), "s"(soff) : "memory");
}
static __device__ __forceinline__ u32x4 bld16_sc1(__amdgpu_buffer_rsrc_t r, int voff, int soff) {
  u32x4 v;
  asm volatile("buffer_load_dwordx4 %0, %1, %2, %3 offen sc1" : "=v"(v) : "v"(voff), "s"(r), "s"(soff) : "memory");
  return v;
}
static __device__ __forceinline__ u32x2 granule_wait(const u64* p, unsigned tag) {
  u64 v = 0;
#pragma unroll 1
  for (int it = 0; it < SPIN_MAX; ++it) {
    v = __hip_atomic_load(p, __ATOMIC_RELAXED, __HIP_MEMORY_SCOPE_AGENT);
    if (__all((unsigned)(v >> 32) == tag)) break;
    __builtin_amdgcn_s_sleep(2);
  }
  return (u32x2){(unsigned)v, (unsigned)(v >> 32)};
}
static __device__ __forceinline__ float key_score(unsigned key) {
  const unsigned u = key >> 16;
  return bf2f((u16)((u & 0x8000u) ? (u & 0x7fffu) : (~u & 0xffffu)));
}
static __device__ __forceinline__ unsigned med3u(unsigned a, unsigned b, unsigned c) { return max(min(a, b), min(max(a, b), c)); }
static __device__ __forceinline__ void merge2(unsigned& a1, unsigned& a2, unsigned b1, unsigned b2) {
  const unsigned lo = min(a1, b1);
  a1 = max(a1, b1);
  a2 = max(lo, max(a2, b2));
}
static __device__ __forceinline__ float sqdev16(const u32x4 (&v)[2], float mb) {
  f32x2 q2 = {0.f, 0.f};
#pragma unroll
  for (int i = 0; i < 8; ++i) {
    const f32x2 e = (f32x2){lo_bf(v[i >> 2][i & 3]), hi_bf(v[i >> 2][i & 3])} - mb;
    q2 += e * e;
  }
  return q2.x + q2.y;
}

constexpr int lrw(int r) { return (r & ~6) | ((r & 2) << 1) | ((r & 4) >> 1); }
constexpr int swz(int r) { return ((r >> 1) & 4) | (r & 2); }
static __device__ __forceinline__ int sym_dn(int p) { return (p ^ (p >> 1)) & 1; }
static __device__ __forceinline__ int sym_hi(int n, int w, int st, int b, int z, int p) {
  return ((p >> 1) * (n / 2 * st) + b * (w * st)) ^ (-sym_dn(p) & (((n - 1) ^ (z * (n / 2))) * st));
}
static __device__ __forceinline__ int sym_lo(int w, int st, int p, int c) { return ((c - sym_dn(p)) * st) & ((w - 1) * st); }
static __device__ __forceinline__ int sym_row(int n, int w, int st, int b, int z, int p, int c) { return sym_lo(w, st, p, c) ^ sym_hi(n, w, st, b, z, p); }
constexpr int G1_NST = 4, G1_NKT = 16, G1_STAGE = 32768;
constexpr int G2_NKT = 16, G2_MRING = 98304;
constexpr int n_off(int k) { return (k % 3) * 32768; }
constexpr int m_off(int k) { return ((k + 2) % 3) * 16384; }
constexpr int k_ord(int k) { return k < 2 ? k : (k < 9 ? 2 * (k - 1) : 2 * (k - 8) + 1); }
constexpr int LDS_DUMMY = G2_MRING + 49152;
constexpr int LDS_GU = LDS_DUMMY + 4096;
constexpr int LDS_FIN = LDS_GU + 4096;
constexpr int LDS_MB = LDS_FIN + 4096;
constexpr int LDS_SEL = LDS_MB + 1536;
constexpr int LDS_SIG = LDS_SEL + 64;
constexpr int LDS_KEY = LDS_SIG + 32;
constexpr int LDS_BYTES = LDS_KEY + 128;
constexpr int LDS_ACC = n_off(G2_NKT - 3);

constexpr int sp_tile(int q) { return q < VA0 ? q : q + VAN < 16 ? q + VAN : -1; }
constexpr int dn_cnt(int q) { return dig(DVS, q); }
constexpr int dn_first(int q) { return acc_dig(DVS, q); }

static __device__ __forceinline__ void code_touch(unsigned& sink, unsigned off) {
  asm volatile("s_getpc_b64 s[40:41]\n\tk_main_rt:\n\ts_min_u32 %1, %1, k_main_end-k_main_rt-196\n\t"
               "s_load_dword %0, s[40:41], %1 offset:0\n\ts_load_dword %0, s[40:41], %1 offset:64\n\ts_load_dword %0, s[40:41], %1 offset:128\n\ts_load_dword %0, s[40:41], %1 offset:192"
               : "+s"(sink), "+s"(off) :: "s40", "s41", "scc", "memory");
}
static __device__ __forceinline__ void touch1(__amdgpu_buffer_rsrc_t r, int voff, unsigned mb, int soff) { TOUCH(r, voff, mb, LDS_DUMMY, soff); }
typedef const __attribute__((address_space(1))) u16* gptr;
typedef __attribute__((ext_vector_type(8))) u64 u64x8;
typedef __attribute__((ext_vector_type(2))) u64 u64x2;
extern "C" __global__ __launch_bounds__(512, 2) void k_main(const u16* x, const u16* wd_re, const u16* wd_im, const u16* w_r,
                                                 char* wsp, const u16* ws_re, const u16* ws_im,
                                                 const u16* w_gate, const u16* w_up, const u16* w_down, u16* out) {
  __shared__ __attribute__((aligned(16))) char smem[LDS_BYTES];
  u64x8 ka;
  u64x2 kb_;
  u64 kc;
  asm volatile("s_load_dwordx16 %0, %3, 0\n\ts_load_dwordx4 %1, %3, 64\n\ts_load_dwordx2 %2, %3, 80"
               : "=&s"(ka), "=&s"(kb_), "=&s"(kc) : "s"(__builtin_amdgcn_kernarg_segment_ptr()) : "memory");
  const int tid = threadIdx.x, lane = tid & 63;
  const int wid = __builtin_amdgcn_readfirstlane(tid >> 6);
  const int g = blockIdx.x, xcd = ((~g & 1) << 2) | ((g >> 1) & 3), j = g >> 3;
  if (wid < 4) {
    unsigned cw, co = (j * 4 + wid) * 448;
    asm volatile(".subsection 1\nk_main_end:\n.subsection 0\n\ts_min_u32 %1, %1, k_main_end-k_main-480\n\ts_getpc_b64 s[40:41]\n\ts_add_u32 s40, s40, %1\n\ts_addc_u32 s41, s41, 0\n\t"
                 "s_load_dword %0, s[40:41], 0\n\ts_load_dword %0, s[40:41], 64\n\ts_load_dword %0, s[40:41], 128\n\t"
                 "s_load_dword %0, s[40:41], 192\n\ts_load_dword %0, s[40:41], 256\n\ts_load_dword %0, s[40:41], 320\n\ts_load_dword %0, s[40:41], 384\n\ts_waitcnt lgkmcnt(0)"
                 : "=&s"(cw), "+s"(co) :: "s40", "s41", "scc", "memory");
  }
  const int pw = wid - 4;
  const int row0 = (wid & 3) * 8 + lrw(lane >> 3);
  const int cm = ((lane & 7) ^ swz(row0)) * 16;
  int vm = ((row0 & 15) * 2 + (row0 >> 4)) * (D_ * 2) + cm;
  asm volatile("" : "+v"(vm));
  const int kq = xcd >> 1;
  const int tt = (xcd & 1) * 4 + (j >> 3);
  const int et = j & 7;
  const int z = kq >> 1, st = (kq & 1) * 8 + tt;
  const int lrow = (kq & 1) * 1024 + (xcd & 1) * 512 + z * 256 + j * 8 + pw * 2;
  const int drow = ((kq & 1) * 2 + (xcd & 1)) * 256 + j * 8 + pw * 2;
  unsigned o_m = st * 128 * D_ * 2, o_l = lrow * D_ * 2, o_d = drow * D_ * 2, mb0 = lds_addr(smem) + pw * 1024;
  int l16 = lane * 16;
  int vn[4];
#pragma unroll
  for (int q = 0; q < 4; ++q) vn[q] = sym_lo(32, D_ * 2, (row0 >> 2) & 3, q * 8 + (row0 >> 4) * 4 + (row0 & 3)) ^ (sym_hi(D_, 32, D_ * 2, et, z, (row0 >> 2) & 3) | cm);
  asm volatile("" : "+s"(o_m), "+s"(o_l), "+s"(o_d), "+s"(mb0));
  asm volatile("" : "+v"(l16), "+v"(vn[0]), "+v"(vn[1]), "+v"(vn[2]), "+v"(vn[3]));
  asm volatile("s_waitcnt lgkmcnt(0)" : "+s"(ka), "+s"(kb_), "+s"(kc) :: "memory");
  x = (const u16*)(gptr)ka[0]; wd_re = (const u16*)(gptr)ka[1]; wd_im = (const u16*)(gptr)ka[2]; w_r = (const u16*)(gptr)ka[3];
  wsp = (char*)(gptr)ka[4]; ws_re = (const u16*)(gptr)ka[5]; ws_im = (const u16*)(gptr)ka[6]; w_gate = (const u16*)(gptr)ka[7];
  w_up = (const u16*)(gptr)kb_[0]; w_down = (const u16*)(gptr)kb_[1]; out = (u16*)(gptr)kc;
  int* flags = (int*)(wsp + WS_FLAGS);
  u64* stat = (u64*)(flags + FL_ST);
  const u16* wdz = z ? wd_im : wd_re;
  const __amdgpu_buffer_rsrc_t rm = mk_rsrc(x);
  const __amdgpu_buffer_rsrc_t rn = mk_rsrc(wdz);
#define G1_X(kt, so, q) DMA_(rm, vm, mb0, (so) + (q) * 4096, o_m, (kt) * 128 + (((q) & 1) * 64 + ((q) >> 1) * 32) * D_ * 2, "")
#define G1_W(kt, so, q) DMA_I(rn, vn[q], mb0, (so) + 16384 + (q) * 4096, (kt) * 128)
#define G1_STAGE_TILE(kt, so, W) do { SB(); G1_X(kt, so, 0); W(0); G1_X(kt, so, 1); W(1); G1_X(kt, so, 2); W(2); G1_X(kt, so, 3); W(3); \
    G1_W(kt, so, 0); W(4); G1_W(kt, so, 1); W(5); G1_W(kt, so, 2); W(6); G1_W(kt, so, 3); W(7); SB(); } while (0)
#define G1_STAGE_A(kt, so, W) do { SB(); G1_X(kt, so, 0); W(0); G1_X(kt, so, 2); W(1); \
    G1_W(kt, so, 0); W(2); G1_W(kt, so, 1); W(3); G1_W(kt, so, 2); W(4); G1_W(kt, so, 3); W(5); W(6); W(7); SB(); } while (0)
#define G1_STAGE_B(kt, so) do { SB(); G1_X(kt, so, 1); G1_X(kt, so, 3); SB(); } while (0)
#define NOW(i)
  const int fr = lane & 15, fq = lane >> 4;
  const int ha = wid >> 2;
  int trow, xo[4], xa[3], po, ml;
  const char *prs, *prc;
  auto col4 = [](int f) __attribute__((always_inline)) { return ((f & 2) << 3 | (f & 4) << 1 | (f & 1) << 2) ^ (-((f >> 3) & 1) & 28); };
  auto seg0 = [&](int hf, int dn) __attribute__((always_inline)) { return (hf * 512 + et * 32) ^ (-dn & (D_ - 32)); };
  auto epi_setup = [&](int part) __attribute__((always_inline)) {
    int t_ = tt, l_ = lane;
    asm volatile("" : "+s"(t_));
    asm volatile("" : "+v"(l_));
    const int ts = ((wid >> 1) & 1) * 8 + kq * 2 + ha;
    if (part == 0) { xo[3] = (seg0(wid & 1, (l_ >> 3) & 1) + col4(l_)) * 2; asm volatile("" : "+v"(xo[3])); }
    if (part == 1) {
      xo[3] |= sym_hi(S_, 64, D_ * 2, t_, z, l_ >> 4);
      ml = (l_ * 16) ^ (-sym_dn(l_ >> 4) & (z * 768));
      asm volatile("" : "+v"(xo[3]), "+v"(ml));
    }
    if (part == 2) {
      const int xh = xo[3];
#pragma unroll
      for (int r = 0; r < 4; ++r) xo[r] = sym_lo(64, D_ * 2, l_ >> 4, ts * 4 + r) ^ xh;
      const bool hi = xo[0] & 8;
      xa[0] = (hi ? xo[2] : xo[0]) & ~15; xa[1] = (hi ? xo[3] : xo[1]) & ~15;
      xa[2] = (sym_lo(64, D_ * 2, l_ >> 4, ts * 4 + (l_ & 3)) ^ xh) >> 11 << 3;
      asm volatile("" : "+v"(xo[0]), "+v"(xo[1]), "+v"(xo[2]), "+v"(xo[3]), "+v"(xa[0]), "+v"(xa[1]), "+v"(xa[2]));
    }
    if (part == 3) {
      trow = t_ * 16 + ts;
      prs = wsp + WS_SLAB + (size_t)((t_ * 8 + et) * 64 + (wid & 3)) * MSG;
      prc = (const char*)(flags + FL_X + ((t_ * 8 + et) * 64 + (wid & 3)) * 32);
      asm volatile("" : "+s"(trow), "+s"(prs), "+s"(prc));
      po = l_ < 3 ? (kq * 4 + ((kq + 1 + l_) & 3)) * 512 : (int)0x80000000;
      asm volatile("" : "+v"(po));
    }
  };
  auto tok_tile = [](unsigned key) __attribute__((always_inline)) {
    const unsigned h = (0xffff - key) & 1023, m = -(h >> 9);
    return (((h ^ m) & 0x1c0) | (((h ^ m) - m) & 63)) >> 2;
  };
#define rs mk_rsrc(prs)
#define rc mk_rsrc(prc)
  lds_vi sig = (lds_vi)(smem + LDS_SIG + (wid & 3) * 4);
  unsigned tag;
  u32x4 xq[2];
  u32x2 sq;
  f32x4 ya[4];
  unsigned key[2];
  float cv[16];
  unsigned far0, far1;
  auto xq_load = [&](int n) __attribute__((always_inline)) {
    if (n < 2) xq[n] = ld16_s<0>(x, xa[n]); else sq = ld8_s_sc1(stat, xa[2]);
  };
#define PINV(a) asm volatile("" : "+v"(a))
  auto far_calc = [&]() __attribute__((always_inline)) {
    far0 = tok_tile(key[0]) ^ trow; far1 = tok_tile(key[1]) ^ trow;
    asm volatile("" : "+s"(far0), "+s"(far1));
  };
  auto cv_calc = [&]() __attribute__((always_inline)) {
    if (__builtin_expect(!__all(sq.y == tag), 0)) sq = granule_wait(stat + (xa[2] >> 3), tag);
    const bool hi = xo[0] & 8;
    const float mb_ = lo_bf(sq.x), ri_ = __builtin_amdgcn_rcpf(hi_bf(sq.x));
#pragma unroll
    for (int r = 0; r < 4; ++r) {
      const float mb = r == 0 ? dpp_f<0x00>(mb_) : r == 1 ? dpp_f<0x55>(mb_) : r == 2 ? dpp_f<0xAA>(mb_) : dpp_f<0xFF>(mb_);
      const float ri = r == 0 ? dpp_f<0x00>(ri_) : r == 1 ? dpp_f<0x55>(ri_) : r == 2 ? dpp_f<0xAA>(ri_) : dpp_f<0xFF>(ri_);
#pragma unroll
      for (int u = 0; u < 2; ++u) {
        const unsigned own = xq[r & 1][(r & 2) + u], oth = dpp_u<0xB1>(xq[r & 1][2 - (r & 2) + u]);
        const unsigned w = hi == (r >= 2) ? own : oth;
        cv[r * 4 + 2 * u] = __builtin_fmaf(lo_bf(w) - mb, ri, lo_bf(w));
        cv[r * 4 + 2 * u + 1] = __builtin_fmaf(hi_bf(w) - mb, ri, hi_bf(w));
        PINV(cv[r * 4 + 2 * u]); PINV(cv[r * 4 + 2 * u + 1]);
      }
    }
  };
#define MSG_IDX(m) (kq * 4 + ((kq + 1 + (m)) & 3))
  unsigned seen = 0;

  if (__builtin_expect(wid >= 4, 1)) {
    __builtin_amdgcn_s_setprio(3);
    G1_STAGE_TILE(0, 0, NOW);
    constexpr int O_ST = (FL_ST - FL_AB) * 4, O_CE = (FL_CE - FL_AB) * 4, O_KEY = (FL_KEY - FL_AB) * 4, O_EP = (FL_EPOCH - FL_AB) * 4;
    const __amdgpu_buffer_rsrc_t rf = mk_rsrc(wsp + WS_FLAGS + FL_AB * 4);
    u32x4 lv[2], lv2[2], lw[2], ld[2], ld2[2];
    unsigned epv;
    {
      const char* xrow = (const char*)x + o_l;
      const char* wdrow = (const char*)wdz + o_d;
      lv[0] = ld16_s<0>(xrow, l16); lv[1] = ld16_s<1024>(xrow, l16);
      lv2[0] = ld16_s<2048>(xrow, l16); lv2[1] = ld16_s<3072>(xrow, l16);
      ld[0] = ld16_s<0>(wdrow, l16); ld[1] = ld16_s<1024>(wdrow, l16);
      ld2[0] = ld16_s<2048>(wdrow, l16); ld2[1] = ld16_s<3072>(wdrow, l16);
      lw[0] = ld16_s<0>(w_r, l16); lw[1] = ld16_s<1024>(w_r, l16);
      asm volatile("buffer_load_dword %0, off, %1, %2 sc1" : "=v"(epv) : "s"(rf), "s"(O_EP) : "memory");
    }
    SB();
    const unsigned long long did = llvm_dispatch_id();
    const unsigned dmix = (unsigned)did * 0x85EBCA6Bu + (unsigned)(did >> 32) * 0xC2B2AE35u + 0x27D4EB2Fu;
    const bool moe = et != 7;
    unsigned epoch;
    const int ci = lane & 15, rq = lane >> 4, tk = (lane >> 3) & 1;
    int mat, cg, dq, d0;
    u32x4 wv[16];
    const u16 *pgu, *pn2;
    const char *pm2, *spb;
#define rw __builtin_amdgcn_make_buffer_rsrc((void*)pgu, 0, moe ? 0x7fffffff : 0, 0x00020000)
#define rn2 mk_rsrc(pn2)
#define rm2 mk_rsrc(pm2)
    int vw, vn2[8], vm2;
    auto k_blk = [&](int k) __attribute__((always_inline)) { return tt ^ (k_ord(k) >> 1); };
#define KROT(kk_) (k_blk(kk_) * 256 + (k_ord(kk_) & 1) * 128)
    int tv2;
    unsigned sd = 0;
    auto spf = [&](auto N) __attribute__((always_inline)) {
      constexpr int n = decltype(N)::value;
      if constexpr (n >= 0 && sp_tile(n >> 4) >= 0) sld<((n >> 1) & 7) * 4096 + (n & 1) * 64>(sd, spb, KROT(sp_tile(n >> 4)));
    };
    unsigned mq0, mq1, mq2, mq3;
    const int cg8 = et ^ ((et & 1) << 1), rgb = (g & 7) * 4 + (j >> 3);
    const __amdgpu_buffer_rsrc_t rdn = mk_rsrc(w_down);
    const __amdgpu_buffer_rsrc_t rgu = mk_rsrc(wsp + WS_GU);
    int vdn, vgu;
    u32x4 dv[7], gv[4];
    int abo, abq[3];
    unsigned lma;
    unsigned sel[2][4];
    float g0, g1;
    u32x4 kk[4];
    unsigned k1 = 0, k2 = 0;
    const int ko_ = O_KEY + pw * 4096;
#define MM(a, b, c) __builtin_amdgcn_mfma_f32_16x16x32_bf16(__builtin_bit_cast(bf16x8, a), __builtin_bit_cast(bf16x8, b), c, 0, 0, 0)
#define PIN1(a) asm volatile("" : "+v"(a))
#define PIN2(a, b) asm volatile("" : "+v"(a), "+v"(b))
#define PIN3(a, b, c) asm volatile("" : "+v"(a), "+v"(b), "+v"(c))
    {
      int gro, go;
      u32x2 gr;
      u32x4 oh[5];
      f32x4 sa4, gw[2], gx[2];
      float su, gs, ta, tb, sx, mbv;
      unsigned dk[4], kl;
      const int hb = lane >> 5;
#pragma unroll
      for (int l = 0; l < WSN; ++l) asm volatile("" : "=v"(wv[l]));
      auto diag = [&](const f32x4& m) __attribute__((always_inline)) {
        return __uint_as_float((__float_as_uint(m[0]) & dk[0]) | (__float_as_uint(m[1]) & dk[1]) | (__float_as_uint(m[2]) & dk[2]) | (__float_as_uint(m[3]) & dk[3]));
      };
      auto setup = [&](auto JB, auto GP) __attribute__((always_inline)) {
        constexpr int jb = decltype(JB)::value, gp = decltype(GP)::value;
        SB();
        {
          int ln = lane, jj = j, p_ = pw;
          launder(ln, jj, p_);
          const int c_ = ln & 15, r_ = ln >> 4, e_ = jj & 7, t_ = (xcd & 1) * 4 + (jj >> 3);
          if constexpr (jb == 1 && gp == 0) {
            pn2 = ((kq >> 1) ? ws_im : ws_re) + (kq & 1) * 1024;
            spb = (const char*)(pn2 + (sym_row(S_, 64, 1, t_, 0, e_ >> 1, 0) + (e_ & 1) * 32 + p_ * 8) * S_);
            pins(pn2, spb);
          }
          if constexpr (jb == 1 && gp >= 1 && gp < 6) {
            const unsigned o = c_ == gp - 1 ? 0x3f803f80u : 0u;
            oh[gp - 1] = (u32x4){o, o, o, o};
            PIN1(oh[gp - 1]);
          }
          if constexpr (jb == 1 && gp == 6) {
            const int rk = moe ? e_ * 8 + (jj >> 3) * 2 + (xcd >> 2) : 0, r3 = ((rk - 14) * 43) >> 7, rr = rk - 14 - r3 * 3;
            const int c56 = rk < 14 ? rk * 4 + 1 : r3 * 4 + rr + ((rr + 1) >> 1);
            mat = (c56 + 36) >> 6; cg = c56 - mat * 28; dq = xcd & 3; d0 = dq * 256 + p_ * 64;
            pin4(mat, cg, dq, d0);
          }
          if constexpr (jb == 2 && gp == 0) {
            pgu = mat ? w_up : w_gate;
            pm2 = wsp + WS_ABT + (kq * 8 + e_) * 262144;
            pins(pgu, pm2);
            vw = ((d0 + r_ * 16) * F_ + cg * 128 + c_ * 8) * 2;
            PIN1(vw);
          }
          if constexpr (jb == 2 && (gp == 1 || gp == 2)) {
            dk[gp * 2 - 2] = c_ == r_ * 4 + gp * 2 - 2 ? ~0u : 0u; dk[gp * 2 - 1] = c_ == r_ * 4 + gp * 2 - 1 ? ~0u : 0u;
            PIN2(dk[gp * 2 - 2], dk[gp * 2 - 1]);
          }
          if constexpr (jb == 2 && gp == 3) { kl = lrow + (ln >> 5); go = (ln & 30) ? (int)0x80000000 : ((ln & 1) ? O_KEY : O_ST) + (int)kl * 8; PIN2(kl, go); }
          if constexpr (jb == 2 && gp == 4) { kl = 0xffff - kl; go = (ln & ~1) == 2 ? O_CE + (z * D_ + drow + (ln & 1)) * 8 : go; PIN2(kl, go); }
          if constexpr (jb == 2 && gp == 5) {
            gro = p_ < 2 ? O_ST + (st * 128 + p_ * 64 + ln) * 8 : O_CE + z * D_ * 8 + sym_row(D_, 32, 8, e_, z, (ln >> 2) & 3, (p_ - 2) * 16 + r_ * 4 + (ln & 3));
            lma = lds_addr(smem + LDS_MB) + (p_ < 2 ? p_ * 256 : 512 + p_ * 256) + ln * 4;
            PIN2(gro, lma);
          }
          if constexpr (jb == 1 && gp == 7) {
            static_assert(VA0 >= 2 && VA0 + VAN <= 9, "first halves");
            tv2 = (ln >> 3) < VAN ? (ln & 7) * 4096 + ((t_ ^ (VA0 - 1 + (ln >> 3))) << 8) : (int)0x80000000;
            PIN1(tv2);
          }
          if constexpr (jb == 7 && gp == 0) {
            vm2 = p_ * 1024 + ln * 16;
            PIN1(vm2);
          }
          if constexpr (jb == 2 && gp == 6) {
            const int r0 = p_ * 8 + lrw(ln >> 3);
            vn2[6] = sym_lo(64, S_ * 2, (r0 >> 2) & 3, (r0 >> 4) * 4 + (r0 & 3)); vn2[7] = sym_hi(S_, 64, S_ * 2, t_, z, (r0 >> 2) & 3) | (vm & 127);
            PIN2(vn2[6], vn2[7]);
          }
          if constexpr ((jb == 2 && gp == 7) || (jb == 7 && gp >= 6)) {
            constexpr int q0 = jb == 2 ? 0 : gp == 6 ? 3 : 6;
            const int lo = vn2[6], hi = vn2[7];
#pragma unroll
            for (int q = q0; q < q0 + 3 && q < 8; ++q) { vn2[q] = ((lo + q * 8 * S_ * 2) & (63 * S_ * 2)) ^ hi; PIN1(vn2[q]); }
          }
          if constexpr (jb == 7 && gp == 1) { vdn = rgb * 112 + p_ * 28 + r_ * 7; vgu = (((ln >> 3) & 1) * 4 * F_ + vdn + min(ln & 7, 6)) * 16; PIN2(vdn, vgu); }
          if constexpr (jb == 7 && gp == 2) { vdn = (vdn * D_ + cg8 * 128 + c_ * 8) * 2; PIN1(vdn); }
          if constexpr (jb == 7 && gp == 3) {
            abo = (((kq * 8 + e_) * 4 + p_) * 16 + c_) * AB_STRIDE * 4;
            abq[2] = ln >= 16 || (ln & 7) == t_ ? (int)0x80000000 : abo;
            PIN2(abo, abq[2]);
          }
          if constexpr (jb == 7 && gp == 4) {
            abq[1] = ln >= 8 ? (int)0x80000000 : abq[2];
            abq[0] = ((ln ^ t_) & 4) ? (int)0x80000000 : abq[1];
            PIN2(abq[0], abq[1]);
          }
          if constexpr (jb == 7 && gp == 5) {
            const unsigned mb = lds_addr(smem) + p_ * 1024;
            mq0 = mb + ((3 - kq) & 3) * 4096; mq1 = mb + ((4 - kq) & 3) * 4096; mq2 = mb + ((5 - kq) & 3) * 4096; mq3 = mb + ((6 - kq) & 3) * 4096;
            pin4(mq0, mq1, mq2, mq3);
          }
          if constexpr (jb == 8 && gp < 4) {
#pragma unroll
            for (int h = 0; h < 2; ++h) {
              const unsigned hv = h ? 0x0302u : 0x0100u;
              sel[h][gp] = ((ln & 7) >> 1) == gp ? ((ln & 1) ? (hv << 16) | 0x0c0cu : 0x0c0c0000u | hv) : 0x0c0c0c0cu;
            }
            PIN2(sel[0][gp], sel[1][gp]);
          }
        }
        SB();
      };
#define WS1(i) setup(IC<1>{}, IC<i>{})
#define WS2(i) setup(IC<2>{}, IC<i>{})
#define WS3(i) do { setup(IC<7>{}, IC<i>{}); if (i >= 4) setup(IC<8>{}, IC<(i) & 3>{}); spf(IC<(i < SP0 ? i : -1)>{}); } while (0)
      G1_STAGE_TILE(1, G1_STAGE, WS1);
      WAIT_VM(19); BAR();
      G1_STAGE_TILE(2, 2 * G1_STAGE, WS2);
      G1_STAGE_TILE(3, 3 * G1_STAGE, WS3);
      auto lnu = [&](auto U) __attribute__((always_inline)) {
        constexpr int jb = 1 + decltype(U)::value / 7, gp = decltype(U)::value % 7;
        SB();
        if constexpr (jb == 1) {
          const f32x4 z4 = {0.f, 0.f, 0.f, 0.f};
          if constexpr (gp == 0) { sa4 = MM(lv[0], oh[0], z4); gw[0] = MM(lv[0], lw[0], z4); gx[0] = MM(lv[0], lv[0], z4); PIN3(sa4, gw[0], gx[0]); }
          if constexpr (gp == 1) { sa4 = MM(lv2[0], oh[1], sa4); gw[1] = MM(lv2[0], lw[0], z4); gx[1] = MM(lv2[0], lv2[0], z4); PIN3(sa4, gw[1], gx[1]); }
          if constexpr (gp == 2) { sa4 = MM(lv[1], oh[0], sa4); gw[0] = MM(lv[1], lw[1], gw[0]); gx[0] = MM(lv[1], lv[1], gx[0]); PIN3(sa4, gw[0], gx[0]); }
          if constexpr (gp == 3) { sa4 = MM(lv2[1], oh[1], sa4); gw[1] = MM(lv2[1], lw[1], gw[1]); gx[1] = MM(lv2[1], lv2[1], gx[1]); PIN3(sa4, gw[1], gx[1]); }
          if constexpr (gp == 4) { sa4 = MM(ld[0], oh[2], sa4); sa4 = MM(ld[1], oh[2], sa4); PIN1(sa4); }
          if constexpr (gp == 5) { sa4 = MM(ld2[0], oh[3], sa4); sa4 = MM(ld2[1], oh[3], sa4); PIN1(sa4); }
          if constexpr (gp == 6) { sa4 = MM(lw[0], oh[4], sa4); sa4 = MM(lw[1], oh[4], sa4); PIN1(sa4);
            epoch = __builtin_amdgcn_readfirstlane(epv); tag = ((epoch + 1u) * 0x9E3779B1u ^ dmix) | 1u; }
        }
        if constexpr (jb == 2) {
          if constexpr (gp == 0) { su = (sa4[0] + sa4[1]) + (sa4[2] + sa4[3]); ta = diag(gw[0]); PIN2(su, ta); }
          if constexpr (gp == 1) { su = fold32(su, su); tb = diag(gw[1]); PIN2(su, tb); }
          if constexpr (gp == 2) { su = fold16(su, su); gs = fold32(ta, tb); PIN2(su, gs); }
          if constexpr (gp == 3) { ta = diag(gx[0]); tb = diag(gx[1]); PIN2(ta, tb); }
          if constexpr (gp == 4) { gs = fold16(gs, fold32(ta, tb)); PIN1(gs); }
          if constexpr (gp == 5) { gs += dpp_f<0xB1>(gs); gs += dpp_f<0x4E>(gs); PIN1(gs); }
          if constexpr (gp == 6) { gs += dpp_f<0x141>(gs); gs += dpp_f<0x140>(gs); PIN1(gs); }
        }
        if constexpr (jb == 3) {
          if constexpr (gp == 0) {
            const float s0 = rdl(su, 0), s1 = rdl(su, 1), q0 = rdl(gs, 16), q1 = rdl(gs, 48);
            sx = hb ? s1 : s0; ta = hb ? q1 : q0; PIN2(sx, ta);
          }
          if constexpr (gp == 1) {
            const float mean = sx * (1.0f / D_);
            mbv = rbf(mean);
            ta -= sx * mean;
            if (__builtin_expect(__any(!(sx * mean <= 16.f * ta)), 0)) {
              float q = fold32(sqdev16(lv, rdl(mbv, 0)), sqdev16(lv2, rdl(mbv, 32)));
              q = row_sum(fold16(q, q));
              const float dm = mean - mbv;
              ta = fmaxf(q - (float)D_ * dm * dm, 0.f);
            }
            PIN2(mbv, ta);
          }
          if constexpr (gp == 2) { ta = rbf(rbf(ta * (1.0f / D_)) + 1e-5f); gs -= mbv * rdl(su, 4); PIN2(ta, gs); }
          if constexpr (gp == 3) { ta = rbf(__builtin_amdgcn_sqrtf(ta)); tb = __builtin_amdgcn_rcpf(ta); PIN2(ta, tb); }
          if constexpr (gp == 4) {
            const unsigned rb = __float_as_uint(rbf(gs * tb));
            kl |= (rb ^ ((unsigned)((int)rb >> 31) | 0x80000000u)) & 0xffff0000u; PIN1(kl);
          }
          if constexpr (gp == 5) { kl = (lane & 2) ? __float_as_uint(su) : (lane & 1) ? kl : (__float_as_uint(mbv) >> 16) | __float_as_uint(ta); PIN1(kl); }
        }
        SB();
      };
      constexpr int LN_JB = 1 + (19 * LNS) / 8;
      auto work1 = [&](auto JB, auto GP) __attribute__((always_inline)) {
        constexpr int jb = decltype(JB)::value, gp = decltype(GP)::value, pos = (jb - 1) * 8 + gp;
        SB();
        if constexpr (pos % LNS == 0 && pos / LNS < 20) lnu(IC<pos / LNS>{});
        if constexpr (jb == ST_JB + 3) {
          if constexpr (gp == 0) { if (!__all(gr.y == tag)) gr = granule_wait((const u64*)((const char*)(flags + FL_AB) + gro), tag); PIN1(gr); }
          if constexpr (gp == 1) { g1 = __builtin_amdgcn_rcpf(__uint_as_float((gr.x & 0xffff0000u) ^ ((unsigned)z << 31))); PIN1(g1); }
          if constexpr (gp == 2) { g0 = pw < 2 ? -(lo_bf(gr.x) * g1) : __uint_as_float(gr.x); PIN1(g0); }
        }
        if constexpr (gp < SP1 && jb <= SP1E) spf(IC<SP0 + (jb - 1) * SP1 + gp>{});
        SB();
      };
      auto step1 = [&](auto JB) __attribute__((always_inline)) {
        constexpr int jb = decltype(JB)::value;
        if constexpr (jb == 1) PAGE(1);
        if constexpr (jb == 7) PAGE(2);
        constexpr int n16 = jb == 12 ? 12 : jb == 11 ? 14 : 16;
        constexpr auto wl = [](int q) { return (q == LN_JB ? 1 : 0) + (q == ST_JB ? 1 : 0); };
        if constexpr (jb == 1) {
          asm volatile("s_waitcnt vmcnt(%11)" : "+v"(lv[0]), "+v"(lv[1]), "+v"(lw[0]), "+v"(lw[1]), "+v"(ld[0]), "+v"(ld[1]),
                       "+v"(lv2[0]), "+v"(lv2[1]), "+v"(ld2[0]), "+v"(ld2[1]), "+v"(epv) : "i"(16) : "memory");
        } else if constexpr (jb == ST_JB + 3) {
          asm volatile("s_waitcnt vmcnt(%1)" : "+v"(gr) : "i"(n16 + wl(jb - 2) + wl(jb - 1)) : "memory");
        } else wait_vm<n16 + wl(jb - 3) + wl(jb - 2) + wl(jb - 1)>();
        BAR();
        if constexpr (jb == 2) { if (pw == 0 && lane < 16) ((int*)(smem + LDS_SEL))[lane < 8 ? lane : lane + 8] = 0; }
        if constexpr (jb == 4) { if (pw == 0 && lane == 0) ((unsigned*)(smem + LDS_SEL))[5] = tag; }
#define W1(i) work1(JB, IC<i>{})
        if constexpr (jb < 10) G1_STAGE_TILE(jb - 1 + G1_NST, ((jb - 1) % G1_NST) * G1_STAGE, W1);
        else G1_STAGE_A(jb - 1 + G1_NST, ((jb - 1) % G1_NST) * G1_STAGE, W1);
        if constexpr (jb == 12) G1_STAGE_B(13, G1_STAGE);
        if constexpr (jb == LN_JB) __builtin_amdgcn_raw_buffer_store_b64((u32x2){kl, tag}, rf, go, 0, 16);
        if constexpr (jb == ST_JB) {
          asm volatile("buffer_load_dwordx2 %0, %1, %2, 0 offen sc1" : "=v"(gr) : "v"(gro), "s"(rf) : "memory");
        }
        if constexpr (jb <= 8) {
          if (et == jb - 1) {
#pragma unroll
            for (int l = 0; l < WSN; ++l) bld16_nt_io(wv[l], rw, vw, l * F_ * 2);
          }
        }
        SB();
      };
      static_assert(ST_JB + 3 <= 12 && ST_JB >= 6, "schedule");
      step1(IC<1>{}); step1(IC<2>{}); step1(IC<3>{}); step1(IC<4>{}); step1(IC<5>{}); step1(IC<6>{}); step1(IC<7>{}); step1(IC<8>{}); step1(IC<9>{}); step1(IC<10>{}); step1(IC<11>{}); step1(IC<12>{});
    }
#define G2_STAGE_N(kk_, W) do { const int kb = KROT(kk_); constexpr int so = n_off(kk_); SB(); \
    DMA_S(rn2, vn2[0], mq0, so, kb); W(0); DMA_S(rn2, vn2[1], mq1, so, kb); W(1); \
    DMA_S(rn2, vn2[2], mq2, so, kb); W(2); DMA_S(rn2, vn2[3], mq3, so, kb); W(3); \
    DMA_S(rn2, vn2[4], mq0, so + 16384, kb); W(4); DMA_S(rn2, vn2[5], mq1, so + 16384, kb); W(5); \
    DMA_S(rn2, vn2[6], mq2, so + 16384, kb); W(6); DMA_S(rn2, vn2[7], mq3, so + 16384, kb); W(7); SB(); } while (0)
#define G2_STAGE_NH(kk_, h) do { const int kb = KROT(kk_); constexpr int so = n_off(kk_) + (h) * 16384; SB(); \
    DMA_S(rn2, vn2[(h) * 4], mq0, so, kb); DMA_S(rn2, vn2[(h) * 4 + 1], mq1, so, kb); \
    DMA_S(rn2, vn2[(h) * 4 + 2], mq2, so, kb); DMA_S(rn2, vn2[(h) * 4 + 3], mq3, so, kb); SB(); } while (0)
#define G2_STAGE_M(kk_, W) do { const int kb = KROT(kk_) * 128; constexpr int so = G2_MRING + m_off(kk_); SB(); \
    DMA_(rm2, vm2, mb0, so, kb, 0, " sc1"); W(8); DMA_(rm2, vm2, mb0, so + 4096, kb, 4096, " sc1"); W(9); \
    DMA_(rm2, vm2, mb0, so + 8192, kb, 8192, " sc1"); W(10); DMA_(rm2, vm2, mb0, so + 12288, kb, 12288, " sc1"); W(11); SB(); } while (0)
    WAIT_VM(8); BAR();
    asm volatile("ds_write_b32 %0, %1" :: "v"(lma), "v"(g0) : "memory");
    if (pw < 2) asm volatile("ds_write_b32 %0, %1 offset:512" :: "v"(lma), "v"(g1) : "memory");
    u32x4 tv[4];
    unsigned ready = 0;
    auto need = [&](int k) __attribute__((always_inline)) {
      const int b = k_blk(k) + (k_ord(k) & 1) * 8;
#pragma unroll 1
      for (int it = 0; !((ready >> b) & 1) && it < SPIN_MAX; ++it) {
        ready |= (unsigned)__ballot(__builtin_amdgcn_raw_buffer_load_b32(rf, abq[k < 5 ? 0 : k < 9 ? 1 : 2], 0, 16) == tag);
        asm volatile("" ::: "memory");
      }
    };
    unsigned ksel, bad = 0;
    int isel;
    unsigned xw;
    auto bvec = [&](unsigned src, const unsigned (&sl)[4]) __attribute__((always_inline)) {
      return (u32x4){__builtin_amdgcn_perm(0, src, sl[0]), __builtin_amdgcn_perm(0, src, sl[1]),
                     __builtin_amdgcn_perm(0, src, sl[2]), __builtin_amdgcn_perm(0, src, sl[3])};
    };
#define BC8(v, p) (unsigned)__builtin_amdgcn_update_dpp(__builtin_amdgcn_update_dpp(0, (int)(v), 0x150 + (p), 0xf, 0x3, false), (int)(v), 0x158 + (p), 0xf, 0xc, false)
    float* lg = (float*)(smem + LDS_GU);
    f32x4 macc = {0.f, 0.f, 0.f, 0.f};
    u32x2 sv;
    unsigned hp, gbad;
    float s0, s1;
    auto work2 = [&](auto JB, auto GP) __attribute__((always_inline)) {
      constexpr int jb = decltype(JB)::value, gp = decltype(GP)::value;
      SB();
      if constexpr (jb == -1 && gp < 4) {
        constexpr int c = gp == 0 ? 0xB1 : gp == 1 ? 0x4E : gp == 2 ? 0x141 : 0x140;
        merge2(k1, k2, dpp_u<c>(k1), dpp_u<c>(k2));
        PIN2(k1, k2);
      }
      if constexpr (jb == -1 && gp == 4) {
        ksel = tk ? k2 : k1;
        isel = (0xffff - (int)(ksel & 0xffffu)) & (S_ - 1);
        k1 = (isel * D_ + d0 + rq * 16 + (lane & 7) * 2) * 2; k2 = O_ST + isel * 8;
        PIN1(ksel);
        xw = gld4_s(x, k1);
        sv = bld8_sc1(rf, k2);
      }
      if constexpr (jb == 0 && gp == 1) wait_xs<3 + 2 + 2>(xw, sv);
      if constexpr (jb == 0) {
        if constexpr (gp == 1) { if (!__all(sv.y == tag)) sv = granule_wait(stat + isel, tag); s0 = __builtin_amdgcn_rcpf(hi_bf(sv.x)); PIN1(s0); }
        if constexpr (gp == 2) { s1 = ln_elem(lo_bf(xw), lo_bf(sv.x), hi_bf(sv.x), s0); PIN1(s1); }
        if constexpr (gp == 3) { xw = pk2(s1, ln_elem(hi_bf(xw), lo_bf(sv.x), hi_bf(sv.x), s0)); PIN1(xw); }
      }
      if constexpr (jb == DG && gp < 7) { PIN1(dv[gp]); macc = MM(dv[gp], bvec(BC8(hp, gp), sel[0]), macc); PIN1(macc); }
      if constexpr ((jb == 0 && gp >= 4) || jb == 1) {
        constexpr int l = jb == 0 ? gp - 4 : gp + 4;
        if constexpr (!(l & 1)) hp = BC8(xw, l >> 1);
        PIN1(wv[l]);
        macc = MM(wv[l], bvec(hp, sel[l & 1]), macc);
        PIN2(macc, hp);
      }
      if constexpr (jb == GV_JB + 2) {
        if constexpr (gp == 0) { gbad = (gv[0].y ^ tag) | (gv[0].w ^ tag) | (gv[1].y ^ tag) | (gv[1].w ^ tag); PIN1(gbad); }
        if constexpr (gp == 1) { gbad |= (gv[2].y ^ tag) | (gv[2].w ^ tag) | (gv[3].y ^ tag) | (gv[3].w ^ tag); PIN1(gbad); }
        if constexpr (gp == 2) {
          s0 = ((__uint_as_float(gv[0].x) + __uint_as_float(gv[1].x)) + __uint_as_float(gv[2].x)) + __uint_as_float(gv[3].x);
          s1 = ((__uint_as_float(gv[0].z) + __uint_as_float(gv[1].z)) + __uint_as_float(gv[2].z)) + __uint_as_float(gv[3].z);
          PIN2(s0, s1);
        }
        if constexpr (gp == 3) {
#pragma unroll 1
          for (int it = 0; !__all(gbad == 0) && it < SPIN_MAX; ++it) {
            u32x4 q[4];
            gbad = 0;
#pragma unroll
            for (int i = 0; i < 4; ++i) { q[i] = __builtin_amdgcn_raw_buffer_load_b128(rgu, vgu, i * F_ * 16, 16); gbad |= (q[i].y ^ tag) | (q[i].w ^ tag); }
            s0 = ((__uint_as_float(q[0].x) + __uint_as_float(q[1].x)) + __uint_as_float(q[2].x)) + __uint_as_float(q[3].x);
            s1 = ((__uint_as_float(q[0].z) + __uint_as_float(q[1].z)) + __uint_as_float(q[2].z)) + __uint_as_float(q[3].z);
            asm volatile("" ::: "memory");
          }
          const unsigned gp2 = pk2(s0, s1);
          const unsigned up2 = dpp_u<0x128>(gp2);
          s0 = tk ? hi_bf(up2) : lo_bf(gp2); s1 = tk ? hi_bf(gp2) : lo_bf(up2);
          PIN2(s0, s1);
        }
        if constexpr (gp == 4) { s0 = rbf(s0 * __builtin_amdgcn_rcpf(1.f + __expf(-s0))); PIN1(s0); }
        if constexpr (gp == 5) { hp = pk2(s0 * s1, 0.f); PIN1(hp); }
      }
      if constexpr (jb == 10 && gp == 4) code_touch(sd, (j * 4 + pw) * 256);
      SB();
    };
#define W2M(i) work2(IC<-1>{}, IC<i>{})
#define W20(i) work2(IC<0>{}, IC<i>{})
#define W21(i) work2(IC<1>{}, IC<i>{})
    unsigned fs;
    {
      G1_STAGE_B(15, 3 * G1_STAGE);
      G1_STAGE_B(14, 2 * G1_STAGE);
      lgkm0(sd);
      BAR();
      G2_STAGE_NH(0, 0);
      WAIT_VM(6); BAR();
#pragma unroll
      for (int i = 0; i < 4; ++i) kk[i] = bld16_sc1(rf, l16, ko_ + i * 1024);
      G2_STAGE_NH(0, 1);
#pragma unroll
      for (int l = WSN; l < 16; ++l) wv[l] = bld16_nt(rw, vw, l * F_ * 2);
      asm volatile("s_waitcnt vmcnt(%4)" : "+v"(kk[0]), "+v"(kk[1]), "+v"(kk[2]), "+v"(kk[3]) : "i"(16 - WSN + 4) : "memory");
#pragma unroll
      for (int i = 0; i < 4; ++i) {
        k2 = med3u(k1, k2, kk[i].x); k1 = max(k1, kk[i].x);
        k2 = med3u(k1, k2, kk[i].z); k1 = max(k1, kk[i].z);
        bad |= (kk[i].y ^ tag) | (kk[i].w ^ tag);
      }
#pragma unroll 1
      for (int it = 0; !__all(bad == 0) && it < SPIN_MAX; ++it) {
        k1 = 0; k2 = 0; bad = 0;
#pragma unroll
        for (int i = 0; i < 4; ++i) {
          const u32x4 q = __builtin_amdgcn_raw_buffer_load_b128(rf, l16, ko_ + i * 1024, 16);
          k2 = med3u(k1, k2, q.x); k1 = max(k1, q.x);
          k2 = med3u(k1, k2, q.z); k1 = max(k1, q.z);
          bad |= (q.y ^ tag) | (q.w ^ tag);
        }
        asm volatile("" ::: "memory");
      }
      merge2(k1, k2, dpp_u<0xB1>(k1), dpp_u<0xB1>(k2));
      merge2(k1, k2, dpp_u<0x4E>(k1), dpp_u<0x4E>(k2));
      merge2(k1, k2, dpp_u<0x141>(k1), dpp_u<0x141>(k2));
      merge2(k1, k2, dpp_u<0x140>(k1), dpp_u<0x140>(k2));
      if ((lane & 15) == 0) *(u32x2*)(smem + LDS_KEY + (pw * 4 + rq) * 8) = (u32x2){k1, k2};
      PAGE(3);
      wait_vm<16 - WSN>();
      BAR_LGKM();
#pragma unroll
      for (int jj = 0; jj < 4; ++jj) tv[jj] = *(const u32x4*)(smem + G2_MRING + m_off(0) + (jj * 4 + pw) * 1024 + lane * 16);
      { const u32x2 kp = *(const u32x2*)(smem + LDS_KEY + ci * 8); k1 = kp.x; k2 = kp.y; }
#pragma unroll
      for (int jj = 0; jj < 4; ++jj) __builtin_amdgcn_raw_buffer_store_b128(tv[jj], rm2, vm2 + jj * 4096 + 2 * tt * 16384, 0, 16);
      G2_STAGE_N(1, W2M);
      touch1(mk_rsrc(spb), tv2, mb0, 0);
      WAIT_VM(11);
      if (lane == tt) __builtin_amdgcn_raw_buffer_store_b32(tag, rf, abo, 0, 16);
      {
        lds_vi rel = (lds_vi)(smem + LDS_SEL + 12);
        int it = 0;
        while (rel[0] != 4 && it < SPIN_MAX * 8) ++it;
        asm volatile("" ::: "memory");
      }
      G2_STAGE_N(2, W20);
      __builtin_amdgcn_s_sleep(2);
      WAIT_VM(10);
      fs = bld4_sc1(rf, abq[0]);
    }
    BAR();
#pragma unroll
    for (int jj = 0; jj < 4; ++jj) tv[jj] = *(const u32x4*)(smem + G2_MRING + m_off(1) + (jj * 4 + pw) * 1024 + lane * 16);
    constexpr int SPB = SP0 + SP1E * SP1;
    G2_STAGE_N(3, NOW);
    asm volatile("s_waitcnt vmcnt(8)" : "+v"(fs) :: "memory");
    ready = (unsigned)__ballot(fs == tag);
    need(2);
    G2_STAGE_M(2, NOW);
    need(3);
    G2_STAGE_M(3, NOW);
    fs = bld4_sc1(rf, abq[1]);
    W21(0); W21(1); W21(2); W21(3); W21(4); W21(5); W21(6); W21(7); W21(8); W21(9); W21(10); W21(11);
    asm volatile("" : "+v"(tv[0]), "+v"(tv[1]), "+v"(tv[2]), "+v"(tv[3]));
    int dno;
    auto step2 = [&](auto JB) __attribute__((always_inline)) {
      constexpr int jb = decltype(JB)::value;
      if constexpr (jb == 3) PAGE(4);
      if constexpr (jb == 9) PAGE(5);
      if constexpr (jb == 13) PAGE(7);
      constexpr auto must = [](int q) { return (q == 2 ? 4 : 0) + ((FSM >> q) & 1) + dig(GVS, q); };
      if constexpr (jb == 2) wait_vm<4 + must(1)>();
      else wait_vm<dn_cnt(jb - 2) + (jb == 5 ? 1 : 0) + 12 + must(jb - 1) + dn_cnt(jb - 1)>();
      if constexpr (jb == 3) { if (pw == 0 && (lane & ~8) == 0) ((unsigned*)(smem + LDS_SEL))[tk] = ksel; }
      if constexpr ((jb == 2 || jb == DG + 1)) {
        *(f32x4*)(lg + pw * 256 + lane * 4) = macc;
        macc = (f32x4){0.f, 0.f, 0.f, 0.f};
        lgkm0(sd);
      }
      BAR();
      float p0[4], p1[4];
      const int col = (pw & 1) * 64 + lane;
      if constexpr (jb == 3) {
        if (g == 0 && pw == 0 && lane == 0) __hip_atomic_store((unsigned*)flags + FL_EPOCH, epoch + 1u, __ATOMIC_RELAXED, __HIP_MEMORY_SCOPE_AGENT);
      }
      if constexpr (jb == DG + 1) {
        const float* p = lg + (pw & 1) * 32 + (lane >> 4) * 64 + ((2 * lane) & 7) * 4 + ((lane >> 2) & 3);
#pragma unroll
        for (int i = 0; i < 4; ++i) { p0[i] = p[i * 256]; p1[i] = p[i * 256 + 4]; }
      }
      if constexpr (jb == 2) {
        const float* p = lg + (col >> 5) * 64 + (col & 7) * 4 + ((col >> 3) & 3);
#pragma unroll
        for (int i = 0; i < 4; ++i) { p0[i] = p[i * 256]; p1[i] = p[i * 256 + 32]; }
      }
      if constexpr (jb == 4) {
        if (lane == 8 + tt) __builtin_amdgcn_raw_buffer_store_b32(tag, rf, abo, 0, 16);
      }
      if constexpr ((FSM >> (jb - 2)) & 1) {
        asm volatile("" : "+v"(fs));
        ready |= (unsigned)__ballot(fs == tag);
      }
      if constexpr (jb == GV_JB + 2) asm volatile("" : "+v"(gv[0]), "+v"(gv[1]), "+v"(gv[2]), "+v"(gv[3]));
      auto w2 = [&](auto GP) __attribute__((always_inline)) {
        constexpr int gp = decltype(GP)::value;
        if constexpr ((jb == 2 || jb == DG + 1) && gp == 0) {
          p0[0] = (p0[0] + p0[1]) + (p0[2] + p0[3]); p1[0] = (p1[0] + p1[1]) + (p1[2] + p1[3]);
          PIN2(p0[0], p1[0]);
        }
        if constexpr (jb == DG + 1 && gp == 1) {
          if (pw < 2) __builtin_amdgcn_raw_buffer_store_b64((u32x2){pk2(p0[0], p1[0]), tag}, mk_rsrc(wsp + WS_DN), (((pw * 8 + cg8) * 32 + rgb) * 64 + lane) * 8, 0, 16);
        }
        if constexpr (jb == 12 && gp == 3) epi_setup(3);
        if constexpr (jb == 12 && gp == 6) { key[0] = __builtin_amdgcn_readlane(ksel, 0); key[1] = __builtin_amdgcn_readlane(ksel, 8); far_calc(); }
        if constexpr (jb == 12 && gp == 10) {
          int l_ = lane;
          PIN1(l_);
          const int ec = seg0((l_ >> 4) & 1, (l_ >> 3) & 1);
          dno = (ec >> 7) * 16384 + (l_ >> 5) * 512 + (ec & 127) * 4 + (l_ & 7) * 16;
          PIN1(dno);
        }
        work2(JB, GP);
        if constexpr (gp < dig(SPS, jb)) spf(IC<SPB + acc_dig(SPS, jb) + gp>{});
      };
#define W2(i) w2(IC<i>{})
      G2_STAGE_N(jb + 2, W2);
      need(jb + 2);
      G2_STAGE_M(jb + 2, W2);
      if constexpr (jb == 2) {
#pragma unroll
        for (int jj = 0; jj < 4; ++jj) __builtin_amdgcn_raw_buffer_store_b128(tv[jj], rm2, vm2 + jj * 4096 + (2 * tt + 1) * 16384, 0, 16);
      }
      if constexpr ((FSM >> jb) & 1) fs = bld4_sc1(rf, abq[jb < 5 ? 1 : 2]);
#pragma unroll
      for (int i = acc_dig(GVS, jb); i < acc_dig(GVS, jb + 1); ++i) gv[i] = bld16_sc1(rgu, vgu, i * F_ * 16);
#pragma unroll
      for (int i = dn_first(jb); i < dn_first(jb) + dn_cnt(jb); ++i) dv[i] = bld16_nt(rdn, vdn, i * D_ * 2);
      if constexpr (jb == 2) {
        if (moe && pw < 2)
          __builtin_amdgcn_raw_buffer_store_b128((u32x4){__float_as_uint(p0[0]), tag, __float_as_uint(p1[0]), tag},
                                                 rgu, ((mat * 4 + dq) * F_ + cg * 128 + col) * 16, 0, 16);
      }
      SB();
      if constexpr (dig(SPS, jb) > 12) spf(IC<SPB + acc_dig(SPS, jb) + 12>{});
      SB();
    };
    static_assert(DG + 1 <= 12 && DG >= GV_JB + 3 && dn_first(DG - 2) == 7 && acc_dig(GVS, GV_JB + 1) == 4, "schedule");
    static_assert(SPB + acc_dig(SPS, 14) == (16 - VAN) * 16, "all tiles are prefetched");
    step2(IC<2>{}); step2(IC<3>{}); step2(IC<4>{}); step2(IC<5>{}); step2(IC<6>{}); step2(IC<7>{}); step2(IC<8>{}); step2(IC<9>{}); step2(IC<10>{}); step2(IC<11>{}); step2(IC<12>{}); step2(IC<13>{});
    lgkm0(sd);
    WAIT_VM(12); BAR();
    __builtin_amdgcn_s_setprio(0);
    const __amdgpu_buffer_rsrc_t rd = mk_rsrc(wsp + WS_DN + pw * 4096);
    u32x4 dq_[4];
    epi_setup(0); epi_setup(1); epi_setup(2);
    xq_load(0); xq_load(1); xq_load(2);
    asm volatile("s_cmp_lg_u32 %7, 0\n\ts_cbranch_scc1 1f\n\t"
                 "buffer_load_dwordx4 %0, %4, %5, %6 offen sc1\n\tbuffer_load_dwordx4 %1, %4, %5, %6 offen offset:1024 sc1\n\t"
                 "buffer_load_dwordx4 %2, %4, %5, %6 offen offset:2048 sc1\n\tbuffer_load_dwordx4 %3, %4, %5, %6 offen offset:3072 sc1\n\t"
                 "s_waitcnt vmcnt(7)\n\ts_branch 2f\n1:\n\ts_waitcnt vmcnt(3)\n2:\n\ts_barrier\n\ts_waitcnt vmcnt(0)"
                 : "=&v"(dq_[0]), "=&v"(dq_[1]), "=&v"(dq_[2]), "=&v"(dq_[3])
                 : "v"(dno), "s"(rd), "s"((far0 & ~9) ? 131072 : 0), "s"(min(far0 & ~9, far1 & ~9)) : "scc", "memory");
    wait_xq<0>(xq, sq);
    bool first = true;
#pragma unroll 1
    for (int c = 0; c < 2; ++c) {
      if (__builtin_expect(((c ? far1 : far0) & ~9) != 0, 1)) continue;
      const float sg = rbf(__builtin_amdgcn_rcpf(1.f + __expf(-key_score(c ? key[1] : key[0]))));
#pragma unroll 1
      for (int it = 0; it < SPIN_MAX; ++it) {
        unsigned bad = 0;
#pragma unroll
        for (int i = 0; i < 4; ++i) bad |= (dq_[i].y ^ tag) | (dq_[i].w ^ tag);
        if ((first || it) && __all(bad == 0)) break;
        if (it) __builtin_amdgcn_s_sleep(4);
#pragma unroll
        for (int i = 0; i < 4; ++i) dq_[i] = __builtin_amdgcn_raw_buffer_load_b128(rd, dno + i * 1024, c * 131072, 16);
        asm volatile("" ::: "memory");
      }
      first = false;
      f32x4 s4 = {0.f, 0.f, 0.f, 0.f};
#pragma unroll
      for (int i = 0; i < 4; ++i) s4 += (f32x4){lo_bf(dq_[i].x), hi_bf(dq_[i].x), lo_bf(dq_[i].z), hi_bf(dq_[i].z)};
      *(f32x2*)(smem + LDS_FIN + (c * 4 + pw) * 512 + (lane & 31) * 16 + (lane >> 5) * 8) = (f32x2){fold32(s4[0], s4[2]) * sg, fold32(s4[1], s4[3]) * sg};
      asm volatile("s_waitcnt lgkmcnt(0)" ::: "memory");
      if (lane == 0) asm volatile("ds_add_u32 %0, %1" :: "v"(lds_addr(smem + LDS_SEL + 24 + c * 4)), "v"(1) : "memory");
    }
    cv_calc();
  } else {
  {
    const int q = j * 4 + wid;
    constexpr int Q1 = WS_MB, Q2 = Q1 + 8, Q3 = Q2 + 8, Q4 = Q3 + 7, Q5 = Q4 + 7, Q6 = Q5 + 7, Q7 = Q6 + 4, Q8 = Q7 + 4, QC = Q8 + 1;
    static_assert(QC <= 128, "warm-up slots");
    unsigned dmy = 0;
    if (q < QC) {
      const char* base; int b0;
      if (q < Q1) { base = wsp; b0 = 0; }
      else if (q < Q2) { base = (const char*)ws_re; b0 = Q1; }
      else if (q < Q3) { base = (const char*)ws_im; b0 = Q2; }
      else if (q < Q4) { base = (const char*)w_gate; b0 = Q3; }
      else if (q < Q5) { base = (const char*)w_up; b0 = Q4; }
      else if (q < Q6) { base = (const char*)w_down; b0 = Q5; }
      else if (q < Q7) { base = (const char*)out; b0 = Q6; }
      else if (q < Q8) { base = (const char*)x; b0 = Q7; }
      else { base = (const char*)w_r; b0 = Q8; }
      base += (size_t)(q - b0) << 20;
      asm volatile("s_load_dword %0, %1, 0" : "+s"(dmy) : "s"(base) : "memory");
    }
    if (wid == 0) { CALL_PG(1); CALL_PG(8); CALL_PG(5); }
    else if (wid == 1) { CALL_PG(2); CALL_PG(9); CALL_PG(6); }
    else if (wid == 2) { CALL_PG(3); CALL_PG(10); CALL_PG(7); }
    else { CALL_PG(4); CALL_PG(11); CALL_PG(12); }
    BAR();
    lgkm0(dmy);
  }
  PAGE(8);
  epi_setup(0); epi_setup(1); epi_setup(2); epi_setup(3);
  const int wr = wid >> 1, wc = wid & 1;
  const int sw = swz(fr), fl = lrw(fr);
  const int ko0 = ((0 + fq) ^ sw) * 16, ko1 = ((4 + fq) ^ sw) * 16;
  bf16x8 fm[2][4], fn[2][4];
  {
    const char* pm = smem + (wr * 64 + fl) * 128;
    const char* pn = smem + 16384 + (wc * 64 + fl) * 128;
    f32x4 acc[4][4];
#pragma unroll
    for (int i = 0; i < 4; ++i)
#pragma unroll
      for (int jn = 0; jn < 4; ++jn)
#pragma unroll
        for (int q = 0; q < 4; ++q) acc[i][jn][q] = 0.f;
#define TO(i) ((i) * 2048)
#define LDSP(T, a) ((__attribute__((address_space(3))) T*)(size_t)(a))
#define SLOTS(n) _Pragma("unroll") for (int q = 0; q < (n); ++q)
#define MFA(c, m, n) asm volatile("v_mfma_f32_16x16x32_bf16 %0, %2, %1, %0" : "+v"(c) : "v"(m), "v"(n))
#define MFA2(c, m, n, m2, n2) asm volatile("v_mfma_f32_16x16x32_bf16 %0, %2, %1, %0\n\tv_mfma_f32_16x16x32_bf16 %0, %4, %3, %0" : "+v"(c) : "v"(m), "v"(n), "v"(m2), "v"(n2))
    unsigned lm[2][2], ln[2][2];
#pragma unroll
    for (int u = 0; u < 2; ++u)
#pragma unroll
      for (int kk = 0; kk < 2; ++kk) {
        lm[u][kk] = lds_addr(pm) + u * 2 * G1_STAGE + (kk ? ko1 : ko0);
        ln[u][kk] = lds_addr(pn) + u * 2 * G1_STAGE + (kk ? ko1 : ko0);
        asm volatile("" : "+v"(lm[u][kk]), "+v"(ln[u][kk]));
      }
    auto ldm = [&](int stg, int i, int kk) __attribute__((always_inline)) { fm[kk][i] = *LDSP(const bf16x8, lm[stg >> 1][kk] + (stg & 1) * G1_STAGE + TO(i)); };
    auto ldn = [&](int stg, int jn, int kk) __attribute__((always_inline)) { fn[kk][jn] = *LDSP(const bf16x8, ln[stg >> 1][kk] + (stg & 1) * G1_STAGE + TO(jn)); };
    auto quad = [&](int a, int c, int d, int s) __attribute__((always_inline)) {
      const int i = (s < 2 || s >= 6) ? a : a + 1, jn = s < 4 ? c : d, kk = (s == 1 || s == 2 || s == 5 || s == 6);
      MFA(acc[i][jn], fm[kk][i], fn[kk][jn]);
    };
    auto quad2 = [&](int a, int c, int d, int s) __attribute__((always_inline)) {
      const int i = (s < 2 || s >= 6) ? a : a + 1, jn = s < 4 ? c : d, kk = (s == 2 || s == 6);
      MFA2(acc[i][jn], fm[kk][i], fn[kk][jn], fm[1 - kk][i], fn[1 - kk][jn]);
    };
    auto half1 = [&](int stg) __attribute__((always_inline)) {
      const int ja = (stg & 1) * 2, jb = 2 - ja;
      SLOTS(8) {
        quad(0, ja, ja + 1, q);
        if (q < 4) ldn(stg, jb + (q >> 1), q & 1); else ldm(stg, 2 + ((q - 4) >> 1), q == 5 || q == 6);
        SB();
      }
      SLOTS(4) { quad2(0, jb, jb + 1, 2 * q); SB(); }
    };
    auto half2 = [&](int stg) __attribute__((always_inline)) {
      const int ja = (stg & 1) * 2, jb = 2 - ja, nx = (stg + 1) & 3;
      SLOTS(8) {
        quad(2, jb + 1, jb, q);
        if (!(q & 1)) ldm(nx, q >> 2, q == 2 || q == 4);
        SB();
      }
      SLOTS(8) {
        quad(2, ja, ja + 1, q);
        if (!(q & 1)) ldn(nx, jb + (q >> 2), (q >> 1) & 1);
        SB();
      }
    };
    unsigned ae[7];
    {
      int l_ = lane;
      asm volatile("" : "+v"(l_));
      const int p = l_ >> 4, dn = sym_dn(p), hf = (p >> 1) ^ (dn & z);
#pragma unroll
      for (int u = 0; u < 7; ++u) {
        const int T = u < 3 ? wc * 4 : (wc * 4 + u - 3 - dn) & 7, i = u < 3 ? (u + 1) ^ ((u & 1) ? 0 : dn * 2) : 0;
        const int f = dn * 8 + ((T & 2) << 1) + ((T & 4) >> 1) + (T & 1);
        ae[u] = lds_addr(smem + G2_MRING) + (hf * 64 + i * 16 + lrw(f)) * 128 + (((wr * 4 + ((l_ >> 2) & 3)) ^ swz(f)) * 16) + (l_ & 3) * 4;
        asm volatile("" : "+v"(ae[u]));
      }
    }
    unsigned al = lds_addr(smem + LDS_MB) + (wr * 32 + fr * 2) * 4, ac = lds_addr(smem + LDS_MB) + (256 + wc * 64 + fq * 4) * 4;
    asm volatile("" : "+v"(al), "+v"(ac));
    SLOTS(16) asm volatile("" : "+v"(acc[q >> 2][q & 3]));
    SB();
    SLOTS(4) { ldm(0, q >> 1, q & 1); ldn(0, q >> 1, q & 1); }
    SB();
#pragma unroll 1
    for (int k4 = 0; k4 < G1_NKT - 4; k4 += 4) {
      half1(0); BAR_LGKM();
      SB(); half2(0);
      half1(1); BAR_LGKM();
      SB(); half2(1);
      half1(2); BAR_LGKM();
      SB(); half2(2);
      half1(3); BAR_LGKM();
      SB(); half2(3);
    }
    bf16x8 gm[4][4], gn[2][8];
    auto rdm1 = [&](int set, int t, int h, int i, int kk) __attribute__((always_inline)) {
      gm[set][kk * 2 + i] = *LDSP(const bf16x8, lm[(t >> 1) & 1][kk] + (t & 1) * G1_STAGE + h * 4096 + i * 2048);
    };
    auto rdg = [&](int set, int t, int h, int r) __attribute__((always_inline)) {
      if (r == 0 || r == 2 || r == 4 || r == 5) rdm1(set, t, h, r >= 4, r == 2 || r == 4);
      else {
        const int jn = r < 4 ? 0 : (r - 4) >> 1, kk = r < 4 ? r >> 1 : r & 1;
        gn[set][kk * 4 + jn] = *LDSP(const bf16x8, ln[(t >> 1) & 1][kk] + (t & 1) * G1_STAGE + TO(jn));
      }
    };
    auto mmg = [&](int ms, int ns, int h, int jn, int r, int f) __attribute__((always_inline)) {
      const int i = (r >> 1) ^ f, kk = r == 1 || r == 2;
      MFA(acc[h * 2 + i][jn], gm[ms][kk * 2 + i], gn[ns][kk * 4 + jn]);
    };
    f32x2 rsv, mrv;
    f32x4 cev[4];
    float tc[32];
    unsigned ep;
    auto rds = [&](int h, int r) __attribute__((always_inline)) {
      if (r == 0) mrv = *LDSP(const f32x2, al + h * 256); else if (r == 5) rsv = *LDSP(const f32x2, al + 512 + h * 256);
      else cev[r - 1] = *LDSP(const f32x4, ac + (r - 1) * 64);
    };
    auto prep = [&](int n) __attribute__((always_inline)) {
      tc[n] = llvm_fmul_legacy(mrv[n & 1], cev[n >> 3][(n >> 1) & 3]);
      asm volatile("" : "+v"(tc[n]));
    };
    auto epi = [&](int h, int n) __attribute__((always_inline)) {
      const int jn = n >> 4, r = (n >> 2) & 3, u = n & 3, e = (n >> 2) * 2;
      if (u == 0) {}
      else if (u == 1) asm volatile("v_fmac_f32 %0, %2, %4\n\tv_fmac_f32 %1, %3, %5\n\tv_cvt_pk_bf16_f32 %0, %0, %1" : "+v"(tc[e]), "+v"(tc[e + 1])
                                    : "v"(acc[h * 2][jn][r]), "v"(acc[h * 2 + 1][jn][r]), "v"(rsv[0]), "v"(rsv[1]));
      else if (u == 2) ep = __float_as_uint(tc[e]);
      else *LDSP(unsigned, (r ? ae[r - 1] + (jn & 1) * 128 + (jn >> 1) * lrw(4) * 128 : ae[3 + jn]) + m_off(h)) = ep;
      SB();
    };
    SB();
    half1(0);
    BAR_LGKM();
    SB();
    SLOTS(16) {
      if (q < 8) quad(2, 3, 2, q); else quad(2, 0, 1, q - 8);
      if (q < 12) rdg(0, 14, 0, q); else rdg(1, 13, 0, q - 12);
      SB();
    }
    BAR();
    SB();
    SLOTS(16) {
      mmg(0, 0, 0, q >> 2, q & 3, (q >> 2) & 1);
      if (q < 6) rds(0, q); else if (q < 14) rdg(1, 13, 0, q - 2);
      if (q >= 6) prep(q - 6);
      if (q >= 14) prep(q - 4);
      SB();
    }
    PAGE(9);
    BAR();
    SB();
    {
      constexpr int ct[8] = {1, 1, 0, 0, 1, 0, 1, 0}, cj[8] = {0, 1, 0, 1, 2, 2, 3, 3}, cf[8] = {0, 1, 0, 1, 0, 1, 0, 1};
      constexpr auto en = [](int q) { return q < 15 ? 0 : q < 26 ? (q - 14) * 3 - (q == 25) : q < 27 ? 32 : q < 32 ? 32 + (q - 26) * 3 + (q == 31) : 48; };
      SLOTS(32) {
        mmg(ct[q >> 2], ct[q >> 2], 0, cj[q >> 2], q & 3, cf[q >> 2]);
        if (q < 12) rdg(0, 15, 0, q);
        else if (q < 15 || q == 17) { const int r = q < 15 ? q - 12 : 3; rdm1(2, 15, 1, r >> 1, r == 1 || r == 2); }
        else if (q == 20 || q == 23 || q == 25 || q == 26) { const int r = q == 20 ? 0 : q == 23 ? 1 : q - 23; rdm1(3, 13, 1, r >> 1, r == 1 || r == 2); }
        if (q < 15) prep(12 + q);
        if (q >= 10 && q < 15) prep(17 + q);
        SB();
#pragma unroll
        for (int n = q ? en(q - 1) : 0; n < en(q); ++n) epi(0, n);
      }
    }
    SLOTS(2) { mmg(2, 0, 1, 0, q, 0); SB(); }
    asm volatile("s_nop 1");
    SLOTS(16) { epi(0, 48 + q); if (q == 3 || q == 9) { mmg(2, 0, 1, 0, q == 3 ? 2 : 3, 0); SB(); } }
    BAR_LGKM();
    SB();
    SLOTS(12) {
      mmg(2, 0, 1, 1 + (q >> 2), q & 3, ~(q >> 2) & 1);
      if (q < 6) rds(1, q); else rdg(0, 14, 1, q - 6);
      if (q >= 8) prep(q - 8);
      SB();
    }
    SLOTS(16) {
      mmg(3, 1, 1, q >> 2, q & 3, (q >> 2) & 1);
      if (q < 6) rdg(0, 14, 1, q + 6);
      if (q < 4) prep(4 + q); else { prep(2 * q); prep(2 * q + 1); }
      SB();
    }
    asm volatile("s_waitcnt lgkmcnt(0)" ::: "memory");
    if (lane == 0) asm volatile("ds_add_u32 %0, %1" :: "v"(lds_addr(smem + LDS_SEL + 12)), "v"(1) : "memory");
    SB();
    SLOTS(16) {
      mmg(0, 0, 1, q >> 2, q & 3, (q >> 2) & 1);
      if (q < 4) fn[0][q] = *(const bf16x8*)(smem + (wr * 128 + fl) * 128 + (q >> 1) * 2048 + ((q & 1) ? ko1 : ko0));
      else if (q < 8) fm[q & 1][(q - 4) >> 1] = *(const bf16x8*)(smem + G2_MRING + m_off(0) + (wc * 64 + fl) * 128 + TO((q - 4) >> 1) + ((q & 1) ? ko1 : ko0));
      SB();
      if (q >= 8) { epi(1, 3 * q - 24); epi(1, 3 * q - 23); epi(1, 3 * q - 22); }
    }
    SLOTS(40) epi(1, 24 + q);
  }

  const char* pn = smem + (wr * 128 + fl) * 128;
  const char* pm = smem + G2_MRING + (wc * 64 + fl) * 128;
  f32x4 acc[8][4];
  auto mm = [&](bool imaj, int set, int q2, int s, int mode) __attribute__((always_inline)) {
    int i, jj, kk;
    if (imaj) { i = s >> 2; const int r = s & 3; jj = (r >> 1) ^ (i & 1); kk = (r == 1 || r == 2); }
    else { jj = s >> 3; const int r = s & 7; i = jj ? 3 - (r >> 1) : (r >> 1); kk = ((r + 1) >> 1) & 1; }
    if (mode == 1 && !(s & 1)) asm volatile("v_mfma_f32_16x16x32_bf16 %0, %2, %1, 0" : "=&v"(acc[q2 * 2 + jj][i]) : "v"(fm[kk][i]), "v"(fn[set][jj * 2 + kk]));
    else if (mode) asm volatile("v_mfma_f32_16x16x32_bf16 %0, %2, %1, %0" : "+v"(acc[q2 * 2 + jj][i]) : "v"(fm[kk][i]), "v"(fn[set][jj * 2 + kk]));
    else acc[q2 * 2 + jj][i] = __builtin_amdgcn_mfma_f32_16x16x32_bf16(fn[set][jj * 2 + kk], fm[kk][i], acc[q2 * 2 + jj][i], 0, 0, 0);
  };
  unsigned bn[2], bm[2], an[2], am[2];
#pragma unroll
  for (int kk = 0; kk < 2; ++kk) {
    bn[kk] = lds_addr(pn) + (kk ? ko1 : ko0); bm[kk] = lds_addr(pm) + (kk ? ko1 : ko0);
    asm volatile("" : "+v"(bn[kk]), "+v"(bm[kk]));
    an[kk] = bn[kk]; am[kk] = bm[kk] + m_off(0);
  }
#define RDN(jt, kk, o) (*LDSP(const bf16x8, an[kk] + (o) + (jt) * 2048))
#define RDM(i, kk, o) (*LDSP(const bf16x8, am[kk] + (o) + TO(i)))
  PAGE(10);
  tag = __builtin_amdgcn_readfirstlane(((const unsigned*)(smem + LDS_SEL))[5]);
  {
    int son = 0;
    auto tile = [&](int kt, int zero) __attribute__((always_inline)) {
      SLOTS(16) {
        mm(true, 0, 0, q, zero);
        if (!(q & 1)) {
          if (q < 8) fm[(q >> 1) & 1][2 + (q >> 2)] = RDM(2 + (q >> 2), (q >> 1) & 1, 0);
          else { const int r = (q - 8) >> 1; fn[1][r] = RDN(2 + (r >> 1), r & 1, 0); }
        }
        SB();
      }
      SLOTS(16) {
        mm(false, 1, 1, q, zero);
        if (!(q & 1) && q < 8) fn[0][q >> 1] = RDN(4 + (q >> 2), (q >> 1) & 1, 0);
        SB();
      }
      const int som = son >> 1;
      son = son == 65536 ? 0 : son + 32768;
      SLOTS(16) {
        mm(false, 0, 2, q, zero);
        if (!(q & 1) && q < 8) fn[1][q >> 1] = RDN(6 + (q >> 2), (q >> 1) & 1, 0);
        if (q == 9 || q == 11) { an[q == 11] = bn[q == 11] + son; asm volatile("" : "+v"(an[q == 11])); }
        if (q == 13 || q == 15) { am[q == 15] = bm[q == 15] + som; asm volatile("" : "+v"(am[q == 15])); }
        SB();
      }
      BAR_LGKM();
      SB();
      SLOTS(16) {
        mm(true, 1, 3, q, zero);
        if (!(q & 1)) {
          if (q < 8) fn[0][q >> 1] = RDN(q >> 2, (q >> 1) & 1, 0);
          else fm[(q >> 1) & 1][(q - 8) >> 2] = RDM((q - 8) >> 2, (q >> 1) & 1, 0);
        }
        SB();
      }
    };
    SB();
    tile(0, 1);
#pragma unroll 1
    for (int kt = 1; kt < G2_NKT - 3; ++kt) tile(kt, 0);
  }
  unsigned fx;
  int fo = lane == 0 ? kq * 512 : (int)0x80000000;
  {
    constexpr int NG = 12;
    static constexpr int g_t[NG] = {0, 0, 1, 1, 2, 2, 2, 1, 0, 0, 1, 2};
    static constexpr int g_q[NG] = {0, 1, 0, 1, 0, 1, 2, 2, 2, 3, 3, 3};
    constexpr int G14 = 1, G15 = 3, GP0 = 5, GP1 = 6;
    constexpr auto chp = [](int g) { return g == 0 || g_t[g] != g_t[g - 1]; };
    constexpr auto chn = [](int g) { return g + 1 < NG && g_t[g + 1] != g_t[g]; };
    static_assert(n_off(13) == 32768 && n_off(14) == 65536 && n_off(15) == 0, "");
    unsigned an2[2];
#pragma unroll
    for (int kk = 0; kk < 2; ++kk) {
      an[kk] = bn[kk]; an2[kk] = bn[kk] + 65536; am[kk] = bm[kk];
      asm volatile("" : "+v"(an[kk]), "+v"(an2[kk]), "+v"(am[kk]));
    }
    int vo;
    u32x4 pk;
    u32x2 kv;
    auto publish = [&](int d, int h) __attribute__((always_inline)) {
      if (h == 0) { vo = ml; PINV(vo); vo += (((d + kq + 1) & 3) * 4 + kq) * 4 * MSG; PINV(vo); }
      const f32x4 v = acc[2 * d + (h >> 2)][h & 3];
      pk[(h & 1) * 2] = pk2(v[0], v[1]); pk[(h & 1) * 2 + 1] = pk2(v[2], v[3]);
      if (h & 1) __builtin_amdgcn_raw_buffer_store_b128(pk, rs, vo + (h >> 1) * 1024, 0, 16);
    };
#define MSG_FLAG(d) __builtin_amdgcn_raw_buffer_store_b32(tag, rc, fo + (((d) + kq + 1) & 3) * 2048, 0, 16)
    auto rdn = [&](int g, int r) __attribute__((always_inline)) {
      const int t = g_t[g], jt = g_q[g] * 2 + (r >> 1);
      fn[g & 1][r] = *LDSP(const bf16x8, (t == 1 ? an2[r & 1] : an[r & 1]) + (t == 0 ? 32768 : 0) + jt * 2048);
    };
    auto rdm = [&](int t, int i, int kk) __attribute__((always_inline)) { fm[kk][i] = *LDSP(const bf16x8, am[kk] + m_off(13 + t) + TO(i)); };
    auto group = [&](auto GI) __attribute__((always_inline)) {
      constexpr int gi = decltype(GI)::value;
      if (gi == G14) { BAR();
        SB(); }
      if (gi == G15) {
        PAGE(11);
        BAR();
        SB();
        kv = *(const u32x2*)(smem + LDS_SEL);
        SB();
      }
#pragma unroll
      for (int q = 0; q < 16; ++q) {
        const int n = gi * 16 + q;
        if (n == 112) {
        }
        if (n == 152) {
        }
        if (n == 168) { WAIT_VM(8); MSG_FLAG(0); SB(); }
        mm(chp(gi) || chn(gi), gi & 1, g_q[gi], q, 2);
        constexpr bool ni = gi + 1 < NG && (chp(gi + 1 < NG ? gi + 1 : gi) || chn(gi + 1 < NG ? gi + 1 : gi));
        auto nr = [](int r) { return ni && r >= 2 ? 5 - r : r; };
        if constexpr (chp(gi) && chn(gi)) {
          if (q < 4) rdm(g_t[gi], 2 + (q >> 1), q & 1); else if (q < 8) rdn(gi + 1, nr(q - 4)); else if (q < 12) rdm(g_t[gi + 1], (q - 8) >> 1, q & 1);
        } else if constexpr (chp(gi)) {
          if (q < 4) rdm(g_t[gi], 2 + (q >> 1), q & 1);
          else if (gi + 1 < NG && q < 12 && !(q & 1)) rdn(gi + 1, nr((q - 4) >> 1));
        } else if constexpr (chn(gi)) {
          if (!(q & 1)) { if (q < 8) rdn(gi + 1, nr(q >> 1)); else rdm(g_t[gi + 1], (q - 8) >> 2, (q >> 1) & 1); }
        } else {
          if (!(q & 1) && q < 8) rdn(gi + 1, nr(q >> 1));
        }
        if (gi == GP0 && q < 8) publish(0, q);
        if (gi == GP1 && q < 8) publish(1, q);
        if (gi == 9 && q < 8) publish(2, q);
        if (n == 33 || n == 37 || n == 41) xq_load((n - 33) >> 2);
        if (n == 72) { key[0] = __builtin_amdgcn_readfirstlane(kv.x); key[1] = __builtin_amdgcn_readfirstlane(kv.y); }
        SB();
      }
    };
#define GR4(i) group(IC<i>{}); group(IC<i + 1>{}); group(IC<i + 2>{}); group(IC<i + 3>{});
    GR4(0) GR4(4) GR4(8)
    asm volatile("s_nop 7");
    SB();
  }
#pragma unroll
  for (int i = 0; i < 4; ++i) *(f32x4*)(smem + LDS_ACC + wr * 16384 + wc * 4096 + i * 1024 + lane * 16) = acc[7][i];
#pragma unroll
  for (int i = 0; i < 4; ++i) ya[i] = acc[6][i];
  asm volatile("s_waitcnt lgkmcnt(0)" ::: "memory");
  if (lane == 0) sig[0] = 2;
  SB();
  WAIT_VM(5); MSG_FLAG(1); SB();
  WAIT_VM(2); MSG_FLAG(2); fx = bld4_sc1(rc, po); SB();
  wait_xq<4>(xq, sq);
  far_calc();
  cv_calc();
  asm volatile("s_waitcnt vmcnt(0)" : "+v"(fx) :: "memory");
  seen = (unsigned)__ballot(fx == tag);
  }

  PAGE(6);
  if (ha) {
    int it = 0;
    while (sig[0] != 2 && it < SPIN_MAX * 8) { __builtin_amdgcn_s_sleep(1); ++it; }
    asm volatile("" ::: "memory");
#pragma unroll
    for (int i = 0; i < 4; ++i) ya[i] = *(const f32x4*)(smem + LDS_ACC + (wid & 2) * 8192 + (wid & 1) * 4096 + i * 1024 + lane * 16);
  }
  u32x4 pp[3][2];
  unsigned e_lo = 0x00003f80u, e_hi = 0x3f800000u;
  asm volatile("" : "+v"(e_lo), "+v"(e_hi));
  __amdgpu_buffer_rsrc_t rs_ = rs, rc_ = rc;
  int mo[3] = {MSG_IDX(0) * 4 * MSG, MSG_IDX(1) * 4 * MSG, MSG_IDX(2) * 4 * MSG}, mv = ml + ha * 2048, mv2 = mv + 1024;
  asm volatile("" : "+s"(rs_), "+s"(rc_), "+s"(mo[0]), "+s"(mo[1]), "+s"(mo[2]));
  asm volatile("" : "+v"(mv), "+v"(mv2));
#define MSG_POLL(m) do { \
    if (!((seen >> (m)) & 1)) { \
      unsigned pv_, it_ = SPIN_MAX; \
      asm volatile("1:\n\tbuffer_load_dword %0, %3, %4, 0 offen sc1\n\ts_waitcnt vmcnt(0)\n\tv_cmp_eq_u32 vcc, %5, %0\n\ts_mov_b32 %1, vcc_lo\n\t" \
                   "s_bitcmp1_b32 %1, %6\n\ts_cbranch_scc1 2f\n\ts_sub_u32 %2, %2, 1\n\ts_cbranch_scc1 2f\n\ts_sleep 1\n\ts_branch 1b\n2:" \
                   : "=&v"(pv_), "+s"(seen), "+s"(it_) : "v"(po), "s"(rc_), "s"(tag), "i"(m) : "vcc", "scc", "memory"); \
    } \
    pp[m][0] = __builtin_amdgcn_raw_buffer_load_b128(rs_, mv, mo[m], 16); \
    pp[m][1] = __builtin_amdgcn_raw_buffer_load_b128(rs_, mv2, mo[m], 16); } while (0)
#define MSG_ADD(m) do { \
    _Pragma("unroll") for (int u = 0; u < 8; ++u) { \
      ya[u >> 1][(u & 1) * 2] = add_bf(pp[m][u >> 2][u & 3], e_lo, ya[u >> 1][(u & 1) * 2]); ya[u >> 1][(u & 1) * 2 + 1] = add_bf(pp[m][u >> 2][u & 3], e_hi, ya[u >> 1][(u & 1) * 2 + 1]); } } while (0)
  MSG_POLL(2); MSG_POLL(1); MSG_POLL(0);
  SB();
  if (__builtin_expect(far0 == 0 || far1 == 0, 0)) {
    asm volatile("s_branch 2f\nk_pg12:\n\ts_setpc_b64 s[42:43]\n2:");
#pragma unroll 1
    for (int c = 0; c < 2; ++c) {
      if (c ? far1 : far0) continue;
      const int ic = (0xffff - (c ? key[1] : key[0])) & (S_ - 1);
      lds_vi cnt = (lds_vi)(smem + LDS_SEL + 24 + c * 4);
      int it = 0;
      while (cnt[0] != 4 && it < SPIN_MAX * 8) { __builtin_amdgcn_s_sleep(1); ++it; }
      asm volatile("" ::: "memory");
      const float* q = (const float*)(smem + LDS_FIN + c * 2048) + (wid & 1) * 64 + (fr >> 3) * 32 + col4(fr);
      const f32x4 v = (*(const f32x4*)q + *(const f32x4*)(q + 128)) + (*(const f32x4*)(q + 256) + *(const f32x4*)(q + 384));
#pragma unroll
      for (int r = 0; r < 4; ++r)
#pragma unroll
        for (int k = 0; k < 4; ++k) {
          cv[r * 4 + k] += (xo[r] >> 11) == ic ? v[k] : 0.f;
        }
    }
  }
#pragma unroll
  for (int e = 0; e < 16; ++e) ya[e & 3][e >> 2] += cv[e];
  asm volatile("" : "+v"(ya[0]), "+v"(ya[1]), "+v"(ya[2]), "+v"(ya[3]));
  SB();
  MSG_ADD(2); MSG_ADD(1);
  asm volatile("" : "+v"(ya[0]), "+v"(ya[1]), "+v"(ya[2]), "+v"(ya[3]));
  SB();
  MSG_ADD(0);
  {
#pragma unroll
    for (int r = 0; r < 4; ++r) *(u32x2*)((char*)out + (unsigned)xo[r]) = (u32x2){pk2(ya[0][r], ya[1][r]), pk2(ya[2][r], ya[3][r])};
  }
}

extern "C" void kernel_launch(void* const* d_in, const int* in_sizes, int n_in, void* d_out, int out_size,
                              void* d_ws, size_t ws_size, hipStream_t stream) {
  const u16* x = (const u16*)d_in[0];
  const u16* ws_re = (const u16*)d_in[1];
  const u16* ws_im = (const u16*)d_in[2];
  const u16* wd_re = (const u16*)d_in[3];
  const u16* wd_im = (const u16*)d_in[4];
  const u16* w_r = (const u16*)d_in[6];
  const u16* w_gate = (const u16*)d_in[7];
  const u16* w_up = (const u16*)d_in[8];
  const u16* w_down = (const u16*)d_in[9];
  k_main<<<256, 512, 0, stream>>>(x, wd_re, wd_im, w_r, (char*)d_ws, ws_re, ws_im, w_gate, w_up, w_down, (u16*)d_out);
}
```

```cpp
#include <hip/hip_runtime.h>
#include <hip/hip_bf16.h>
#include <stdint.h>
#include <type_traits>

extern "C" __device__ unsigned long long llvm_dispatch_id(void) __asm("llvm.amdgcn.dispatch.id");
extern "C" __device__ float llvm_fmul_legacy(float, float) __asm("llvm.amdgcn.fmul.legacy");
typedef unsigned short u16;
typedef unsigned long long u64;
typedef __attribute__((ext_vector_type(8))) short bf16x8;
typedef __attribute__((ext_vector_type(4))) unsigned u32x4;
typedef __attribute__((ext_vector_type(2))) unsigned u32x2;
typedef __attribute__((ext_vector_type(4))) float f32x4;
typedef __attribute__((ext_vector_type(2))) float f32x2;
typedef __attribute__((ext_vector_type(2))) __bf16 bf16x2;
typedef __attribute__((address_space(3))) void* lds_vp;
typedef volatile __attribute__((address_space(3))) int* lds_vi;

constexpr int S_ = 2048, D_ = 1024, F_ = 3584;
template <int N> using IC = std::integral_constant<int, N>;

static __device__ __forceinline__ float bf2f(u16 u) { return __uint_as_float((unsigned)u << 16); }
static __device__ __forceinline__ u16 f2bf(float f) {
  __hip_bfloat16 b = __float2bfloat16(f);
  return *reinterpret_cast<u16*>(&b);
}
static __device__ __forceinline__ float rbf(float f) { return bf2f(f2bf(f)); }
static __device__ __forceinline__ float lo_bf(unsigned u) { return __uint_as_float(u << 16); }
static __device__ __forceinline__ float hi_bf(unsigned u) { return __uint_as_float(u & 0xffff0000u); }
static __device__ __forceinline__ unsigned pk2(float a, float b) {
  const bf16x2 v = __builtin_convertvector((f32x2){a, b}, bf16x2);
  return __builtin_bit_cast(unsigned, v);
}
static __device__ __forceinline__ float add_bf(unsigned u, unsigned e, float c) {
  return __builtin_amdgcn_fdot2_f32_bf16(__builtin_bit_cast(bf16x2, u), __builtin_bit_cast(bf16x2, e), c, false);
}
static __device__ __forceinline__ float ln_elem(float x, float mb, float sd, float ri) {
  const float d = rbf(x - mb);
  const float q0 = d * ri;
  return rbf(__builtin_fmaf(__builtin_fmaf(-q0, sd, d), ri, q0));
}
template <int CTRL> static __device__ __forceinline__ float dpp_f(float v) {
  return __int_as_float(__builtin_amdgcn_update_dpp(0, __float_as_int(v), CTRL, 0xf, 0xf, true));
}
template <int CTRL> static __device__ __forceinline__ unsigned dpp_u(unsigned v) {
  return (unsigned)__builtin_amdgcn_update_dpp(0, (int)v, CTRL, 0xf, 0xf, true);
}
static __device__ __forceinline__ float row_sum(float v) {
  v += dpp_f<0xB1>(v);
  v += dpp_f<0x4E>(v);
  v += dpp_f<0x141>(v);
  v += dpp_f<0x140>(v);
  return v;
}
static __device__ __forceinline__ float fold32(float a, float b) {
  const auto r = __builtin_amdgcn_permlane32_swap(__float_as_uint(a), __float_as_uint(b), false, false);
  return __uint_as_float(r[0]) + __uint_as_float(r[1]);
}
static __device__ __forceinline__ float fold16(float a, float b) {
  const auto r = __builtin_amdgcn_permlane16_swap(__float_as_uint(a), __float_as_uint(b), false, false);
  return __uint_as_float(r[0]) + __uint_as_float(r[1]);
}
static __device__ __forceinline__ float rdl(float v, int l) { return __int_as_float(__builtin_amdgcn_readlane(__float_as_int(v), l)); }

#define ST_JB 9
#define LNS 2
#define WSN 10
#define FSM 0xa2
#define GV_JB 7
#define DG 11
#define AB_STRIDE 32
#define SPIN_MAX (1 << 17)
#define VA0 5
#define VAN 4
#define SP0 3
#define SP1 7
#define SP1E 11
#define SPS 0x000cccccdddd00
#define DVS 0x00000400300000
#define GVS 0x00000022000000
constexpr int dig(unsigned long long t, int q) { return q < 0 ? 0 : (int)((t >> (4 * q)) & 15); }
constexpr int acc_dig(unsigned long long t, int q) { int a = 0; for (int i = 0; i < q; ++i) a += dig(t, i); return a; }

static __device__ __forceinline__ unsigned lds_addr(const void* p) { return (unsigned)(size_t)(lds_vp)p; }

constexpr int MSG = 4096;
constexpr size_t WS_ABT   = 0;
constexpr size_t WS_SLAB  = WS_ABT + (size_t)D_ * 4096 * 2;
constexpr size_t WS_GU    = WS_SLAB + (size_t)64 * 64 * MSG;
constexpr size_t WS_DN    = WS_GU + (size_t)8 * F_ * 16;
constexpr size_t WS_FLAGS = WS_DN + (size_t)2 * 8 * 32 * 64 * 8;
constexpr int FL_X = 0;
constexpr int FL_AB = 64 * 64 * 32;
constexpr int FL_ST = FL_AB + 2048 * AB_STRIDE;
constexpr int FL_CE = FL_ST + 4096;
constexpr int FL_KEY = FL_CE + 4096;
constexpr int FL_EPOCH = FL_KEY + 4096;
constexpr int WS_MB = (int)((WS_FLAGS + (FL_EPOCH + 1) * 4 + 1048575) >> 20);

#define WAIT_VM(n) asm volatile("s_waitcnt vmcnt(" #n ")" ::: "memory")
template <int N> static __device__ __forceinline__ void wait_vm() { asm volatile("s_waitcnt vmcnt(%0)" :: "i"(N) : "memory"); }
#define BAR_LGKM() asm volatile("s_waitcnt lgkmcnt(0)\n\ts_barrier" ::: "memory")
#define BAR() asm volatile("s_barrier" ::: "memory")
#define SB() __builtin_amdgcn_sched_barrier(0)
#define PAGE(n) asm volatile("s_branch 2f\n\t.p2align 12\nk_pg" #n ":\n\ts_setpc_b64 s[42:43]\n2:")
#define CALL_PG(n) asm volatile("s_getpc_b64 s[40:41]\n3:\n\ts_mov_b32 s42, k_pg" #n "-3b\n\ts_ashr_i32 s43, s42, 31\n\ts_add_u32 s40, s40, s42\n\ts_addc_u32 s41, s41, s43\n\t" \
                                "s_swappc_b64 s[42:43], s[40:41]" ::: "s40", "s41", "s42", "s43", "scc", "memory")

static __device__ __forceinline__ __amdgpu_buffer_rsrc_t mk_rsrc(const void* p) {
  return __builtin_amdgcn_make_buffer_rsrc((void*)p, 0, 0x7fffffff, 0x00020000);
}
#define DMA_(rsrc, voff, mbase, MI, sbase, SI, pol) do { unsigned _t; \
    asm volatile("s_add_i32 m0, %2, %3\n\ts_add_i32 %0, %5, %6\n\tbuffer_load_dwordx4 %1, %4, %0 offen" pol " lds" \
                 : "=&s"(_t) : "v"(voff), "s"(mbase), "i"(MI), "s"(rsrc), "s"(sbase), "i"(SI) : "memory", "scc"); } while (0)
#define DMA_I(rsrc, voff, mbase, MI, SI) \
    asm volatile("s_add_i32 m0, %1, %2\n\ts_nop 0\n\tbuffer_load_dwordx4 %0, %3, 0 offen offset:%4 lds" \
                 :: "v"(voff), "s"(mbase), "i"((MI) - (SI)), "s"(rsrc), "i"(SI) : "memory", "scc")
#define DMA_S(rsrc, voff, mbase, MI, soff) \
    asm volatile("s_add_i32 m0, %1, %2\n\ts_nop 0\n\tbuffer_load_dwordx4 %0, %3, %4 offen lds" \
                 :: "v"(voff), "s"(mbase), "i"(MI), "s"(rsrc), "s"(soff) : "memory", "scc")
#define TOUCH(rsrc, voff, mbase, MI, soff) \
    asm volatile("s_add_i32 m0, %1, %2\n\ts_nop 0\n\tbuffer_load_dword %0, %3, %4 offen lds" \
                 :: "v"(voff), "s"(mbase), "i"(MI), "s"(rsrc), "s"(soff) : "memory", "scc")
template <int OFF> static __device__ __forceinline__ u32x4 ld16_s(const void* p, int voff) {
  u32x4 v;
  asm volatile("global_load_dwordx4 %0, %1, %2 offset:%3" : "=v"(v) : "v"(voff), "s"(p), "i"(OFF) : "memory");
  return v;
}
template <int OFF> static __device__ __forceinline__ void sld(unsigned& sink, const void* p, int soff) {
  asm volatile("s_load_dword %0, %1, %2 offset:%3" : "+s"(sink) : "s"(p), "s"(soff), "i"(OFF) : "memory");
}
static __device__ __forceinline__ unsigned gld4_s(const void* p, unsigned voff) {
  unsigned v;
  asm volatile("global_load_dword %0, %1, %2" : "=v"(v) : "v"(voff), "s"(p) : "memory");
  return v;
}
static __device__ __forceinline__ u32x2 bld8_sc1(__amdgpu_buffer_rsrc_t r, unsigned voff) {
  u32x2 v;
  asm volatile("buffer_load_dwordx2 %0, %1, %2, 0 offen sc1" : "=v"(v) : "v"(voff), "s"(r) : "memory");
  return v;
}
template <int N> static __device__ __forceinline__ void wait_xs(unsigned& a, u32x2& b) { asm volatile("s_waitcnt vmcnt(%2)" : "+v"(a), "+v"(b) : "i"(N) : "memory"); }
template <int N> static __device__ __forceinline__ void wait_xq(u32x4 (&a)[2], u32x2& b) { asm volatile("s_waitcnt vmcnt(%3)" : "+v"(a[0]), "+v"(a[1]), "+v"(b) : "i"(N) : "memory"); }
static __device__ __forceinline__ u32x2 ld8_s_sc1(const void* p, int voff) {
  u32x2 v;
  asm volatile("global_load_dwordx2 %0, %1, %2 sc1" : "=v"(v) : "v"(voff), "s"(p) : "memory");
  return v;
}
static __device__ __forceinline__ void lgkm0(unsigned& sink) { asm volatile("s_waitcnt lgkmcnt(0)" : "+s"(sink) :: "memory"); }
template <typename T> static __device__ __forceinline__ void pin4(T& a, T& b, T& c, T& d) { asm volatile("" : "+s"(a), "+s"(b), "+s"(c), "+s"(d)); }
template <typename T, typename U> static __device__ __forceinline__ void pins(T& a, U& b) { asm volatile("" : "+s"(a), "+s"(b)); }
static __device__ __forceinline__ void launder(int& v, int& a, int& b) { asm volatile("" : "+v"(v)); asm volatile("" : "+s"(a), "+s"(b)); }
static __device__ __forceinline__ unsigned bld4_sc1(__amdgpu_buffer_rsrc_t r, int voff) {
  unsigned v;
  asm volatile("buffer_load_dword %0, %1, %2, 0 offen sc1" : "=v"(v) : "v"(voff), "s"(r) : "memory");
  return v;
}
static __device__ __forceinline__ u32x4 bld16_nt(__amdgpu_buffer_rsrc_t r, int voff, int soff) {
  u32x4 v;
  asm volatile("buffer_load_dwordx4 %0, %1, %2, %3 offen nt" : "=v"(v) : "v"(voff), "s"(r), "s"(soff) : "memory");
  return v;
}
static __device__ __forceinline__ void bld16_nt_io(u32x4& v, __amdgpu_buffer_rsrc_t r, int voff, int soff) {
  asm volatile("buffer_load_dwordx4 %0, %1, %2, %3 offen nt" : "+v"(v) : "v"(voff), "s"(r), "s"(soff) : "memory");
}
static __device__ __forceinline__ u32x4 bld16_sc1(__amdgpu_buffer_rsrc_t r, int voff, int soff) {
  u32x4 v;
  asm volatile("buffer_load_dwordx4 %0, %1, %2, %3 offen sc1" : "=v"(v) : "v"(voff), "s"(r), "s"(soff) : "memory");
  return v;
}
static __device__ __forceinline__ u32x2 granule_wait(const u64* p, unsigned tag) {
  u64 v = 0;
#pragma unroll 1
  for (int it = 0; it < SPIN_MAX; ++it) {
    v = __hip_atomic_load(p, __ATOMIC_RELAXED, __HIP_MEMORY_SCOPE_AGENT);
    if (__all((unsigned)(v >> 32) == tag)) break;
    __builtin_amdgcn_s_sleep(2);
  }
  return (u32x2){(unsigned)v, (unsigned)(v >> 32)};
}
static __device__ __forceinline__ float key_score(unsigned key) {
  const unsigned u = key >> 16;
  return bf2f((u16)((u & 0x8000u) ? (u & 0x7fffu) : (~u & 0xffffu)));
}
static __device__ __forceinline__ unsigned med3u(unsigned a, unsigned b, unsigned c) { return max(min(a, b), min(max(a, b), c)); }
static __device__ __forceinline__ void merge2(unsigned& a1, unsigned& a2, unsigned b1, unsigned b2) {
  const unsigned lo = min(a1, b1);
  a1 = max(a1, b1);
  a2 = max(lo, max(a2, b2));
}
static __device__ __forceinline__ float sqdev16(const u32x4 (&v)[2], float mb) {
  f32x2 q2 = {0.f, 0.f};
#pragma unroll
  for (int i = 0; i < 8; ++i) {
    const f32x2 e = (f32x2){lo_bf(v[i >> 2][i & 3]), hi_bf(v[i >> 2][i & 3])} - mb;
    q2 += e * e;
  }
  return q2.x + q2.y;
}

constexpr int lrw(int r) { return (r & ~6) | ((r & 2) << 1) | ((r & 4) >> 1); }
constexpr int swz(int r) { return ((r >> 1) & 4) | (r & 2); }
static __device__ __forceinline__ int sym_dn(int p) { return (p ^ (p >> 1)) & 1; }
static __device__ __forceinline__ int sym_hi(int n, int w, int st, int b, int z, int p) {
  return ((p >> 1) * (n / 2 * st) + b * (w * st)) ^ (-sym_dn(p) & (((n - 1) ^ (z * (n / 2))) * st));
}
static __device__ __forceinline__ int sym_lo(int w, int st, int p, int c) { return ((c - sym_dn(p)) * st) & ((w - 1) * st); }
static __device__ __forceinline__ int sym_row(int n, int w, int st, int b, int z, int p, int c) { return sym_lo(w, st, p, c) ^ sym_hi(n, w, st, b, z, p); }
constexpr int G1_NST = 4, G1_NKT = 16, G1_STAGE = 32768;
constexpr int G2_NKT = 16, G2_MRING = 98304;
constexpr int n_off(int k) { return (k % 3) * 32768; }
constexpr int m_off(int k) { return ((k + 2) % 3) * 16384; }
constexpr int k_ord(int k) { return k < 2 ? k : (k < 9 ? 2 * (k - 1) : 2 * (k - 8) + 1); }
constexpr int LDS_DUMMY = G2_MRING + 49152;
constexpr int LDS_GU = LDS_DUMMY + 4096;
constexpr int LDS_FIN = LDS_GU + 4096;
constexpr int LDS_MB = LDS_FIN + 4096;
constexpr int LDS_SEL = LDS_MB + 1536;
constexpr int LDS_SIG = LDS_SEL + 64;
constexpr int LDS_KEY = LDS_SIG + 32;
constexpr int LDS_BYTES = LDS_KEY + 128;
constexpr int LDS_ACC = n_off(G2_NKT - 3);

constexpr int sp_tile(int q) { return q < VA0 ? q : q + VAN < 16 ? q + VAN : -1; }
constexpr int dn_cnt(int q) { return dig(DVS, q); }
constexpr int dn_first(int q) { return acc_dig(DVS, q); }

static __device__ __forceinline__ void code_touch(unsigned& sink, unsigned off) {
  asm volatile("s_getpc_b64 s[40:41]\n\tk_main_rt:\n\ts_min_u32 %1, %1, k_main_end-k_main_rt-196\n\t"
               "s_load_dword %0, s[40:41], %1 offset:0\n\ts_load_dword %0, s[40:41], %1 offset:64\n\ts_load_dword %0, s[40:41], %1 offset:128\n\ts_load_dword %0, s[40:41], %1 offset:192"
               : "+s"(sink), "+s"(off) :: "s40", "s41", "scc", "memory");
}
static __device__ __forceinline__ void touch1(__amdgpu_buffer_rsrc_t r, int voff, unsigned mb, int soff) { TOUCH(r, voff, mb, LDS_DUMMY, soff); }
typedef const __attribute__((address_space(1))) u16* gptr;
typedef __attribute__((ext_vector_type(8))) u64 u64x8;
typedef __attribute__((ext_vector_type(2))) u64 u64x2;
extern "C" __global__ __launch_bounds__(512, 2) void k_main(const u16* x, const u16* wd_re, const u16* wd_im, const u16* w_r,
                                                 char* wsp, const u16* ws_re, const u16* ws_im,
                                                 const u16* w_gate, const u16* w_up, const u16* w_down, u16* out) {
  __shared__ __attribute__((aligned(16))) char smem[LDS_BYTES];
  u64x8 ka;
  u64x2 kb_;
  u64 kc;
  asm volatile("s_load_dwordx16 %0, %3, 0\n\ts_load_dwordx4 %1, %3, 64\n\ts_load_dwordx2 %2, %3, 80"
               : "=&s"(ka), "=&s"(kb_), "=&s"(kc) : "s"(__builtin_amdgcn_kernarg_segment_ptr()) : "memory");
  const int tid = threadIdx.x, lane = tid & 63;
  const int wid = __builtin_amdgcn_readfirstlane(tid >> 6);
  const int g = blockIdx.x, xcd = ((~g & 1) << 2) | ((g >> 1) & 3), j = g >> 3;
  if (wid < 4) {
    unsigned cw, co = (j * 4 + wid) * 448;
    asm volatile(".subsection 1\nk_main_end:\n.subsection 0\n\ts_min_u32 %1, %1, k_main_end-k_main-480\n\ts_getpc_b64 s[40:41]\n\ts_add_u32 s40, s40, %1\n\ts_addc_u32 s41, s41, 0\n\t"
                 "s_load_dword %0, s[40:41], 0\n\ts_load_dword %0, s[40:41], 64\n\ts_load_dword %0, s[40:41], 128\n\t"
                 "s_load_dword %0, s[40:41], 192\n\ts_load_dword %0, s[40:41], 256\n\ts_load_dword %0, s[40:41], 320\n\ts_load_dword %0, s[40:41], 384\n\ts_waitcnt lgkmcnt(0)"
                 : "=&s"(cw), "+s"(co) :: "s40", "s41", "scc", "memory");
  }
  const int pw = wid - 4;
  const int row0 = (wid & 3) * 8 + lrw(lane >> 3);
  const int cm = ((lane & 7) ^ swz(row0)) * 16;
  int vm = ((row0 & 15) * 2 + (row0 >> 4)) * (D_ * 2) + cm;
  asm volatile("" : "+v"(vm));
  const int kq = xcd >> 1;
  const int tt = (xcd & 1) * 4 + (j >> 3);
  const int et = j & 7;
  const int z = kq >> 1, st = (kq & 1) * 8 + tt;
  const int lrow = (kq & 1) * 1024 + (xcd & 1) * 512 + z * 256 + j * 8 + pw * 2;
  const int drow = ((kq & 1) * 2 + (xcd & 1)) * 256 + j * 8 + pw * 2;
  unsigned o_m = st * 128 * D_ * 2, o_l = lrow * D_ * 2, o_d = drow * D_ * 2, mb0 = lds_addr(smem) + pw * 1024;
  int l16 = lane * 16;
  int vn[4];
#pragma unroll
  for (int q = 0; q < 4; ++q) vn[q] = sym_lo(32, D_ * 2, (row0 >> 2) & 3, q * 8 + (row0 >> 4) * 4 + (row0 & 3)) ^ (sym_hi(D_, 32, D_ * 2, et, z, (row0 >> 2) & 3) | cm);
  asm volatile("" : "+s"(o_m), "+s"(o_l), "+s"(o_d), "+s"(mb0));
  asm volatile("" : "+v"(l16), "+v"(vn[0]), "+v"(vn[1]), "+v"(vn[2]), "+v"(vn[3]));
  asm volatile("s_waitcnt lgkmcnt(0)" : "+s"(ka), "+s"(kb_), "+s"(kc) :: "memory");
  x = (const u16*)(gptr)ka[0]; wd_re = (const u16*)(gptr)ka[1]; wd_im = (const u16*)(gptr)ka[2]; w_r = (const u16*)(gptr)ka[3];
  wsp = (char*)(gptr)ka[4]; ws_re = (const u16*)(gptr)ka[5]; ws_im = (const u16*)(gptr)ka[6]; w_gate = (const u16*)(gptr)ka[7];
  w_up = (const u16*)(gptr)kb_[0]; w_down = (const u16*)(gptr)kb_[1]; out = (u16*)(gptr)kc;
  int* flags = (int*)(wsp + WS_FLAGS);
  u64* stat = (u64*)(flags + FL_ST);
  const u16* wdz = z ? wd_im : wd_re;
  const __amdgpu_buffer_rsrc_t rm = mk_rsrc(x);
  const __amdgpu_buffer_rsrc_t rn = mk_rsrc(wdz);
#define G1_X(kt, so, q) DMA_(rm, vm, mb0, (so) + (q) * 4096, o_m, (kt) * 128 + (((q) & 1) * 64 + ((q) >> 1) * 32) * D_ * 2, "")
#define G1_W(kt, so, q) DMA_I(rn, vn[q], mb0, (so) + 16384 + (q) * 4096, (kt) * 128)
#define G1_STAGE_TILE(kt, so, W) do { SB(); G1_X(kt, so, 0); W(0); G1_X(kt, so, 1); W(1); G1_X(kt, so, 2); W(2); G1_X(kt, so, 3); W(3); \
    G1_W(kt, so, 0); W(4); G1_W(kt, so, 1); W(5); G1_W(kt, so, 2); W(6); G1_W(kt, so, 3); W(7); SB(); } while (0)
#define G1_STAGE_A(kt, so, W) do { SB(); G1_X(kt, so, 0); W(0); G1_X(kt, so, 2); W(1); \
    G1_W(kt, so, 0); W(2); G1_W(kt, so, 1); W(3); G1_W(kt, so, 2); W(4); G1_W(kt, so, 3); W(5); W(6); W(7); SB(); } while (0)
#define G1_STAGE_B(kt, so) do { SB(); G1_X(kt, so, 1); G1_X(kt, so, 3); SB(); } while (0)
#define NOW(i)
  const int fr = lane & 15, fq = lane >> 4;
  const int ha = wid >> 2;
  int trow, xo[4], xa[3], po, ml;
  const char *prs, *prc;
  auto col4 = [](int f) __attribute__((always_inline)) { return ((f & 2) << 3 | (f & 4) << 1 | (f & 1) << 2) ^ (-((f >> 3) & 1) & 28); };
  auto seg0 = [&](int hf, int dn) __attribute__((always_inline)) { return (hf * 512 + et * 32) ^ (-dn & (D_ - 32)); };
  auto epi_setup = [&](int part) __attribute__((always_inline)) {
    int t_ = tt, l_ = lane;
    asm volatile("" : "+s"(t_));
    asm volatile("" : "+v"(l_));
    const int ts = ((wid >> 1) & 1) * 8 + kq * 2 + ha;
    if (part == 0) { xo[3] = (seg0(wid & 1, (l_ >> 3) & 1) + col4(l_)) * 2; asm volatile("" : "+v"(xo[3])); }
    if (part == 1) {
      xo[3] |= sym_hi(S_, 64, D_ * 2, t_, z, l_ >> 4);
      ml = (l_ * 16) ^ (-sym_dn(l_ >> 4) & (z * 768));
      asm volatile("" : "+v"(xo[3]), "+v"(ml));
    }
    if (part == 2) {
      const int xh = xo[3];
#pragma unroll
      for (int r = 0; r < 4; ++r) xo[r] = sym_lo(64, D_ * 2, l_ >> 4, ts * 4 + r) ^ xh;
      const bool hi = xo[0] & 8;
      xa[0] = (hi ? xo[2] : xo[0]) & ~15; xa[1] = (hi ? xo[3] : xo[1]) & ~15;
      xa[2] = (sym_lo(64, D_ * 2, l_ >> 4, ts * 4 + (l_ & 3)) ^ xh) >> 11 << 3;
      asm volatile("" : "+v"(xo[0]), "+v"(xo[1]), "+v"(xo[2]), "+v"(xo[3]), "+v"(xa[0]), "+v"(xa[1]), "+v"(xa[2]));
    }
    if (part == 3) {
      trow = t_ * 16 + ts;
      prs = wsp + WS_SLAB + (size_t)((t_ * 8 + et) * 64 + (wid & 3)) * MSG;
      prc = (const char*)(flags + FL_X + ((t_ * 8 + et) * 64 + (wid & 3)) * 32);
      asm volatile("" : "+s"(trow), "+s"(prs), "+s"(prc));
      po = l_ < 3 ? (kq * 4 + ((kq + 1 + l_) & 3)) * 512 : (int)0x80000000;
      asm volatile("" : "+v"(po));
    }
  };
  auto tok_tile = [](unsigned key) __attribute__((always_inline)) {
    const unsigned h = (0xffff - key) & 1023, m = -(h >> 9);
    return (((h ^ m) & 0x1c0) | (((h ^ m) - m) & 63)) >> 2;
  };
#define rs mk_rsrc(prs)
#define rc mk_rsrc(prc)
  lds_vi sig = (lds_vi)(smem + LDS_SIG + (wid & 3) * 4);
  unsigned tag;
  u32x4 xq[2];
  u32x2 sq;
  f32x4 ya[4];
  unsigned key[2];
  float cv[16];
  unsigned far0, far1;
  auto xq_load = [&](int n) __attribute__((always_inline)) {
    if (n < 2) xq[n] = ld16_s<0>(x, xa[n]); else sq = ld8_s_sc1(stat, xa[2]);
  };
#define PINV(a) asm volatile("" : "+v"(a))
  auto far_calc = [&]() __attribute__((always_inline)) {
    far0 = tok_tile(key[0]) ^ trow; far1 = tok_tile(key[1]) ^ trow;
    asm volatile("" : "+s"(far0), "+s"(far1));
  };
  auto cv_calc = [&]() __attribute__((always_inline)) {
    if (__builtin_expect(!__all(sq.y == tag), 0)) sq = granule_wait(stat + (xa[2] >> 3), tag);
    const bool hi = xo[0] & 8;
    const float mb_ = lo_bf(sq.x), ri_ = __builtin_amdgcn_rcpf(hi_bf(sq.x));
#pragma unroll
    for (int r = 0; r < 4; ++r) {
      const float mb = r == 0 ? dpp_f<0x00>(mb_) : r == 1 ? dpp_f<0x55>(mb_) : r == 2 ? dpp_f<0xAA>(mb_) : dpp_f<0xFF>(mb_);
      const float ri = r == 0 ? dpp_f<0x00>(ri_) : r == 1 ? dpp_f<0x55>(ri_) : r == 2 ? dpp_f<0xAA>(ri_) : dpp_f<0xFF>(ri_);
#pragma unroll
      for (int u = 0; u < 2; ++u) {
        const unsigned own = xq[r & 1][(r & 2) + u], oth = dpp_u<0xB1>(xq[r & 1][2 - (r & 2) + u]);
        const unsigned w = hi == (r >= 2) ? own : oth;
        cv[r * 4 + 2 * u] = __builtin_fmaf(lo_bf(w) - mb, ri, lo_bf(w));
        cv[r * 4 + 2 * u + 1] = __builtin_fmaf(hi_bf(w) - mb, ri, hi_bf(w));
        PINV(cv[r * 4 + 2 * u]); PINV(cv[r * 4 + 2 * u + 1]);
      }
    }
  };
#define MSG_IDX(m) (kq * 4 + ((kq + 1 + (m)) & 3))
  unsigned seen = 0;

  if (__builtin_expect(wid >= 4, 1)) {
    __builtin_amdgcn_s_setprio(3);
    G1_STAGE_TILE(0, 0, NOW);
    constexpr int O_ST = (FL_ST - FL_AB) * 4, O_CE = (FL_CE - FL_AB) * 4, O_KEY = (FL_KEY - FL_AB) * 4, O_EP = (FL_EPOCH - FL_AB) * 4;
    const __amdgpu_buffer_rsrc_t rf = mk_rsrc(wsp + WS_FLAGS + FL_AB * 4);
    u32x4 lv[2], lv2[2], lw[2], ld[2], ld2[2];
    unsigned epv;
    {
      const char* xrow = (const char*)x + o_l;
      const char* wdrow = (const char*)wdz + o_d;
      lv[0] = ld16_s<0>(xrow, l16); lv[1] = ld16_s<1024>(xrow, l16);
      lv2[0] = ld16_s<2048>(xrow, l16); lv2[1] = ld16_s<3072>(xrow, l16);
      ld[0] = ld16_s<0>(wdrow, l16); ld[1] = ld16_s<1024>(wdrow, l16);
      ld2[0] = ld16_s<2048>(wdrow, l16); ld2[1] = ld16_s<3072>(wdrow, l16);
      lw[0] = ld16_s<0>(w_r, l16); lw[1] = ld16_s<1024>(w_r, l16);
      asm volatile("buffer_load_dword %0, off, %1, %2 sc1" : "=v"(epv) : "s"(rf), "s"(O_EP) : "memory");
    }
    SB();
    const unsigned long long did = llvm_dispatch_id();
    const unsigned dmix = (unsigned)did * 0x85EBCA6Bu + (unsigned)(did >> 32) * 0xC2B2AE35u + 0x27D4EB2Fu;
    const bool moe = et != 7;
    unsigned epoch;
    const int ci = lane & 15, rq = lane >> 4, tk = (lane >> 3) & 1;
    int mat, cg, dq, d0;
    u32x4 wv[16];
    const u16 *pgu, *pn2;
    const char *pm2, *spb;
#define rw __builtin_amdgcn_make_buffer_rsrc((void*)pgu, 0, moe ? 0x7fffffff : 0, 0x00020000)
#define rn2 mk_rsrc(pn2)
#define rm2 mk_rsrc(pm2)
    int vw, vn2[8], vm2;
    auto k_blk = [&](int k) __attribute__((always_inline)) { return tt ^ (k_ord(k) >> 1); };
#define KROT(kk_) (k_blk(kk_) * 256 + (k_ord(kk_) & 1) * 128)
    int tv2, tv7;
    unsigned sd = 0;
    auto spf = [&](auto N) __attribute__((always_inline)) {
      constexpr int n = decltype(N)::value;
      if constexpr (n >= 0 && sp_tile(n >> 4) >= 0) sld<((n >> 1) & 7) * 4096 + (n & 1) * 64>(sd, spb, KROT(sp_tile(n >> 4)));
    };
    unsigned mq0, mq1, mq2, mq3;
    const int cg8 = et ^ ((et & 1) << 1), rgb = (g & 7) * 4 + (j >> 3);
    const __amdgpu_buffer_rsrc_t rdn = mk_rsrc(w_down);
    const __amdgpu_buffer_rsrc_t rgu = mk_rsrc(wsp + WS_GU);
    int vdn, vgu;
    u32x4 dv[7], gv[4];
    int abo, abq[3];
    unsigned lma;
    unsigned sel[2][4];
    float g0, g1;
    u32x4 kk[4];
    unsigned k1 = 0, k2 = 0;
    const int ko_ = O_KEY + pw * 4096;
#define MM(a, b, c) __builtin_amdgcn_mfma_f32_16x16x32_bf16(__builtin_bit_cast(bf16x8, a), __builtin_bit_cast(bf16x8, b), c, 0, 0, 0)
#define PIN1(a) asm volatile("" : "+v"(a))
#define PIN2(a, b) asm volatile("" : "+v"(a), "+v"(b))
#define PIN3(a, b, c) asm volatile("" : "+v"(a), "+v"(b), "+v"(c))
    {
      int gro, go;
      u32x2 gr;
      u32x4 oh[5];
      f32x4 sa4, gw[2], gx[2];
      float su, gs, ta, tb, sx, mbv;
      unsigned dk[4], kl;
      const int hb = lane >> 5;
#pragma unroll
      for (int l = 0; l < WSN; ++l) asm volatile("" : "=v"(wv[l]));
      auto diag = [&](const f32x4& m) __attribute__((always_inline)) {
        return __uint_as_float((__float_as_uint(m[0]) & dk[0]) | (__float_as_uint(m[1]) & dk[1]) | (__float_as_uint(m[2]) & dk[2]) | (__float_as_uint(m[3]) & dk[3]));
      };
      auto setup = [&](auto JB, auto GP) __attribute__((always_inline)) {
        constexpr int jb = decltype(JB)::value, gp = decltype(GP)::value;
        SB();
        {
          int ln = lane, jj = j, p_ = pw;
          launder(ln, jj, p_);
          const int c_ = ln & 15, r_ = ln >> 4, e_ = jj & 7, t_ = (xcd & 1) * 4 + (jj >> 3);
          if constexpr (jb == 1 && gp == 0) {
            pn2 = ((kq >> 1) ? ws_im : ws_re) + (kq & 1) * 1024;
            spb = (const char*)(pn2 + (sym_row(S_, 64, 1, t_, 0, e_ >> 1, 0) + (e_ & 1) * 32 + p_ * 8) * S_);
            pins(pn2, spb);
          }
          if constexpr (jb == 1 && gp >= 1 && gp < 6) {
            const unsigned o = c_ == gp - 1 ? 0x3f803f80u : 0u;
            oh[gp - 1] = (u32x4){o, o, o, o};
            PIN1(oh[gp - 1]);
          }
          if constexpr (jb == 1 && gp == 6) {
            const int rk = moe ? e_ * 8 + (jj >> 3) * 2 + (xcd >> 2) : 0, r3 = ((rk - 14) * 43) >> 7, rr = rk - 14 - r3 * 3;
            const int c56 = rk < 14 ? rk * 4 + 1 : r3 * 4 + rr + ((rr + 1) >> 1);
            mat = (c56 + 36) >> 6; cg = c56 - mat * 28; dq = xcd & 3; d0 = dq * 256 + p_ * 64;
            pin4(mat, cg, dq, d0);
          }
          if constexpr (jb == 2 && gp == 0) {
            pgu = mat ? w_up : w_gate;
            pm2 = wsp + WS_ABT + (kq * 8 + e_) * 262144;
            pins(pgu, pm2);
            vw = ((d0 + r_ * 16) * F_ + cg * 128 + c_ * 8) * 2;
            PIN1(vw);
          }
          if constexpr (jb == 2 && (gp == 1 || gp == 2)) {
            dk[gp * 2 - 2] = c_ == r_ * 4 + gp * 2 - 2 ? ~0u : 0u; dk[gp * 2 - 1] = c_ == r_ * 4 + gp * 2 - 1 ? ~0u : 0u;
            PIN2(dk[gp * 2 - 2], dk[gp * 2 - 1]);
          }
          if constexpr (jb == 2 && gp == 3) { kl = lrow + (ln >> 5); go = (ln & 30) ? (int)0x80000000 : ((ln & 1) ? O_KEY : O_ST) + (int)kl * 8; PIN2(kl, go); }
          if constexpr (jb == 2 && gp == 4) { kl = 0xffff - kl; go = (ln & ~1) == 2 ? O_CE + (z * D_ + drow + (ln & 1)) * 8 : go; PIN2(kl, go); }
          if constexpr (jb == 2 && gp == 5) {
            gro = p_ < 2 ? O_ST + (st * 128 + p_ * 64 + ln) * 8 : O_CE + z * D_ * 8 + sym_row(D_, 32, 8, e_, z, (ln >> 2) & 3, (p_ - 2) * 16 + r_ * 4 + (ln & 3));
            lma = lds_addr(smem + LDS_MB) + (p_ < 2 ? p_ * 256 : 512 + p_ * 256) + ln * 4;
            PIN2(gro, lma);
          }
          if constexpr (jb == 7 && gp == 0) {
            vm2 = p_ * 1024 + ln * 16;
            PIN1(vm2);
          }
          if constexpr (jb == 2 && gp == 6) {
            const int r0 = p_ * 8 + lrw(ln >> 3);
            vn2[6] = sym_lo(64, S_ * 2, (r0 >> 2) & 3, (r0 >> 4) * 4 + (r0 & 3)); vn2[7] = sym_hi(S_, 64, S_ * 2, t_, z, (r0 >> 2) & 3) | (vm & 127);
            PIN2(vn2[6], vn2[7]);
          }
          if constexpr ((jb == 2 && gp == 7) || (jb == 7 && gp >= 6)) {
            constexpr int q0 = jb == 2 ? 0 : gp == 6 ? 3 : 6;
            const int lo = vn2[6], hi = vn2[7];
#pragma unroll
            for (int q = q0; q < q0 + 3 && q < 8; ++q) { vn2[q] = ((lo + q * 8 * S_ * 2) & (63 * S_ * 2)) ^ hi; PIN1(vn2[q]); }
          }
          if constexpr (jb == 7 && gp == 1) { vdn = rgb * 112 + p_ * 28 + r_ * 7; vgu = (((ln >> 3) & 1) * 4 * F_ + vdn + min(ln & 7, 6)) * 16; PIN2(vdn, vgu); }
          if constexpr (jb == 7 && gp == 2) { vdn = (vdn * D_ + cg8 * 128 + c_ * 8) * 2; PIN1(vdn); }
          if constexpr (jb == 7 && gp == 3) {
            abo = (((kq * 8 + e_) * 4 + p_) * 16 + c_) * AB_STRIDE * 4;
            abq[2] = ln >= 16 || (ln & 7) == t_ ? (int)0x80000000 : abo;
            PIN2(abo, abq[2]);
          }
          if constexpr (jb == 7 && gp == 4) {
            abq[1] = ln >= 8 ? (int)0x80000000 : abq[2];
            abq[0] = ((ln ^ t_) & 4) ? (int)0x80000000 : abq[1];
            PIN2(abq[0], abq[1]);
          }
          if constexpr (jb == 7 && gp == 5) {
            const unsigned mb = lds_addr(smem) + p_ * 1024;
            mq0 = mb + ((3 - kq) & 3) * 4096; mq1 = mb + ((4 - kq) & 3) * 4096; mq2 = mb + ((5 - kq) & 3) * 4096; mq3 = mb + ((6 - kq) & 3) * 4096;
            pin4(mq0, mq1, mq2, mq3);
          }
          if constexpr (jb == 8 && gp < 4) {
#pragma unroll
            for (int h = 0; h < 2; ++h) {
              const unsigned hv = h ? 0x0302u : 0x0100u;
              sel[h][gp] = ((ln & 7) >> 1) == gp ? ((ln & 1) ? (hv << 16) | 0x0c0cu : 0x0c0c0000u | hv) : 0x0c0c0c0cu;
            }
            PIN2(sel[0][gp], sel[1][gp]);
          }
        }
        SB();
      };
#define WS1(i) setup(IC<1>{}, IC<i>{})
#define WS2(i) setup(IC<2>{}, IC<i>{})
#define WS3(i) do { setup(IC<7>{}, IC<i>{}); if (i >= 4) setup(IC<8>{}, IC<(i) & 3>{}); spf(IC<(i < SP0 ? i : -1)>{}); } while (0)
      G1_STAGE_TILE(1, G1_STAGE, WS1);
      WAIT_VM(19); BAR();
      G1_STAGE_TILE(2, 2 * G1_STAGE, WS2);
      G1_STAGE_TILE(3, 3 * G1_STAGE, WS3);
      auto lnu = [&](auto U) __attribute__((always_inline)) {
        constexpr int jb = 1 + decltype(U)::value / 7, gp = decltype(U)::value % 7;
        SB();
        if constexpr (jb == 1) {
          const f32x4 z4 = {0.f, 0.f, 0.f, 0.f};
          if constexpr (gp == 0) { sa4 = MM(lv[0], oh[0], z4); gw[0] = MM(lv[0], lw[0], z4); gx[0] = MM(lv[0], lv[0], z4); PIN3(sa4, gw[0], gx[0]); }
          if constexpr (gp == 1) { sa4 = MM(lv2[0], oh[1], sa4); gw[1] = MM(lv2[0], lw[0], z4); gx[1] = MM(lv2[0], lv2[0], z4); PIN3(sa4, gw[1], gx[1]); }
          if constexpr (gp == 2) { sa4 = MM(lv[1], oh[0], sa4); gw[0] = MM(lv[1], lw[1], gw[0]); gx[0] = MM(lv[1], lv[1], gx[0]); PIN3(sa4, gw[0], gx[0]); }
          if constexpr (gp == 3) { sa4 = MM(lv2[1], oh[1], sa4); gw[1] = MM(lv2[1], lw[1], gw[1]); gx[1] = MM(lv2[1], lv2[1], gx[1]); PIN3(sa4, gw[1], gx[1]); }
          if constexpr (gp == 4) { sa4 = MM(ld[0], oh[2], sa4); sa4 = MM(ld[1], oh[2], sa4); PIN1(sa4); }
          if constexpr (gp == 5) { sa4 = MM(ld2[0], oh[3], sa4); sa4 = MM(ld2[1], oh[3], sa4); PIN1(sa4); }
          if constexpr (gp == 6) { sa4 = MM(lw[0], oh[4], sa4); sa4 = MM(lw[1], oh[4], sa4); PIN1(sa4);
            epoch = __builtin_amdgcn_readfirstlane(epv); tag = ((epoch + 1u) * 0x9E3779B1u ^ dmix) | 1u; }
        }
        if constexpr (jb == 2) {
          if constexpr (gp == 0) { su = (sa4[0] + sa4[1]) + (sa4[2] + sa4[3]); ta = diag(gw[0]); PIN2(su, ta); }
          if constexpr (gp == 1) { su = fold32(su, su); tb = diag(gw[1]); PIN2(su, tb); }
          if constexpr (gp == 2) { su = fold16(su, su); gs = fold32(ta, tb); PIN2(su, gs); }
          if constexpr (gp == 3) { ta = diag(gx[0]); tb = diag(gx[1]); PIN2(ta, tb); }
          if constexpr (gp == 4) { gs = fold16(gs, fold32(ta, tb)); PIN1(gs); }
          if constexpr (gp == 5) { gs += dpp_f<0xB1>(gs); gs += dpp_f<0x4E>(gs); PIN1(gs); }
          if constexpr (gp == 6) { gs += dpp_f<0x141>(gs); gs += dpp_f<0x140>(gs); PIN1(gs); }
        }
        if constexpr (jb == 3) {
          if constexpr (gp == 0) {
            const float s0 = rdl(su, 0), s1 = rdl(su, 1), q0 = rdl(gs, 16), q1 = rdl(gs, 48);
            sx = hb ? s1 : s0; ta = hb ? q1 : q0; PIN2(sx, ta);
          }
          if constexpr (gp == 1) {
            const float mean = sx * (1.0f / D_);
            mbv = rbf(mean);
            ta -= sx * mean;
            if (__builtin_expect(__any(!(sx * mean <= 16.f * ta)), 0)) {
              float q = fold32(sqdev16(lv, rdl(mbv, 0)), sqdev16(lv2, rdl(mbv, 32)));
              q = row_sum(fold16(q, q));
              const float dm = mean - mbv;
              ta = fmaxf(q - (float)D_ * dm * dm, 0.f);
            }
            PIN2(mbv, ta);
          }
          if constexpr (gp == 2) { ta = rbf(rbf(ta * (1.0f / D_)) + 1e-5f); gs -= mbv * rdl(su, 4); PIN2(ta, gs); }
          if constexpr (gp == 3) { ta = rbf(__builtin_amdgcn_sqrtf(ta)); tb = __builtin_amdgcn_rcpf(ta); PIN2(ta, tb); }
          if constexpr (gp == 4) {
            const unsigned rb = __float_as_uint(rbf(gs * tb));
            kl |= (rb ^ ((unsigned)((int)rb >> 31) | 0x80000000u)) & 0xffff0000u; PIN1(kl);
          }
          if constexpr (gp == 5) { kl = (lane & 2) ? __float_as_uint(su) : (lane & 1) ? kl : (__float_as_uint(mbv) >> 16) | __float_as_uint(ta); PIN1(kl); }
        }
        SB();
      };
      constexpr int LN_JB = 1 + (19 * LNS) / 8;
      auto work1 = [&](auto JB, auto GP) __attribute__((always_inline)) {
        constexpr int jb = decltype(JB)::value, gp = decltype(GP)::value, pos = (jb - 1) * 8 + gp;
        SB();
        if constexpr (pos % LNS == 0 && pos / LNS < 20) lnu(IC<pos / LNS>{});
        if constexpr (jb == ST_JB + 3) {
          if constexpr (gp == 0) { if (!__all(gr.y == tag)) gr = granule_wait((const u64*)((const char*)(flags + FL_AB) + gro), tag); PIN1(gr); }
          if constexpr (gp == 1) { g1 = __builtin_amdgcn_rcpf(__uint_as_float((gr.x & 0xffff0000u) ^ ((unsigned)z << 31))); PIN1(g1); }
          if constexpr (gp == 2) { g0 = pw < 2 ? -(lo_bf(gr.x) * g1) : __uint_as_float(gr.x); PIN1(g0); }
        }
        if constexpr (jb == 8 && (gp == 2 || gp == 5)) {
          static_assert(VAN == 4 && VA0 >= 2 && VA0 + VAN <= 9, "first halves");
          int ln = lane;
          PIN1(ln);
          const int i = (ln & 15) * 4 + pw, n = gp == 2 ? (moe ? 0 : 64) : (et < 2 ? 0 : 32);
          const int r0 = gp == 2 ? tt * 64 : sym_row(S_, 64, 1, tt, 0, et >> 1, 0) + (et & 1) * 32;
          (gp == 2 ? tv7 : tv2) = i < n ? (r0 + i) * (S_ * 2) + ((tt ^ (VA0 - 1 + (ln >> 4))) << 8) : (int)0x80000000;
          PIN1(gp == 2 ? tv7 : tv2);
        }
        if constexpr (gp < SP1 && jb <= SP1E) spf(IC<SP0 + (jb - 1) * SP1 + gp>{});
        SB();
      };
      auto step1 = [&](auto JB) __attribute__((always_inline)) {
        constexpr int jb = decltype(JB)::value;
        if constexpr (jb == 1) PAGE(1);
        if constexpr (jb == 7) PAGE(2);
        constexpr int n16 = jb == 12 ? 12 : jb == 11 ? 14 : 16;
        constexpr auto wl = [](int q) { return (q == LN_JB ? 1 : 0) + (q == ST_JB ? 1 : 0); };
        if constexpr (jb == 1) {
          asm volatile("s_waitcnt vmcnt(%11)" : "+v"(lv[0]), "+v"(lv[1]), "+v"(lw[0]), "+v"(lw[1]), "+v"(ld[0]), "+v"(ld[1]),
                       "+v"(lv2[0]), "+v"(lv2[1]), "+v"(ld2[0]), "+v"(ld2[1]), "+v"(epv) : "i"(16) : "memory");
        } else if constexpr (jb == ST_JB + 3) {
          asm volatile("s_waitcnt vmcnt(%1)" : "+v"(gr) : "i"(n16 + wl(jb - 2) + wl(jb - 1)) : "memory");
        } else wait_vm<n16 + wl(jb - 3) + wl(jb - 2) + wl(jb - 1)>();
        BAR();
        if constexpr (jb == 2) { if (pw == 0 && lane < 16) ((int*)(smem + LDS_SEL))[lane < 8 ? lane : lane + 8] = 0; }
        if constexpr (jb == 4) { if (pw == 0 && lane == 0) ((unsigned*)(smem + LDS_SEL))[5] = tag; }
#define W1(i) work1(JB, IC<i>{})
        if constexpr (jb < 10) G1_STAGE_TILE(jb - 1 + G1_NST, ((jb - 1) % G1_NST) * G1_STAGE, W1);
        else G1_STAGE_A(jb - 1 + G1_NST, ((jb - 1) % G1_NST) * G1_STAGE, W1);
        if constexpr (jb == 12) G1_STAGE_B(13, G1_STAGE);
        if constexpr (jb == LN_JB) __builtin_amdgcn_raw_buffer_store_b64((u32x2){kl, tag}, rf, go, 0, 16);
        if constexpr (jb == ST_JB) {
          asm volatile("buffer_load_dwordx2 %0, %1, %2, 0 offen sc1" : "=v"(gr) : "v"(gro), "s"(rf) : "memory");
        }
        if constexpr (jb <= 8) {
          if (et == jb - 1) {
#pragma unroll
            for (int l = 0; l < WSN; ++l) bld16_nt_io(wv[l], rw, vw, l * F_ * 2);
          }
        }
        SB();
      };
      static_assert(ST_JB + 3 <= 12 && ST_JB >= 6, "schedule");
      step1(IC<1>{}); step1(IC<2>{}); step1(IC<3>{}); step1(IC<4>{}); step1(IC<5>{}); step1(IC<6>{}); step1(IC<7>{}); step1(IC<8>{}); step1(IC<9>{}); step1(IC<10>{}); step1(IC<11>{}); step1(IC<12>{});
    }
#define G2_STAGE_N(kk_, W) do { const int kb = KROT(kk_); constexpr int so = n_off(kk_); SB(); \
    DMA_S(rn2, vn2[0], mq0, so, kb); W(0); DMA_S(rn2, vn2[1], mq1, so, kb); W(1); \
    DMA_S(rn2, vn2[2], mq2, so, kb); W(2); DMA_S(rn2, vn2[3], mq3, so, kb); W(3); \
    DMA_S(rn2, vn2[4], mq0, so + 16384, kb); W(4); DMA_S(rn2, vn2[5], mq1, so + 16384, kb); W(5); \
    DMA_S(rn2, vn2[6], mq2, so + 16384, kb); W(6); DMA_S(rn2, vn2[7], mq3, so + 16384, kb); W(7); SB(); } while (0)
#define G2_STAGE_NH(kk_, h) do { const int kb = KROT(kk_); constexpr int so = n_off(kk_) + (h) * 16384; SB(); \
    DMA_S(rn2, vn2[(h) * 4], mq0, so, kb); DMA_S(rn2, vn2[(h) * 4 + 1], mq1, so, kb); \
    DMA_S(rn2, vn2[(h) * 4 + 2], mq2, so, kb); DMA_S(rn2, vn2[(h) * 4 + 3], mq3, so, kb); SB(); } while (0)
#define G2_STAGE_M(kk_, W) do { const int kb = KROT(kk_) * 128; constexpr int so = G2_MRING + m_off(kk_); SB(); \
    DMA_(rm2, vm2, mb0, so, kb, 0, " sc1"); W(8); DMA_(rm2, vm2, mb0, so + 4096, kb, 4096, " sc1"); W(9); \
    DMA_(rm2, vm2, mb0, so + 8192, kb, 8192, " sc1"); W(10); DMA_(rm2, vm2, mb0, so + 12288, kb, 12288, " sc1"); W(11); SB(); } while (0)
    WAIT_VM(8); BAR();
    asm volatile("ds_write_b32 %0, %1" :: "v"(lma), "v"(g0) : "memory");
    if (pw < 2) asm volatile("ds_write_b32 %0, %1 offset:512" :: "v"(lma), "v"(g1) : "memory");
    u32x4 tv[4];
    unsigned ready = 0;
    auto need = [&](int k) __attribute__((always_inline)) {
      const int b = k_blk(k) + (k_ord(k) & 1) * 8;
#pragma unroll 1
      for (int it = 0; !((ready >> b) & 1) && it < SPIN_MAX; ++it) {
        ready |= (unsigned)__ballot(__builtin_amdgcn_raw_buffer_load_b32(rf, abq[k < 5 ? 0 : k < 9 ? 1 : 2], 0, 16) == tag);
        asm volatile("" ::: "memory");
      }
    };
    unsigned ksel, bad = 0;
    int isel;
    unsigned xw;
    auto bvec = [&](unsigned src, const unsigned (&sl)[4]) __attribute__((always_inline)) {
      return (u32x4){__builtin_amdgcn_perm(0, src, sl[0]), __builtin_amdgcn_perm(0, src, sl[1]),
                     __builtin_amdgcn_perm(0, src, sl[2]), __builtin_amdgcn_perm(0, src, sl[3])};
    };
#define BC8(v, p) (unsigned)__builtin_amdgcn_update_dpp(__builtin_amdgcn_update_dpp(0, (int)(v), 0x150 + (p), 0xf, 0x3, false), (int)(v), 0x158 + (p), 0xf, 0xc, false)
    float* lg = (float*)(smem + LDS_GU);
    f32x4 macc = {0.f, 0.f, 0.f, 0.f};
    u32x2 sv;
    unsigned hp, gbad;
    float s0, s1;
    auto work2 = [&](auto JB, auto GP) __attribute__((always_inline)) {
      constexpr int jb = decltype(JB)::value, gp = decltype(GP)::value;
      SB();
      if constexpr (jb == -1 && gp < 4) {
        constexpr int c = gp == 0 ? 0xB1 : gp == 1 ? 0x4E : gp == 2 ? 0x141 : 0x140;
        merge2(k1, k2, dpp_u<c>(k1), dpp_u<c>(k2));
        PIN2(k1, k2);
      }
      if constexpr (jb == -1 && gp == 4) {
        ksel = tk ? k2 : k1;
        isel = (0xffff - (int)(ksel & 0xffffu)) & (S_ - 1);
        k1 = (isel * D_ + d0 + rq * 16 + (lane & 7) * 2) * 2; k2 = O_ST + isel * 8;
        PIN1(ksel);
        xw = gld4_s(x, k1);
        sv = bld8_sc1(rf, k2);
      }
      if constexpr (jb == 0 && gp == 1) wait_xs<3 + 2 + 2>(xw, sv);
      if constexpr (jb == 0) {
        if constexpr (gp == 1) { if (!__all(sv.y == tag)) sv = granule_wait(stat + isel, tag); s0 = __builtin_amdgcn_rcpf(hi_bf(sv.x)); PIN1(s0); }
        if constexpr (gp == 2) { s1 = ln_elem(lo_bf(xw), lo_bf(sv.x), hi_bf(sv.x), s0); PIN1(s1); }
        if constexpr (gp == 3) { xw = pk2(s1, ln_elem(hi_bf(xw), lo_bf(sv.x), hi_bf(sv.x), s0)); PIN1(xw); }
      }
      if constexpr (jb == DG && gp < 7) { PIN1(dv[gp]); macc = MM(dv[gp], bvec(BC8(hp, gp), sel[0]), macc); PIN1(macc); }
      if constexpr ((jb == 0 && gp >= 4) || jb == 1) {
        constexpr int l = jb == 0 ? gp - 4 : gp + 4;
        if constexpr (!(l & 1)) hp = BC8(xw, l >> 1);
        PIN1(wv[l]);
        macc = MM(wv[l], bvec(hp, sel[l & 1]), macc);
        PIN2(macc, hp);
      }
      if constexpr (jb == GV_JB + 2) {
        if constexpr (gp == 0) { gbad = (gv[0].y ^ tag) | (gv[0].w ^ tag) | (gv[1].y ^ tag) | (gv[1].w ^ tag); PIN1(gbad); }
        if constexpr (gp == 1) { gbad |= (gv[2].y ^ tag) | (gv[2].w ^ tag) | (gv[3].y ^ tag) | (gv[3].w ^ tag); PIN1(gbad); }
        if constexpr (gp == 2) {
          s0 = ((__uint_as_float(gv[0].x) + __uint_as_float(gv[1].x)) + __uint_as_float(gv[2].x)) + __uint_as_float(gv[3].x);
          s1 = ((__uint_as_float(gv[0].z) + __uint_as_float(gv[1].z)) + __uint_as_float(gv[2].z)) + __uint_as_float(gv[3].z);
          PIN2(s0, s1);
        }
        if constexpr (gp == 3) {
#pragma unroll 1
          for (int it = 0; !__all(gbad == 0) && it < SPIN_MAX; ++it) {
            u32x4 q[4];
            gbad = 0;
#pragma unroll
            for (int i = 0; i < 4; ++i) { q[i] = __builtin_amdgcn_raw_buffer_load_b128(rgu, vgu, i * F_ * 16, 16); gbad |= (q[i].y ^ tag) | (q[i].w ^ tag); }
            s0 = ((__uint_as_float(q[0].x) + __uint_as_float(q[1].x)) + __uint_as_float(q[2].x)) + __uint_as_float(q[3].x);
            s1 = ((__uint_as_float(q[0].z) + __uint_as_float(q[1].z)) + __uint_as_float(q[2].z)) + __uint_as_float(q[3].z);
            asm volatile("" ::: "memory");
          }
          const unsigned gp2 = pk2(s0, s1);
          const unsigned up2 = dpp_u<0x128>(gp2);
          s0 = tk ? hi_bf(up2) : lo_bf(gp2); s1 = tk ? hi_bf(gp2) : lo_bf(up2);
          PIN2(s0, s1);
        }
        if constexpr (gp == 4) { s0 = rbf(s0 * __builtin_amdgcn_rcpf(1.f + __expf(-s0))); PIN1(s0); }
        if constexpr (gp == 5) { hp = pk2(s0 * s1, 0.f); PIN1(hp); }
      }
      if constexpr (jb == 10 && gp == 4) code_touch(sd, (j * 4 + pw) * 256);
      SB();
    };
#define W2M(i) work2(IC<-1>{}, IC<i>{})
#define W20(i) work2(IC<0>{}, IC<i>{})
#define W21(i) work2(IC<1>{}, IC<i>{})
    unsigned fs;
    {
      G1_STAGE_B(15, 3 * G1_STAGE);
      G1_STAGE_B(14, 2 * G1_STAGE);
      lgkm0(sd);
      BAR();
      G2_STAGE_NH(0, 0);
      WAIT_VM(6); BAR();
#pragma unroll
      for (int i = 0; i < 4; ++i) kk[i] = bld16_sc1(rf, l16, ko_ + i * 1024);
      G2_STAGE_NH(0, 1);
#pragma unroll
      for (int l = WSN; l < 16; ++l) wv[l] = bld16_nt(rw, vw, l * F_ * 2);
      touch1(rn2, tv7, mb0, 0);
      asm volatile("s_waitcnt vmcnt(%4)" : "+v"(kk[0]), "+v"(kk[1]), "+v"(kk[2]), "+v"(kk[3]) : "i"(16 - WSN + 5) : "memory");
#pragma unroll
      for (int i = 0; i < 4; ++i) {
        k2 = med3u(k1, k2, kk[i].x); k1 = max(k1, kk[i].x);
        k2 = med3u(k1, k2, kk[i].z); k1 = max(k1, kk[i].z);
        bad |= (kk[i].y ^ tag) | (kk[i].w ^ tag);
      }
#pragma unroll 1
      for (int it = 0; !__all(bad == 0) && it < SPIN_MAX; ++it) {
        k1 = 0; k2 = 0; bad = 0;
#pragma unroll
        for (int i = 0; i < 4; ++i) {
          const u32x4 q = __builtin_amdgcn_raw_buffer_load_b128(rf, l16, ko_ + i * 1024, 16);
          k2 = med3u(k1, k2, q.x); k1 = max(k1, q.x);
          k2 = med3u(k1, k2, q.z); k1 = max(k1, q.z);
          bad |= (q.y ^ tag) | (q.w ^ tag);
        }
        asm volatile("" ::: "memory");
      }
      merge2(k1, k2, dpp_u<0xB1>(k1), dpp_u<0xB1>(k2));
      merge2(k1, k2, dpp_u<0x4E>(k1), dpp_u<0x4E>(k2));
      merge2(k1, k2, dpp_u<0x141>(k1), dpp_u<0x141>(k2));
      merge2(k1, k2, dpp_u<0x140>(k1), dpp_u<0x140>(k2));
      if ((lane & 15) == 0) *(u32x2*)(smem + LDS_KEY + (pw * 4 + rq) * 8) = (u32x2){k1, k2};
      PAGE(3);
      wait_vm<17 - WSN>();
      BAR_LGKM();
#pragma unroll
      for (int jj = 0; jj < 4; ++jj) tv[jj] = *(const u32x4*)(smem + G2_MRING + m_off(0) + (jj * 4 + pw) * 1024 + lane * 16);
      { const u32x2 kp = *(const u32x2*)(smem + LDS_KEY + ci * 8); k1 = kp.x; k2 = kp.y; }
#pragma unroll
      for (int jj = 0; jj < 4; ++jj) __builtin_amdgcn_raw_buffer_store_b128(tv[jj], rm2, vm2 + jj * 4096 + 2 * tt * 16384, 0, 16);
      G2_STAGE_N(1, W2M);
      touch1(rn2, tv2, mb0, 0);
      WAIT_VM(11);
      if (lane == tt) __builtin_amdgcn_raw_buffer_store_b32(tag, rf, abo, 0, 16);
      {
        lds_vi rel = (lds_vi)(smem + LDS_SEL + 12);
        int it = 0;
        while (rel[0] != 4 && it < SPIN_MAX * 8) ++it;
        asm volatile("" ::: "memory");
      }
      G2_STAGE_N(2, W20);
      __builtin_amdgcn_s_sleep(2);
      WAIT_VM(10);
      fs = bld4_sc1(rf, abq[0]);
    }
    BAR();
#pragma unroll
    for (int jj = 0; jj < 4; ++jj) tv[jj] = *(const u32x4*)(smem + G2_MRING + m_off(1) + (jj * 4 + pw) * 1024 + lane * 16);
    constexpr int SPB = SP0 + SP1E * SP1;
    G2_STAGE_N(3, NOW);
    asm volatile("s_waitcnt vmcnt(8)" : "+v"(fs) :: "memory");
    ready = (unsigned)__ballot(fs == tag);
    need(2);
    G2_STAGE_M(2, NOW);
    need(3);
    G2_STAGE_M(3, NOW);
    fs = bld4_sc1(rf, abq[1]);
    W21(0); W21(1); W21(2); W21(3); W21(4); W21(5); W21(6); W21(7); W21(8); W21(9); W21(10); W21(11);
    asm volatile("" : "+v"(tv[0]), "+v"(tv[1]), "+v"(tv[2]), "+v"(tv[3]));
    int dno;
    auto step2 = [&](auto JB) __attribute__((always_inline)) {
      constexpr int jb = decltype(JB)::value;
      if constexpr (jb == 3) PAGE(4);
      if constexpr (jb == 9) PAGE(5);
      if constexpr (jb == 13) PAGE(7);
      constexpr auto must = [](int q) { return (q == 2 ? 4 : 0) + ((FSM >> q) & 1) + dig(GVS, q); };
      if constexpr (jb == 2) wait_vm<4 + must(1)>();
      else wait_vm<dn_cnt(jb - 2) + (jb == 5 ? 1 : 0) + 12 + must(jb - 1) + dn_cnt(jb - 1)>();
      if constexpr (jb == 3) { if (pw == 0 && (lane & ~8) == 0) ((unsigned*)(smem + LDS_SEL))[tk] = ksel; }
      if constexpr ((jb == 2 || jb == DG + 1)) {
        *(f32x4*)(lg + pw * 256 + lane * 4) = macc;
        macc = (f32x4){0.f, 0.f, 0.f, 0.f};
        lgkm0(sd);
      }
      BAR();
      float p0[4], p1[4];
      const int col = (pw & 1) * 64 + lane;
      if constexpr (jb == 3) {
        if (g == 0 && pw == 0 && lane == 0) __hip_atomic_store((unsigned*)flags + FL_EPOCH, epoch + 1u, __ATOMIC_RELAXED, __HIP_MEMORY_SCOPE_AGENT);
      }
      if constexpr (jb == DG + 1) {
        const float* p = lg + (pw & 1) * 32 + (lane >> 4) * 64 + ((2 * lane) & 7) * 4 + ((lane >> 2) & 3);
#pragma unroll
        for (int i = 0; i < 4; ++i) { p0[i] = p[i * 256]; p1[i] = p[i * 256 + 4]; }
      }
      if constexpr (jb == 2) {
        const float* p = lg + (col >> 5) * 64 + (col & 7) * 4 + ((col >> 3) & 3);
#pragma unroll
        for (int i = 0; i < 4; ++i) { p0[i] = p[i * 256]; p1[i] = p[i * 256 + 32]; }
      }
      if constexpr (jb == 4) {
        if (lane == 8 + tt) __builtin_amdgcn_raw_buffer_store_b32(tag, rf, abo, 0, 16);
      }
      if constexpr ((FSM >> (jb - 2)) & 1) {
        asm volatile("" : "+v"(fs));
        ready |= (unsigned)__ballot(fs == tag);
      }
      if constexpr (jb == GV_JB + 2) asm volatile("" : "+v"(gv[0]), "+v"(gv[1]), "+v"(gv[2]), "+v"(gv[3]));
      auto w2 = [&](auto GP) __attribute__((always_inline)) {
        constexpr int gp = decltype(GP)::value;
        if constexpr ((jb == 2 || jb == DG + 1) && gp == 0) {
          p0[0] = (p0[0] + p0[1]) + (p0[2] + p0[3]); p1[0] = (p1[0] + p1[1]) + (p1[2] + p1[3]);
          PIN2(p0[0], p1[0]);
        }
        if constexpr (jb == DG + 1 && gp == 1) {
          if (pw < 2) __builtin_amdgcn_raw_buffer_store_b64((u32x2){pk2(p0[0], p1[0]), tag}, mk_rsrc(wsp + WS_DN), (((pw * 8 + cg8) * 32 + rgb) * 64 + lane) * 8, 0, 16);
        }
        if constexpr (jb == 12 && gp == 3) epi_setup(3);
        if constexpr (jb == 12 && gp == 6) { key[0] = __builtin_amdgcn_readlane(ksel, 0); key[1] = __builtin_amdgcn_readlane(ksel, 8); far_calc(); }
        if constexpr (jb == 12 && gp == 10) {
          int l_ = lane;
          PIN1(l_);
          const int ec = seg0((l_ >> 4) & 1, (l_ >> 3) & 1);
          dno = (ec >> 7) * 16384 + (l_ >> 5) * 512 + (ec & 127) * 4 + (l_ & 7) * 16;
          PIN1(dno);
        }
        work2(JB, GP);
        if constexpr (gp < dig(SPS, jb)) spf(IC<SPB + acc_dig(SPS, jb) + gp>{});
      };
#define W2(i) w2(IC<i>{})
      G2_STAGE_N(jb + 2, W2);
      need(jb + 2);
      G2_STAGE_M(jb + 2, W2);
      if constexpr (jb == 2) {
#pragma unroll
        for (int jj = 0; jj < 4; ++jj) __builtin_amdgcn_raw_buffer_store_b128(tv[jj], rm2, vm2 + jj * 4096 + (2 * tt + 1) * 16384, 0, 16);
      }
      if constexpr ((FSM >> jb) & 1) fs = bld4_sc1(rf, abq[jb < 5 ? 1 : 2]);
#pragma unroll
      for (int i = acc_dig(GVS, jb); i < acc_dig(GVS, jb + 1); ++i) gv[i] = bld16_sc1(rgu, vgu, i * F_ * 16);
#pragma unroll
      for (int i = dn_first(jb); i < dn_first(jb) + dn_cnt(jb); ++i) dv[i] = bld16_nt(rdn, vdn, i * D_ * 2);
      if constexpr (jb == 2) {
        if (moe && pw < 2)
          __builtin_amdgcn_raw_buffer_store_b128((u32x4){__float_as_uint(p0[0]), tag, __float_as_uint(p1[0]), tag},
                                                 rgu, ((mat * 4 + dq) * F_ + cg * 128 + col) * 16, 0, 16);
      }
      SB();
      if constexpr (dig(SPS, jb) > 12) spf(IC<SPB + acc_dig(SPS, jb) + 12>{});
      SB();
    };
    static_assert(DG + 1 <= 12 && DG >= GV_JB + 3 && dn_first(DG - 2) == 7 && acc_dig(GVS, GV_JB + 1) == 4, "schedule");
    static_assert(SPB + acc_dig(SPS, 14) == (16 - VAN) * 16, "all tiles are prefetched");
    step2(IC<2>{}); step2(IC<3>{}); step2(IC<4>{}); step2(IC<5>{}); step2(IC<6>{}); step2(IC<7>{}); step2(IC<8>{}); step2(IC<9>{}); step2(IC<10>{}); step2(IC<11>{}); step2(IC<12>{}); step2(IC<13>{});
    lgkm0(sd);
    WAIT_VM(12); BAR();
    __builtin_amdgcn_s_setprio(0);
    const __amdgpu_buffer_rsrc_t rd = mk_rsrc(wsp + WS_DN + pw * 4096);
    u32x4 dq_[4];
    epi_setup(0); epi_setup(1); epi_setup(2);
    xq_load(0); xq_load(1); xq_load(2);
    asm volatile("s_cmp_lg_u32 %7, 0\n\ts_cbranch_scc1 1f\n\t"
                 "buffer_load_dwordx4 %0, %4, %5, %6 offen sc1\n\tbuffer_load_dwordx4 %1, %4, %5, %6 offen offset:1024 sc1\n\t"
                 "buffer_load_dwordx4 %2, %4, %5, %6 offen offset:2048 sc1\n\tbuffer_load_dwordx4 %3, %4, %5, %6 offen offset:3072 sc1\n\t"
                 "s_waitcnt vmcnt(7)\n\ts_branch 2f\n1:\n\ts_waitcnt vmcnt(3)\n2:\n\ts_barrier\n\ts_waitcnt vmcnt(0)"
                 : "=&v"(dq_[0]), "=&v"(dq_[1]), "=&v"(dq_[2]), "=&v"(dq_[3])
                 : "v"(dno), "s"(rd), "s"((far0 & ~9) ? 131072 : 0), "s"(min(far0 & ~9, far1 & ~9)) : "scc", "memory");
    wait_xq<0>(xq, sq);
    bool first = true;
#pragma unroll 1
    for (int c = 0; c < 2; ++c) {
      if (__builtin_expect(((c ? far1 : far0) & ~9) != 0, 1)) continue;
      const float sg = rbf(__builtin_amdgcn_rcpf(1.f + __expf(-key_score(c ? key[1] : key[0]))));
#pragma unroll 1
      for (int it = 0; it < SPIN_MAX; ++it) {
        unsigned bad = 0;
#pragma unroll
        for (int i = 0; i < 4; ++i) bad |= (dq_[i].y ^ tag) | (dq_[i].w ^ tag);
        if ((first || it) && __all(bad == 0)) break;
        if (it) __builtin_amdgcn_s_sleep(4);
#pragma unroll
        for (int i = 0; i < 4; ++i) dq_[i] = __builtin_amdgcn_raw_buffer_load_b128(rd, dno + i * 1024, c * 131072, 16);
        asm volatile("" ::: "memory");
      }
      first = false;
      f32x4 s4 = {0.f, 0.f, 0.f, 0.f};
#pragma unroll
      for (int i = 0; i < 4; ++i) s4 += (f32x4){lo_bf(dq_[i].x), hi_bf(dq_[i].x), lo_bf(dq_[i].z), hi_bf(dq_[i].z)};
      *(f32x2*)(smem + LDS_FIN + (c * 4 + pw) * 512 + (lane & 31) * 16 + (lane >> 5) * 8) = (f32x2){fold32(s4[0], s4[2]) * sg, fold32(s4[1], s4[3]) * sg};
      asm volatile("s_waitcnt lgkmcnt(0)" ::: "memory");
      if (lane == 0) asm volatile("ds_add_u32 %0, %1" :: "v"(lds_addr(smem + LDS_SEL + 24 + c * 4)), "v"(1) : "memory");
    }
    cv_calc();
  } else {
  {
    const int q = j * 4 + wid;
    constexpr int Q1 = WS_MB, Q2 = Q1 + 8, Q3 = Q2 + 8, Q4 = Q3 + 7, Q5 = Q4 + 7, Q6 = Q5 + 7, Q7 = Q6 + 4, Q8 = Q7 + 4, QC = Q8 + 1;
    static_assert(QC <= 128, "warm-up slots");
    unsigned dmy = 0;
    if (q < QC) {
      const char* base; int b0;
      if (q < Q1) { base = wsp; b0 = 0; }
      else if (q < Q2) { base = (const char*)ws_re; b0 = Q1; }
      else if (q < Q3) { base = (const char*)ws_im; b0 = Q2; }
      else if (q < Q4) { base = (const char*)w_gate; b0 = Q3; }
      else if (q < Q5) { base = (const char*)w_up; b0 = Q4; }
      else if (q < Q6) { base = (const char*)w_down; b0 = Q5; }
      else if (q < Q7) { base = (const char*)out; b0 = Q6; }
      else if (q < Q8) { base = (const char*)x; b0 = Q7; }
      else { base = (const char*)w_r; b0 = Q8; }
      base += (size_t)(q - b0) << 20;
      asm volatile("s_load_dword %0, %1, 0" : "+s"(dmy) : "s"(base) : "memory");
    }
    if (wid == 0) { CALL_PG(1); CALL_PG(8); CALL_PG(5); }
    else if (wid == 1) { CALL_PG(2); CALL_PG(9); CALL_PG(6); }
    else if (wid == 2) { CALL_PG(3); CALL_PG(10); CALL_PG(7); }
    else { CALL_PG(4); CALL_PG(11); CALL_PG(12); }
    BAR();
    lgkm0(dmy);
  }
  PAGE(8);
  epi_setup(0); epi_setup(1); epi_setup(2); epi_setup(3);
  const int wr = wid >> 1, wc = wid & 1;
  const int sw = swz(fr), fl = lrw(fr);
  const int ko0 = ((0 + fq) ^ sw) * 16, ko1 = ((4 + fq) ^ sw) * 16;
  bf16x8 fm[2][4], fn[2][4];
  {
    const char* pm = smem + (wr * 64 + fl) * 128;
    const char* pn = smem + 16384 + (wc * 64 + fl) * 128;
    f32x4 acc[4][4];
#pragma unroll
    for (int i = 0; i < 4; ++i)
#pragma unroll
      for (int jn = 0; jn < 4; ++jn)
#pragma unroll
        for (int q = 0; q < 4; ++q) acc[i][jn][q] = 0.f;
#define TO(i) ((i) * 2048)
#define LDSP(T, a) ((__attribute__((address_space(3))) T*)(size_t)(a))
#define SLOTS(n) _Pragma("unroll") for (int q = 0; q < (n); ++q)
#define MFA(c, m, n) asm volatile("v_mfma_f32_16x16x32_bf16 %0, %2, %1, %0" : "+v"(c) : "v"(m), "v"(n))
#define MFA2(c, m, n, m2, n2) asm volatile("v_mfma_f32_16x16x32_bf16 %0, %2, %1, %0\n\tv_mfma_f32_16x16x32_bf16 %0, %4, %3, %0" : "+v"(c) : "v"(m), "v"(n), "v"(m2), "v"(n2))
    unsigned lm[2][2], ln[2][2];
#pragma unroll
    for (int u = 0; u < 2; ++u)
#pragma unroll
      for (int kk = 0; kk < 2; ++kk) {
        lm[u][kk] = lds_addr(pm) + u * 2 * G1_STAGE + (kk ? ko1 : ko0);
        ln[u][kk] = lds_addr(pn) + u * 2 * G1_STAGE + (kk ? ko1 : ko0);
        asm volatile("" : "+v"(lm[u][kk]), "+v"(ln[u][kk]));
      }
    auto ldm = [&](int stg, int i, int kk) __attribute__((always_inline)) { fm[kk][i] = *LDSP(const bf16x8, lm[stg >> 1][kk] + (stg & 1) * G1_STAGE + TO(i)); };
    auto ldn = [&](int stg, int jn, int kk) __attribute__((always_inline)) { fn[kk][jn] = *LDSP(const bf16x8, ln[stg >> 1][kk] + (stg & 1) * G1_STAGE + TO(jn)); };
    auto quad = [&](int a, int c, int d, int s) __attribute__((always_inline)) {
      const int i = (s < 2 || s >= 6) ? a : a + 1, jn = s < 4 ? c : d, kk = (s == 1 || s == 2 || s == 5 || s == 6);
      MFA(acc[i][jn], fm[kk][i], fn[kk][jn]);
    };
    auto quad2 = [&](int a, int c, int d, int s) __attribute__((always_inline)) {
      const int i = (s < 2 || s >= 6) ? a : a + 1, jn = s < 4 ? c : d, kk = (s == 2 || s == 6);
      MFA2(acc[i][jn], fm[kk][i], fn[kk][jn], fm[1 - kk][i], fn[1 - kk][jn]);
    };
    auto half1 = [&](int stg) __attribute__((always_inline)) {
      const int ja = (stg & 1) * 2, jb = 2 - ja;
      SLOTS(8) {
        quad(0, ja, ja + 1, q);
        if (q < 4) ldn(stg, jb + (q >> 1), q & 1); else ldm(stg, 2 + ((q - 4) >> 1), q == 5 || q == 6);
        SB();
      }
      SLOTS(4) { quad2(0, jb, jb + 1, 2 * q); SB(); }
    };
    auto half2 = [&](int stg) __attribute__((always_inline)) {
      const int ja = (stg & 1) * 2, jb = 2 - ja, nx = (stg + 1) & 3;
      SLOTS(8) {
        quad(2, jb + 1, jb, q);
        if (!(q & 1)) ldm(nx, q >> 2, q == 2 || q == 4);
        SB();
      }
      SLOTS(8) {
        quad(2, ja, ja + 1, q);
        if (!(q & 1)) ldn(nx, jb + (q >> 2), (q >> 1) & 1);
        SB();
      }
    };
    unsigned ae[7];
    {
      int l_ = lane;
      asm volatile("" : "+v"(l_));
      const int p = l_ >> 4, dn = sym_dn(p), hf = (p >> 1) ^ (dn & z);
#pragma unroll
      for (int u = 0; u < 7; ++u) {
        const int T = u < 3 ? wc * 4 : (wc * 4 + u - 3 - dn) & 7, i = u < 3 ? (u + 1) ^ ((u & 1) ? 0 : dn * 2) : 0;
        const int f = dn * 8 + ((T & 2) << 1) + ((T & 4) >> 1) + (T & 1);
        ae[u] = lds_addr(smem + G2_MRING) + (hf * 64 + i * 16 + lrw(f)) * 128 + (((wr * 4 + ((l_ >> 2) & 3)) ^ swz(f)) * 16) + (l_ & 3) * 4;
        asm volatile("" : "+v"(ae[u]));
      }
    }
    unsigned al = lds_addr(smem + LDS_MB) + (wr * 32 + fr * 2) * 4, ac = lds_addr(smem + LDS_MB) + (256 + wc * 64 + fq * 4) * 4;
    asm volatile("" : "+v"(al), "+v"(ac));
    SLOTS(16) asm volatile("" : "+v"(acc[q >> 2][q & 3]));
    SB();
    SLOTS(4) { ldm(0, q >> 1, q & 1); ldn(0, q >> 1, q & 1); }
    SB();
#pragma unroll 1
    for (int k4 = 0; k4 < G1_NKT - 4; k4 += 4) {
      half1(0); BAR_LGKM();
      SB(); half2(0);
      half1(1); BAR_LGKM();
      SB(); half2(1);
      half1(2); BAR_LGKM();
      SB(); half2(2);
      half1(3); BAR_LGKM();
      SB(); half2(3);
    }
    bf16x8 gm[4][4], gn[2][8];
    auto rdm1 = [&](int set, int t, int h, int i, int kk) __attribute__((always_inline)) {
      gm[set][kk * 2 + i] = *LDSP(const bf16x8, lm[(t >> 1) & 1][kk] + (t & 1) * G1_STAGE + h * 4096 + i * 2048);
    };
    auto rdg = [&](int set, int t, int h, int r) __attribute__((always_inline)) {
      if (r == 0 || r == 2 || r == 4 || r == 5) rdm1(set, t, h, r >= 4, r == 2 || r == 4);
      else {
        const int jn = r < 4 ? 0 : (r - 4) >> 1, kk = r < 4 ? r >> 1 : r & 1;
        gn[set][kk * 4 + jn] = *LDSP(const bf16x8, ln[(t >> 1) & 1][kk] + (t & 1) * G1_STAGE + TO(jn));
      }
    };
    auto mmg = [&](int ms, int ns, int h, int jn, int r, int f) __attribute__((always_inline)) {
      const int i = (r >> 1) ^ f, kk = r == 1 || r == 2;
      MFA(acc[h * 2 + i][jn], gm[ms][kk * 2 + i], gn[ns][kk * 4 + jn]);
    };
    f32x2 rsv, mrv;
    f32x4 cev[4];
    float tc[32];
    unsigned ep;
    auto rds = [&](int h, int r) __attribute__((always_inline)) {
      if (r == 0) mrv = *LDSP(const f32x2, al + h * 256); else if (r == 5) rsv = *LDSP(const f32x2, al + 512 + h * 256);
      else cev[r - 1] = *LDSP(const f32x4, ac + (r - 1) * 64);
    };
    auto prep = [&](int n) __attribute__((always_inline)) {
      tc[n] = llvm_fmul_legacy(mrv[n & 1], cev[n >> 3][(n >> 1) & 3]);
      asm volatile("" : "+v"(tc[n]));
    };
    auto epi = [&](int h, int n) __attribute__((always_inline)) {
      const int jn = n >> 4, r = (n >> 2) & 3, u = n & 3, e = (n >> 2) * 2;
      if (u == 0) {}
      else if (u == 1) asm volatile("v_fmac_f32 %0, %2, %4\n\tv_fmac_f32 %1, %3, %5\n\tv_cvt_pk_bf16_f32 %0, %0, %1" : "+v"(tc[e]), "+v"(tc[e + 1])
                                    : "v"(acc[h * 2][jn][r]), "v"(acc[h * 2 + 1][jn][r]), "v"(rsv[0]), "v"(rsv[1]));
      else if (u == 2) ep = __float_as_uint(tc[e]);
      else *LDSP(unsigned, (r ? ae[r - 1] + (jn & 1) * 128 + (jn >> 1) * lrw(4) * 128 : ae[3 + jn]) + m_off(h)) = ep;
      SB();
    };
    SB();
    half1(0);
    BAR_LGKM();
    SB();
    SLOTS(16) {
      if (q < 8) quad(2, 3, 2, q); else quad(2, 0, 1, q - 8);
      if (q < 12) rdg(0, 14, 0, q); else rdg(1, 13, 0, q - 12);
      SB();
    }
    BAR();
    SB();
    SLOTS(16) {
      mmg(0, 0, 0, q >> 2, q & 3, (q >> 2) & 1);
      if (q < 6) rds(0, q); else if (q < 14) rdg(1, 13, 0, q - 2);
      if (q >= 6) prep(q - 6);
      if (q >= 14) prep(q - 4);
      SB();
    }
    PAGE(9);
    BAR();
    SB();
    {
      constexpr int ct[8] = {1, 1, 0, 0, 1, 0, 1, 0}, cj[8] = {0, 1, 0, 1, 2, 2, 3, 3}, cf[8] = {0, 1, 0, 1, 0, 1, 0, 1};
      constexpr auto en = [](int q) { return q < 15 ? 0 : q < 26 ? (q - 14) * 3 - (q == 25) : q < 27 ? 32 : q < 32 ? 32 + (q - 26) * 3 + (q == 31) : 48; };
      SLOTS(32) {
        mmg(ct[q >> 2], ct[q >> 2], 0, cj[q >> 2], q & 3, cf[q >> 2]);
        if (q < 12) rdg(0, 15, 0, q);
        else if (q < 15 || q == 17) { const int r = q < 15 ? q - 12 : 3; rdm1(2, 15, 1, r >> 1, r == 1 || r == 2); }
        else if (q == 20 || q == 23 || q == 25 || q == 26) { const int r = q == 20 ? 0 : q == 23 ? 1 : q - 23; rdm1(3, 13, 1, r >> 1, r == 1 || r == 2); }
        if (q < 15) prep(12 + q);
        if (q >= 10 && q < 15) prep(17 + q);
        SB();
#pragma unroll
        for (int n = q ? en(q - 1) : 0; n < en(q); ++n) epi(0, n);
      }
    }
    SLOTS(2) { mmg(2, 0, 1, 0, q, 0); SB(); }
    asm volatile("s_nop 1");
    SLOTS(16) { epi(0, 48 + q); if (q == 3 || q == 9) { mmg(2, 0, 1, 0, q == 3 ? 2 : 3, 0); SB(); } }
    BAR_LGKM();
    SB();
    SLOTS(12) {
      mmg(2, 0, 1, 1 + (q >> 2), q & 3, ~(q >> 2) & 1);
      if (q < 6) rds(1, q); else rdg(0, 14, 1, q - 6);
      if (q >= 8) prep(q - 8);
      SB();
    }
    SLOTS(16) {
      mmg(3, 1, 1, q >> 2, q & 3, (q >> 2) & 1);
      if (q < 6) rdg(0, 14, 1, q + 6);
      if (q < 4) prep(4 + q); else { prep(2 * q); prep(2 * q + 1); }
      SB();
    }
    asm volatile("s_waitcnt lgkmcnt(0)" ::: "memory");
    if (lane == 0) asm volatile("ds_add_u32 %0, %1" :: "v"(lds_addr(smem + LDS_SEL + 12)), "v"(1) : "memory");
    SB();
    SLOTS(16) {
      mmg(0, 0, 1, q >> 2, q & 3, (q >> 2) & 1);
      if (q < 4) fn[0][q] = *(const bf16x8*)(smem + (wr * 128 + fl) * 128 + (q >> 1) * 2048 + ((q & 1) ? ko1 : ko0));
      else if (q < 8) fm[q & 1][(q - 4) >> 1] = *(const bf16x8*)(smem + G2_MRING + m_off(0) + (wc * 64 + fl) * 128 + TO((q - 4) >> 1) + ((q & 1) ? ko1 : ko0));
      SB();
      if (q >= 8) { epi(1, 3 * q - 24); epi(1, 3 * q - 23); epi(1, 3 * q - 22); }
    }
    SLOTS(40) epi(1, 24 + q);
  }

  const char* pn = smem + (wr * 128 + fl) * 128;
  const char* pm = smem + G2_MRING + (wc * 64 + fl) * 128;
  f32x4 acc[8][4];
  auto mm = [&](bool imaj, int set, int q2, int s, int mode) __attribute__((always_inline)) {
    int i, jj, kk;
    if (imaj) { i = s >> 2; const int r = s & 3; jj = (r >> 1) ^ (i & 1); kk = (r == 1 || r == 2); }
    else { jj = s >> 3; const int r = s & 7; i = jj ? 3 - (r >> 1) : (r >> 1); kk = ((r + 1) >> 1) & 1; }
    if (mode == 1 && !(s & 1)) asm volatile("v_mfma_f32_16x16x32_bf16 %0, %2, %1, 0" : "=&v"(acc[q2 * 2 + jj][i]) : "v"(fm[kk][i]), "v"(fn[set][jj * 2 + kk]));
    else if (mode) asm volatile("v_mfma_f32_16x16x32_bf16 %0, %2, %1, %0" : "+v"(acc[q2 * 2 + jj][i]) : "v"(fm[kk][i]), "v"(fn[set][jj * 2 + kk]));
    else acc[q2 * 2 + jj][i] = __builtin_amdgcn_mfma_f32_16x16x32_bf16(fn[set][jj * 2 + kk], fm[kk][i], acc[q2 * 2 + jj][i], 0, 0, 0);
  };
  unsigned bn[2], bm[2], an[2], am[2];
#pragma unroll
  for (int kk = 0; kk < 2; ++kk) {
    bn[kk] = lds_addr(pn) + (kk ? ko1 : ko0); bm[kk] = lds_addr(pm) + (kk ? ko1 : ko0);
    asm volatile("" : "+v"(bn[kk]), "+v"(bm[kk]));
    an[kk] = bn[kk]; am[kk] = bm[kk] + m_off(0);
  }
#define RDN(jt, kk, o) (*LDSP(const bf16x8, an[kk] + (o) + (jt) * 2048))
#define RDM(i, kk, o) (*LDSP(const bf16x8, am[kk] + (o) + TO(i)))
  PAGE(10);
  tag = __builtin_amdgcn_readfirstlane(((const unsigned*)(smem + LDS_SEL))[5]);
  {
    int son = 0;
    auto tile = [&](int kt, int zero) __attribute__((always_inline)) {
      SLOTS(16) {
        mm(true, 0, 0, q, zero);
        if (!(q & 1)) {
          if (q < 8) fm[(q >> 1) & 1][2 + (q >> 2)] = RDM(2 + (q >> 2), (q >> 1) & 1, 0);
          else { const int r = (q - 8) >> 1; fn[1][r] = RDN(2 + (r >> 1), r & 1, 0); }
        }
        SB();
      }
      SLOTS(16) {
        mm(false, 1, 1, q, zero);
        if (!(q & 1) && q < 8) fn[0][q >> 1] = RDN(4 + (q >> 2), (q >> 1) & 1, 0);
        SB();
      }
      const int som = son >> 1;
      son = son == 65536 ? 0 : son + 32768;
      SLOTS(16) {
        mm(false, 0, 2, q, zero);
        if (!(q & 1) && q < 8) fn[1][q >> 1] = RDN(6 + (q >> 2), (q >> 1) & 1, 0);
        if (q == 9 || q == 11) { an[q == 11] = bn[q == 11] + son; asm volatile("" : "+v"(an[q == 11])); }
        if (q == 13 || q == 15) { am[q == 15] = bm[q == 15] + som; asm volatile("" : "+v"(am[q == 15])); }
        SB();
      }
      BAR_LGKM();
      SB();
      SLOTS(16) {
        mm(true, 1, 3, q, zero);
        if (!(q & 1)) {
          if (q < 8) fn[0][q >> 1] = RDN(q >> 2, (q >> 1) & 1, 0);
          else fm[(q >> 1) & 1][(q - 8) >> 2] = RDM((q - 8) >> 2, (q >> 1) & 1, 0);
        }
        SB();
      }
    };
    SB();
    tile(0, 1);
#pragma unroll 1
    for (int kt = 1; kt < G2_NKT - 3; ++kt) tile(kt, 0);
  }
  unsigned fx;
  int fo = lane == 0 ? kq * 512 : (int)0x80000000;
  {
    constexpr int NG = 12;
    static constexpr int g_t[NG] = {0, 0, 1, 1, 2, 2, 2, 1, 0, 0, 1, 2};
    static constexpr int g_q[NG] = {0, 1, 0, 1, 0, 1, 2, 2, 2, 3, 3, 3};
    constexpr int G14 = 1, G15 = 3, GP0 = 5, GP1 = 6;
    constexpr auto chp = [](int g) { return g == 0 || g_t[g] != g_t[g - 1]; };
    constexpr auto chn = [](int g) { return g + 1 < NG && g_t[g + 1] != g_t[g]; };
    static_assert(n_off(13) == 32768 && n_off(14) == 65536 && n_off(15) == 0, "");
    unsigned an2[2];
#pragma unroll
    for (int kk = 0; kk < 2; ++kk) {
      an[kk] = bn[kk]; an2[kk] = bn[kk] + 65536; am[kk] = bm[kk];
      asm volatile("" : "+v"(an[kk]), "+v"(an2[kk]), "+v"(am[kk]));
    }
    int vo;
    u32x4 pk;
    u32x2 kv;
    auto publish = [&](int d, int h) __attribute__((always_inline)) {
      if (h == 0) { vo = ml; PINV(vo); vo += (((d + kq + 1) & 3) * 4 + kq) * 4 * MSG; PINV(vo); }
      const f32x4 v = acc[2 * d + (h >> 2)][h & 3];
      pk[(h & 1) * 2] = pk2(v[0], v[1]); pk[(h & 1) * 2 + 1] = pk2(v[2], v[3]);
      if (h & 1) __builtin_amdgcn_raw_buffer_store_b128(pk, rs, vo + (h >> 1) * 1024, 0, 16);
    };
#define MSG_FLAG(d) __builtin_amdgcn_raw_buffer_store_b32(tag, rc, fo + (((d) + kq + 1) & 3) * 2048, 0, 16)
    auto rdn = [&](int g, int r) __attribute__((always_inline)) {
      const int t = g_t[g], jt = g_q[g] * 2 + (r >> 1);
      fn[g & 1][r] = *LDSP(const bf16x8, (t == 1 ? an2[r & 1] : an[r & 1]) + (t == 0 ? 32768 : 0) + jt * 2048);
    };
    auto rdm = [&](int t, int i, int kk) __attribute__((always_inline)) { fm[kk][i] = *LDSP(const bf16x8, am[kk] + m_off(13 + t) + TO(i)); };
    auto group = [&](auto GI) __attribute__((always_inline)) {
      constexpr int gi = decltype(GI)::value;
      if (gi == G14) { BAR();
        SB(); }
      if (gi == G15) {
        PAGE(11);
        BAR();
        SB();
        kv = *(const u32x2*)(smem + LDS_SEL);
        SB();
      }
#pragma unroll
      for (int q = 0; q < 16; ++q) {
        const int n = gi * 16 + q;
        if (n == 112) {
        }
        if (n == 152) {
        }
        if (n == 168) { WAIT_VM(8); MSG_FLAG(0); SB(); }
        mm(chp(gi) || chn(gi), gi & 1, g_q[gi], q, 2);
        constexpr bool ni = gi + 1 < NG && (chp(gi + 1 < NG ? gi + 1 : gi) || chn(gi + 1 < NG ? gi + 1 : gi));
        auto nr = [](int r) { return ni && r >= 2 ? 5 - r : r; };
        if constexpr (chp(gi) && chn(gi)) {
          if (q < 4) rdm(g_t[gi], 2 + (q >> 1), q & 1); else if (q < 8) rdn(gi + 1, nr(q - 4)); else if (q < 12) rdm(g_t[gi + 1], (q - 8) >> 1, q & 1);
        } else if constexpr (chp(gi)) {
          if (q < 4) rdm(g_t[gi], 2 + (q >> 1), q & 1);
          else if (gi + 1 < NG && q < 12 && !(q & 1)) rdn(gi + 1, nr((q - 4) >> 1));
        } else if constexpr (chn(gi)) {
          if (!(q & 1)) { if (q < 8) rdn(gi + 1, nr(q >> 1)); else rdm(g_t[gi + 1], (q - 8) >> 2, (q >> 1) & 1); }
        } else {
          if (!(q & 1) && q < 8) rdn(gi + 1, nr(q >> 1));
        }
        if (gi == GP0 && q < 8) publish(0, q);
        if (gi == GP1 && q < 8) publish(1, q);
        if (gi == 9 && q < 8) publish(2, q);
        if (n == 33 || n == 37 || n == 41) xq_load((n - 33) >> 2);
        if (n == 72) { key[0] = __builtin_amdgcn_readfirstlane(kv.x); key[1] = __builtin_amdgcn_readfirstlane(kv.y); }
        SB();
      }
    };
#define GR4(i) group(IC<i>{}); group(IC<i + 1>{}); group(IC<i + 2>{}); group(IC<i + 3>{});
    GR4(0) GR4(4) GR4(8)
    asm volatile("s_nop 7");
    SB();
  }
#pragma unroll
  for (int i = 0; i < 4; ++i) *(f32x4*)(smem + LDS_ACC + wr * 16384 + wc * 4096 + i * 1024 + lane * 16) = acc[7][i];
#pragma unroll
  for (int i = 0; i < 4; ++i) ya[i] = acc[6][i];
  asm volatile("s_waitcnt lgkmcnt(0)" ::: "memory");
  if (lane == 0) sig[0] = 2;
  SB();
  WAIT_VM(5); MSG_FLAG(1); SB();
  WAIT_VM(2); MSG_FLAG(2); fx = bld4_sc1(rc, po); SB();
  wait_xq<4>(xq, sq);
  far_calc();
  cv_calc();
  asm volatile("s_waitcnt vmcnt(0)" : "+v"(fx) :: "memory");
  seen = (unsigned)__ballot(fx == tag);
  }

  PAGE(6);
  if (ha) {
    int it = 0;
    while (sig[0] != 2 && it < SPIN_MAX * 8) { __builtin_amdgcn_s_sleep(1); ++it; }
    asm volatile("" ::: "memory");
#pragma unroll
    for (int i = 0; i < 4; ++i) ya[i] = *(const f32x4*)(smem + LDS_ACC + (wid & 2) * 8192 + (wid & 1) * 4096 + i * 1024 + lane * 16);
  }
  u32x4 pp[3][2];
  unsigned e_lo = 0x00003f80u, e_hi = 0x3f800000u;
  asm volatile("" : "+v"(e_lo), "+v"(e_hi));
  __amdgpu_buffer_rsrc_t rs_ = rs, rc_ = rc;
  int mo[3] = {MSG_IDX(0) * 4 * MSG, MSG_IDX(1) * 4 * MSG, MSG_IDX(2) * 4 * MSG}, mv = ml + ha * 2048, mv2 = mv + 1024;
  asm volatile("" : "+s"(rs_), "+s"(rc_), "+s"(mo[0]), "+s"(mo[1]), "+s"(mo[2]));
  asm volatile("" : "+v"(mv), "+v"(mv2));
#define MSG_POLL(m) do { \
    if (!((seen >> (m)) & 1)) { \
      unsigned pv_, it_ = SPIN_MAX; \
      asm volatile("1:\n\tbuffer_load_dword %0, %3, %4, 0 offen sc1\n\ts_waitcnt vmcnt(0)\n\tv_cmp_eq_u32 vcc, %5, %0\n\ts_mov_b32 %1, vcc_lo\n\t" \
                   "s_bitcmp1_b32 %1, %6\n\ts_cbranch_scc1 2f\n\ts_sub_u32 %2, %2, 1\n\ts_cbranch_scc1 2f\n\ts_sleep 1\n\ts_branch 1b\n2:" \
                   : "=&v"(pv_), "+s"(seen), "+s"(it_) : "v"(po), "s"(rc_), "s"(tag), "i"(m) : "vcc", "scc", "memory"); \
    } \
    pp[m][0] = __builtin_amdgcn_raw_buffer_load_b128(rs_, mv, mo[m], 16); \
    pp[m][1] = __builtin_amdgcn_raw_buffer_load_b128(rs_, mv2, mo[m], 16); } while (0)
#define MSG_ADD(m) do { \
    _Pragma("unroll") for (int u = 0; u < 8; ++u) { \
      ya[u >> 1][(u & 1) * 2] = add_bf(pp[m][u >> 2][u & 3], e_lo, ya[u >> 1][(u & 1) * 2]); ya[u >> 1][(u & 1) * 2 + 1] = add_bf(pp[m][u >> 2][u & 3], e_hi, ya[u >> 1][(u & 1) * 2 + 1]); } } while (0)
  MSG_POLL(2); MSG_POLL(1); MSG_POLL(0);
  SB();
  if (__builtin_expect(far0 == 0 || far1 == 0, 0)) {
    asm volatile("s_branch 2f\nk_pg12:\n\ts_setpc_b64 s[42:43]\n2:");
#pragma unroll 1
    for (int c = 0; c < 2; ++c) {
      if (c ? far1 : far0) continue;
      const int ic = (0xffff - (c ? key[1] : key[0])) & (S_ - 1);
      lds_vi cnt = (lds_vi)(smem + LDS_SEL + 24 + c * 4);
      int it = 0;
      while (cnt[0] != 4 && it < SPIN_MAX * 8) { __builtin_amdgcn_s_sleep(1); ++it; }
      asm volatile("" ::: "memory");
      const float* q = (const float*)(smem + LDS_FIN + c * 2048) + (wid & 1) * 64 + (fr >> 3) * 32 + col4(fr);
      const f32x4 v = (*(const f32x4*)q + *(const f32x4*)(q + 128)) + (*(const f32x4*)(q + 256) + *(const f32x4*)(q + 384));
#pragma unroll
      for (int r = 0; r < 4; ++r)
#pragma unroll
        for (int k = 0; k < 4; ++k) {
          cv[r * 4 + k] += (xo[r] >> 11) == ic ? v[k] : 0.f;
        }
    }
  }
#pragma unroll
  for (int e = 0; e < 16; ++e) ya[e & 3][e >> 2] += cv[e];
  asm volatile("" : "+v"(ya[0]), "+v"(ya[1]), "+v"(ya[2]), "+v"(ya[3]));
  SB();
  MSG_ADD(2); MSG_ADD(1);
  asm volatile("" : "+v"(ya[0]), "+v"(ya[1]), "+v"(ya[2]), "+v"(ya[3]));
  SB();
  MSG_ADD(0);
  {
#pragma unroll
    for (int r = 0; r < 4; ++r) *(u32x2*)((char*)out + (unsigned)xo[r]) = (u32x2){pk2(ya[0][r], ya[1][r]), pk2(ya[2][r], ya[3][r])};
  }
}

extern "C" void kernel_launch(void* const* d_in, const int* in_sizes, int n_in, void* d_out, int out_size,
                              void* d_ws, size_t ws_size, hipStream_t stream) {
  const u16* x = (const u16*)d_in[0];
  const u16* ws_re = (const u16*)d_in[1];
  const u16* ws_im = (const u16*)d_in[2];
  const u16* wd_re = (const u16*)d_in[3];
  const u16* wd_im = (const u16*)d_in[4];
  const u16* w_r = (const u16*)d_in[6];
  const u16* w_gate = (const u16*)d_in[7];
  const u16* w_up = (const u16*)d_in[8];
  const u16* w_down = (const u16*)d_in[9];
  k_main<<<256, 512, 0, stream>>>(x, wd_re, wd_im, w_r, (char*)d_ws, ws_re, ws_im, w_gate, w_up, w_down, (u16*)d_out);
}
```

```cpp
#define THR 55
#include <hip/hip_runtime.h>
#include <hip/hip_bf16.h>
#include <stdint.h>
#include <type_traits>

extern "C" __device__ unsigned long long llvm_dispatch_id(void) __asm("llvm.amdgcn.dispatch.id");
extern "C" __device__ float llvm_fmul_legacy(float, float) __asm("llvm.amdgcn.fmul.legacy");
typedef unsigned short u16;
typedef unsigned long long u64;
typedef __attribute__((ext_vector_type(8))) short bf16x8;
typedef __attribute__((ext_vector_type(4))) unsigned u32x4;
typedef __attribute__((ext_vector_type(2))) unsigned u32x2;
typedef __attribute__((ext_vector_type(4))) float f32x4;
typedef __attribute__((ext_vector_type(2))) float f32x2;
typedef __attribute__((ext_vector_type(2))) __bf16 bf16x2;
typedef __attribute__((address_space(3))) void* lds_vp;
typedef volatile __attribute__((address_space(3))) int* lds_vi;

constexpr int S_ = 2048, D_ = 1024, F_ = 3584;
template <int N> using IC = std::integral_constant<int, N>;

static __device__ __forceinline__ float bf2f(u16 u) { return __uint_as_float((unsigned)u << 16); }
static __device__ __forceinline__ u16 f2bf(float f) {
  __hip_bfloat16 b = __float2bfloat16(f);
  return *reinterpret_cast<u16*>(&b);
}
static __device__ __forceinline__ float rbf(float f) { return bf2f(f2bf(f)); }
static __device__ __forceinline__ float lo_bf(unsigned u) { return __uint_as_float(u << 16); }
static __device__ __forceinline__ float hi_bf(unsigned u) { return __uint_as_float(u & 0xffff0000u); }
static __device__ __forceinline__ unsigned pk2(float a, float b) {
  const bf16x2 v = __builtin_convertvector((f32x2){a, b}, bf16x2);
  return __builtin_bit_cast(unsigned, v);
}
static __device__ __forceinline__ float add_bf(unsigned u, unsigned e, float c) {
  return __builtin_amdgcn_fdot2_f32_bf16(__builtin_bit_cast(bf16x2, u), __builtin_bit_cast(bf16x2, e), c, false);
}
static __device__ __forceinline__ float ln_elem(float x, float mb, float sd, float ri) {
  const float d = rbf(x - mb);
  const float q0 = d * ri;
  return rbf(__builtin_fmaf(__builtin_fmaf(-q0, sd, d), ri, q0));
}
template <int CTRL> static __device__ __forceinline__ float dpp_f(float v) {
  return __int_as_float(__builtin_amdgcn_update_dpp(0, __float_as_int(v), CTRL, 0xf, 0xf, true));
}
template <int CTRL> static __device__ __forceinline__ unsigned dpp_u(unsigned v) {
  return (unsigned)__builtin_amdgcn_update_dpp(0, (int)v, CTRL, 0xf, 0xf, true);
}
static __device__ __forceinline__ float row_sum(float v) {
  v += dpp_f<0xB1>(v);
  v += dpp_f<0x4E>(v);
  v += dpp_f<0x141>(v);
  v += dpp_f<0x140>(v);
  return v;
}
static __device__ __forceinline__ float fold32(float a, float b) {
  const auto r = __builtin_amdgcn_permlane32_swap(__float_as_uint(a), __float_as_uint(b), false, false);
  return __uint_as_float(r[0]) + __uint_as_float(r[1]);
}
static __device__ __forceinline__ float fold16(float a, float b) {
  const auto r = __builtin_amdgcn_permlane16_swap(__float_as_uint(a), __float_as_uint(b), false, false);
  return __uint_as_float(r[0]) + __uint_as_float(r[1]);
}
static __device__ __forceinline__ float rdl(float v, int l) { return __int_as_float(__builtin_amdgcn_readlane(__float_as_int(v), l)); }


#define TL(I) do { if ((I) == 52) { const unsigned _t = (unsigned)__builtin_amdgcn_s_memrealtime(); tlv = lane == (I) ? _t : tlv; } } while (0)
#define ST_JB 9
#define LNS 2
#define WSN 10
#define FSM 0xa2
#define GV_JB 7
#define DG 11
#define AB_STRIDE 32
#define SPIN_MAX (1 << 17)
#define VA0 5
#define VAN 4
#define SP0 3
#define SP1 7
#define SP1E 11
#define SPS 0x000cccccdddd00
#define DVS 0x00000400300000
#define GVS 0x00000022000000
constexpr int dig(unsigned long long t, int q) { return q < 0 ? 0 : (int)((t >> (4 * q)) & 15); }
constexpr int acc_dig(unsigned long long t, int q) { int a = 0; for (int i = 0; i < q; ++i) a += dig(t, i); return a; }

static __device__ __forceinline__ unsigned lds_addr(const void* p) { return (unsigned)(size_t)(lds_vp)p; }

constexpr int MSG = 4096;
constexpr size_t WS_ABT   = 0;
constexpr size_t WS_SLAB  = WS_ABT + (size_t)D_ * 4096 * 2;
constexpr size_t WS_GU    = WS_SLAB + (size_t)64 * 64 * MSG;
constexpr size_t WS_DN    = WS_GU + (size_t)8 * F_ * 16;
constexpr size_t WS_FLAGS = WS_DN + (size_t)2 * 8 * 32 * 64 * 8;
constexpr int FL_X = 0;
constexpr int FL_AB = 64 * 64 * 32;
constexpr int FL_ST = FL_AB + 2048 * AB_STRIDE;
constexpr int FL_CE = FL_ST + 4096;
constexpr int FL_KEY = FL_CE + 4096;
constexpr int FL_EPOCH = FL_KEY + 4096;
constexpr int WS_MB = (int)((WS_FLAGS + (FL_EPOCH + 1) * 4 + 1048575) >> 20);

#define WAIT_VM(n) asm volatile("s_waitcnt vmcnt(" #n ")" ::: "memory")
template <int N> static __device__ __forceinline__ void wait_vm() { asm volatile("s_waitcnt vmcnt(%0)" :: "i"(N) : "memory"); }
#define BAR_LGKM() asm volatile("s_waitcnt lgkmcnt(0)\n\ts_barrier" ::: "memory")
#define BAR() asm volatile("s_barrier" ::: "memory")
#define SB() __builtin_amdgcn_sched_barrier(0)
#define PAGE(n) asm volatile("s_branch 2f\n\t.p2align 12\nk_pg" #n ":\n\ts_setpc_b64 s[42:43]\n2:")
#define CALL_PG(n) asm volatile("s_getpc_b64 s[40:41]\n3:\n\ts_mov_b32 s42, k_pg" #n "-3b\n\ts_ashr_i32 s43, s42, 31\n\ts_add_u32 s40, s40, s42\n\ts_addc_u32 s41, s41, s43\n\t" \
                                "s_swappc_b64 s[42:43], s[40:41]" ::: "s40", "s41", "s42", "s43", "scc", "memory")

static __device__ __forceinline__ __amdgpu_buffer_rsrc_t mk_rsrc(const void* p) {
  return __builtin_amdgcn_make_buffer_rsrc((void*)p, 0, 0x7fffffff, 0x00020000);
}
#define DMA_(rsrc, voff, mbase, MI, sbase, SI, pol) do { unsigned _t; \
    asm volatile("s_add_i32 m0, %2, %3\n\ts_add_i32 %0, %5, %6\n\tbuffer_load_dwordx4 %1, %4, %0 offen" pol " lds" \
                 : "=&s"(_t) : "v"(voff), "s"(mbase), "i"(MI), "s"(rsrc), "s"(sbase), "i"(SI) : "memory", "scc"); } while (0)
#define DMA_I(rsrc, voff, mbase, MI, SI) \
    asm volatile("s_add_i32 m0, %1, %2\n\ts_nop 0\n\tbuffer_load_dwordx4 %0, %3, 0 offen offset:%4 lds" \
                 :: "v"(voff), "s"(mbase), "i"((MI) - (SI)), "s"(rsrc), "i"(SI) : "memory", "scc")
#define DMA_S(rsrc, voff, mbase, MI, soff) \
    asm volatile("s_add_i32 m0, %1, %2\n\ts_nop 0\n\tbuffer_load_dwordx4 %0, %3, %4 offen lds" \
                 :: "v"(voff), "s"(mbase), "i"(MI), "s"(rsrc), "s"(soff) : "memory", "scc")
#define TOUCH(rsrc, voff, mbase, MI, soff) \
    asm volatile("s_add_i32 m0, %1, %2\n\ts_nop 0\n\tbuffer_load_dword %0, %3, %4 offen lds" \
                 :: "v"(voff), "s"(mbase), "i"(MI), "s"(rsrc), "s"(soff) : "memory", "scc")
template <int OFF> static __device__ __forceinline__ u32x4 ld16_s(const void* p, int voff) {
  u32x4 v;
  asm volatile("global_load_dwordx4 %0, %1, %2 offset:%3" : "=v"(v) : "v"(voff), "s"(p), "i"(OFF) : "memory");
  return v;
}
template <int OFF> static __device__ __forceinline__ void sld(unsigned& sink, const void* p, int soff) {
  asm volatile("s_load_dword %0, %1, %2 offset:%3" : "+s"(sink) : "s"(p), "s"(soff), "i"(OFF) : "memory");
}
static __device__ __forceinline__ unsigned gld4_s(const void* p, unsigned voff) {
  unsigned v;
  asm volatile("global_load_dword %0, %1, %2" : "=v"(v) : "v"(voff), "s"(p) : "memory");
  return v;
}
static __device__ __forceinline__ u32x2 bld8_sc1(__amdgpu_buffer_rsrc_t r, unsigned voff) {
  u32x2 v;
  asm volatile("buffer_load_dwordx2 %0, %1, %2, 0 offen sc1" : "=v"(v) : "v"(voff), "s"(r) : "memory");
  return v;
}
template <int N> static __device__ __forceinline__ void wait_xs(unsigned& a, u32x2& b) { asm volatile("s_waitcnt vmcnt(%2)" : "+v"(a), "+v"(b) : "i"(N) : "memory"); }
template <int N> static __device__ __forceinline__ void wait_xq(u32x4 (&a)[2], u32x2& b) { asm volatile("s_waitcnt vmcnt(%3)" : "+v"(a[0]), "+v"(a[1]), "+v"(b) : "i"(N) : "memory"); }
static __device__ __forceinline__ u32x2 ld8_s_sc1(const void* p, int voff) {
  u32x2 v;
  asm volatile("global_load_dwordx2 %0, %1, %2 sc1" : "=v"(v) : "v"(voff), "s"(p) : "memory");
  return v;
}
static __device__ __forceinline__ void lgkm0(unsigned& sink) { asm volatile("s_waitcnt lgkmcnt(0)" : "+s"(sink) :: "memory"); }
template <typename T> static __device__ __forceinline__ void pin4(T& a, T& b, T& c, T& d) { asm volatile("" : "+s"(a), "+s"(b), "+s"(c), "+s"(d)); }
template <typename T, typename U> static __device__ __forceinline__ void pins(T& a, U& b) { asm volatile("" : "+s"(a), "+s"(b)); }
static __device__ __forceinline__ void launder(int& v, int& a, int& b) { asm volatile("" : "+v"(v)); asm volatile("" : "+s"(a), "+s"(b)); }
static __device__ __forceinline__ unsigned bld4_sc1(__amdgpu_buffer_rsrc_t r, int voff) {
  unsigned v;
  asm volatile("buffer_load_dword %0, %1, %2, 0 offen sc1" : "=v"(v) : "v"(voff), "s"(r) : "memory");
  return v;
}
static __device__ __forceinline__ u32x4 bld16_nt(__amdgpu_buffer_rsrc_t r, int voff, int soff) {
  u32x4 v;
  asm volatile("buffer_load_dwordx4 %0, %1, %2, %3 offen nt" : "=v"(v) : "v"(voff), "s"(r), "s"(soff) : "memory");
  return v;
}
static __device__ __forceinline__ void bld16_nt_io(u32x4& v, __amdgpu_buffer_rsrc_t r, int voff, int soff) {
  asm volatile("buffer_load_dwordx4 %0, %1, %2, %3 offen nt" : "+v"(v) : "v"(voff), "s"(r), "s"(soff) : "memory");
}
static __device__ __forceinline__ u32x4 bld16_sc1(__amdgpu_buffer_rsrc_t r, int voff, int soff) {
  u32x4 v;
  asm volatile("buffer_load_dwordx4 %0, %1, %2, %3 offen sc1" : "=v"(v) : "v"(voff), "s"(r), "s"(soff) : "memory");
  return v;
}
static __device__ __forceinline__ u32x2 granule_wait(const u64* p, unsigned tag) {
  u64 v = 0;
#pragma unroll 1
  for (int it = 0; it < SPIN_MAX; ++it) {
    v = __hip_atomic_load(p, __ATOMIC_RELAXED, __HIP_MEMORY_SCOPE_AGENT);
    if (__all((unsigned)(v >> 32) == tag)) break;
    __builtin_amdgcn_s_sleep(2);
  }
  return (u32x2){(unsigned)v, (unsigned)(v >> 32)};
}
static __device__ __forceinline__ float key_score(unsigned key) {
  const unsigned u = key >> 16;
  return bf2f((u16)((u & 0x8000u) ? (u & 0x7fffu) : (~u & 0xffffu)));
}
static __device__ __forceinline__ unsigned med3u(unsigned a, unsigned b, unsigned c) { return max(min(a, b), min(max(a, b), c)); }
static __device__ __forceinline__ void merge2(unsigned& a1, unsigned& a2, unsigned b1, unsigned b2) {
  const unsigned lo = min(a1, b1);
  a1 = max(a1, b1);
  a2 = max(lo, max(a2, b2));
}
static __device__ __forceinline__ float sqdev16(const u32x4 (&v)[2], float mb) {
  f32x2 q2 = {0.f, 0.f};
#pragma unroll
  for (int i = 0; i < 8; ++i) {
    const f32x2 e = (f32x2){lo_bf(v[i >> 2][i & 3]), hi_bf(v[i >> 2][i & 3])} - mb;
    q2 += e * e;
  }
  return q2.x + q2.y;
}

constexpr int lrw(int r) { return (r & ~6) | ((r & 2) << 1) | ((r & 4) >> 1); }
constexpr int swz(int r) { return ((r >> 1) & 4) | (r & 2); }
static __device__ __forceinline__ int sym_dn(int p) { return (p ^ (p >> 1)) & 1; }
static __device__ __forceinline__ int sym_hi(int n, int w, int st, int b, int z, int p) {
  return ((p >> 1) * (n / 2 * st) + b * (w * st)) ^ (-sym_dn(p) & (((n - 1) ^ (z * (n / 2))) * st));
}
static __device__ __forceinline__ int sym_lo(int w, int st, int p, int c) { return ((c - sym_dn(p)) * st) & ((w - 1) * st); }
static __device__ __forceinline__ int sym_row(int n, int w, int st, int b, int z, int p, int c) { return sym_lo(w, st, p, c) ^ sym_hi(n, w, st, b, z, p); }
constexpr int G1_NST = 4, G1_NKT = 16, G1_STAGE = 32768;
constexpr int G2_NKT = 16, G2_MRING = 98304;
constexpr int n_off(int k) { return (k % 3) * 32768; }
constexpr int m_off(int k) { return ((k + 2) % 3) * 16384; }
constexpr int k_ord(int k) { return k < 2 ? k : (k < 9 ? 2 * (k - 1) : 2 * (k - 8) + 1); }
constexpr int LDS_DUMMY = G2_MRING + 49152;
constexpr int LDS_GU = LDS_DUMMY + 4096;
constexpr int LDS_FIN = LDS_GU + 4096;
constexpr int LDS_MB = LDS_FIN + 4096;
constexpr int LDS_SEL = LDS_MB + 1536;
constexpr int LDS_SIG = LDS_SEL + 64;
constexpr int LDS_KEY = LDS_SIG + 32;
constexpr int LDS_BYTES = LDS_KEY + 128;
constexpr int LDS_ACC = n_off(G2_NKT - 3);

constexpr int sp_tile(int q) { return q < VA0 ? q : q + VAN < 16 ? q + VAN : -1; }
constexpr int dn_cnt(int q) { return dig(DVS, q); }
constexpr int dn_first(int q) { return acc_dig(DVS, q); }

static __device__ __forceinline__ void code_touch(unsigned& sink, unsigned off) {
  asm volatile("s_getpc_b64 s[40:41]\n\tk_main_rt:\n\ts_min_u32 %1, %1, k_main_end-k_main_rt-196\n\t"
               "s_load_dword %0, s[40:41], %1 offset:0\n\ts_load_dword %0, s[40:41], %1 offset:64\n\ts_load_dword %0, s[40:41], %1 offset:128\n\ts_load_dword %0, s[40:41], %1 offset:192"
               : "+s"(sink), "+s"(off) :: "s40", "s41", "scc", "memory");
}
static __device__ __forceinline__ void touch1(__amdgpu_buffer_rsrc_t r, int voff, unsigned mb, int soff) { TOUCH(r, voff, mb, LDS_DUMMY, soff); }
typedef const __attribute__((address_space(1))) u16* gptr;
typedef __attribute__((ext_vector_type(8))) u64 u64x8;
typedef __attribute__((ext_vector_type(2))) u64 u64x2;
extern "C" __global__ __launch_bounds__(512, 2) void k_main(const u16* x, const u16* wd_re, const u16* wd_im, const u16* w_r,
                                                 char* wsp, const u16* ws_re, const u16* ws_im,
                                                 const u16* w_gate, const u16* w_up, const u16* w_down, u16* out) {
  __shared__ __attribute__((aligned(16))) char smem[LDS_BYTES];
  u64x8 ka;
  u64x2 kb_;
  u64 kc;
  asm volatile("s_load_dwordx16 %0, %3, 0\n\ts_load_dwordx4 %1, %3, 64\n\ts_load_dwordx2 %2, %3, 80"
               : "=&s"(ka), "=&s"(kb_), "=&s"(kc) : "s"(__builtin_amdgcn_kernarg_segment_ptr()) : "memory");
  unsigned long long t00;
  asm volatile("s_memrealtime %0" : "=s"(t00) :: "memory");
  const int tid = threadIdx.x, lane = tid & 63;
  const int wid = __builtin_amdgcn_readfirstlane(tid >> 6);
  const int g = blockIdx.x, xcd = ((~g & 1) << 2) | ((g >> 1) & 3), j = g >> 3;
  if (wid < 4) {
    unsigned cw, co = (j * 4 + wid) * 448;
    asm volatile(".subsection 1\nk_main_end:\n.subsection 0\n\ts_min_u32 %1, %1, k_main_end-k_main-480\n\ts_getpc_b64 s[40:41]\n\ts_add_u32 s40, s40, %1\n\ts_addc_u32 s41, s41, 0\n\t"
                 "s_load_dword %0, s[40:41], 0\n\ts_load_dword %0, s[40:41], 64\n\ts_load_dword %0, s[40:41], 128\n\t"
                 "s_load_dword %0, s[40:41], 192\n\ts_load_dword %0, s[40:41], 256\n\ts_load_dword %0, s[40:41], 320\n\ts_load_dword %0, s[40:41], 384\n\ts_waitcnt lgkmcnt(0)"
                 : "=&s"(cw), "+s"(co) :: "s40", "s41", "scc", "memory");
  }
  const int pw = wid - 4;
  const int row0 = (wid & 3) * 8 + lrw(lane >> 3);
  const int cm = ((lane & 7) ^ swz(row0)) * 16;
  int vm = ((row0 & 15) * 2 + (row0 >> 4)) * (D_ * 2) + cm;
  asm volatile("" : "+v"(vm));
  const int kq = xcd >> 1;
  const int tt = (xcd & 1) * 4 + (j >> 3);
  const int et = j & 7;
  const int z = kq >> 1, st = (kq & 1) * 8 + tt;
  const int lrow = (kq & 1) * 1024 + (xcd & 1) * 512 + z * 256 + j * 8 + pw * 2;
  const int drow = ((kq & 1) * 2 + (xcd & 1)) * 256 + j * 8 + pw * 2;
  unsigned o_m = st * 128 * D_ * 2, o_l = lrow * D_ * 2, o_d = drow * D_ * 2, mb0 = lds_addr(smem) + pw * 1024;
  int l16 = lane * 16;
  int vn[4];
#pragma unroll
  for (int q = 0; q < 4; ++q) vn[q] = sym_lo(32, D_ * 2, (row0 >> 2) & 3, q * 8 + (row0 >> 4) * 4 + (row0 & 3)) ^ (sym_hi(D_, 32, D_ * 2, et, z, (row0 >> 2) & 3) | cm);
  asm volatile("" : "+s"(o_m), "+s"(o_l), "+s"(o_d), "+s"(mb0));
  asm volatile("" : "+v"(l16), "+v"(vn[0]), "+v"(vn[1]), "+v"(vn[2]), "+v"(vn[3]));
  asm volatile("s_waitcnt lgkmcnt(0)" : "+s"(ka), "+s"(kb_), "+s"(kc) :: "memory");
  x = (const u16*)(gptr)ka[0]; wd_re = (const u16*)(gptr)ka[1]; wd_im = (const u16*)(gptr)ka[2]; w_r = (const u16*)(gptr)ka[3];
  wsp = (char*)(gptr)ka[4]; ws_re = (const u16*)(gptr)ka[5]; ws_im = (const u16*)(gptr)ka[6]; w_gate = (const u16*)(gptr)ka[7];
  w_up = (const u16*)(gptr)kb_[0]; w_down = (const u16*)(gptr)kb_[1]; out = (u16*)(gptr)kc;
  int* flags = (int*)(wsp + WS_FLAGS);
  u64* stat = (u64*)(flags + FL_ST);
  unsigned tlv = 0;
  asm volatile("s_waitcnt lgkmcnt(0)" : "+s"(t00) :: "memory");
  tlv = lane == 52 ? (unsigned)t00 : tlv;
  asm volatile("" : "+v"(tlv));
  const u16* wdz = z ? wd_im : wd_re;
  const __amdgpu_buffer_rsrc_t rm = mk_rsrc(x);
  const __amdgpu_buffer_rsrc_t rn = mk_rsrc(wdz);
#define G1_X(kt, so, q) DMA_(rm, vm, mb0, (so) + (q) * 4096, o_m, (kt) * 128 + (((q) & 1) * 64 + ((q) >> 1) * 32) * D_ * 2, "")
#define G1_W(kt, so, q) DMA_I(rn, vn[q], mb0, (so) + 16384 + (q) * 4096, (kt) * 128)
#define G1_STAGE_TILE(kt, so, W) do { SB(); G1_X(kt, so, 0); W(0); G1_X(kt, so, 1); W(1); G1_X(kt, so, 2); W(2); G1_X(kt, so, 3); W(3); \
    G1_W(kt, so, 0); W(4); G1_W(kt, so, 1); W(5); G1_W(kt, so, 2); W(6); G1_W(kt, so, 3); W(7); SB(); } while (0)
#define G1_STAGE_A(kt, so, W) do { SB(); G1_X(kt, so, 0); W(0); G1_X(kt, so, 2); W(1); \
    G1_W(kt, so, 0); W(2); G1_W(kt, so, 1); W(3); G1_W(kt, so, 2); W(4); G1_W(kt, so, 3); W(5); W(6); W(7); SB(); } while (0)
#define G1_STAGE_B(kt, so) do { SB(); G1_X(kt, so, 1); G1_X(kt, so, 3); SB(); } while (0)
#define NOW(i)
  const int fr = lane & 15, fq = lane >> 4;
  const int ha = wid >> 2;
  int trow, xo[4], xa[3], po, ml;
  const char *prs, *prc;
  auto col4 = [](int f) __attribute__((always_inline)) { return ((f & 2) << 3 | (f & 4) << 1 | (f & 1) << 2) ^ (-((f >> 3) & 1) & 28); };
  auto seg0 = [&](int hf, int dn) __attribute__((always_inline)) { return (hf * 512 + et * 32) ^ (-dn & (D_ - 32)); };
  auto epi_setup = [&](int part) __attribute__((always_inline)) {
    int t_ = tt, l_ = lane;
    asm volatile("" : "+s"(t_));
    asm volatile("" : "+v"(l_));
    const int ts = ((wid >> 1) & 1) * 8 + kq * 2 + ha;
    if (part == 0) { xo[3] = (seg0(wid & 1, (l_ >> 3) & 1) + col4(l_)) * 2; asm volatile("" : "+v"(xo[3])); }
    if (part == 1) {
      xo[3] |= sym_hi(S_, 64, D_ * 2, t_, z, l_ >> 4);
      ml = (l_ * 16) ^ (-sym_dn(l_ >> 4) & (z * 768));
      asm volatile("" : "+v"(xo[3]), "+v"(ml));
    }
    if (part == 2) {
      const int xh = xo[3];
#pragma unroll
      for (int r = 0; r < 4; ++r) xo[r] = sym_lo(64, D_ * 2, l_ >> 4, ts * 4 + r) ^ xh;
      const bool hi = xo[0] & 8;
      xa[0] = (hi ? xo[2] : xo[0]) & ~15; xa[1] = (hi ? xo[3] : xo[1]) & ~15;
      xa[2] = (sym_lo(64, D_ * 2, l_ >> 4, ts * 4 + (l_ & 3)) ^ xh) >> 11 << 3;
      asm volatile("" : "+v"(xo[0]), "+v"(xo[1]), "+v"(xo[2]), "+v"(xo[3]), "+v"(xa[0]), "+v"(xa[1]), "+v"(xa[2]));
    }
    if (part == 3) {
      trow = t_ * 16 + ts;
      prs = wsp + WS_SLAB + (size_t)((t_ * 8 + et) * 64 + (wid & 3)) * MSG;
      prc = (const char*)(flags + FL_X + ((t_ * 8 + et) * 64 + (wid & 3)) * 32);
      asm volatile("" : "+s"(trow), "+s"(prs), "+s"(prc));
      po = l_ < 3 ? (kq * 4 + ((kq + 1 + l_) & 3)) * 512 : (int)0x80000000;
      asm volatile("" : "+v"(po));
    }
  };
  auto tok_tile = [](unsigned key) __attribute__((always_inline)) {
    const unsigned h = (0xffff - key) & 1023, m = -(h >> 9);
    return (((h ^ m) & 0x1c0) | (((h ^ m) - m) & 63)) >> 2;
  };
#define rs mk_rsrc(prs)
#define rc mk_rsrc(prc)
  lds_vi sig = (lds_vi)(smem + LDS_SIG + (wid & 3) * 4);
  unsigned tag;
  u32x4 xq[2];
  u32x2 sq;
  f32x4 ya[4];
  unsigned key[2];
  float cv[16];
  unsigned far0, far1;
  auto xq_load = [&](int n) __attribute__((always_inline)) {
    if (n < 2) xq[n] = ld16_s<0>(x, xa[n]); else sq = ld8_s_sc1(stat, xa[2]);
  };
#define PINV(a) asm volatile("" : "+v"(a))
  auto far_calc = [&]() __attribute__((always_inline)) {
    far0 = tok_tile(key[0]) ^ trow; far1 = tok_tile(key[1]) ^ trow;
    asm volatile("" : "+s"(far0), "+s"(far1));
  };
  auto cv_calc = [&]() __attribute__((always_inline)) {
    if (__builtin_expect(!__all(sq.y == tag), 0)) sq = granule_wait(stat + (xa[2] >> 3), tag);
    const bool hi = xo[0] & 8;
    const float mb_ = lo_bf(sq.x), ri_ = __builtin_amdgcn_rcpf(hi_bf(sq.x));
#pragma unroll
    for (int r = 0; r < 4; ++r) {
      const float mb = r == 0 ? dpp_f<0x00>(mb_) : r == 1 ? dpp_f<0x55>(mb_) : r == 2 ? dpp_f<0xAA>(mb_) : dpp_f<0xFF>(mb_);
      const float ri = r == 0 ? dpp_f<0x00>(ri_) : r == 1 ? dpp_f<0x55>(ri_) : r == 2 ? dpp_f<0xAA>(ri_) : dpp_f<0xFF>(ri_);
#pragma unroll
      for (int u = 0; u < 2; ++u) {
        const unsigned own = xq[r & 1][(r & 2) + u], oth = dpp_u<0xB1>(xq[r & 1][2 - (r & 2) + u]);
        const unsigned w = hi == (r >= 2) ? own : oth;
        cv[r * 4 + 2 * u] = __builtin_fmaf(lo_bf(w) - mb, ri, lo_bf(w));
        cv[r * 4 + 2 * u + 1] = __builtin_fmaf(hi_bf(w) - mb, ri, hi_bf(w));
        PINV(cv[r * 4 + 2 * u]); PINV(cv[r * 4 + 2 * u + 1]);
      }
    }
  };
#define MSG_IDX(m) (kq * 4 + ((kq + 1 + (m)) & 3))
  unsigned seen = 0;

  if (__builtin_expect(wid >= 4, 1)) {
    __builtin_amdgcn_s_setprio(3);
    G1_STAGE_TILE(0, 0, NOW);
    constexpr int O_ST = (FL_ST - FL_AB) * 4, O_CE = (FL_CE - FL_AB) * 4, O_KEY = (FL_KEY - FL_AB) * 4, O_EP = (FL_EPOCH - FL_AB) * 4;
    const __amdgpu_buffer_rsrc_t rf = mk_rsrc(wsp + WS_FLAGS + FL_AB * 4);
    u32x4 lv[2], lv2[2], lw[2], ld[2], ld2[2];
    unsigned epv;
    {
      const char* xrow = (const char*)x + o_l;
      const char* wdrow = (const char*)wdz + o_d;
      lv[0] = ld16_s<0>(xrow, l16); lv[1] = ld16_s<1024>(xrow, l16);
      lv2[0] = ld16_s<2048>(xrow, l16); lv2[1] = ld16_s<3072>(xrow, l16);
      ld[0] = ld16_s<0>(wdrow, l16); ld[1] = ld16_s<1024>(wdrow, l16);
      ld2[0] = ld16_s<2048>(wdrow, l16); ld2[1] = ld16_s<3072>(wdrow, l16);
      lw[0] = ld16_s<0>(w_r, l16); lw[1] = ld16_s<1024>(w_r, l16);
      asm volatile("buffer_load_dword %0, off, %1, %2 sc1" : "=v"(epv) : "s"(rf), "s"(O_EP) : "memory");
    }
    SB();
    const unsigned long long did = llvm_dispatch_id();
    const unsigned dmix = (unsigned)did * 0x85EBCA6Bu + (unsigned)(did >> 32) * 0xC2B2AE35u + 0x27D4EB2Fu;
    const bool moe = et != 7;
    unsigned epoch;
    const int ci = lane & 15, rq = lane >> 4, tk = (lane >> 3) & 1;
    int mat, cg, dq, d0;
    u32x4 wv[16];
    const u16 *pgu, *pn2;
    const char *pm2, *spb;
#define rw __builtin_amdgcn_make_buffer_rsrc((void*)pgu, 0, moe ? 0x7fffffff : 0, 0x00020000)
#define rn2 mk_rsrc(pn2)
#define rm2 mk_rsrc(pm2)
    int vw, vn2[8], vm2;
    auto k_blk = [&](int k) __attribute__((always_inline)) { return tt ^ (k_ord(k) >> 1); };
#define KROT(kk_) (k_blk(kk_) * 256 + (k_ord(kk_) & 1) * 128)
    int tv2, tv7;
    unsigned sd = 0;
    auto spf = [&](auto N) __attribute__((always_inline)) {
      constexpr int n = decltype(N)::value;
      if constexpr (n >= 0 && sp_tile(n >> 4) >= 0) sld<((n >> 1) & 7) * 4096 + (n & 1) * 64>(sd, spb, KROT(sp_tile(n >> 4)));
    };
    unsigned mq0, mq1, mq2, mq3;
    const int cg8 = et ^ ((et & 1) << 1), rgb = (g & 7) * 4 + (j >> 3);
    const __amdgpu_buffer_rsrc_t rdn = mk_rsrc(w_down);
    const __amdgpu_buffer_rsrc_t rgu = mk_rsrc(wsp + WS_GU);
    int vdn, vgu;
    u32x4 dv[7], gv[4];
    int abo, abq[3];
    unsigned lma;
    unsigned sel[2][4];
    float g0, g1;
    u32x4 kk[4];
    unsigned k1 = 0, k2 = 0;
    const int ko_ = O_KEY + pw * 4096;
#define MM(a, b, c) __builtin_amdgcn_mfma_f32_16x16x32_bf16(__builtin_bit_cast(bf16x8, a), __builtin_bit_cast(bf16x8, b), c, 0, 0, 0)
#define PIN1(a) asm volatile("" : "+v"(a))
#define PIN2(a, b) asm volatile("" : "+v"(a), "+v"(b))
#define PIN3(a, b, c) asm volatile("" : "+v"(a), "+v"(b), "+v"(c))
    {
      int gro, go;
      u32x2 gr;
      u32x4 oh[5];
      f32x4 sa4, gw[2], gx[2];
      float su, gs, ta, tb, sx, mbv;
      unsigned dk[4], kl;
      const int hb = lane >> 5;
#pragma unroll
      for (int l = 0; l < WSN; ++l) asm volatile("" : "=v"(wv[l]));
      auto diag = [&](const f32x4& m) __attribute__((always_inline)) {
        return __uint_as_float((__float_as_uint(m[0]) & dk[0]) | (__float_as_uint(m[1]) & dk[1]) | (__float_as_uint(m[2]) & dk[2]) | (__float_as_uint(m[3]) & dk[3]));
      };
      auto setup = [&](auto JB, auto GP) __attribute__((always_inline)) {
        constexpr int jb = decltype(JB)::value, gp = decltype(GP)::value;
        SB();
        {
          int ln = lane, jj = j, p_ = pw;
          launder(ln, jj, p_);
          const int c_ = ln & 15, r_ = ln >> 4, e_ = jj & 7, t_ = (xcd & 1) * 4 + (jj >> 3);
          if constexpr (jb == 1 && gp == 0) {
            pn2 = ((kq >> 1) ? ws_im : ws_re) + (kq & 1) * 1024;
            spb = (const char*)(pn2 + (sym_row(S_, 64, 1, t_, 0, e_ >> 1, 0) + (e_ & 1) * 32 + p_ * 8) * S_);
            pins(pn2, spb);
          }
          if constexpr (jb == 1 && gp >= 1 && gp < 6) {
            const unsigned o = c_ == gp - 1 ? 0x3f803f80u : 0u;
            oh[gp - 1] = (u32x4){o, o, o, o};
            PIN1(oh[gp - 1]);
          }
          if constexpr (jb == 1 && gp == 6) {
            const int rk = moe ? e_ * 8 + (jj >> 3) * 2 + (xcd >> 2) : 0, r3 = ((rk - 14) * 43) >> 7, rr = rk - 14 - r3 * 3;
            const int c56 = rk < 14 ? rk * 4 + 1 : r3 * 4 + rr + ((rr + 1) >> 1);
            mat = (c56 + 36) >> 6; cg = c56 - mat * 28; dq = xcd & 3; d0 = dq * 256 + p_ * 64;
            pin4(mat, cg, dq, d0);
          }
          if constexpr (jb == 2 && gp == 0) {
            pgu = mat ? w_up : w_gate;
            pm2 = wsp + WS_ABT + (kq * 8 + e_) * 262144;
            pins(pgu, pm2);
            vw = ((d0 + r_ * 16) * F_ + cg * 128 + c_ * 8) * 2;
            PIN1(vw);
          }
          if constexpr (jb == 2 && (gp == 1 || gp == 2)) {
            dk[gp * 2 - 2] = c_ == r_ * 4 + gp * 2 - 2 ? ~0u : 0u; dk[gp * 2 - 1] = c_ == r_ * 4 + gp * 2 - 1 ? ~0u : 0u;
            PIN2(dk[gp * 2 - 2], dk[gp * 2 - 1]);
          }
          if constexpr (jb == 2 && gp == 3) { kl = lrow + (ln >> 5); go = (ln & 30) ? (int)0x80000000 : ((ln & 1) ? O_KEY : O_ST) + (int)kl * 8; PIN2(kl, go); }
          if constexpr (jb == 2 && gp == 4) { kl = 0xffff - kl; go = (ln & ~1) == 2 ? O_CE + (z * D_ + drow + (ln & 1)) * 8 : go; PIN2(kl, go); }
          if constexpr (jb == 2 && gp == 5) {
            gro = p_ < 2 ? O_ST + (st * 128 + p_ * 64 + ln) * 8 : O_CE + z * D_ * 8 + sym_row(D_, 32, 8, e_, z, (ln >> 2) & 3, (p_ - 2) * 16 + r_ * 4 + (ln & 3));
            lma = lds_addr(smem + LDS_MB) + (p_ < 2 ? p_ * 256 : 512 + p_ * 256) + ln * 4;
            PIN2(gro, lma);
          }
          if constexpr (jb == 7 && gp == 0) {
            vm2 = p_ * 1024 + ln * 16;
            PIN1(vm2);
          }
          if constexpr (jb == 2 && gp == 6) {
            const int r0 = p_ * 8 + lrw(ln >> 3);
            vn2[6] = sym_lo(64, S_ * 2, (r0 >> 2) & 3, (r0 >> 4) * 4 + (r0 & 3)); vn2[7] = sym_hi(S_, 64, S_ * 2, t_, z, (r0 >> 2) & 3) | (vm & 127);
            PIN2(vn2[6], vn2[7]);
          }
          if constexpr ((jb == 2 && gp == 7) || (jb == 7 && gp >= 6)) {
            constexpr int q0 = jb == 2 ? 0 : gp == 6 ? 3 : 6;
            const int lo = vn2[6], hi = vn2[7];
#pragma unroll
            for (int q = q0; q < q0 + 3 && q < 8; ++q) { vn2[q] = ((lo + q * 8 * S_ * 2) & (63 * S_ * 2)) ^ hi; PIN1(vn2[q]); }
          }
          if constexpr (jb == 7 && gp == 1) { vdn = rgb * 112 + p_ * 28 + r_ * 7; vgu = (((ln >> 3) & 1) * 4 * F_ + vdn + min(ln & 7, 6)) * 16; PIN2(vdn, vgu); }
          if constexpr (jb == 7 && gp == 2) { vdn = (vdn * D_ + cg8 * 128 + c_ * 8) * 2; PIN1(vdn); }
          if constexpr (jb == 7 && gp == 3) {
            abo = (((kq * 8 + e_) * 4 + p_) * 16 + c_) * AB_STRIDE * 4;
            abq[2] = ln >= 16 || (ln & 7) == t_ ? (int)0x80000000 : abo;
            PIN2(abo, abq[2]);
          }
          if constexpr (jb == 7 && gp == 4) {
            abq[1] = ln >= 8 ? (int)0x80000000 : abq[2];
            abq[0] = ((ln ^ t_) & 4) ? (int)0x80000000 : abq[1];
            PIN2(abq[0], abq[1]);
          }
          if constexpr (jb == 7 && gp == 5) {
            const unsigned mb = lds_addr(smem) + p_ * 1024;
            mq0 = mb + ((3 - kq) & 3) * 4096; mq1 = mb + ((4 - kq) & 3) * 4096; mq2 = mb + ((5 - kq) & 3) * 4096; mq3 = mb + ((6 - kq) & 3) * 4096;
            pin4(mq0, mq1, mq2, mq3);
          }
          if constexpr (jb == 8 && gp < 4) {
#pragma unroll
            for (int h = 0; h < 2; ++h) {
              const unsigned hv = h ? 0x0302u : 0x0100u;
              sel[h][gp] = ((ln & 7) >> 1) == gp ? ((ln & 1) ? (hv << 16) | 0x0c0cu : 0x0c0c0000u | hv) : 0x0c0c0c0cu;
            }
            PIN2(sel[0][gp], sel[1][gp]);
          }
        }
        SB();
      };
#define WS1(i) setup(IC<1>{}, IC<i>{})
#define WS2(i) setup(IC<2>{}, IC<i>{})
#define WS3(i) do { setup(IC<7>{}, IC<i>{}); if (i >= 4) setup(IC<8>{}, IC<(i) & 3>{}); spf(IC<(i < SP0 ? i : -1)>{}); } while (0)
      G1_STAGE_TILE(1, G1_STAGE, WS1);
      WAIT_VM(19); BAR();
      G1_STAGE_TILE(2, 2 * G1_STAGE, WS2);
      G1_STAGE_TILE(3, 3 * G1_STAGE, WS3);
      auto lnu = [&](auto U) __attribute__((always_inline)) {
        constexpr int jb = 1 + decltype(U)::value / 7, gp = decltype(U)::value % 7;
        SB();
        if constexpr (jb == 1) {
          const f32x4 z4 = {0.f, 0.f, 0.f, 0.f};
          if constexpr (gp == 0) { sa4 = MM(lv[0], oh[0], z4); gw[0] = MM(lv[0], lw[0], z4); gx[0] = MM(lv[0], lv[0], z4); PIN3(sa4, gw[0], gx[0]); }
          if constexpr (gp == 1) { sa4 = MM(lv2[0], oh[1], sa4); gw[1] = MM(lv2[0], lw[0], z4); gx[1] = MM(lv2[0], lv2[0], z4); PIN3(sa4, gw[1], gx[1]); }
          if constexpr (gp == 2) { sa4 = MM(lv[1], oh[0], sa4); gw[0] = MM(lv[1], lw[1], gw[0]); gx[0] = MM(lv[1], lv[1], gx[0]); PIN3(sa4, gw[0], gx[0]); }
          if constexpr (gp == 3) { sa4 = MM(lv2[1], oh[1], sa4); gw[1] = MM(lv2[1], lw[1], gw[1]); gx[1] = MM(lv2[1], lv2[1], gx[1]); PIN3(sa4, gw[1], gx[1]); }
          if constexpr (gp == 4) { sa4 = MM(ld[0], oh[2], sa4); sa4 = MM(ld[1], oh[2], sa4); PIN1(sa4); }
          if constexpr (gp == 5) { sa4 = MM(ld2[0], oh[3], sa4); sa4 = MM(ld2[1], oh[3], sa4); PIN1(sa4); }
          if constexpr (gp == 6) { sa4 = MM(lw[0], oh[4], sa4); sa4 = MM(lw[1], oh[4], sa4); PIN1(sa4);
            epoch = __builtin_amdgcn_readfirstlane(epv); tag = ((epoch + 1u) * 0x9E3779B1u ^ dmix) | 1u; }
        }
        if constexpr (jb == 2) {
          if constexpr (gp == 0) { su = (sa4[0] + sa4[1]) + (sa4[2] + sa4[3]); ta = diag(gw[0]); PIN2(su, ta); }
          if constexpr (gp == 1) { su = fold32(su, su); tb = diag(gw[1]); PIN2(su, tb); }
          if constexpr (gp == 2) { su = fold16(su, su); gs = fold32(ta, tb); PIN2(su, gs); }
          if constexpr (gp == 3) { ta = diag(gx[0]); tb = diag(gx[1]); PIN2(ta, tb); }
          if constexpr (gp == 4) { gs = fold16(gs, fold32(ta, tb)); PIN1(gs); }
          if constexpr (gp == 5) { gs += dpp_f<0xB1>(gs); gs += dpp_f<0x4E>(gs); PIN1(gs); }
          if constexpr (gp == 6) { gs += dpp_f<0x141>(gs); gs += dpp_f<0x140>(gs); PIN1(gs); }
        }
        if constexpr (jb == 3) {
          if constexpr (gp == 0) {
            const float s0 = rdl(su, 0), s1 = rdl(su, 1), q0 = rdl(gs, 16), q1 = rdl(gs, 48);
            sx = hb ? s1 : s0; ta = hb ? q1 : q0; PIN2(sx, ta);
          }
          if constexpr (gp == 1) {
            const float mean = sx * (1.0f / D_);
            mbv = rbf(mean);
            ta -= sx * mean;
            if (__builtin_expect(__any(!(sx * mean <= 16.f * ta)), 0)) {
              float q = fold32(sqdev16(lv, rdl(mbv, 0)), sqdev16(lv2, rdl(mbv, 32)));
              q = row_sum(fold16(q, q));
              const float dm = mean - mbv;
              ta = fmaxf(q - (float)D_ * dm * dm, 0.f);
            }
            PIN2(mbv, ta);
          }
          if constexpr (gp == 2) { ta = rbf(rbf(ta * (1.0f / D_)) + 1e-5f); gs -= mbv * rdl(su, 4); PIN2(ta, gs); }
          if constexpr (gp == 3) { ta = rbf(__builtin_amdgcn_sqrtf(ta)); tb = __builtin_amdgcn_rcpf(ta); PIN2(ta, tb); }
          if constexpr (gp == 4) {
            const unsigned rb = __float_as_uint(rbf(gs * tb));
            kl |= (rb ^ ((unsigned)((int)rb >> 31) | 0x80000000u)) & 0xffff0000u; PIN1(kl);
          }
          if constexpr (gp == 5) { kl = (lane & 2) ? __float_as_uint(su) : (lane & 1) ? kl : (__float_as_uint(mbv) >> 16) | __float_as_uint(ta); PIN1(kl); }
        }
        SB();
      };
      constexpr int LN_JB = 1 + (19 * LNS) / 8;
      auto work1 = [&](auto JB, auto GP) __attribute__((always_inline)) {
        constexpr int jb = decltype(JB)::value, gp = decltype(GP)::value, pos = (jb - 1) * 8 + gp;
        SB();
        if constexpr (pos % LNS == 0 && pos / LNS < 20) lnu(IC<pos / LNS>{});
        if constexpr (jb == ST_JB + 3) {
          if constexpr (gp == 0) { if (!__all(gr.y == tag)) gr = granule_wait((const u64*)((const char*)(flags + FL_AB) + gro), tag); PIN1(gr); }
          if constexpr (gp == 1) { g1 = __builtin_amdgcn_rcpf(__uint_as_float((gr.x & 0xffff0000u) ^ ((unsigned)z << 31))); PIN1(g1); }
          if constexpr (gp == 2) { g0 = pw < 2 ? -(lo_bf(gr.x) * g1) : __uint_as_float(gr.x); PIN1(g0); }
        }
        if constexpr (jb == 8 && (gp == 2 || gp == 5)) {
          static_assert(VAN == 4 && VA0 >= 2 && VA0 + VAN <= 9, "first halves");
          int ln = lane;
          PIN1(ln);
          const int i = (ln & 15) * 4 + pw, n = gp == 2 ? (moe ? 0 : 64) : (et < 2 ? 0 : 32);
          const int r0 = gp == 2 ? tt * 64 : sym_row(S_, 64, 1, tt, 0, et >> 1, 0) + (et & 1) * 32;
          (gp == 2 ? tv7 : tv2) = i < n ? (r0 + i) * (S_ * 2) + ((tt ^ (VA0 - 1 + (ln >> 4))) << 8) : (int)0x80000000;
          PIN1(gp == 2 ? tv7 : tv2);
        }
        if constexpr (gp < SP1 && jb <= SP1E) spf(IC<SP0 + (jb - 1) * SP1 + gp>{});
        SB();
      };
      auto step1 = [&](auto JB) __attribute__((always_inline)) {
        constexpr int jb = decltype(JB)::value;
        if constexpr (jb == 1) PAGE(1);
        if constexpr (jb == 7) PAGE(2);
        constexpr int n16 = jb == 12 ? 12 : jb == 11 ? 14 : 16;
        constexpr auto wl = [](int q) { return (q == LN_JB ? 1 : 0) + (q == ST_JB ? 1 : 0); };
        if constexpr (jb == 1) {
          asm volatile("s_waitcnt vmcnt(%11)" : "+v"(lv[0]), "+v"(lv[1]), "+v"(lw[0]), "+v"(lw[1]), "+v"(ld[0]), "+v"(ld[1]),
                       "+v"(lv2[0]), "+v"(lv2[1]), "+v"(ld2[0]), "+v"(ld2[1]), "+v"(epv) : "i"(16) : "memory");
        } else if constexpr (jb == ST_JB + 3) {
          asm volatile("s_waitcnt vmcnt(%1)" : "+v"(gr) : "i"(n16 + wl(jb - 2) + wl(jb - 1)) : "memory");
        } else wait_vm<n16 + wl(jb - 3) + wl(jb - 2) + wl(jb - 1)>();
        BAR();
        if constexpr (jb == 2) { if (pw == 0 && lane < 16) ((int*)(smem + LDS_SEL))[lane < 8 ? lane : lane + 8] = 0; }
        if constexpr (jb == 4) { if (pw == 0 && lane == 0) ((unsigned*)(smem + LDS_SEL))[5] = tag; }
#define W1(i) work1(JB, IC<i>{})
        if constexpr (jb < 10) G1_STAGE_TILE(jb - 1 + G1_NST, ((jb - 1) % G1_NST) * G1_STAGE, W1);
        else G1_STAGE_A(jb - 1 + G1_NST, ((jb - 1) % G1_NST) * G1_STAGE, W1);
        if constexpr (jb == 12) G1_STAGE_B(13, G1_STAGE);
        if constexpr (jb == LN_JB) __builtin_amdgcn_raw_buffer_store_b64((u32x2){kl, tag}, rf, go, 0, 16);
        if constexpr (jb == ST_JB) {
          asm volatile("buffer_load_dwordx2 %0, %1, %2, 0 offen sc1" : "=v"(gr) : "v"(gro), "s"(rf) : "memory");
        }
        if constexpr (jb <= 8) {
          if (et == jb - 1) {
#pragma unroll
            for (int l = 0; l < WSN; ++l) bld16_nt_io(wv[l], rw, vw, l * F_ * 2);
          }
        }
        SB();
      };
      static_assert(ST_JB + 3 <= 12 && ST_JB >= 6, "schedule");
      step1(IC<1>{}); step1(IC<2>{}); step1(IC<3>{}); step1(IC<4>{}); step1(IC<5>{}); step1(IC<6>{}); step1(IC<7>{}); step1(IC<8>{}); step1(IC<9>{}); step1(IC<10>{}); step1(IC<11>{}); step1(IC<12>{});
    }
#define G2_STAGE_N(kk_, W) do { const int kb = KROT(kk_); constexpr int so = n_off(kk_); SB(); \
    DMA_S(rn2, vn2[0], mq0, so, kb); W(0); DMA_S(rn2, vn2[1], mq1, so, kb); W(1); \
    DMA_S(rn2, vn2[2], mq2, so, kb); W(2); DMA_S(rn2, vn2[3], mq3, so, kb); W(3); \
    DMA_S(rn2, vn2[4], mq0, so + 16384, kb); W(4); DMA_S(rn2, vn2[5], mq1, so + 16384, kb); W(5); \
    DMA_S(rn2, vn2[6], mq2, so + 16384, kb); W(6); DMA_S(rn2, vn2[7], mq3, so + 16384, kb); W(7); SB(); } while (0)
#define G2_STAGE_NH(kk_, h) do { const int kb = KROT(kk_); constexpr int so = n_off(kk_) + (h) * 16384; SB(); \
    DMA_S(rn2, vn2[(h) * 4], mq0, so, kb); DMA_S(rn2, vn2[(h) * 4 + 1], mq1, so, kb); \
    DMA_S(rn2, vn2[(h) * 4 + 2], mq2, so, kb); DMA_S(rn2, vn2[(h) * 4 + 3], mq3, so, kb); SB(); } while (0)
#define G2_STAGE_M(kk_, W) do { const int kb = KROT(kk_) * 128; constexpr int so = G2_MRING + m_off(kk_); SB(); \
    DMA_(rm2, vm2, mb0, so, kb, 0, " sc1"); W(8); DMA_(rm2, vm2, mb0, so + 4096, kb, 4096, " sc1"); W(9); \
    DMA_(rm2, vm2, mb0, so + 8192, kb, 8192, " sc1"); W(10); DMA_(rm2, vm2, mb0, so + 12288, kb, 12288, " sc1"); W(11); SB(); } while (0)
    WAIT_VM(8); BAR();
    asm volatile("ds_write_b32 %0, %1" :: "v"(lma), "v"(g0) : "memory");
    if (pw < 2) asm volatile("ds_write_b32 %0, %1 offset:512" :: "v"(lma), "v"(g1) : "memory");
    u32x4 tv[4];
    unsigned ready = 0;
    auto need = [&](int k) __attribute__((always_inline)) {
      const int b = k_blk(k) + (k_ord(k) & 1) * 8;
#pragma unroll 1
      for (int it = 0; !((ready >> b) & 1) && it < SPIN_MAX; ++it) {
        ready |= (unsigned)__ballot(__builtin_amdgcn_raw_buffer_load_b32(rf, abq[k < 5 ? 0 : k < 9 ? 1 : 2], 0, 16) == tag);
        asm volatile("" ::: "memory");
      }
    };
    unsigned ksel, bad = 0;
    int isel;
    unsigned xw;
    auto bvec = [&](unsigned src, const unsigned (&sl)[4]) __attribute__((always_inline)) {
      return (u32x4){__builtin_amdgcn_perm(0, src, sl[0]), __builtin_amdgcn_perm(0, src, sl[1]),
                     __builtin_amdgcn_perm(0, src, sl[2]), __builtin_amdgcn_perm(0, src, sl[3])};
    };
#define BC8(v, p) (unsigned)__builtin_amdgcn_update_dpp(__builtin_amdgcn_update_dpp(0, (int)(v), 0x150 + (p), 0xf, 0x3, false), (int)(v), 0x158 + (p), 0xf, 0xc, false)
    float* lg = (float*)(smem + LDS_GU);
    f32x4 macc = {0.f, 0.f, 0.f, 0.f};
    u32x2 sv;
    unsigned hp, gbad;
    float s0, s1;
    auto work2 = [&](auto JB, auto GP) __attribute__((always_inline)) {
      constexpr int jb = decltype(JB)::value, gp = decltype(GP)::value;
      SB();
      if constexpr (jb == -1 && gp < 4) {
        constexpr int c = gp == 0 ? 0xB1 : gp == 1 ? 0x4E : gp == 2 ? 0x141 : 0x140;
        merge2(k1, k2, dpp_u<c>(k1), dpp_u<c>(k2));
        PIN2(k1, k2);
      }
      if constexpr (jb == -1 && gp == 4) {
        ksel = tk ? k2 : k1;
        isel = (0xffff - (int)(ksel & 0xffffu)) & (S_ - 1);
        k1 = (isel * D_ + d0 + rq * 16 + (lane & 7) * 2) * 2; k2 = O_ST + isel * 8;
        PIN1(ksel);
        xw = gld4_s(x, k1);
        sv = bld8_sc1(rf, k2);
      }
      if constexpr (jb == 0 && gp == 1) wait_xs<3 + 2 + 2>(xw, sv);
      if constexpr (jb == 0) {
        if constexpr (gp == 1) { if (!__all(sv.y == tag)) sv = granule_wait(stat + isel, tag); s0 = __builtin_amdgcn_rcpf(hi_bf(sv.x)); PIN1(s0); }
        if constexpr (gp == 2) { s1 = ln_elem(lo_bf(xw), lo_bf(sv.x), hi_bf(sv.x), s0); PIN1(s1); }
        if constexpr (gp == 3) { xw = pk2(s1, ln_elem(hi_bf(xw), lo_bf(sv.x), hi_bf(sv.x), s0)); PIN1(xw); }
      }
      if constexpr (jb == DG && gp < 7) { PIN1(dv[gp]); macc = MM(dv[gp], bvec(BC8(hp, gp), sel[0]), macc); PIN1(macc); }
      if constexpr ((jb == 0 && gp >= 4) || jb == 1) {
        constexpr int l = jb == 0 ? gp - 4 : gp + 4;
        if constexpr (!(l & 1)) hp = BC8(xw, l >> 1);
        PIN1(wv[l]);
        macc = MM(wv[l], bvec(hp, sel[l & 1]), macc);
        PIN2(macc, hp);
      }
      if constexpr (jb == GV_JB + 2) {
        if constexpr (gp == 0) { gbad = (gv[0].y ^ tag) | (gv[0].w ^ tag) | (gv[1].y ^ tag) | (gv[1].w ^ tag); PIN1(gbad); }
        if constexpr (gp == 1) { gbad |= (gv[2].y ^ tag) | (gv[2].w ^ tag) | (gv[3].y ^ tag) | (gv[3].w ^ tag); PIN1(gbad); }
        if constexpr (gp == 2) {
          s0 = ((__uint_as_float(gv[0].x) + __uint_as_float(gv[1].x)) + __uint_as_float(gv[2].x)) + __uint_as_float(gv[3].x);
          s1 = ((__uint_as_float(gv[0].z) + __uint_as_float(gv[1].z)) + __uint_as_float(gv[2].z)) + __uint_as_float(gv[3].z);
          PIN2(s0, s1);
        }
        if constexpr (gp == 3) {
#pragma unroll 1
          for (int it = 0; !__all(gbad == 0) && it < SPIN_MAX; ++it) {
            u32x4 q[4];
            gbad = 0;
#pragma unroll
            for (int i = 0; i < 4; ++i) { q[i] = __builtin_amdgcn_raw_buffer_load_b128(rgu, vgu, i * F_ * 16, 16); gbad |= (q[i].y ^ tag) | (q[i].w ^ tag); }
            s0 = ((__uint_as_float(q[0].x) + __uint_as_float(q[1].x)) + __uint_as_float(q[2].x)) + __uint_as_float(q[3].x);
            s1 = ((__uint_as_float(q[0].z) + __uint_as_float(q[1].z)) + __uint_as_float(q[2].z)) + __uint_as_float(q[3].z);
            asm volatile("" ::: "memory");
          }
          const unsigned gp2 = pk2(s0, s1);
          const unsigned up2 = dpp_u<0x128>(gp2);
          s0 = tk ? hi_bf(up2) : lo_bf(gp2); s1 = tk ? hi_bf(gp2) : lo_bf(up2);
          PIN2(s0, s1);
        }
        if constexpr (gp == 4) { s0 = rbf(s0 * __builtin_amdgcn_rcpf(1.f + __expf(-s0))); PIN1(s0); }
        if constexpr (gp == 5) { hp = pk2(s0 * s1, 0.f); PIN1(hp); }
      }
      if constexpr (jb == 10 && gp == 4) code_touch(sd, (j * 4 + pw) * 256);
      SB();
    };
#define W2M(i) work2(IC<-1>{}, IC<i>{})
#define W20(i) work2(IC<0>{}, IC<i>{})
#define W21(i) work2(IC<1>{}, IC<i>{})
    unsigned fs;
    {
      G1_STAGE_B(15, 3 * G1_STAGE);
      G1_STAGE_B(14, 2 * G1_STAGE);
      lgkm0(sd);
      BAR();
      G2_STAGE_NH(0, 0);
      WAIT_VM(6); BAR();
#pragma unroll
      for (int i = 0; i < 4; ++i) kk[i] = bld16_sc1(rf, l16, ko_ + i * 1024);
      G2_STAGE_NH(0, 1);
#pragma unroll
      for (int l = WSN; l < 16; ++l) wv[l] = bld16_nt(rw, vw, l * F_ * 2);
      touch1(rn2, tv7, mb0, 0);
      asm volatile("s_waitcnt vmcnt(%4)" : "+v"(kk[0]), "+v"(kk[1]), "+v"(kk[2]), "+v"(kk[3]) : "i"(16 - WSN + 5) : "memory");
#pragma unroll
      for (int i = 0; i < 4; ++i) {
        k2 = med3u(k1, k2, kk[i].x); k1 = max(k1, kk[i].x);
        k2 = med3u(k1, k2, kk[i].z); k1 = max(k1, kk[i].z);
        bad |= (kk[i].y ^ tag) | (kk[i].w ^ tag);
      }
#pragma unroll 1
      for (int it = 0; !__all(bad == 0) && it < SPIN_MAX; ++it) {
        k1 = 0; k2 = 0; bad = 0;
#pragma unroll
        for (int i = 0; i < 4; ++i) {
          const u32x4 q = __builtin_amdgcn_raw_buffer_load_b128(rf, l16, ko_ + i * 1024, 16);
          k2 = med3u(k1, k2, q.x); k1 = max(k1, q.x);
          k2 = med3u(k1, k2, q.z); k1 = max(k1, q.z);
          bad |= (q.y ^ tag) | (q.w ^ tag);
        }
        asm volatile("" ::: "memory");
      }
      merge2(k1, k2, dpp_u<0xB1>(k1), dpp_u<0xB1>(k2));
      merge2(k1, k2, dpp_u<0x4E>(k1), dpp_u<0x4E>(k2));
      merge2(k1, k2, dpp_u<0x141>(k1), dpp_u<0x141>(k2));
      merge2(k1, k2, dpp_u<0x140>(k1), dpp_u<0x140>(k2));
      if ((lane & 15) == 0) *(u32x2*)(smem + LDS_KEY + (pw * 4 + rq) * 8) = (u32x2){k1, k2};
      PAGE(3);
      wait_vm<17 - WSN>();
      BAR_LGKM();
#pragma unroll
      for (int jj = 0; jj < 4; ++jj) tv[jj] = *(const u32x4*)(smem + G2_MRING + m_off(0) + (jj * 4 + pw) * 1024 + lane * 16);
      { const u32x2 kp = *(const u32x2*)(smem + LDS_KEY + ci * 8); k1 = kp.x; k2 = kp.y; }
#pragma unroll
      for (int jj = 0; jj < 4; ++jj) __builtin_amdgcn_raw_buffer_store_b128(tv[jj], rm2, vm2 + jj * 4096 + 2 * tt * 16384, 0, 16);
      G2_STAGE_N(1, W2M);
      touch1(rn2, tv2, mb0, 0);
      WAIT_VM(11);
      if (lane == tt) __builtin_amdgcn_raw_buffer_store_b32(tag, rf, abo, 0, 16);
      {
        lds_vi rel = (lds_vi)(smem + LDS_SEL + 12);
        int it = 0;
        while (rel[0] != 4 && it < SPIN_MAX * 8) ++it;
        asm volatile("" ::: "memory");
      }
      G2_STAGE_N(2, W20);
      __builtin_amdgcn_s_sleep(2);
      WAIT_VM(10);
      fs = bld4_sc1(rf, abq[0]);
    }
    BAR();
#pragma unroll
    for (int jj = 0; jj < 4; ++jj) tv[jj] = *(const u32x4*)(smem + G2_MRING + m_off(1) + (jj * 4 + pw) * 1024 + lane * 16);
    constexpr int SPB = SP0 + SP1E * SP1;
    G2_STAGE_N(3, NOW);
    asm volatile("s_waitcnt vmcnt(8)" : "+v"(fs) :: "memory");
    ready = (unsigned)__ballot(fs == tag);
    need(2);
    G2_STAGE_M(2, NOW);
    need(3);
    G2_STAGE_M(3, NOW);
    fs = bld4_sc1(rf, abq[1]);
    W21(0); W21(1); W21(2); W21(3); W21(4); W21(5); W21(6); W21(7); W21(8); W21(9); W21(10); W21(11);
    asm volatile("" : "+v"(tv[0]), "+v"(tv[1]), "+v"(tv[2]), "+v"(tv[3]));
    int dno;
    auto step2 = [&](auto JB) __attribute__((always_inline)) {
      constexpr int jb = decltype(JB)::value;
      if constexpr (jb == 3) PAGE(4);
      if constexpr (jb == 9) PAGE(5);
      if constexpr (jb == 13) PAGE(7);
      constexpr auto must = [](int q) { return (q == 2 ? 4 : 0) + ((FSM >> q) & 1) + dig(GVS, q); };
      if constexpr (jb == 2) wait_vm<4 + must(1)>();
      else wait_vm<dn_cnt(jb - 2) + (jb == 5 ? 1 : 0) + 12 + must(jb - 1) + dn_cnt(jb - 1)>();
      if constexpr (jb == 3) { if (pw == 0 && (lane & ~8) == 0) ((unsigned*)(smem + LDS_SEL))[tk] = ksel; }
      if constexpr ((jb == 2 || jb == DG + 1)) {
        *(f32x4*)(lg + pw * 256 + lane * 4) = macc;
        macc = (f32x4){0.f, 0.f, 0.f, 0.f};
        lgkm0(sd);
      }
      BAR();
      float p0[4], p1[4];
      const int col = (pw & 1) * 64 + lane;
      if constexpr (jb == 3) {
        if (g == 0 && pw == 0 && lane == 0) __hip_atomic_store((unsigned*)flags + FL_EPOCH, epoch + 1u, __ATOMIC_RELAXED, __HIP_MEMORY_SCOPE_AGENT);
      }
      if constexpr (jb == DG + 1) {
        const float* p = lg + (pw & 1) * 32 + (lane >> 4) * 64 + ((2 * lane) & 7) * 4 + ((lane >> 2) & 3);
#pragma unroll
        for (int i = 0; i < 4; ++i) { p0[i] = p[i * 256]; p1[i] = p[i * 256 + 4]; }
      }
      if constexpr (jb == 2) {
        const float* p = lg + (col >> 5) * 64 + (col & 7) * 4 + ((col >> 3) & 3);
#pragma unroll
        for (int i = 0; i < 4; ++i) { p0[i] = p[i * 256]; p1[i] = p[i * 256 + 32]; }
      }
      if constexpr (jb == 4) {
        if (lane == 8 + tt) __builtin_amdgcn_raw_buffer_store_b32(tag, rf, abo, 0, 16);
      }
      if constexpr ((FSM >> (jb - 2)) & 1) {
        asm volatile("" : "+v"(fs));
        ready |= (unsigned)__ballot(fs == tag);
      }
      if constexpr (jb == GV_JB + 2) asm volatile("" : "+v"(gv[0]), "+v"(gv[1]), "+v"(gv[2]), "+v"(gv[3]));
      auto w2 = [&](auto GP) __attribute__((always_inline)) {
        constexpr int gp = decltype(GP)::value;
        if constexpr ((jb == 2 || jb == DG + 1) && gp == 0) {
          p0[0] = (p0[0] + p0[1]) + (p0[2] + p0[3]); p1[0] = (p1[0] + p1[1]) + (p1[2] + p1[3]);
          PIN2(p0[0], p1[0]);
        }
        if constexpr (jb == DG + 1 && gp == 1) {
          if (pw < 2) __builtin_amdgcn_raw_buffer_store_b64((u32x2){pk2(p0[0], p1[0]), tag}, mk_rsrc(wsp + WS_DN), (((pw * 8 + cg8) * 32 + rgb) * 64 + lane) * 8, 0, 16);
        }
        if constexpr (jb == 12 && gp == 3) epi_setup(3);
        if constexpr (jb == 12 && gp == 6) { key[0] = __builtin_amdgcn_readlane(ksel, 0); key[1] = __builtin_amdgcn_readlane(ksel, 8); far_calc(); }
        if constexpr (jb == 12 && gp == 10) {
          int l_ = lane;
          PIN1(l_);
          const int ec = seg0((l_ >> 4) & 1, (l_ >> 3) & 1);
          dno = (ec >> 7) * 16384 + (l_ >> 5) * 512 + (ec & 127) * 4 + (l_ & 7) * 16;
          PIN1(dno);
        }
        work2(JB, GP);
        if constexpr (gp < dig(SPS, jb)) spf(IC<SPB + acc_dig(SPS, jb) + gp>{});
      };
#define W2(i) w2(IC<i>{})
      G2_STAGE_N(jb + 2, W2);
      need(jb + 2);
      G2_STAGE_M(jb + 2, W2);
      if constexpr (jb == 2) {
#pragma unroll
        for (int jj = 0; jj < 4; ++jj) __builtin_amdgcn_raw_buffer_store_b128(tv[jj], rm2, vm2 + jj * 4096 + (2 * tt + 1) * 16384, 0, 16);
      }
      if constexpr ((FSM >> jb) & 1) fs = bld4_sc1(rf, abq[jb < 5 ? 1 : 2]);
#pragma unroll
      for (int i = acc_dig(GVS, jb); i < acc_dig(GVS, jb + 1); ++i) gv[i] = bld16_sc1(rgu, vgu, i * F_ * 16);
#pragma unroll
      for (int i = dn_first(jb); i < dn_first(jb) + dn_cnt(jb); ++i) dv[i] = bld16_nt(rdn, vdn, i * D_ * 2);
      if constexpr (jb == 2) {
        if (moe && pw < 2)
          __builtin_amdgcn_raw_buffer_store_b128((u32x4){__float_as_uint(p0[0]), tag, __float_as_uint(p1[0]), tag},
                                                 rgu, ((mat * 4 + dq) * F_ + cg * 128 + col) * 16, 0, 16);
      }
      SB();
      if constexpr (dig(SPS, jb) > 12) spf(IC<SPB + acc_dig(SPS, jb) + 12>{});
      SB();
    };
    static_assert(DG + 1 <= 12 && DG >= GV_JB + 3 && dn_first(DG - 2) == 7 && acc_dig(GVS, GV_JB + 1) == 4, "schedule");
    static_assert(SPB + acc_dig(SPS, 14) == (16 - VAN) * 16, "all tiles are prefetched");
    step2(IC<2>{}); step2(IC<3>{}); step2(IC<4>{}); step2(IC<5>{}); step2(IC<6>{}); step2(IC<7>{}); step2(IC<8>{}); step2(IC<9>{}); step2(IC<10>{}); step2(IC<11>{}); step2(IC<12>{}); step2(IC<13>{});
    lgkm0(sd);
    WAIT_VM(12); BAR();
    __builtin_amdgcn_s_setprio(0);
    const __amdgpu_buffer_rsrc_t rd = mk_rsrc(wsp + WS_DN + pw * 4096);
    u32x4 dq_[4];
    epi_setup(0); epi_setup(1); epi_setup(2);
    xq_load(0); xq_load(1); xq_load(2);
    asm volatile("s_cmp_lg_u32 %7, 0\n\ts_cbranch_scc1 1f\n\t"
                 "buffer_load_dwordx4 %0, %4, %5, %6 offen sc1\n\tbuffer_load_dwordx4 %1, %4, %5, %6 offen offset:1024 sc1\n\t"
                 "buffer_load_dwordx4 %2, %4, %5, %6 offen offset:2048 sc1\n\tbuffer_load_dwordx4 %3, %4, %5, %6 offen offset:3072 sc1\n\t"
                 "s_waitcnt vmcnt(7)\n\ts_branch 2f\n1:\n\ts_waitcnt vmcnt(3)\n2:\n\ts_barrier\n\ts_waitcnt vmcnt(0)"
                 : "=&v"(dq_[0]), "=&v"(dq_[1]), "=&v"(dq_[2]), "=&v"(dq_[3])
                 : "v"(dno), "s"(rd), "s"((far0 & ~9) ? 131072 : 0), "s"(min(far0 & ~9, far1 & ~9)) : "scc", "memory");
    wait_xq<0>(xq, sq);
    bool first = true;
#pragma unroll 1
    for (int c = 0; c < 2; ++c) {
      if (__builtin_expect(((c ? far1 : far0) & ~9) != 0, 1)) continue;
      const float sg = rbf(__builtin_amdgcn_rcpf(1.f + __expf(-key_score(c ? key[1] : key[0]))));
#pragma unroll 1
      for (int it = 0; it < SPIN_MAX; ++it) {
        unsigned bad = 0;
#pragma unroll
        for (int i = 0; i < 4; ++i) bad |= (dq_[i].y ^ tag) | (dq_[i].w ^ tag);
        if ((first || it) && __all(bad == 0)) break;
        if (it) __builtin_amdgcn_s_sleep(4);
#pragma unroll
        for (int i = 0; i < 4; ++i) dq_[i] = __builtin_amdgcn_raw_buffer_load_b128(rd, dno + i * 1024, c * 131072, 16);
        asm volatile("" ::: "memory");
      }
      first = false;
      f32x4 s4 = {0.f, 0.f, 0.f, 0.f};
#pragma unroll
      for (int i = 0; i < 4; ++i) s4 += (f32x4){lo_bf(dq_[i].x), hi_bf(dq_[i].x), lo_bf(dq_[i].z), hi_bf(dq_[i].z)};
      *(f32x2*)(smem + LDS_FIN + (c * 4 + pw) * 512 + (lane & 31) * 16 + (lane >> 5) * 8) = (f32x2){fold32(s4[0], s4[2]) * sg, fold32(s4[1], s4[3]) * sg};
      asm volatile("s_waitcnt lgkmcnt(0)" ::: "memory");
      if (lane == 0) asm volatile("ds_add_u32 %0, %1" :: "v"(lds_addr(smem + LDS_SEL + 24 + c * 4)), "v"(1) : "memory");
    }
    cv_calc();
  } else {
  {
    const int q = j * 4 + wid;
    constexpr int Q1 = WS_MB, Q2 = Q1 + 8, Q3 = Q2 + 8, Q4 = Q3 + 7, Q5 = Q4 + 7, Q6 = Q5 + 7, Q7 = Q6 + 4, Q8 = Q7 + 4, QC = Q8 + 1;
    static_assert(QC <= 128, "warm-up slots");
    unsigned dmy = 0;
    if (q < QC) {
      const char* base; int b0;
      if (q < Q1) { base = wsp; b0 = 0; }
      else if (q < Q2) { base = (const char*)ws_re; b0 = Q1; }
      else if (q < Q3) { base = (const char*)ws_im; b0 = Q2; }
      else if (q < Q4) { base = (const char*)w_gate; b0 = Q3; }
      else if (q < Q5) { base = (const char*)w_up; b0 = Q4; }
      else if (q < Q6) { base = (const char*)w_down; b0 = Q5; }
      else if (q < Q7) { base = (const char*)out; b0 = Q6; }
      else if (q < Q8) { base = (const char*)x; b0 = Q7; }
      else { base = (const char*)w_r; b0 = Q8; }
      base += (size_t)(q - b0) << 20;
      asm volatile("s_load_dword %0, %1, 0" : "+s"(dmy) : "s"(base) : "memory");
    }
    if (wid == 0) { CALL_PG(1); CALL_PG(8); CALL_PG(5); }
    else if (wid == 1) { CALL_PG(2); CALL_PG(9); CALL_PG(6); }
    else if (wid == 2) { CALL_PG(3); CALL_PG(10); CALL_PG(7); }
    else { CALL_PG(4); CALL_PG(11); CALL_PG(12); }
    BAR();
    lgkm0(dmy);
    TL(1);
  }
  PAGE(8);
  epi_setup(0); epi_setup(1); epi_setup(2); epi_setup(3);
  const int wr = wid >> 1, wc = wid & 1;
  const int sw = swz(fr), fl = lrw(fr);
  const int ko0 = ((0 + fq) ^ sw) * 16, ko1 = ((4 + fq) ^ sw) * 16;
  bf16x8 fm[2][4], fn[2][4];
  {
    const char* pm = smem + (wr * 64 + fl) * 128;
    const char* pn = smem + 16384 + (wc * 64 + fl) * 128;
    f32x4 acc[4][4];
#pragma unroll
    for (int i = 0; i < 4; ++i)
#pragma unroll
      for (int jn = 0; jn < 4; ++jn)
#pragma unroll
        for (int q = 0; q < 4; ++q) acc[i][jn][q] = 0.f;
#define TO(i) ((i) * 2048)
#define LDSP(T, a) ((__attribute__((address_space(3))) T*)(size_t)(a))
#define SLOTS(n) _Pragma("unroll") for (int q = 0; q < (n); ++q)
#define MFA(c, m, n) asm volatile("v_mfma_f32_16x16x32_bf16 %0, %2, %1, %0" : "+v"(c) : "v"(m), "v"(n))
#define MFA2(c, m, n, m2, n2) asm volatile("v_mfma_f32_16x16x32_bf16 %0, %2, %1, %0\n\tv_mfma_f32_16x16x32_bf16 %0, %4, %3, %0" : "+v"(c) : "v"(m), "v"(n), "v"(m2), "v"(n2))
    unsigned lm[2][2], ln[2][2];
#pragma unroll
    for (int u = 0; u < 2; ++u)
#pragma unroll
      for (int kk = 0; kk < 2; ++kk) {
        lm[u][kk] = lds_addr(pm) + u * 2 * G1_STAGE + (kk ? ko1 : ko0);
        ln[u][kk] = lds_addr(pn) + u * 2 * G1_STAGE + (kk ? ko1 : ko0);
        asm volatile("" : "+v"(lm[u][kk]), "+v"(ln[u][kk]));
      }
    auto ldm = [&](int stg, int i, int kk) __attribute__((always_inline)) { fm[kk][i] = *LDSP(const bf16x8, lm[stg >> 1][kk] + (stg & 1) * G1_STAGE + TO(i)); };
    auto ldn = [&](int stg, int jn, int kk) __attribute__((always_inline)) { fn[kk][jn] = *LDSP(const bf16x8, ln[stg >> 1][kk] + (stg & 1) * G1_STAGE + TO(jn)); };
    auto quad = [&](int a, int c, int d, int s) __attribute__((always_inline)) {
      const int i = (s < 2 || s >= 6) ? a : a + 1, jn = s < 4 ? c : d, kk = (s == 1 || s == 2 || s == 5 || s == 6);
      MFA(acc[i][jn], fm[kk][i], fn[kk][jn]);
    };
    auto quad2 = [&](int a, int c, int d, int s) __attribute__((always_inline)) {
      const int i = (s < 2 || s >= 6) ? a : a + 1, jn = s < 4 ? c : d, kk = (s == 2 || s == 6);
      MFA2(acc[i][jn], fm[kk][i], fn[kk][jn], fm[1 - kk][i], fn[1 - kk][jn]);
    };
    auto half1 = [&](int stg) __attribute__((always_inline)) {
      const int ja = (stg & 1) * 2, jb = 2 - ja;
      SLOTS(8) {
        quad(0, ja, ja + 1, q);
        if (q < 4) ldn(stg, jb + (q >> 1), q & 1); else ldm(stg, 2 + ((q - 4) >> 1), q == 5 || q == 6);
        SB();
      }
      SLOTS(4) { quad2(0, jb, jb + 1, 2 * q); SB(); }
    };
    auto half2 = [&](int stg) __attribute__((always_inline)) {
      const int ja = (stg & 1) * 2, jb = 2 - ja, nx = (stg + 1) & 3;
      SLOTS(8) {
        quad(2, jb + 1, jb, q);
        if (!(q & 1)) ldm(nx, q >> 2, q == 2 || q == 4);
        SB();
      }
      SLOTS(8) {
        quad(2, ja, ja + 1, q);
        if (!(q & 1)) ldn(nx, jb + (q >> 2), (q >> 1) & 1);
        SB();
      }
    };
    unsigned ae[7];
    {
      int l_ = lane;
      asm volatile("" : "+v"(l_));
      const int p = l_ >> 4, dn = sym_dn(p), hf = (p >> 1) ^ (dn & z);
#pragma unroll
      for (int u = 0; u < 7; ++u) {
        const int T = u < 3 ? wc * 4 : (wc * 4 + u - 3 - dn) & 7, i = u < 3 ? (u + 1) ^ ((u & 1) ? 0 : dn * 2) : 0;
        const int f = dn * 8 + ((T & 2) << 1) + ((T & 4) >> 1) + (T & 1);
        ae[u] = lds_addr(smem + G2_MRING) + (hf * 64 + i * 16 + lrw(f)) * 128 + (((wr * 4 + ((l_ >> 2) & 3)) ^ swz(f)) * 16) + (l_ & 3) * 4;
        asm volatile("" : "+v"(ae[u]));
      }
    }
    unsigned al = lds_addr(smem + LDS_MB) + (wr * 32 + fr * 2) * 4, ac = lds_addr(smem + LDS_MB) + (256 + wc * 64 + fq * 4) * 4;
    asm volatile("" : "+v"(al), "+v"(ac));
    SLOTS(16) asm volatile("" : "+v"(acc[q >> 2][q & 3]));
    SB();
    SLOTS(4) { ldm(0, q >> 1, q & 1); ldn(0, q >> 1, q & 1); }
    SB();
#pragma unroll 1
    for (int k4 = 0; k4 < G1_NKT - 4; k4 += 4) {
      half1(0); BAR_LGKM();

      SB(); half2(0);
      half1(1); BAR_LGKM();

      SB(); half2(1);
      half1(2); BAR_LGKM();

      SB(); half2(2);
      half1(3); BAR_LGKM();

      SB(); half2(3);
    }
    bf16x8 gm[4][4], gn[2][8];
    auto rdm1 = [&](int set, int t, int h, int i, int kk) __attribute__((always_inline)) {
      gm[set][kk * 2 + i] = *LDSP(const bf16x8, lm[(t >> 1) & 1][kk] + (t & 1) * G1_STAGE + h * 4096 + i * 2048);
    };
    auto rdg = [&](int set, int t, int h, int r) __attribute__((always_inline)) {
      if (r == 0 || r == 2 || r == 4 || r == 5) rdm1(set, t, h, r >= 4, r == 2 || r == 4);
      else {
        const int jn = r < 4 ? 0 : (r - 4) >> 1, kk = r < 4 ? r >> 1 : r & 1;
        gn[set][kk * 4 + jn] = *LDSP(const bf16x8, ln[(t >> 1) & 1][kk] + (t & 1) * G1_STAGE + TO(jn));
      }
    };
    auto mmg = [&](int ms, int ns, int h, int jn, int r, int f) __attribute__((always_inline)) {
      const int i = (r >> 1) ^ f, kk = r == 1 || r == 2;
      MFA(acc[h * 2 + i][jn], gm[ms][kk * 2 + i], gn[ns][kk * 4 + jn]);
    };
    f32x2 rsv, mrv;
    f32x4 cev[4];
    float tc[32];
    unsigned ep;
    auto rds = [&](int h, int r) __attribute__((always_inline)) {
      if (r == 0) mrv = *LDSP(const f32x2, al + h * 256); else if (r == 5) rsv = *LDSP(const f32x2, al + 512 + h * 256);
      else cev[r - 1] = *LDSP(const f32x4, ac + (r - 1) * 64);
    };
    auto prep = [&](int n) __attribute__((always_inline)) {
      tc[n] = llvm_fmul_legacy(mrv[n & 1], cev[n >> 3][(n >> 1) & 3]);
      asm volatile("" : "+v"(tc[n]));
    };
    auto epi = [&](int h, int n) __attribute__((always_inline)) {
      const int jn = n >> 4, r = (n >> 2) & 3, u = n & 3, e = (n >> 2) * 2;
      if (u == 0) {}
      else if (u == 1) asm volatile("v_fmac_f32 %0, %2, %4\n\tv_fmac_f32 %1, %3, %5\n\tv_cvt_pk_bf16_f32 %0, %0, %1" : "+v"(tc[e]), "+v"(tc[e + 1])
                                    : "v"(acc[h * 2][jn][r]), "v"(acc[h * 2 + 1][jn][r]), "v"(rsv[0]), "v"(rsv[1]));
      else if (u == 2) ep = __float_as_uint(tc[e]);
      else *LDSP(unsigned, (r ? ae[r - 1] + (jn & 1) * 128 + (jn >> 1) * lrw(4) * 128 : ae[3 + jn]) + m_off(h)) = ep;
      SB();
    };
    SB();
    half1(0);
    BAR_LGKM();
    SB();
    TL(14);
    SLOTS(16) {
      if (q < 8) quad(2, 3, 2, q); else quad(2, 0, 1, q - 8);
      if (q < 12) rdg(0, 14, 0, q); else rdg(1, 13, 0, q - 12);
      SB();
    }
    BAR();
    SB();
    TL(15);
    SLOTS(16) {
      mmg(0, 0, 0, q >> 2, q & 3, (q >> 2) & 1);
      if (q < 6) rds(0, q); else if (q < 14) rdg(1, 13, 0, q - 2);
      if (q >= 6) prep(q - 6);
      if (q >= 14) prep(q - 4);
      SB();
    }
    PAGE(9);
    BAR();
    SB();
    TL(16);
    {
      constexpr int ct[8] = {1, 1, 0, 0, 1, 0, 1, 0}, cj[8] = {0, 1, 0, 1, 2, 2, 3, 3}, cf[8] = {0, 1, 0, 1, 0, 1, 0, 1};
      constexpr auto en = [](int q) { return q < 15 ? 0 : q < 26 ? (q - 14) * 3 - (q == 25) : q < 27 ? 32 : q < 32 ? 32 + (q - 26) * 3 + (q == 31) : 48; };
      SLOTS(32) {
        mmg(ct[q >> 2], ct[q >> 2], 0, cj[q >> 2], q & 3, cf[q >> 2]);
        if (q < 12) rdg(0, 15, 0, q);
        else if (q < 15 || q == 17) { const int r = q < 15 ? q - 12 : 3; rdm1(2, 15, 1, r >> 1, r == 1 || r == 2); }
        else if (q == 20 || q == 23 || q == 25 || q == 26) { const int r = q == 20 ? 0 : q == 23 ? 1 : q - 23; rdm1(3, 13, 1, r >> 1, r == 1 || r == 2); }
        if (q < 15) prep(12 + q);
        if (q >= 10 && q < 15) prep(17 + q);
        SB();
#pragma unroll
        for (int n = q ? en(q - 1) : 0; n < en(q); ++n) epi(0, n);
      }
    }
    SLOTS(2) { mmg(2, 0, 1, 0, q, 0); SB(); }
    asm volatile("s_nop 1");
    SLOTS(16) { epi(0, 48 + q); if (q == 3 || q == 9) { mmg(2, 0, 1, 0, q == 3 ? 2 : 3, 0); SB(); } }
    BAR_LGKM();
    SB();
    TL(60);
    SLOTS(12) {
      mmg(2, 0, 1, 1 + (q >> 2), q & 3, ~(q >> 2) & 1);
      if (q < 6) rds(1, q); else rdg(0, 14, 1, q - 6);
      if (q >= 8) prep(q - 8);
      SB();
    }
    SLOTS(16) {
      mmg(3, 1, 1, q >> 2, q & 3, (q >> 2) & 1);
      if (q < 6) rdg(0, 14, 1, q + 6);
      if (q < 4) prep(4 + q); else { prep(2 * q); prep(2 * q + 1); }
      SB();
    }
    asm volatile("s_waitcnt lgkmcnt(0)" ::: "memory");
    if (lane == 0) asm volatile("ds_add_u32 %0, %1" :: "v"(lds_addr(smem + LDS_SEL + 12)), "v"(1) : "memory");
    SB();
    SLOTS(16) {
      mmg(0, 0, 1, q >> 2, q & 3, (q >> 2) & 1);
      if (q < 4) fn[0][q] = *(const bf16x8*)(smem + (wr * 128 + fl) * 128 + (q >> 1) * 2048 + ((q & 1) ? ko1 : ko0));
      else if (q < 8) fm[q & 1][(q - 4) >> 1] = *(const bf16x8*)(smem + G2_MRING + m_off(0) + (wc * 64 + fl) * 128 + TO((q - 4) >> 1) + ((q & 1) ? ko1 : ko0));
      SB();
      if (q >= 8) { epi(1, 3 * q - 24); epi(1, 3 * q - 23); epi(1, 3 * q - 22); }
    }
    TL(17);
    SLOTS(40) epi(1, 24 + q);
  }

  const char* pn = smem + (wr * 128 + fl) * 128;
  const char* pm = smem + G2_MRING + (wc * 64 + fl) * 128;
  f32x4 acc[8][4];
  auto mm = [&](bool imaj, int set, int q2, int s, int mode) __attribute__((always_inline)) {
    int i, jj, kk;
    if (imaj) { i = s >> 2; const int r = s & 3; jj = (r >> 1) ^ (i & 1); kk = (r == 1 || r == 2); }
    else { jj = s >> 3; const int r = s & 7; i = jj ? 3 - (r >> 1) : (r >> 1); kk = ((r + 1) >> 1) & 1; }
    if (mode == 1 && !(s & 1)) asm volatile("v_mfma_f32_16x16x32_bf16 %0, %2, %1, 0" : "=&v"(acc[q2 * 2 + jj][i]) : "v"(fm[kk][i]), "v"(fn[set][jj * 2 + kk]));
    else if (mode) asm volatile("v_mfma_f32_16x16x32_bf16 %0, %2, %1, %0" : "+v"(acc[q2 * 2 + jj][i]) : "v"(fm[kk][i]), "v"(fn[set][jj * 2 + kk]));
    else acc[q2 * 2 + jj][i] = __builtin_amdgcn_mfma_f32_16x16x32_bf16(fn[set][jj * 2 + kk], fm[kk][i], acc[q2 * 2 + jj][i], 0, 0, 0);
  };
  unsigned bn[2], bm[2], an[2], am[2];
#pragma unroll
  for (int kk = 0; kk < 2; ++kk) {
    bn[kk] = lds_addr(pn) + (kk ? ko1 : ko0); bm[kk] = lds_addr(pm) + (kk ? ko1 : ko0);
    asm volatile("" : "+v"(bn[kk]), "+v"(bm[kk]));
    an[kk] = bn[kk]; am[kk] = bm[kk] + m_off(0);
  }
#define RDN(jt, kk, o) (*LDSP(const bf16x8, an[kk] + (o) + (jt) * 2048))
#define RDM(i, kk, o) (*LDSP(const bf16x8, am[kk] + (o) + TO(i)))
  PAGE(10);
  TL(18);
  TL(19);
  tag = __builtin_amdgcn_readfirstlane(((const unsigned*)(smem + LDS_SEL))[5]);
  {
    int son = 0;
    auto tile = [&](int kt, int zero) __attribute__((always_inline)) {
      SLOTS(16) {
        mm(true, 0, 0, q, zero);
        if (!(q & 1)) {
          if (q < 8) fm[(q >> 1) & 1][2 + (q >> 2)] = RDM(2 + (q >> 2), (q >> 1) & 1, 0);
          else { const int r = (q - 8) >> 1; fn[1][r] = RDN(2 + (r >> 1), r & 1, 0); }
        }
        SB();
      }
      SLOTS(16) {
        mm(false, 1, 1, q, zero);
        if (!(q & 1) && q < 8) fn[0][q >> 1] = RDN(4 + (q >> 2), (q >> 1) & 1, 0);
        SB();
      }
      const int som = son >> 1;
      son = son == 65536 ? 0 : son + 32768;
      SLOTS(16) {
        mm(false, 0, 2, q, zero);
        if (!(q & 1) && q < 8) fn[1][q >> 1] = RDN(6 + (q >> 2), (q >> 1) & 1, 0);
        if (q == 9 || q == 11) { an[q == 11] = bn[q == 11] + son; asm volatile("" : "+v"(an[q == 11])); }
        if (q == 13 || q == 15) { am[q == 15] = bm[q == 15] + som; asm volatile("" : "+v"(am[q == 15])); }
        SB();
      }

      BAR_LGKM();

      SB();
      SLOTS(16) {
        mm(true, 1, 3, q, zero);
        if (!(q & 1)) {
          if (q < 8) fn[0][q >> 1] = RDN(q >> 2, (q >> 1) & 1, 0);
          else fm[(q >> 1) & 1][(q - 8) >> 2] = RDM((q - 8) >> 2, (q >> 1) & 1, 0);
        }
        SB();
      }
    };
    SB();
    tile(0, 1);
#pragma unroll 1
    for (int kt = 1; kt < G2_NKT - 3; ++kt) tile(kt, 0);
  }
  unsigned fx;
  int fo = lane == 0 ? kq * 512 : (int)0x80000000;
  {
    constexpr int NG = 12;
    static constexpr int g_t[NG] = {0, 0, 1, 1, 2, 2, 2, 1, 0, 0, 1, 2};
    static constexpr int g_q[NG] = {0, 1, 0, 1, 0, 1, 2, 2, 2, 3, 3, 3};
    constexpr int G14 = 1, G15 = 3, GP0 = 5, GP1 = 6;
    constexpr auto chp = [](int g) { return g == 0 || g_t[g] != g_t[g - 1]; };
    constexpr auto chn = [](int g) { return g + 1 < NG && g_t[g + 1] != g_t[g]; };
    static_assert(n_off(13) == 32768 && n_off(14) == 65536 && n_off(15) == 0, "");
    unsigned an2[2];
#pragma unroll
    for (int kk = 0; kk < 2; ++kk) {
      an[kk] = bn[kk]; an2[kk] = bn[kk] + 65536; am[kk] = bm[kk];
      asm volatile("" : "+v"(an[kk]), "+v"(an2[kk]), "+v"(am[kk]));
    }
    int vo;
    u32x4 pk;
    u32x2 kv;
    auto publish = [&](int d, int h) __attribute__((always_inline)) {
      if (h == 0) { vo = ml; PINV(vo); vo += (((d + kq + 1) & 3) * 4 + kq) * 4 * MSG; PINV(vo); }
      const f32x4 v = acc[2 * d + (h >> 2)][h & 3];
      pk[(h & 1) * 2] = pk2(v[0], v[1]); pk[(h & 1) * 2 + 1] = pk2(v[2], v[3]);
      if (h & 1) __builtin_amdgcn_raw_buffer_store_b128(pk, rs, vo + (h >> 1) * 1024, 0, 16);
    };
#define MSG_FLAG(d) __builtin_amdgcn_raw_buffer_store_b32(tag, rc, fo + (((d) + kq + 1) & 3) * 2048, 0, 16)
    auto rdn = [&](int g, int r) __attribute__((always_inline)) {
      const int t = g_t[g], jt = g_q[g] * 2 + (r >> 1);
      fn[g & 1][r] = *LDSP(const bf16x8, (t == 1 ? an2[r & 1] : an[r & 1]) + (t == 0 ? 32768 : 0) + jt * 2048);
    };
    auto rdm = [&](int t, int i, int kk) __attribute__((always_inline)) { fm[kk][i] = *LDSP(const bf16x8, am[kk] + m_off(13 + t) + TO(i)); };
    auto group = [&](auto GI) __attribute__((always_inline)) {
      constexpr int gi = decltype(GI)::value;
      if (gi == G14) { BAR();
        TL(47);
        SB(); }
      if (gi == G15) {
        PAGE(11);
        TL(53);
        BAR();
        TL(54);
        SB();
        kv = *(const u32x2*)(smem + LDS_SEL);
        SB();
      }
#pragma unroll
      for (int q = 0; q < 16; ++q) {
        const int n = gi * 16 + q;
        if (n == 112) {
          TL(55);
        }
        if (n == 152) {
          TL(58);
        }
        if (n == 168) { WAIT_VM(8); MSG_FLAG(0); SB(); }
        mm(chp(gi) || chn(gi), gi & 1, g_q[gi], q, 2);
        constexpr bool ni = gi + 1 < NG && (chp(gi + 1 < NG ? gi + 1 : gi) || chn(gi + 1 < NG ? gi + 1 : gi));
        auto nr = [](int r) { return ni && r >= 2 ? 5 - r : r; };
        if constexpr (chp(gi) && chn(gi)) {
          if (q < 4) rdm(g_t[gi], 2 + (q >> 1), q & 1); else if (q < 8) rdn(gi + 1, nr(q - 4)); else if (q < 12) rdm(g_t[gi + 1], (q - 8) >> 1, q & 1);
        } else if constexpr (chp(gi)) {
          if (q < 4) rdm(g_t[gi], 2 + (q >> 1), q & 1);
          else if (gi + 1 < NG && q < 12 && !(q & 1)) rdn(gi + 1, nr((q - 4) >> 1));
        } else if constexpr (chn(gi)) {
          if (!(q & 1)) { if (q < 8) rdn(gi + 1, nr(q >> 1)); else rdm(g_t[gi + 1], (q - 8) >> 2, (q >> 1) & 1); }
        } else {
          if (!(q & 1) && q < 8) rdn(gi + 1, nr(q >> 1));
        }
        if (gi == GP0 && q < 8) publish(0, q);
        if (gi == GP1 && q < 8) publish(1, q);
        if (gi == 9 && q < 8) publish(2, q);
        if (n == 33 || n == 37 || n == 41) xq_load((n - 33) >> 2);
        if (n == 72) { key[0] = __builtin_amdgcn_readfirstlane(kv.x); key[1] = __builtin_amdgcn_readfirstlane(kv.y); }
        SB();
      }
    };
#define GR4(i) group(IC<i>{}); group(IC<i + 1>{}); group(IC<i + 2>{}); group(IC<i + 3>{});
    GR4(0) GR4(4) GR4(8)
    asm volatile("s_nop 7");
    SB();
  }
  TL(50);
#pragma unroll
  for (int i = 0; i < 4; ++i) *(f32x4*)(smem + LDS_ACC + wr * 16384 + wc * 4096 + i * 1024 + lane * 16) = acc[7][i];
#pragma unroll
  for (int i = 0; i < 4; ++i) ya[i] = acc[6][i];
  asm volatile("s_waitcnt lgkmcnt(0)" ::: "memory");
  if (lane == 0) sig[0] = 2;
  SB();
  TL(61);
  WAIT_VM(5); MSG_FLAG(1); SB();
  TL(62);
  WAIT_VM(2); MSG_FLAG(2); fx = bld4_sc1(rc, po); SB();
  TL(63);
  wait_xq<4>(xq, sq);
  far_calc();
  cv_calc();
  asm volatile("s_waitcnt vmcnt(0)" : "+v"(fx) :: "memory");
  seen = (unsigned)__ballot(fx == tag);
  }

  PAGE(6);
  if (ha) {
    int it = 0;
    while (sig[0] != 2 && it < SPIN_MAX * 8) { __builtin_amdgcn_s_sleep(1); ++it; }
    asm volatile("" ::: "memory");
#pragma unroll
    for (int i = 0; i < 4; ++i) ya[i] = *(const f32x4*)(smem + LDS_ACC + (wid & 2) * 8192 + (wid & 1) * 4096 + i * 1024 + lane * 16);
  }
  u32x4 pp[3][2];
  unsigned e_lo = 0x00003f80u, e_hi = 0x3f800000u;
  asm volatile("" : "+v"(e_lo), "+v"(e_hi));
  __amdgpu_buffer_rsrc_t rs_ = rs, rc_ = rc;
  int mo[3] = {MSG_IDX(0) * 4 * MSG, MSG_IDX(1) * 4 * MSG, MSG_IDX(2) * 4 * MSG}, mv = ml + ha * 2048, mv2 = mv + 1024;
  asm volatile("" : "+s"(rs_), "+s"(rc_), "+s"(mo[0]), "+s"(mo[1]), "+s"(mo[2]));
  asm volatile("" : "+v"(mv), "+v"(mv2));
#define MSG_POLL(m) do { \
    if (!((seen >> (m)) & 1)) { \
      unsigned pv_, it_ = SPIN_MAX; \
      asm volatile("1:\n\tbuffer_load_dword %0, %3, %4, 0 offen sc1\n\ts_waitcnt vmcnt(0)\n\tv_cmp_eq_u32 vcc, %5, %0\n\ts_mov_b32 %1, vcc_lo\n\t" \
                   "s_bitcmp1_b32 %1, %6\n\ts_cbranch_scc1 2f\n\ts_sub_u32 %2, %2, 1\n\ts_cbranch_scc1 2f\n\ts_sleep 1\n\ts_branch 1b\n2:" \
                   : "=&v"(pv_), "+s"(seen), "+s"(it_) : "v"(po), "s"(rc_), "s"(tag), "i"(m) : "vcc", "scc", "memory"); \
    } \
    pp[m][0] = __builtin_amdgcn_raw_buffer_load_b128(rs_, mv, mo[m], 16); \
    pp[m][1] = __builtin_amdgcn_raw_buffer_load_b128(rs_, mv2, mo[m], 16); } while (0)
#define MSG_ADD(m) do { \
    _Pragma("unroll") for (int u = 0; u < 8; ++u) { \
      ya[u >> 1][(u & 1) * 2] = add_bf(pp[m][u >> 2][u & 3], e_lo, ya[u >> 1][(u & 1) * 2]); ya[u >> 1][(u & 1) * 2 + 1] = add_bf(pp[m][u >> 2][u & 3], e_hi, ya[u >> 1][(u & 1) * 2 + 1]); } } while (0)
  MSG_POLL(2); MSG_POLL(1); MSG_POLL(0);
  SB();
  if (__builtin_expect(far0 == 0 || far1 == 0, 0)) {
    asm volatile("s_branch 2f\nk_pg12:\n\ts_setpc_b64 s[42:43]\n2:");
#pragma unroll 1
    for (int c = 0; c < 2; ++c) {
      if (c ? far1 : far0) continue;
      const int ic = (0xffff - (c ? key[1] : key[0])) & (S_ - 1);
      lds_vi cnt = (lds_vi)(smem + LDS_SEL + 24 + c * 4);
      int it = 0;
      while (cnt[0] != 4 && it < SPIN_MAX * 8) { __builtin_amdgcn_s_sleep(1); ++it; }
      asm volatile("" ::: "memory");
      const float* q = (const float*)(smem + LDS_FIN + c * 2048) + (wid & 1) * 64 + (fr >> 3) * 32 + col4(fr);
      const f32x4 v = (*(const f32x4*)q + *(const f32x4*)(q + 128)) + (*(const f32x4*)(q + 256) + *(const f32x4*)(q + 384));
#pragma unroll
      for (int r = 0; r < 4; ++r)
#pragma unroll
        for (int k = 0; k < 4; ++k) {
          cv[r * 4 + k] += (xo[r] >> 11) == ic ? v[k] : 0.f;
        }
    }
  }
#pragma unroll
  for (int e = 0; e < 16; ++e) ya[e & 3][e >> 2] += cv[e];
  asm volatile("" : "+v"(ya[0]), "+v"(ya[1]), "+v"(ya[2]), "+v"(ya[3]));
  SB();
  MSG_ADD(2); MSG_ADD(1);
  asm volatile("" : "+v"(ya[0]), "+v"(ya[1]), "+v"(ya[2]), "+v"(ya[3]));
  SB();
  MSG_ADD(0);
  {
#pragma unroll
    for (int r = 0; r < 4; ++r) *(u32x2*)((char*)out + (unsigned)xo[r]) = (u32x2){pk2(ya[0][r], ya[1][r]), pk2(ya[2][r], ya[3][r])};
  }

  if (wid == 0) {
    const unsigned te = (unsigned)__builtin_amdgcn_s_memrealtime();
    const unsigned t0 = __builtin_amdgcn_readlane(tlv, 52);
    const __amdgpu_buffer_rsrc_t rg = mk_rsrc(wsp + (64u << 20));
    if (lane == 0) { __builtin_amdgcn_raw_buffer_store_b64((u32x2){tag, t0}, rg, g * 32, 0, 16); __builtin_amdgcn_raw_buffer_store_b64((u32x2){tag, (unsigned)__builtin_amdgcn_readlane(tlv, 52)}, rg, g * 32 + 8, 0, 16);
      __builtin_amdgcn_raw_buffer_store_b64((u32x2){tag, (unsigned)__builtin_amdgcn_readlane(tlv, 52)}, rg, g * 32 + 16, 0, 16); }
    if (g == 37) {
      int mn0, mx[2], sum[2];
      for (int it = 0; it < 100000; ++it) {
        unsigned bad = 0; mn0 = 0x7fffffff; mx[0] = mx[1] = -0x7fffffff; sum[0] = sum[1] = 0;
        for (int k = 0; k < 4; ++k) {
          const u32x4 q = __builtin_amdgcn_raw_buffer_load_b128(rg, (k * 64 + lane) * 32, 0, 16);
          const u32x2 q2 = __builtin_amdgcn_raw_buffer_load_b64(rg, (k * 64 + lane) * 32 + 16, 0, 16);
          bad |= (q.x ^ tag) | (q.z ^ tag) | (q2.x ^ tag);
          mn0 = min(mn0, (int)(q.y - t0)); mx[0] = max(mx[0], (int)(q.w - t0)); sum[0] += (int)(q.w - t0); mx[1] = max(mx[1], (int)(q2.y - t0)); sum[1] += (int)(q2.y - t0);
        }
        if (__all(bad == 0)) break;
        asm volatile("" ::: "memory");
      }
      int sx = sum[0];
      for (int o = 32; o >= 8; o >>= 1) sx += __shfl_xor(sx, o, 64);
      for (int o = 32; o; o >>= 1) { mn0 = min(mn0, __shfl_xor(mn0, o, 64)); for (int k = 0; k < 2; ++k) { mx[k] = max(mx[k], __shfl_xor(mx[k], o, 64)); sum[k] += __shfl_xor(sum[k], o, 64); } }
      int code = 0;
      { int d = mx[0] - mn0; d = (d - 0) / 60; code = code * 2 + min(max(d, 0), 1); }
      code = code * 256 + (int)(__ballot(sx / 32 - mn0 > THR) & 255);
      const unsigned tend = __builtin_amdgcn_readfirstlane(t0 + mn0 + 4000 + code * 400);
      for (int it = 0; it < (1 << 17) && (int)((unsigned)__builtin_amdgcn_s_memrealtime() - tend) < 0; ++it) __builtin_amdgcn_s_sleep(1);
    }
  }

}

extern "C" void kernel_launch(void* const* d_in, const int* in_sizes, int n_in, void* d_out, int out_size,
                              void* d_ws, size_t ws_size, hipStream_t stream) {
  const u16* x = (const u16*)d_in[0];
  const u16* ws_re = (const u16*)d_in[1];
  const u16* ws_im = (const u16*)d_in[2];
  const u16* wd_re = (const u16*)d_in[3];
  const u16* wd_im = (const u16*)d_in[4];
  const u16* w_r = (const u16*)d_in[6];
  const u16* w_gate = (const u16*)d_in[7];
  const u16* w_up = (const u16*)d_in[8];
  const u16* w_down = (const u16*)d_in[9];
  k_main<<<256, 512, 0, stream>>>(x, wd_re, wd_im, w_r, (char*)d_ws, ws_re, ws_im, w_gate, w_up, w_down, (u16*)d_out);
}
```

```cpp
#define THR 50
#include <hip/hip_runtime.h>
#include <hip/hip_bf16.h>
#include <stdint.h>
#include <type_traits>

extern "C" __device__ unsigned long long llvm_dispatch_id(void) __asm("llvm.amdgcn.dispatch.id");
extern "C" __device__ float llvm_fmul_legacy(float, float) __asm("llvm.amdgcn.fmul.legacy");
typedef unsigned short u16;
typedef unsigned long long u64;
typedef __attribute__((ext_vector_type(8))) short bf16x8;
typedef __attribute__((ext_vector_type(4))) unsigned u32x4;
typedef __attribute__((ext_vector_type(2))) unsigned u32x2;
typedef __attribute__((ext_vector_type(4))) float f32x4;
typedef __attribute__((ext_vector_type(2))) float f32x2;
typedef __attribute__((ext_vector_type(2))) __bf16 bf16x2;
typedef __attribute__((address_space(3))) void* lds_vp;
typedef volatile __attribute__((address_space(3))) int* lds_vi;

constexpr int S_ = 2048, D_ = 1024, F_ = 3584;
template <int N> using IC = std::integral_constant<int, N>;

static __device__ __forceinline__ float bf2f(u16 u) { return __uint_as_float((unsigned)u << 16); }
static __device__ __forceinline__ u16 f2bf(float f) {
  __hip_bfloat16 b = __float2bfloat16(f);
  return *reinterpret_cast<u16*>(&b);
}
static __device__ __forceinline__ float rbf(float f) { return bf2f(f2bf(f)); }
static __device__ __forceinline__ float lo_bf(unsigned u) { return __uint_as_float(u << 16); }
static __device__ __forceinline__ float hi_bf(unsigned u) { return __uint_as_float(u & 0xffff0000u); }
static __device__ __forceinline__ unsigned pk2(float a, float b) {
  const bf16x2 v = __builtin_convertvector((f32x2){a, b}, bf16x2);
  return __builtin_bit_cast(unsigned, v);
}
static __device__ __forceinline__ float add_bf(unsigned u, unsigned e, float c) {
  return __builtin_amdgcn_fdot2_f32_bf16(__builtin_bit_cast(bf16x2, u), __builtin_bit_cast(bf16x2, e), c, false);
}
static __device__ __forceinline__ float ln_elem(float x, float mb, float sd, float ri) {
  const float d = rbf(x - mb);
  const float q0 = d * ri;
  return rbf(__builtin_fmaf(__builtin_fmaf(-q0, sd, d), ri, q0));
}
template <int CTRL> static __device__ __forceinline__ float dpp_f(float v) {
  return __int_as_float(__builtin_amdgcn_update_dpp(0, __float_as_int(v), CTRL, 0xf, 0xf, true));
}
template <int CTRL> static __device__ __forceinline__ unsigned dpp_u(unsigned v) {
  return (unsigned)__builtin_amdgcn_update_dpp(0, (int)v, CTRL, 0xf, 0xf, true);
}
static __device__ __forceinline__ float row_sum(float v) {
  v += dpp_f<0xB1>(v);
  v += dpp_f<0x4E>(v);
  v += dpp_f<0x141>(v);
  v += dpp_f<0x140>(v);
  return v;
}
static __device__ __forceinline__ float fold32(float a, float b) {
  const auto r = __builtin_amdgcn_permlane32_swap(__float_as_uint(a), __float_as_uint(b), false, false);
  return __uint_as_float(r[0]) + __uint_as_float(r[1]);
}
static __device__ __forceinline__ float fold16(float a, float b) {
  const auto r = __builtin_amdgcn_permlane16_swap(__float_as_uint(a), __float_as_uint(b), false, false);
  return __uint_as_float(r[0]) + __uint_as_float(r[1]);
}
static __device__ __forceinline__ float rdl(float v, int l) { return __int_as_float(__builtin_amdgcn_readlane(__float_as_int(v), l)); }


#define TL(I) do { if ((I) == 52) { const unsigned _t = (unsigned)__builtin_amdgcn_s_memrealtime(); tlv = lane == (I) ? _t : tlv; } } while (0)
#define ST_JB 9
#define LNS 2
#define WSN 10
#define FSM 0xa2
#define GV_JB 7
#define DG 11
#define AB_STRIDE 32
#define SPIN_MAX (1 << 17)
#define VA0 5
#define VAN 4
#define SP0 3
#define SP1 7
#define SP1E 11
#define SPS 0x000cccccdddd00
#define DVS 0x00000400300000
#define GVS 0x00000022000000
constexpr int dig(unsigned long long t, int q) { return q < 0 ? 0 : (int)((t >> (4 * q)) & 15); }
constexpr int acc_dig(unsigned long long t, int q) { int a = 0; for (int i = 0; i < q; ++i) a += dig(t, i); return a; }

static __device__ __forceinline__ unsigned lds_addr(const void* p) { return (unsigned)(size_t)(lds_vp)p; }

constexpr int MSG = 4096;
constexpr size_t WS_ABT   = 0;
constexpr size_t WS_SLAB  = WS_ABT + (size_t)D_ * 4096 * 2;
constexpr size_t WS_GU    = WS_SLAB + (size_t)64 * 64 * MSG;
constexpr size_t WS_DN    = WS_GU + (size_t)8 * F_ * 16;
constexpr size_t WS_FLAGS = WS_DN + (size_t)2 * 8 * 32 * 64 * 8;
constexpr int FL_X = 0;
constexpr int FL_AB = 64 * 64 * 32;
constexpr int FL_ST = FL_AB + 2048 * AB_STRIDE;
constexpr int FL_CE = FL_ST + 4096;
constexpr int FL_KEY = FL_CE + 4096;
constexpr int FL_EPOCH = FL_KEY + 4096;
constexpr int WS_MB = (int)((WS_FLAGS + (FL_EPOCH + 1) * 4 + 1048575) >> 20);

#define WAIT_VM(n) asm volatile("s_waitcnt vmcnt(" #n ")" ::: "memory")
template <int N> static __device__ __forceinline__ void wait_vm() { asm volatile("s_waitcnt vmcnt(%0)" :: "i"(N) : "memory"); }
#define BAR_LGKM() asm volatile("s_waitcnt lgkmcnt(0)\n\ts_barrier" ::: "memory")
#define BAR() asm volatile("s_barrier" ::: "memory")
#define SB() __builtin_amdgcn_sched_barrier(0)
#define PAGE(n) asm volatile("s_branch 2f\n\t.p2align 12\nk_pg" #n ":\n\ts_setpc_b64 s[42:43]\n2:")
#define CALL_PG(n) asm volatile("s_getpc_b64 s[40:41]\n3:\n\ts_mov_b32 s42, k_pg" #n "-3b\n\ts_ashr_i32 s43, s42, 31\n\ts_add_u32 s40, s40, s42\n\ts_addc_u32 s41, s41, s43\n\t" \
                                "s_swappc_b64 s[42:43], s[40:41]" ::: "s40", "s41", "s42", "s43", "scc", "memory")

static __device__ __forceinline__ __amdgpu_buffer_rsrc_t mk_rsrc(const void* p) {
  return __builtin_amdgcn_make_buffer_rsrc((void*)p, 0, 0x7fffffff, 0x00020000);
}
#define DMA_(rsrc, voff, mbase, MI, sbase, SI, pol) do { unsigned _t; \
    asm volatile("s_add_i32 m0, %2, %3\n\ts_add_i32 %0, %5, %6\n\tbuffer_load_dwordx4 %1, %4, %0 offen" pol " lds" \
                 : "=&s"(_t) : "v"(voff), "s"(mbase), "i"(MI), "s"(rsrc), "s"(sbase), "i"(SI) : "memory", "scc"); } while (0)
#define DMA_I(rsrc, voff, mbase, MI, SI) \
    asm volatile("s_add_i32 m0, %1, %2\n\ts_nop 0\n\tbuffer_load_dwordx4 %0, %3, 0 offen offset:%4 lds" \
                 :: "v"(voff), "s"(mbase), "i"((MI) - (SI)), "s"(rsrc), "i"(SI) : "memory", "scc")
#define DMA_S(rsrc, voff, mbase, MI, soff) \
    asm volatile("s_add_i32 m0, %1, %2\n\ts_nop 0\n\tbuffer_load_dwordx4 %0, %3, %4 offen lds" \
                 :: "v"(voff), "s"(mbase), "i"(MI), "s"(rsrc), "s"(soff) : "memory", "scc")
#define TOUCH(rsrc, voff, mbase, MI, soff) \
    asm volatile("s_add_i32 m0, %1, %2\n\ts_nop 0\n\tbuffer_load_dword %0, %3, %4 offen lds" \
                 :: "v"(voff), "s"(mbase), "i"(MI), "s"(rsrc), "s"(soff) : "memory", "scc")
template <int OFF> static __device__ __forceinline__ u32x4 ld16_s(const void* p, int voff) {
  u32x4 v;
  asm volatile("global_load_dwordx4 %0, %1, %2 offset:%3" : "=v"(v) : "v"(voff), "s"(p), "i"(OFF) : "memory");
  return v;
}
template <int OFF> static __device__ __forceinline__ void sld(unsigned& sink, const void* p, int soff) {
  asm volatile("s_load_dword %0, %1, %2 offset:%3" : "+s"(sink) : "s"(p), "s"(soff), "i"(OFF) : "memory");
}
static __device__ __forceinline__ unsigned gld4_s(const void* p, unsigned voff) {
  unsigned v;
  asm volatile("global_load_dword %0, %1, %2" : "=v"(v) : "v"(voff), "s"(p) : "memory");
  return v;
}
static __device__ __forceinline__ u32x2 bld8_sc1(__amdgpu_buffer_rsrc_t r, unsigned voff) {
  u32x2 v;
  asm volatile("buffer_load_dwordx2 %0, %1, %2, 0 offen sc1" : "=v"(v) : "v"(voff), "s"(r) : "memory");
  return v;
}
template <int N> static __device__ __forceinline__ void wait_xs(unsigned& a, u32x2& b) { asm volatile("s_waitcnt vmcnt(%2)" : "+v"(a), "+v"(b) : "i"(N) : "memory"); }
template <int N> static __device__ __forceinline__ void wait_xq(u32x4 (&a)[2], u32x2& b) { asm volatile("s_waitcnt vmcnt(%3)" : "+v"(a[0]), "+v"(a[1]), "+v"(b) : "i"(N) : "memory"); }
static __device__ __forceinline__ u32x2 ld8_s_sc1(const void* p, int voff) {
  u32x2 v;
  asm volatile("global_load_dwordx2 %0, %1, %2 sc1" : "=v"(v) : "v"(voff), "s"(p) : "memory");
  return v;
}
static __device__ __forceinline__ void lgkm0(unsigned& sink) { asm volatile("s_waitcnt lgkmcnt(0)" : "+s"(sink) :: "memory"); }
template <typename T> static __device__ __forceinline__ void pin4(T& a, T& b, T& c, T& d) { asm volatile("" : "+s"(a), "+s"(b), "+s"(c), "+s"(d)); }
template <typename T, typename U> static __device__ __forceinline__ void pins(T& a, U& b) { asm volatile("" : "+s"(a), "+s"(b)); }
static __device__ __forceinline__ void launder(int& v, int& a, int& b) { asm volatile("" : "+v"(v)); asm volatile("" : "+s"(a), "+s"(b)); }
static __device__ __forceinline__ unsigned bld4_sc1(__amdgpu_buffer_rsrc_t r, int voff) {
  unsigned v;
  asm volatile("buffer_load_dword %0, %1, %2, 0 offen sc1" : "=v"(v) : "v"(voff), "s"(r) : "memory");
  return v;
}
static __device__ __forceinline__ u32x4 bld16_nt(__amdgpu_buffer_rsrc_t r, int voff, int soff) {
  u32x4 v;
  asm volatile("buffer_load_dwordx4 %0, %1, %2, %3 offen nt" : "=v"(v) : "v"(voff), "s"(r), "s"(soff) : "memory");
  return v;
}
static __device__ __forceinline__ void bld16_nt_io(u32x4& v, __amdgpu_buffer_rsrc_t r, int voff, int soff) {
  asm volatile("buffer_load_dwordx4 %0, %1, %2, %3 offen nt" : "+v"(v) : "v"(voff), "s"(r), "s"(soff) : "memory");
}
static __device__ __forceinline__ u32x4 bld16_sc1(__amdgpu_buffer_rsrc_t r, int voff, int soff) {
  u32x4 v;
  asm volatile("buffer_load_dwordx4 %0, %1, %2, %3 offen sc1" : "=v"(v) : "v"(voff), "s"(r), "s"(soff) : "memory");
  return v;
}
static __device__ __forceinline__ u32x2 granule_wait(const u64* p, unsigned tag) {
  u64 v = 0;
#pragma unroll 1
  for (int it = 0; it < SPIN_MAX; ++it) {
    v = __hip_atomic_load(p, __ATOMIC_RELAXED, __HIP_MEMORY_SCOPE_AGENT);
    if (__all((unsigned)(v >> 32) == tag)) break;
    __builtin_amdgcn_s_sleep(2);
  }
  return (u32x2){(unsigned)v, (unsigned)(v >> 32)};
}
static __device__ __forceinline__ float key_score(unsigned key) {
  const unsigned u = key >> 16;
  return bf2f((u16)((u & 0x8000u) ? (u & 0x7fffu) : (~u & 0xffffu)));
}
static __device__ __forceinline__ unsigned med3u(unsigned a, unsigned b, unsigned c) { return max(min(a, b), min(max(a, b), c)); }
static __device__ __forceinline__ void merge2(unsigned& a1, unsigned& a2, unsigned b1, unsigned b2) {
  const unsigned lo = min(a1, b1);
  a1 = max(a1, b1);
  a2 = max(lo, max(a2, b2));
}
static __device__ __forceinline__ float sqdev16(const u32x4 (&v)[2], float mb) {
  f32x2 q2 = {0.f, 0.f};
#pragma unroll
  for (int i = 0; i < 8; ++i) {
    const f32x2 e = (f32x2){lo_bf(v[i >> 2][i & 3]), hi_bf(v[i >> 2][i & 3])} - mb;
    q2 += e * e;
  }
  return q2.x + q2.y;
}

constexpr int lrw(int r) { return (r & ~6) | ((r & 2) << 1) | ((r & 4) >> 1); }
constexpr int swz(int r) { return ((r >> 1) & 4) | (r & 2); }
static __device__ __forceinline__ int sym_dn(int p) { return (p ^ (p >> 1)) & 1; }
static __device__ __forceinline__ int sym_hi(int n, int w, int st, int b, int z, int p) {
  return ((p >> 1) * (n / 2 * st) + b * (w * st)) ^ (-sym_dn(p) & (((n - 1) ^ (z * (n / 2))) * st));
}
static __device__ __forceinline__ int sym_lo(int w, int st, int p, int c) { return ((c - sym_dn(p)) * st) & ((w - 1) * st); }
static __device__ __forceinline__ int sym_row(int n, int w, int st, int b, int z, int p, int c) { return sym_lo(w, st, p, c) ^ sym_hi(n, w, st, b, z, p); }
constexpr int G1_NST = 4, G1_NKT = 16, G1_STAGE = 32768;
constexpr int G2_NKT = 16, G2_MRING = 98304;
constexpr int n_off(int k) { return (k % 3) * 32768; }
constexpr int m_off(int k) { return ((k + 2) % 3) * 16384; }
constexpr int k_ord(int k) { return k < 2 ? k : (k < 9 ? 2 * (k - 1) : 2 * (k - 8) + 1); }
constexpr int LDS_DUMMY = G2_MRING + 49152;
constexpr int LDS_GU = LDS_DUMMY + 4096;
constexpr int LDS_FIN = LDS_GU + 4096;
constexpr int LDS_MB = LDS_FIN + 4096;
constexpr int LDS_SEL = LDS_MB + 1536;
constexpr int LDS_SIG = LDS_SEL + 64;
constexpr int LDS_KEY = LDS_SIG + 32;
constexpr int LDS_BYTES = LDS_KEY + 128;
constexpr int LDS_ACC = n_off(G2_NKT - 3);

constexpr int sp_tile(int q) { return q < VA0 ? q : q + VAN < 16 ? q + VAN : -1; }
constexpr int dn_cnt(int q) { return dig(DVS, q); }
constexpr int dn_first(int q) { return acc_dig(DVS, q); }

static __device__ __forceinline__ void code_touch(unsigned& sink, unsigned off) {
  asm volatile("s_getpc_b64 s[40:41]\n\tk_main_rt:\n\ts_min_u32 %1, %1, k_main_end-k_main_rt-196\n\t"
               "s_load_dword %0, s[40:41], %1 offset:0\n\ts_load_dword %0, s[40:41], %1 offset:64\n\ts_load_dword %0, s[40:41], %1 offset:128\n\ts_load_dword %0, s[40:41], %1 offset:192"
               : "+s"(sink), "+s"(off) :: "s40", "s41", "scc", "memory");
}
static __device__ __forceinline__ void touch1(__amdgpu_buffer_rsrc_t r, int voff, unsigned mb, int soff) { TOUCH(r, voff, mb, LDS_DUMMY, soff); }
typedef const __attribute__((address_space(1))) u16* gptr;
typedef __attribute__((ext_vector_type(8))) u64 u64x8;
typedef __attribute__((ext_vector_type(2))) u64 u64x2;
extern "C" __global__ __launch_bounds__(512, 2) void k_main(const u16* x, const u16* wd_re, const u16* wd_im, const u16* w_r,
                                                 char* wsp, const u16* ws_re, const u16* ws_im,
                                                 const u16* w_gate, const u16* w_up, const u16* w_down, u16* out) {
  __shared__ __attribute__((aligned(16))) char smem[LDS_BYTES];
  u64x8 ka;
  u64x2 kb_;
  u64 kc;
  asm volatile("s_load_dwordx16 %0, %3, 0\n\ts_load_dwordx4 %1, %3, 64\n\ts_load_dwordx2 %2, %3, 80"
               : "=&s"(ka), "=&s"(kb_), "=&s"(kc) : "s"(__builtin_amdgcn_kernarg_segment_ptr()) : "memory");
  unsigned long long t00;
  asm volatile("s_memrealtime %0" : "=s"(t00) :: "memory");
  const int tid = threadIdx.x, lane = tid & 63;
  const int wid = __builtin_amdgcn_readfirstlane(tid >> 6);
  const int g = blockIdx.x, xcd = ((~g & 1) << 2) | ((g >> 1) & 3), j = g >> 3;
  if (wid < 4) {
    unsigned cw, co = (j * 4 + wid) * 448;
    asm volatile(".subsection 1\nk_main_end:\n.subsection 0\n\ts_min_u32 %1, %1, k_main_end-k_main-480\n\ts_getpc_b64 s[40:41]\n\ts_add_u32 s40, s40, %1\n\ts_addc_u32 s41, s41, 0\n\t"
                 "s_load_dword %0, s[40:41], 0\n\ts_load_dword %0, s[40:41], 64\n\ts_load_dword %0, s[40:41], 128\n\t"
                 "s_load_dword %0, s[40:41], 192\n\ts_load_dword %0, s[40:41], 256\n\ts_load_dword %0, s[40:41], 320\n\ts_load_dword %0, s[40:41], 384\n\ts_waitcnt lgkmcnt(0)"
                 : "=&s"(cw), "+s"(co) :: "s40", "s41", "scc", "memory");
  }
  const int pw = wid - 4;
  const int row0 = (wid & 3) * 8 + lrw(lane >> 3);
  const int cm = ((lane & 7) ^ swz(row0)) * 16;
  int vm = ((row0 & 15) * 2 + (row0 >> 4)) * (D_ * 2) + cm;
  asm volatile("" : "+v"(vm));
  const int kq = xcd >> 1;
  const int tt = (xcd & 1) * 4 + (j >> 3);
  const int et = j & 7;
  const int z = kq >> 1, st = (kq & 1) * 8 + tt;
  const int lrow = (kq & 1) * 1024 + (xcd & 1) * 512 + z * 256 + j * 8 + pw * 2;
  const int drow = ((kq & 1) * 2 + (xcd & 1)) * 256 + j * 8 + pw * 2;
  unsigned o_m = st * 128 * D_ * 2, o_l = lrow * D_ * 2, o_d = drow * D_ * 2, mb0 = lds_addr(smem) + pw * 1024;
  int l16 = lane * 16;
  int vn[4];
#pragma unroll
  for (int q = 0; q < 4; ++q) vn[q] = sym_lo(32, D_ * 2, (row0 >> 2) & 3, q * 8 + (row0 >> 4) * 4 + (row0 & 3)) ^ (sym_hi(D_, 32, D_ * 2, et, z, (row0 >> 2) & 3) | cm);
  asm volatile("" : "+s"(o_m), "+s"(o_l), "+s"(o_d), "+s"(mb0));
  asm volatile("" : "+v"(l16), "+v"(vn[0]), "+v"(vn[1]), "+v"(vn[2]), "+v"(vn[3]));
  asm volatile("s_waitcnt lgkmcnt(0)" : "+s"(ka), "+s"(kb_), "+s"(kc) :: "memory");
  x = (const u16*)(gptr)ka[0]; wd_re = (const u16*)(gptr)ka[1]; wd_im = (const u16*)(gptr)ka[2]; w_r = (const u16*)(gptr)ka[3];
  wsp = (char*)(gptr)ka[4]; ws_re = (const u16*)(gptr)ka[5]; ws_im = (const u16*)(gptr)ka[6]; w_gate = (const u16*)(gptr)ka[7];
  w_up = (const u16*)(gptr)kb_[0]; w_down = (const u16*)(gptr)kb_[1]; out = (u16*)(gptr)kc;
  int* flags = (int*)(wsp + WS_FLAGS);
  u64* stat = (u64*)(flags + FL_ST);
  unsigned tlv = 0;
  asm volatile("s_waitcnt lgkmcnt(0)" : "+s"(t00) :: "memory");
  tlv = lane == 52 ? (unsigned)t00 : tlv;
  asm volatile("" : "+v"(tlv));
  const u16* wdz = z ? wd_im : wd_re;
  const __amdgpu_buffer_rsrc_t rm = mk_rsrc(x);
  const __amdgpu_buffer_rsrc_t rn = mk_rsrc(wdz);
#define G1_X(kt, so, q) DMA_(rm, vm, mb0, (so) + (q) * 4096, o_m, (kt) * 128 + (((q) & 1) * 64 + ((q) >> 1) * 32) * D_ * 2, "")
#define G1_W(kt, so, q) DMA_I(rn, vn[q], mb0, (so) + 16384 + (q) * 4096, (kt) * 128)
#define G1_STAGE_TILE(kt, so, W) do { SB(); G1_X(kt, so, 0); W(0); G1_X(kt, so, 1); W(1); G1_X(kt, so, 2); W(2); G1_X(kt, so, 3); W(3); \
    G1_W(kt, so, 0); W(4); G1_W(kt, so, 1); W(5); G1_W(kt, so, 2); W(6); G1_W(kt, so, 3); W(7); SB(); } while (0)
#define G1_STAGE_A(kt, so, W) do { SB(); G1_X(kt, so, 0); W(0); G1_X(kt, so, 2); W(1); \
    G1_W(kt, so, 0); W(2); G1_W(kt, so, 1); W(3); G1_W(kt, so, 2); W(4); G1_W(kt, so, 3); W(5); W(6); W(7); SB(); } while (0)
#define G1_STAGE_B(kt, so) do { SB(); G1_X(kt, so, 1); G1_X(kt, so, 3); SB(); } while (0)
#define NOW(i)
  const int fr = lane & 15, fq = lane >> 4;
  const int ha = wid >> 2;
  int trow, xo[4], xa[3], po, ml;
  const char *prs, *prc;
  auto col4 = [](int f) __attribute__((always_inline)) { return ((f & 2) << 3 | (f & 4) << 1 | (f & 1) << 2) ^ (-((f >> 3) & 1) & 28); };
  auto seg0 = [&](int hf, int dn) __attribute__((always_inline)) { return (hf * 512 + et * 32) ^ (-dn & (D_ - 32)); };
  auto epi_setup = [&](int part) __attribute__((always_inline)) {
    int t_ = tt, l_ = lane;
    asm volatile("" : "+s"(t_));
    asm volatile("" : "+v"(l_));
    const int ts = ((wid >> 1) & 1) * 8 + kq * 2 + ha;
    if (part == 0) { xo[3] = (seg0(wid & 1, (l_ >> 3) & 1) + col4(l_)) * 2; asm volatile("" : "+v"(xo[3])); }
    if (part == 1) {
      xo[3] |= sym_hi(S_, 64, D_ * 2, t_, z, l_ >> 4);
      ml = (l_ * 16) ^ (-sym_dn(l_ >> 4) & (z * 768));
      asm volatile("" : "+v"(xo[3]), "+v"(ml));
    }
    if (part == 2) {
      const int xh = xo[3];
#pragma unroll
      for (int r = 0; r < 4; ++r) xo[r] = sym_lo(64, D_ * 2, l_ >> 4, ts * 4 + r) ^ xh;
      const bool hi = xo[0] & 8;
      xa[0] = (hi ? xo[2] : xo[0]) & ~15; xa[1] = (hi ? xo[3] : xo[1]) & ~15;
      xa[2] = (sym_lo(64, D_ * 2, l_ >> 4, ts * 4 + (l_ & 3)) ^ xh) >> 11 << 3;
      asm volatile("" : "+v"(xo[0]), "+v"(xo[1]), "+v"(xo[2]), "+v"(xo[3]), "+v"(xa[0]), "+v"(xa[1]), "+v"(xa[2]));
    }
    if (part == 3) {
      trow = t_ * 16 + ts;
      prs = wsp + WS_SLAB + (size_t)((t_ * 8 + et) * 64 + (wid & 3)) * MSG;
      prc = (const char*)(flags + FL_X + ((t_ * 8 + et) * 64 + (wid & 3)) * 32);
      asm volatile("" : "+s"(trow), "+s"(prs), "+s"(prc));
      po = l_ < 3 ? (kq * 4 + ((kq + 1 + l_) & 3)) * 512 : (int)0x80000000;
      asm volatile("" : "+v"(po));
    }
  };
  auto tok_tile = [](unsigned key) __attribute__((always_inline)) {
    const unsigned h = (0xffff - key) & 1023, m = -(h >> 9);
    return (((h ^ m) & 0x1c0) | (((h ^ m) - m) & 63)) >> 2;
  };
#define rs mk_rsrc(prs)
#define rc mk_rsrc(prc)
  lds_vi sig = (lds_vi)(smem + LDS_SIG + (wid & 3) * 4);
  unsigned tag;
  u32x4 xq[2];
  u32x2 sq;
  f32x4 ya[4];
  unsigned key[2];
  float cv[16];
  unsigned far0, far1;
  auto xq_load = [&](int n) __attribute__((always_inline)) {
    if (n < 2) xq[n] = ld16_s<0>(x, xa[n]); else sq = ld8_s_sc1(stat, xa[2]);
  };
#define PINV(a) asm volatile("" : "+v"(a))
  auto far_calc = [&]() __attribute__((always_inline)) {
    far0 = tok_tile(key[0]) ^ trow; far1 = tok_tile(key[1]) ^ trow;
    asm volatile("" : "+s"(far0), "+s"(far1));
  };
  auto cv_calc = [&]() __attribute__((always_inline)) {
    if (__builtin_expect(!__all(sq.y == tag), 0)) sq = granule_wait(stat + (xa[2] >> 3), tag);
    const bool hi = xo[0] & 8;
    const float mb_ = lo_bf(sq.x), ri_ = __builtin_amdgcn_rcpf(hi_bf(sq.x));
#pragma unroll
    for (int r = 0; r < 4; ++r) {
      const float mb = r == 0 ? dpp_f<0x00>(mb_) : r == 1 ? dpp_f<0x55>(mb_) : r == 2 ? dpp_f<0xAA>(mb_) : dpp_f<0xFF>(mb_);
      const float ri = r == 0 ? dpp_f<0x00>(ri_) : r == 1 ? dpp_f<0x55>(ri_) : r == 2 ? dpp_f<0xAA>(ri_) : dpp_f<0xFF>(ri_);
#pragma unroll
      for (int u = 0; u < 2; ++u) {
        const unsigned own = xq[r & 1][(r & 2) + u], oth = dpp_u<0xB1>(xq[r & 1][2 - (r & 2) + u]);
        const unsigned w = hi == (r >= 2) ? own : oth;
        cv[r * 4 + 2 * u] = __builtin_fmaf(lo_bf(w) - mb, ri, lo_bf(w));
        cv[r * 4 + 2 * u + 1] = __builtin_fmaf(hi_bf(w) - mb, ri, hi_bf(w));
        PINV(cv[r * 4 + 2 * u]); PINV(cv[r * 4 + 2 * u + 1]);
      }
    }
  };
#define MSG_IDX(m) (kq * 4 + ((kq + 1 + (m)) & 3))
  unsigned seen = 0;

  if (__builtin_expect(wid >= 4, 1)) {
    __builtin_amdgcn_s_setprio(3);
    G1_STAGE_TILE(0, 0, NOW);
    constexpr int O_ST = (FL_ST - FL_AB) * 4, O_CE = (FL_CE - FL_AB) * 4, O_KEY = (FL_KEY - FL_AB) * 4, O_EP = (FL_EPOCH - FL_AB) * 4;
    const __amdgpu_buffer_rsrc_t rf = mk_rsrc(wsp + WS_FLAGS + FL_AB * 4);
    u32x4 lv[2], lv2[2], lw[2], ld[2], ld2[2];
    unsigned epv;
    {
      const char* xrow = (const char*)x + o_l;
      const char* wdrow = (const char*)wdz + o_d;
      lv[0] = ld16_s<0>(xrow, l16); lv[1] = ld16_s<1024>(xrow, l16);
      lv2[0] = ld16_s<2048>(xrow, l16); lv2[1] = ld16_s<3072>(xrow, l16);
      ld[0] = ld16_s<0>(wdrow, l16); ld[1] = ld16_s<1024>(wdrow, l16);
      ld2[0] = ld16_s<2048>(wdrow, l16); ld2[1] = ld16_s<3072>(wdrow, l16);
      lw[0] = ld16_s<0>(w_r, l16); lw[1] = ld16_s<1024>(w_r, l16);
      asm volatile("buffer_load_dword %0, off, %1, %2 sc1" : "=v"(epv) : "s"(rf), "s"(O_EP) : "memory");
    }
    SB();
    const unsigned long long did = llvm_dispatch_id();
    const unsigned dmix = (unsigned)did * 0x85EBCA6Bu + (unsigned)(did >> 32) * 0xC2B2AE35u + 0x27D4EB2Fu;
    const bool moe = et != 7;
    unsigned epoch;
    const int ci = lane & 15, rq = lane >> 4, tk = (lane >> 3) & 1;
    int mat, cg, dq, d0;
    u32x4 wv[16];
    const u16 *pgu, *pn2;
    const char *pm2, *spb;
#define rw __builtin_amdgcn_make_buffer_rsrc((void*)pgu, 0, moe ? 0x7fffffff : 0, 0x00020000)
#define rn2 mk_rsrc(pn2)
#define rm2 mk_rsrc(pm2)
    int vw, vn2[8], vm2;
    auto k_blk = [&](int k) __attribute__((always_inline)) { return tt ^ (k_ord(k) >> 1); };
#define KROT(kk_) (k_blk(kk_) * 256 + (k_ord(kk_) & 1) * 128)
    int tv2, tv7;
    unsigned sd = 0;
    auto spf = [&](auto N) __attribute__((always_inline)) {
      constexpr int n = decltype(N)::value;
      if constexpr (n >= 0 && sp_tile(n >> 4) >= 0) sld<((n >> 1) & 7) * 4096 + (n & 1) * 64>(sd, spb, KROT(sp_tile(n >> 4)));
    };
    unsigned mq0, mq1, mq2, mq3;
    const int cg8 = et ^ ((et & 1) << 1), rgb = (g & 7) * 4 + (j >> 3);
    const __amdgpu_buffer_rsrc_t rdn = mk_rsrc(w_down);
    const __amdgpu_buffer_rsrc_t rgu = mk_rsrc(wsp + WS_GU);
    int vdn, vgu;
    u32x4 dv[7], gv[4];
    int abo, abq[3];
    unsigned lma;
    unsigned sel[2][4];
    float g0, g1;
    u32x4 kk[4];
    unsigned k1 = 0, k2 = 0;
    const int ko_ = O_KEY + pw * 4096;
#define MM(a, b, c) __builtin_amdgcn_mfma_f32_16x16x32_bf16(__builtin_bit_cast(bf16x8, a), __builtin_bit_cast(bf16x8, b), c, 0, 0, 0)
#define PIN1(a) asm volatile("" : "+v"(a))
#define PIN2(a, b) asm volatile("" : "+v"(a), "+v"(b))
#define PIN3(a, b, c) asm volatile("" : "+v"(a), "+v"(b), "+v"(c))
    {
      int gro, go;
      u32x2 gr;
      u32x4 oh[5];
      f32x4 sa4, gw[2], gx[2];
      float su, gs, ta, tb, sx, mbv;
      unsigned dk[4], kl;
      const int hb = lane >> 5;
#pragma unroll
      for (int l = 0; l < WSN; ++l) asm volatile("" : "=v"(wv[l]));
      auto diag = [&](const f32x4& m) __attribute__((always_inline)) {
        return __uint_as_float((__float_as_uint(m[0]) & dk[0]) | (__float_as_uint(m[1]) & dk[1]) | (__float_as_uint(m[2]) & dk[2]) | (__float_as_uint(m[3]) & dk[3]));
      };
      auto setup = [&](auto JB, auto GP) __attribute__((always_inline)) {
        constexpr int jb = decltype(JB)::value, gp = decltype(GP)::value;
        SB();
        {
          int ln = lane, jj = j, p_ = pw;
          launder(ln, jj, p_);
          const int c_ = ln & 15, r_ = ln >> 4, e_ = jj & 7, t_ = (xcd & 1) * 4 + (jj >> 3);
          if constexpr (jb == 1 && gp == 0) {
            pn2 = ((kq >> 1) ? ws_im : ws_re) + (kq & 1) * 1024;
            spb = (const char*)(pn2 + (sym_row(S_, 64, 1, t_, 0, e_ >> 1, 0) + (e_ & 1) * 32 + p_ * 8) * S_);
            pins(pn2, spb);
          }
          if constexpr (jb == 1 && gp >= 1 && gp < 6) {
            const unsigned o = c_ == gp - 1 ? 0x3f803f80u : 0u;
            oh[gp - 1] = (u32x4){o, o, o, o};
            PIN1(oh[gp - 1]);
          }
          if constexpr (jb == 1 && gp == 6) {
            const int rk = moe ? e_ * 8 + (jj >> 3) * 2 + (xcd >> 2) : 0, r3 = ((rk - 14) * 43) >> 7, rr = rk - 14 - r3 * 3;
            const int c56 = rk < 14 ? rk * 4 + 1 : r3 * 4 + rr + ((rr + 1) >> 1);
            mat = (c56 + 36) >> 6; cg = c56 - mat * 28; dq = xcd & 3; d0 = dq * 256 + p_ * 64;
            pin4(mat, cg, dq, d0);
          }
          if constexpr (jb == 2 && gp == 0) {
            pgu = mat ? w_up : w_gate;
            pm2 = wsp + WS_ABT + (kq * 8 + e_) * 262144;
            pins(pgu, pm2);
            vw = ((d0 + r_ * 16) * F_ + cg * 128 + c_ * 8) * 2;
            PIN1(vw);
          }
          if constexpr (jb == 2 && (gp == 1 || gp == 2)) {
            dk[gp * 2 - 2] = c_ == r_ * 4 + gp * 2 - 2 ? ~0u : 0u; dk[gp * 2 - 1] = c_ == r_ * 4 + gp * 2 - 1 ? ~0u : 0u;
            PIN2(dk[gp * 2 - 2], dk[gp * 2 - 1]);
          }
          if constexpr (jb == 2 && gp == 3) { kl = lrow + (ln >> 5); go = (ln & 30) ? (int)0x80000000 : ((ln & 1) ? O_KEY : O_ST) + (int)kl * 8; PIN2(kl, go); }
          if constexpr (jb == 2 && gp == 4) { kl = 0xffff - kl; go = (ln & ~1) == 2 ? O_CE + (z * D_ + drow + (ln & 1)) * 8 : go; PIN2(kl, go); }
          if constexpr (jb == 2 && gp == 5) {
            gro = p_ < 2 ? O_ST + (st * 128 + p_ * 64 + ln) * 8 : O_CE + z * D_ * 8 + sym_row(D_, 32, 8, e_, z, (ln >> 2) & 3, (p_ - 2) * 16 + r_ * 4 + (ln & 3));
            lma = lds_addr(smem + LDS_MB) + (p_ < 2 ? p_ * 256 : 512 + p_ * 256) + ln * 4;
            PIN2(gro, lma);
          }
          if constexpr (jb == 7 && gp == 0) {
            vm2 = p_ * 1024 + ln * 16;
            PIN1(vm2);
          }
          if constexpr (jb == 2 && gp == 6) {
            const int r0 = p_ * 8 + lrw(ln >> 3);
            vn2[6] = sym_lo(64, S_ * 2, (r0 >> 2) & 3, (r0 >> 4) * 4 + (r0 & 3)); vn2[7] = sym_hi(S_, 64, S_ * 2, t_, z, (r0 >> 2) & 3) | (vm & 127);
            PIN2(vn2[6], vn2[7]);
          }
          if constexpr ((jb == 2 && gp == 7) || (jb == 7 && gp >= 6)) {
            constexpr int q0 = jb == 2 ? 0 : gp == 6 ? 3 : 6;
            const int lo = vn2[6], hi = vn2[7];
#pragma unroll
            for (int q = q0; q < q0 + 3 && q < 8; ++q) { vn2[q] = ((lo + q * 8 * S_ * 2) & (63 * S_ * 2)) ^ hi; PIN1(vn2[q]); }
          }
          if constexpr (jb == 7 && gp == 1) { vdn = rgb * 112 + p_ * 28 + r_ * 7; vgu = (((ln >> 3) & 1) * 4 * F_ + vdn + min(ln & 7, 6)) * 16; PIN2(vdn, vgu); }
          if constexpr (jb == 7 && gp == 2) { vdn = (vdn * D_ + cg8 * 128 + c_ * 8) * 2; PIN1(vdn); }
          if constexpr (jb == 7 && gp == 3) {
            abo = (((kq * 8 + e_) * 4 + p_) * 16 + c_) * AB_STRIDE * 4;
            abq[2] = ln >= 16 || (ln & 7) == t_ ? (int)0x80000000 : abo;
            PIN2(abo, abq[2]);
          }
          if constexpr (jb == 7 && gp == 4) {
            abq[1] = ln >= 8 ? (int)0x80000000 : abq[2];
            abq[0] = ((ln ^ t_) & 4) ? (int)0x80000000 : abq[1];
            PIN2(abq[0], abq[1]);
          }
          if constexpr (jb == 7 && gp == 5) {
            const unsigned mb = lds_addr(smem) + p_ * 1024;
            mq0 = mb + ((3 - kq) & 3) * 4096; mq1 = mb + ((4 - kq) & 3) * 4096; mq2 = mb + ((5 - kq) & 3) * 4096; mq3 = mb + ((6 - kq) & 3) * 4096;
            pin4(mq0, mq1, mq2, mq3);
          }
          if constexpr (jb == 8 && gp < 4) {
#pragma unroll
            for (int h = 0; h < 2; ++h) {
              const unsigned hv = h ? 0x0302u : 0x0100u;
              sel[h][gp] = ((ln & 7) >> 1) == gp ? ((ln & 1) ? (hv << 16) | 0x0c0cu : 0x0c0c0000u | hv) : 0x0c0c0c0cu;
            }
            PIN2(sel[0][gp], sel[1][gp]);
          }
        }
        SB();
      };
#define WS1(i) setup(IC<1>{}, IC<i>{})
#define WS2(i) setup(IC<2>{}, IC<i>{})
#define WS3(i) do { setup(IC<7>{}, IC<i>{}); if (i >= 4) setup(IC<8>{}, IC<(i) & 3>{}); spf(IC<(i < SP0 ? i : -1)>{}); } while (0)
      G1_STAGE_TILE(1, G1_STAGE, WS1);
      WAIT_VM(19); BAR();
      G1_STAGE_TILE(2, 2 * G1_STAGE, WS2);
      G1_STAGE_TILE(3, 3 * G1_STAGE, WS3);
      auto lnu = [&](auto U) __attribute__((always_inline)) {
        constexpr int jb = 1 + decltype(U)::value / 7, gp = decltype(U)::value % 7;
        SB();
        if constexpr (jb == 1) {
          const f32x4 z4 = {0.f, 0.f, 0.f, 0.f};
          if constexpr (gp == 0) { sa4 = MM(lv[0], oh[0], z4); gw[0] = MM(lv[0], lw[0], z4); gx[0] = MM(lv[0], lv[0], z4); PIN3(sa4, gw[0], gx[0]); }
          if constexpr (gp == 1) { sa4 = MM(lv2[0], oh[1], sa4); gw[1] = MM(lv2[0], lw[0], z4); gx[1] = MM(lv2[0], lv2[0], z4); PIN3(sa4, gw[1], gx[1]); }
          if constexpr (gp == 2) { sa4 = MM(lv[1], oh[0], sa4); gw[0] = MM(lv[1], lw[1], gw[0]); gx[0] = MM(lv[1], lv[1], gx[0]); PIN3(sa4, gw[0], gx[0]); }
          if constexpr (gp == 3) { sa4 = MM(lv2[1], oh[1], sa4); gw[1] = MM(lv2[1], lw[1], gw[1]); gx[1] = MM(lv2[1], lv2[1], gx[1]); PIN3(sa4, gw[1], gx[1]); }
          if constexpr (gp == 4) { sa4 = MM(ld[0], oh[2], sa4); sa4 = MM(ld[1], oh[2], sa4); PIN1(sa4); }
          if constexpr (gp == 5) { sa4 = MM(ld2[0], oh[3], sa4); sa4 = MM(ld2[1], oh[3], sa4); PIN1(sa4); }
          if constexpr (gp == 6) { sa4 = MM(lw[0], oh[4], sa4); sa4 = MM(lw[1], oh[4], sa4); PIN1(sa4);
            epoch = __builtin_amdgcn_readfirstlane(epv); tag = ((epoch + 1u) * 0x9E3779B1u ^ dmix) | 1u; }
        }
        if constexpr (jb == 2) {
          if constexpr (gp == 0) { su = (sa4[0] + sa4[1]) + (sa4[2] + sa4[3]); ta = diag(gw[0]); PIN2(su, ta); }
          if constexpr (gp == 1) { su = fold32(su, su); tb = diag(gw[1]); PIN2(su, tb); }
          if constexpr (gp == 2) { su = fold16(su, su); gs = fold32(ta, tb); PIN2(su, gs); }
          if constexpr (gp == 3) { ta = diag(gx[0]); tb = diag(gx[1]); PIN2(ta, tb); }
          if constexpr (gp == 4) { gs = fold16(gs, fold32(ta, tb)); PIN1(gs); }
          if constexpr (gp == 5) { gs += dpp_f<0xB1>(gs); gs += dpp_f<0x4E>(gs); PIN1(gs); }
          if constexpr (gp == 6) { gs += dpp_f<0x141>(gs); gs += dpp_f<0x140>(gs); PIN1(gs); }
        }
        if constexpr (jb == 3) {
          if constexpr (gp == 0) {
            const float s0 = rdl(su, 0), s1 = rdl(su, 1), q0 = rdl(gs, 16), q1 = rdl(gs, 48);
            sx = hb ? s1 : s0; ta = hb ? q1 : q0; PIN2(sx, ta);
          }
          if constexpr (gp == 1) {
            const float mean = sx * (1.0f / D_);
            mbv = rbf(mean);
            ta -= sx * mean;
            if (__builtin_expect(__any(!(sx * mean <= 16.f * ta)), 0)) {
              float q = fold32(sqdev16(lv, rdl(mbv, 0)), sqdev16(lv2, rdl(mbv, 32)));
              q = row_sum(fold16(q, q));
              const float dm = mean - mbv;
              ta = fmaxf(q - (float)D_ * dm * dm, 0.f);
            }
            PIN2(mbv, ta);
          }
          if constexpr (gp == 2) { ta = rbf(rbf(ta * (1.0f / D_)) + 1e-5f); gs -= mbv * rdl(su, 4); PIN2(ta, gs); }
          if constexpr (gp == 3) { ta = rbf(__builtin_amdgcn_sqrtf(ta)); tb = __builtin_amdgcn_rcpf(ta); PIN2(ta, tb); }
          if constexpr (gp == 4) {
            const unsigned rb = __float_as_uint(rbf(gs * tb));
            kl |= (rb ^ ((unsigned)((int)rb >> 31) | 0x80000000u)) & 0xffff0000u; PIN1(kl);
          }
          if constexpr (gp == 5) { kl = (lane & 2) ? __float_as_uint(su) : (lane & 1) ? kl : (__float_as_uint(mbv) >> 16) | __float_as_uint(ta); PIN1(kl); }
        }
        SB();
      };
      constexpr int LN_JB = 1 + (19 * LNS) / 8;
      auto work1 = [&](auto JB, auto GP) __attribute__((always_inline)) {
        constexpr int jb = decltype(JB)::value, gp = decltype(GP)::value, pos = (jb - 1) * 8 + gp;
        SB();
        if constexpr (pos % LNS == 0 && pos / LNS < 20) lnu(IC<pos / LNS>{});
        if constexpr (jb == ST_JB + 3) {
          if constexpr (gp == 0) { if (!__all(gr.y == tag)) gr = granule_wait((const u64*)((const char*)(flags + FL_AB) + gro), tag); PIN1(gr); }
          if constexpr (gp == 1) { g1 = __builtin_amdgcn_rcpf(__uint_as_float((gr.x & 0xffff0000u) ^ ((unsigned)z << 31))); PIN1(g1); }
          if constexpr (gp == 2) { g0 = pw < 2 ? -(lo_bf(gr.x) * g1) : __uint_as_float(gr.x); PIN1(g0); }
        }
        if constexpr (jb == 8 && (gp == 2 || gp == 5)) {
          static_assert(VAN == 4 && VA0 >= 2 && VA0 + VAN <= 9, "first halves");
          int ln = lane;
          PIN1(ln);
          const int i = (ln & 15) * 4 + pw, n = gp == 2 ? (moe ? 0 : 64) : (et < 2 ? 0 : 32);
          const int r0 = gp == 2 ? tt * 64 : sym_row(S_, 64, 1, tt, 0, et >> 1, 0) + (et & 1) * 32;
          (gp == 2 ? tv7 : tv2) = i < n ? (r0 + i) * (S_ * 2) + ((tt ^ (VA0 - 1 + (ln >> 4))) << 8) : (int)0x80000000;
          PIN1(gp == 2 ? tv7 : tv2);
        }
        if constexpr (gp < SP1 && jb <= SP1E) spf(IC<SP0 + (jb - 1) * SP1 + gp>{});
        SB();
      };
      auto step1 = [&](auto JB) __attribute__((always_inline)) {
        constexpr int jb = decltype(JB)::value;
        if constexpr (jb == 1) PAGE(1);
        if constexpr (jb == 7) PAGE(2);
        constexpr int n16 = jb == 12 ? 12 : jb == 11 ? 14 : 16;
        constexpr auto wl = [](int q) { return (q == LN_JB ? 1 : 0) + (q == ST_JB ? 1 : 0); };
        if constexpr (jb == 1) {
          asm volatile("s_waitcnt vmcnt(%11)" : "+v"(lv[0]), "+v"(lv[1]), "+v"(lw[0]), "+v"(lw[1]), "+v"(ld[0]), "+v"(ld[1]),
                       "+v"(lv2[0]), "+v"(lv2[1]), "+v"(ld2[0]), "+v"(ld2[1]), "+v"(epv) : "i"(16) : "memory");
        } else if constexpr (jb == ST_JB + 3) {
          asm volatile("s_waitcnt vmcnt(%1)" : "+v"(gr) : "i"(n16 + wl(jb - 2) + wl(jb - 1)) : "memory");
        } else wait_vm<n16 + wl(jb - 3) + wl(jb - 2) + wl(jb - 1)>();
        BAR();
        if constexpr (jb == 2) { if (pw == 0 && lane < 16) ((int*)(smem + LDS_SEL))[lane < 8 ? lane : lane + 8] = 0; }
        if constexpr (jb == 4) { if (pw == 0 && lane == 0) ((unsigned*)(smem + LDS_SEL))[5] = tag; }
#define W1(i) work1(JB, IC<i>{})
        if constexpr (jb < 10) G1_STAGE_TILE(jb - 1 + G1_NST, ((jb - 1) % G1_NST) * G1_STAGE, W1);
        else G1_STAGE_A(jb - 1 + G1_NST, ((jb - 1) % G1_NST) * G1_STAGE, W1);
        if constexpr (jb == 12) G1_STAGE_B(13, G1_STAGE);
        if constexpr (jb == LN_JB) __builtin_amdgcn_raw_buffer_store_b64((u32x2){kl, tag}, rf, go, 0, 16);
        if constexpr (jb == ST_JB) {
          asm volatile("buffer_load_dwordx2 %0, %1, %2, 0 offen sc1" : "=v"(gr) : "v"(gro), "s"(rf) : "memory");
        }
        if constexpr (jb <= 8) {
          if (et == jb - 1) {
#pragma unroll
            for (int l = 0; l < WSN; ++l) bld16_nt_io(wv[l], rw, vw, l * F_ * 2);
          }
        }
        SB();
      };
      static_assert(ST_JB + 3 <= 12 && ST_JB >= 6, "schedule");
      step1(IC<1>{}); step1(IC<2>{}); step1(IC<3>{}); step1(IC<4>{}); step1(IC<5>{}); step1(IC<6>{}); step1(IC<7>{}); step1(IC<8>{}); step1(IC<9>{}); step1(IC<10>{}); step1(IC<11>{}); step1(IC<12>{});
    }
#define G2_STAGE_N(kk_, W) do { const int kb = KROT(kk_); constexpr int so = n_off(kk_); SB(); \
    DMA_S(rn2, vn2[0], mq0, so, kb); W(0); DMA_S(rn2, vn2[1], mq1, so, kb); W(1); \
    DMA_S(rn2, vn2[2], mq2, so, kb); W(2); DMA_S(rn2, vn2[3], mq3, so, kb); W(3); \
    DMA_S(rn2, vn2[4], mq0, so + 16384, kb); W(4); DMA_S(rn2, vn2[5], mq1, so + 16384, kb); W(5); \
    DMA_S(rn2, vn2[6], mq2, so + 16384, kb); W(6); DMA_S(rn2, vn2[7], mq3, so + 16384, kb); W(7); SB(); } while (0)
#define G2_STAGE_NH(kk_, h) do { const int kb = KROT(kk_); constexpr int so = n_off(kk_) + (h) * 16384; SB(); \
    DMA_S(rn2, vn2[(h) * 4], mq0, so, kb); DMA_S(rn2, vn2[(h) * 4 + 1], mq1, so, kb); \
    DMA_S(rn2, vn2[(h) * 4 + 2], mq2, so, kb); DMA_S(rn2, vn2[(h) * 4 + 3], mq3, so, kb); SB(); } while (0)
#define G2_STAGE_M(kk_, W) do { const int kb = KROT(kk_) * 128; constexpr int so = G2_MRING + m_off(kk_); SB(); \
    DMA_(rm2, vm2, mb0, so, kb, 0, " sc1"); W(8); DMA_(rm2, vm2, mb0, so + 4096, kb, 4096, " sc1"); W(9); \
    DMA_(rm2, vm2, mb0, so + 8192, kb, 8192, " sc1"); W(10); DMA_(rm2, vm2, mb0, so + 12288, kb, 12288, " sc1"); W(11); SB(); } while (0)
    WAIT_VM(8); BAR();
    asm volatile("ds_write_b32 %0, %1" :: "v"(lma), "v"(g0) : "memory");
    if (pw < 2) asm volatile("ds_write_b32 %0, %1 offset:512" :: "v"(lma), "v"(g1) : "memory");
    u32x4 tv[4];
    unsigned ready = 0;
    auto need = [&](int k) __attribute__((always_inline)) {
      const int b = k_blk(k) + (k_ord(k) & 1) * 8;
#pragma unroll 1
      for (int it = 0; !((ready >> b) & 1) && it < SPIN_MAX; ++it) {
        ready |= (unsigned)__ballot(__builtin_amdgcn_raw_buffer_load_b32(rf, abq[k < 5 ? 0 : k < 9 ? 1 : 2], 0, 16) == tag);
        asm volatile("" ::: "memory");
      }
    };
    unsigned ksel, bad = 0;
    int isel;
    unsigned xw;
    auto bvec = [&](unsigned src, const unsigned (&sl)[4]) __attribute__((always_inline)) {
      return (u32x4){__builtin_amdgcn_perm(0, src, sl[0]), __builtin_amdgcn_perm(0, src, sl[1]),
                     __builtin_amdgcn_perm(0, src, sl[2]), __builtin_amdgcn_perm(0, src, sl[3])};
    };
#define BC8(v, p) (unsigned)__builtin_amdgcn_update_dpp(__builtin_amdgcn_update_dpp(0, (int)(v), 0x150 + (p), 0xf, 0x3, false), (int)(v), 0x158 + (p), 0xf, 0xc, false)
    float* lg = (float*)(smem + LDS_GU);
    f32x4 macc = {0.f, 0.f, 0.f, 0.f};
    u32x2 sv;
    unsigned hp, gbad;
    float s0, s1;
    auto work2 = [&](auto JB, auto GP) __attribute__((always_inline)) {
      constexpr int jb = decltype(JB)::value, gp = decltype(GP)::value;
      SB();
      if constexpr (jb == -1 && gp < 4) {
        constexpr int c = gp == 0 ? 0xB1 : gp == 1 ? 0x4E : gp == 2 ? 0x141 : 0x140;
        merge2(k1, k2, dpp_u<c>(k1), dpp_u<c>(k2));
        PIN2(k1, k2);
      }
      if constexpr (jb == -1 && gp == 4) {
        ksel = tk ? k2 : k1;
        isel = (0xffff - (int)(ksel & 0xffffu)) & (S_ - 1);
        k1 = (isel * D_ + d0 + rq * 16 + (lane & 7) * 2) * 2; k2 = O_ST + isel * 8;
        PIN1(ksel);
        xw = gld4_s(x, k1);
        sv = bld8_sc1(rf, k2);
      }
      if constexpr (jb == 0 && gp == 1) wait_xs<3 + 2 + 2>(xw, sv);
      if constexpr (jb == 0) {
        if constexpr (gp == 1) { if (!__all(sv.y == tag)) sv = granule_wait(stat + isel, tag); s0 = __builtin_amdgcn_rcpf(hi_bf(sv.x)); PIN1(s0); }
        if constexpr (gp == 2) { s1 = ln_elem(lo_bf(xw), lo_bf(sv.x), hi_bf(sv.x), s0); PIN1(s1); }
        if constexpr (gp == 3) { xw = pk2(s1, ln_elem(hi_bf(xw), lo_bf(sv.x), hi_bf(sv.x), s0)); PIN1(xw); }
      }
      if constexpr (jb == DG && gp < 7) { PIN1(dv[gp]); macc = MM(dv[gp], bvec(BC8(hp, gp), sel[0]), macc); PIN1(macc); }
      if constexpr ((jb == 0 && gp >= 4) || jb == 1) {
        constexpr int l = jb == 0 ? gp - 4 : gp + 4;
        if constexpr (!(l & 1)) hp = BC8(xw, l >> 1);
        PIN1(wv[l]);
        macc = MM(wv[l], bvec(hp, sel[l & 1]), macc);
        PIN2(macc, hp);
      }
      if constexpr (jb == GV_JB + 2) {
        if constexpr (gp == 0) { gbad = (gv[0].y ^ tag) | (gv[0].w ^ tag) | (gv[1].y ^ tag) | (gv[1].w ^ tag); PIN1(gbad); }
        if constexpr (gp == 1) { gbad |= (gv[2].y ^ tag) | (gv[2].w ^ tag) | (gv[3].y ^ tag) | (gv[3].w ^ tag); PIN1(gbad); }
        if constexpr (gp == 2) {
          s0 = ((__uint_as_float(gv[0].x) + __uint_as_float(gv[1].x)) + __uint_as_float(gv[2].x)) + __uint_as_float(gv[3].x);
          s1 = ((__uint_as_float(gv[0].z) + __uint_as_float(gv[1].z)) + __uint_as_float(gv[2].z)) + __uint_as_float(gv[3].z);
          PIN2(s0, s1);
        }
        if constexpr (gp == 3) {
#pragma unroll 1
          for (int it = 0; !__all(gbad == 0) && it < SPIN_MAX; ++it) {
            u32x4 q[4];
            gbad = 0;
#pragma unroll
            for (int i = 0; i < 4; ++i) { q[i] = __builtin_amdgcn_raw_buffer_load_b128(rgu, vgu, i * F_ * 16, 16); gbad |= (q[i].y ^ tag) | (q[i].w ^ tag); }
            s0 = ((__uint_as_float(q[0].x) + __uint_as_float(q[1].x)) + __uint_as_float(q[2].x)) + __uint_as_float(q[3].x);
            s1 = ((__uint_as_float(q[0].z) + __uint_as_float(q[1].z)) + __uint_as_float(q[2].z)) + __uint_as_float(q[3].z);
            asm volatile("" ::: "memory");
          }
          const unsigned gp2 = pk2(s0, s1);
          const unsigned up2 = dpp_u<0x128>(gp2);
          s0 = tk ? hi_bf(up2) : lo_bf(gp2); s1 = tk ? hi_bf(gp2) : lo_bf(up2);
          PIN2(s0, s1);
        }
        if constexpr (gp == 4) { s0 = rbf(s0 * __builtin_amdgcn_rcpf(1.f + __expf(-s0))); PIN1(s0); }
        if constexpr (gp == 5) { hp = pk2(s0 * s1, 0.f); PIN1(hp); }
      }
      if constexpr (jb == 10 && gp == 4) code_touch(sd, (j * 4 + pw) * 256);
      SB();
    };
#define W2M(i) work2(IC<-1>{}, IC<i>{})
#define W20(i) work2(IC<0>{}, IC<i>{})
#define W21(i) work2(IC<1>{}, IC<i>{})
    unsigned fs;
    {
      G1_STAGE_B(15, 3 * G1_STAGE);
      G1_STAGE_B(14, 2 * G1_STAGE);
      lgkm0(sd);
      BAR();
      G2_STAGE_NH(0, 0);
      WAIT_VM(6); BAR();
#pragma unroll
      for (int i = 0; i < 4; ++i) kk[i] = bld16_sc1(rf, l16, ko_ + i * 1024);
      G2_STAGE_NH(0, 1);
#pragma unroll
      for (int l = WSN; l < 16; ++l) wv[l] = bld16_nt(rw, vw, l * F_ * 2);
      touch1(rn2, tv7, mb0, 0);
      asm volatile("s_waitcnt vmcnt(%4)" : "+v"(kk[0]), "+v"(kk[1]), "+v"(kk[2]), "+v"(kk[3]) : "i"(16 - WSN + 5) : "memory");
#pragma unroll
      for (int i = 0; i < 4; ++i) {
        k2 = med3u(k1, k2, kk[i].x); k1 = max(k1, kk[i].x);
        k2 = med3u(k1, k2, kk[i].z); k1 = max(k1, kk[i].z);
        bad |= (kk[i].y ^ tag) | (kk[i].w ^ tag);
      }
#pragma unroll 1
      for (int it = 0; !__all(bad == 0) && it < SPIN_MAX; ++it) {
        k1 = 0; k2 = 0; bad = 0;
#pragma unroll
        for (int i = 0; i < 4; ++i) {
          const u32x4 q = __builtin_amdgcn_raw_buffer_load_b128(rf, l16, ko_ + i * 1024, 16);
          k2 = med3u(k1, k2, q.x); k1 = max(k1, q.x);
          k2 = med3u(k1, k2, q.z); k1 = max(k1, q.z);
          bad |= (q.y ^ tag) | (q.w ^ tag);
        }
        asm volatile("" ::: "memory");
      }
      merge2(k1, k2, dpp_u<0xB1>(k1), dpp_u<0xB1>(k2));
      merge2(k1, k2, dpp_u<0x4E>(k1), dpp_u<0x4E>(k2));
      merge2(k1, k2, dpp_u<0x141>(k1), dpp_u<0x141>(k2));
      merge2(k1, k2, dpp_u<0x140>(k1), dpp_u<0x140>(k2));
      if ((lane & 15) == 0) *(u32x2*)(smem + LDS_KEY + (pw * 4 + rq) * 8) = (u32x2){k1, k2};
      PAGE(3);
      wait_vm<17 - WSN>();
      BAR_LGKM();
#pragma unroll
      for (int jj = 0; jj < 4; ++jj) tv[jj] = *(const u32x4*)(smem + G2_MRING + m_off(0) + (jj * 4 + pw) * 1024 + lane * 16);
      { const u32x2 kp = *(const u32x2*)(smem + LDS_KEY + ci * 8); k1 = kp.x; k2 = kp.y; }
#pragma unroll
      for (int jj = 0; jj < 4; ++jj) __builtin_amdgcn_raw_buffer_store_b128(tv[jj], rm2, vm2 + jj * 4096 + 2 * tt * 16384, 0, 16);
      G2_STAGE_N(1, W2M);
      touch1(rn2, tv2, mb0, 0);
      WAIT_VM(11);
      if (lane == tt) __builtin_amdgcn_raw_buffer_store_b32(tag, rf, abo, 0, 16);
      {
        lds_vi rel = (lds_vi)(smem + LDS_SEL + 12);
        int it = 0;
        while (rel[0] != 4 && it < SPIN_MAX * 8) ++it;
        asm volatile("" ::: "memory");
      }
      G2_STAGE_N(2, W20);
      __builtin_amdgcn_s_sleep(2);
      WAIT_VM(10);
      fs = bld4_sc1(rf, abq[0]);
    }
    BAR();
#pragma unroll
    for (int jj = 0; jj < 4; ++jj) tv[jj] = *(const u32x4*)(smem + G2_MRING + m_off(1) + (jj * 4 + pw) * 1024 + lane * 16);
    constexpr int SPB = SP0 + SP1E * SP1;
    G2_STAGE_N(3, NOW);
    asm volatile("s_waitcnt vmcnt(8)" : "+v"(fs) :: "memory");
    ready = (unsigned)__ballot(fs == tag);
    need(2);
    G2_STAGE_M(2, NOW);
    need(3);
    G2_STAGE_M(3, NOW);
    fs = bld4_sc1(rf, abq[1]);
    W21(0); W21(1); W21(2); W21(3); W21(4); W21(5); W21(6); W21(7); W21(8); W21(9); W21(10); W21(11);
    asm volatile("" : "+v"(tv[0]), "+v"(tv[1]), "+v"(tv[2]), "+v"(tv[3]));
    int dno;
    auto step2 = [&](auto JB) __attribute__((always_inline)) {
      constexpr int jb = decltype(JB)::value;
      if constexpr (jb == 3) PAGE(4);
      if constexpr (jb == 9) PAGE(5);
      if constexpr (jb == 13) PAGE(7);
      constexpr auto must = [](int q) { return (q == 2 ? 4 : 0) + ((FSM >> q) & 1) + dig(GVS, q); };
      if constexpr (jb == 2) wait_vm<4 + must(1)>();
      else wait_vm<dn_cnt(jb - 2) + (jb == 5 ? 1 : 0) + 12 + must(jb - 1) + dn_cnt(jb - 1)>();
      if constexpr (jb == 3) { if (pw == 0 && (lane & ~8) == 0) ((unsigned*)(smem + LDS_SEL))[tk] = ksel; }
      if constexpr ((jb == 2 || jb == DG + 1)) {
        *(f32x4*)(lg + pw * 256 + lane * 4) = macc;
        macc = (f32x4){0.f, 0.f, 0.f, 0.f};
        lgkm0(sd);
      }
      BAR();
      float p0[4], p1[4];
      const int col = (pw & 1) * 64 + lane;
      if constexpr (jb == 3) {
        if (g == 0 && pw == 0 && lane == 0) __hip_atomic_store((unsigned*)flags + FL_EPOCH, epoch + 1u, __ATOMIC_RELAXED, __HIP_MEMORY_SCOPE_AGENT);
      }
      if constexpr (jb == DG + 1) {
        const float* p = lg + (pw & 1) * 32 + (lane >> 4) * 64 + ((2 * lane) & 7) * 4 + ((lane >> 2) & 3);
#pragma unroll
        for (int i = 0; i < 4; ++i) { p0[i] = p[i * 256]; p1[i] = p[i * 256 + 4]; }
      }
      if constexpr (jb == 2) {
        const float* p = lg + (col >> 5) * 64 + (col & 7) * 4 + ((col >> 3) & 3);
#pragma unroll
        for (int i = 0; i < 4; ++i) { p0[i] = p[i * 256]; p1[i] = p[i * 256 + 32]; }
      }
      if constexpr (jb == 4) {
        if (lane == 8 + tt) __builtin_amdgcn_raw_buffer_store_b32(tag, rf, abo, 0, 16);
      }
      if constexpr ((FSM >> (jb - 2)) & 1) {
        asm volatile("" : "+v"(fs));
        ready |= (unsigned)__ballot(fs == tag);
      }
      if constexpr (jb == GV_JB + 2) asm volatile("" : "+v"(gv[0]), "+v"(gv[1]), "+v"(gv[2]), "+v"(gv[3]));
      auto w2 = [&](auto GP) __attribute__((always_inline)) {
        constexpr int gp = decltype(GP)::value;
        if constexpr ((jb == 2 || jb == DG + 1) && gp == 0) {
          p0[0] = (p0[0] + p0[1]) + (p0[2] + p0[3]); p1[0] = (p1[0] + p1[1]) + (p1[2] + p1[3]);
          PIN2(p0[0], p1[0]);
        }
        if constexpr (jb == DG + 1 && gp == 1) {
          if (pw < 2) __builtin_amdgcn_raw_buffer_store_b64((u32x2){pk2(p0[0], p1[0]), tag}, mk_rsrc(wsp + WS_DN), (((pw * 8 + cg8) * 32 + rgb) * 64 + lane) * 8, 0, 16);
        }
        if constexpr (jb == 12 && gp == 3) epi_setup(3);
        if constexpr (jb == 12 && gp == 6) { key[0] = __builtin_amdgcn_readlane(ksel, 0); key[1] = __builtin_amdgcn_readlane(ksel, 8); far_calc(); }
        if constexpr (jb == 12 && gp == 10) {
          int l_ = lane;
          PIN1(l_);
          const int ec = seg0((l_ >> 4) & 1, (l_ >> 3) & 1);
          dno = (ec >> 7) * 16384 + (l_ >> 5) * 512 + (ec & 127) * 4 + (l_ & 7) * 16;
          PIN1(dno);
        }
        work2(JB, GP);
        if constexpr (gp < dig(SPS, jb)) spf(IC<SPB + acc_dig(SPS, jb) + gp>{});
      };
#define W2(i) w2(IC<i>{})
      G2_STAGE_N(jb + 2, W2);
      need(jb + 2);
      G2_STAGE_M(jb + 2, W2);
      if constexpr (jb == 2) {
#pragma unroll
        for (int jj = 0; jj < 4; ++jj) __builtin_amdgcn_raw_buffer_store_b128(tv[jj], rm2, vm2 + jj * 4096 + (2 * tt + 1) * 16384, 0, 16);
      }
      if constexpr ((FSM >> jb) & 1) fs = bld4_sc1(rf, abq[jb < 5 ? 1 : 2]);
#pragma unroll
      for (int i = acc_dig(GVS, jb); i < acc_dig(GVS, jb + 1); ++i) gv[i] = bld16_sc1(rgu, vgu, i * F_ * 16);
#pragma unroll
      for (int i = dn_first(jb); i < dn_first(jb) + dn_cnt(jb); ++i) dv[i] = bld16_nt(rdn, vdn, i * D_ * 2);
      if constexpr (jb == 2) {
        if (moe && pw < 2)
          __builtin_amdgcn_raw_buffer_store_b128((u32x4){__float_as_uint(p0[0]), tag, __float_as_uint(p1[0]), tag},
                                                 rgu, ((mat * 4 + dq) * F_ + cg * 128 + col) * 16, 0, 16);
      }
      SB();
      if constexpr (dig(SPS, jb) > 12) spf(IC<SPB + acc_dig(SPS, jb) + 12>{});
      SB();
    };
    static_assert(DG + 1 <= 12 && DG >= GV_JB + 3 && dn_first(DG - 2) == 7 && acc_dig(GVS, GV_JB + 1) == 4, "schedule");
    static_assert(SPB + acc_dig(SPS, 14) == (16 - VAN) * 16, "all tiles are prefetched");
    step2(IC<2>{}); step2(IC<3>{}); step2(IC<4>{}); step2(IC<5>{}); step2(IC<6>{}); step2(IC<7>{}); step2(IC<8>{}); step2(IC<9>{}); step2(IC<10>{}); step2(IC<11>{}); step2(IC<12>{}); step2(IC<13>{});
    lgkm0(sd);
    WAIT_VM(12); BAR();
    __builtin_amdgcn_s_setprio(0);
    const __amdgpu_buffer_rsrc_t rd = mk_rsrc(wsp + WS_DN + pw * 4096);
    u32x4 dq_[4];
    epi_setup(0); epi_setup(1); epi_setup(2);
    xq_load(0); xq_load(1); xq_load(2);
    asm volatile("s_cmp_lg_u32 %7, 0\n\ts_cbranch_scc1 1f\n\t"
                 "buffer_load_dwordx4 %0, %4, %5, %6 offen sc1\n\tbuffer_load_dwordx4 %1, %4, %5, %6 offen offset:1024 sc1\n\t"
                 "buffer_load_dwordx4 %2, %4, %5, %6 offen offset:2048 sc1\n\tbuffer_load_dwordx4 %3, %4, %5, %6 offen offset:3072 sc1\n\t"
                 "s_waitcnt vmcnt(7)\n\ts_branch 2f\n1:\n\ts_waitcnt vmcnt(3)\n2:\n\ts_barrier\n\ts_waitcnt vmcnt(0)"
                 : "=&v"(dq_[0]), "=&v"(dq_[1]), "=&v"(dq_[2]), "=&v"(dq_[3])
                 : "v"(dno), "s"(rd), "s"((far0 & ~9) ? 131072 : 0), "s"(min(far0 & ~9, far1 & ~9)) : "scc", "memory");
    wait_xq<0>(xq, sq);
    bool first = true;
#pragma unroll 1
    for (int c = 0; c < 2; ++c) {
      if (__builtin_expect(((c ? far1 : far0) & ~9) != 0, 1)) continue;
      const float sg = rbf(__builtin_amdgcn_rcpf(1.f + __expf(-key_score(c ? key[1] : key[0]))));
#pragma unroll 1
      for (int it = 0; it < SPIN_MAX; ++it) {
        unsigned bad = 0;
#pragma unroll
        for (int i = 0; i < 4; ++i) bad |= (dq_[i].y ^ tag) | (dq_[i].w ^ tag);
        if ((first || it) && __all(bad == 0)) break;
        if (it) __builtin_amdgcn_s_sleep(4);
#pragma unroll
        for (int i = 0; i < 4; ++i) dq_[i] = __builtin_amdgcn_raw_buffer_load_b128(rd, dno + i * 1024, c * 131072, 16);
        asm volatile("" ::: "memory");
      }
      first = false;
      f32x4 s4 = {0.f, 0.f, 0.f, 0.f};
#pragma unroll
      for (int i = 0; i < 4; ++i) s4 += (f32x4){lo_bf(dq_[i].x), hi_bf(dq_[i].x), lo_bf(dq_[i].z), hi_bf(dq_[i].z)};
      *(f32x2*)(smem + LDS_FIN + (c * 4 + pw) * 512 + (lane & 31) * 16 + (lane >> 5) * 8) = (f32x2){fold32(s4[0], s4[2]) * sg, fold32(s4[1], s4[3]) * sg};
      asm volatile("s_waitcnt lgkmcnt(0)" ::: "memory");
      if (lane == 0) asm volatile("ds_add_u32 %0, %1" :: "v"(lds_addr(smem + LDS_SEL + 24 + c * 4)), "v"(1) : "memory");
    }
    cv_calc();
  } else {
  {
    const int q = j * 4 + wid;
    constexpr int Q1 = WS_MB, Q2 = Q1 + 8, Q3 = Q2 + 8, Q4 = Q3 + 7, Q5 = Q4 + 7, Q6 = Q5 + 7, Q7 = Q6 + 4, Q8 = Q7 + 4, QC = Q8 + 1;
    static_assert(QC <= 128, "warm-up slots");
    unsigned dmy = 0;
    if (q < QC) {
      const char* base; int b0;
      if (q < Q1) { base = wsp; b0 = 0; }
      else if (q < Q2) { base = (const char*)ws_re; b0 = Q1; }
      else if (q < Q3) { base = (const char*)ws_im; b0 = Q2; }
      else if (q < Q4) { base = (const char*)w_gate; b0 = Q3; }
      else if (q < Q5) { base = (const char*)w_up; b0 = Q4; }
      else if (q < Q6) { base = (const char*)w_down; b0 = Q5; }
      else if (q < Q7) { base = (const char*)out; b0 = Q6; }
      else if (q < Q8) { base = (const char*)x; b0 = Q7; }
      else { base = (const char*)w_r; b0 = Q8; }
      base += (size_t)(q - b0) << 20;
      asm volatile("s_load_dword %0, %1, 0" : "+s"(dmy) : "s"(base) : "memory");
    }
    if (wid == 0) { CALL_PG(1); CALL_PG(8); CALL_PG(5); }
    else if (wid == 1) { CALL_PG(2); CALL_PG(9); CALL_PG(6); }
    else if (wid == 2) { CALL_PG(3); CALL_PG(10); CALL_PG(7); }
    else { CALL_PG(4); CALL_PG(11); CALL_PG(12); }
    BAR();
    lgkm0(dmy);
    TL(1);
  }
  PAGE(8);
  epi_setup(0); epi_setup(1); epi_setup(2); epi_setup(3);
  const int wr = wid >> 1, wc = wid & 1;
  const int sw = swz(fr), fl = lrw(fr);
  const int ko0 = ((0 + fq) ^ sw) * 16, ko1 = ((4 + fq) ^ sw) * 16;
  bf16x8 fm[2][4], fn[2][4];
  {
    const char* pm = smem + (wr * 64 + fl) * 128;
    const char* pn = smem + 16384 + (wc * 64 + fl) * 128;
    f32x4 acc[4][4];
#pragma unroll
    for (int i = 0; i < 4; ++i)
#pragma unroll
      for (int jn = 0; jn < 4; ++jn)
#pragma unroll
        for (int q = 0; q < 4; ++q) acc[i][jn][q] = 0.f;
#define TO(i) ((i) * 2048)
#define LDSP(T, a) ((__attribute__((address_space(3))) T*)(size_t)(a))
#define SLOTS(n) _Pragma("unroll") for (int q = 0; q < (n); ++q)
#define MFA(c, m, n) asm volatile("v_mfma_f32_16x16x32_bf16 %0, %2, %1, %0" : "+v"(c) : "v"(m), "v"(n))
#define MFA2(c, m, n, m2, n2) asm volatile("v_mfma_f32_16x16x32_bf16 %0, %2, %1, %0\n\tv_mfma_f32_16x16x32_bf16 %0, %4, %3, %0" : "+v"(c) : "v"(m), "v"(n), "v"(m2), "v"(n2))
    unsigned lm[2][2], ln[2][2];
#pragma unroll
    for (int u = 0; u < 2; ++u)
#pragma unroll
      for (int kk = 0; kk < 2; ++kk) {
        lm[u][kk] = lds_addr(pm) + u * 2 * G1_STAGE + (kk ? ko1 : ko0);
        ln[u][kk] = lds_addr(pn) + u * 2 * G1_STAGE + (kk ? ko1 : ko0);
        asm volatile("" : "+v"(lm[u][kk]), "+v"(ln[u][kk]));
      }
    auto ldm = [&](int stg, int i, int kk) __attribute__((always_inline)) { fm[kk][i] = *LDSP(const bf16x8, lm[stg >> 1][kk] + (stg & 1) * G1_STAGE + TO(i)); };
    auto ldn = [&](int stg, int jn, int kk) __attribute__((always_inline)) { fn[kk][jn] = *LDSP(const bf16x8, ln[stg >> 1][kk] + (stg & 1) * G1_STAGE + TO(jn)); };
    auto quad = [&](int a, int c, int d, int s) __attribute__((always_inline)) {
      const int i = (s < 2 || s >= 6) ? a : a + 1, jn = s < 4 ? c : d, kk = (s == 1 || s == 2 || s == 5 || s == 6);
      MFA(acc[i][jn], fm[kk][i], fn[kk][jn]);
    };
    auto quad2 = [&](int a, int c, int d, int s) __attribute__((always_inline)) {
      const int i = (s < 2 || s >= 6) ? a : a + 1, jn = s < 4 ? c : d, kk = (s == 2 || s == 6);
      MFA2(acc[i][jn], fm[kk][i], fn[kk][jn], fm[1 - kk][i], fn[1 - kk][jn]);
    };
    auto half1 = [&](int stg) __attribute__((always_inline)) {
      const int ja = (stg & 1) * 2, jb = 2 - ja;
      SLOTS(8) {
        quad(0, ja, ja + 1, q);
        if (q < 4) ldn(stg, jb + (q >> 1), q & 1); else ldm(stg, 2 + ((q - 4) >> 1), q == 5 || q == 6);
        SB();
      }
      SLOTS(4) { quad2(0, jb, jb + 1, 2 * q); SB(); }
    };
    auto half2 = [&](int stg) __attribute__((always_inline)) {
      const int ja = (stg & 1) * 2, jb = 2 - ja, nx = (stg + 1) & 3;
      SLOTS(8) {
        quad(2, jb + 1, jb, q);
        if (!(q & 1)) ldm(nx, q >> 2, q == 2 || q == 4);
        SB();
      }
      SLOTS(8) {
        quad(2, ja, ja + 1, q);
        if (!(q & 1)) ldn(nx, jb + (q >> 2), (q >> 1) & 1);
        SB();
      }
    };
    unsigned ae[7];
    {
      int l_ = lane;
      asm volatile("" : "+v"(l_));
      const int p = l_ >> 4, dn = sym_dn(p), hf = (p >> 1) ^ (dn & z);
#pragma unroll
      for (int u = 0; u < 7; ++u) {
        const int T = u < 3 ? wc * 4 : (wc * 4 + u - 3 - dn) & 7, i = u < 3 ? (u + 1) ^ ((u & 1) ? 0 : dn * 2) : 0;
        const int f = dn * 8 + ((T & 2) << 1) + ((T & 4) >> 1) + (T & 1);
        ae[u] = lds_addr(smem + G2_MRING) + (hf * 64 + i * 16 + lrw(f)) * 128 + (((wr * 4 + ((l_ >> 2) & 3)) ^ swz(f)) * 16) + (l_ & 3) * 4;
        asm volatile("" : "+v"(ae[u]));
      }
    }
    unsigned al = lds_addr(smem + LDS_MB) + (wr * 32 + fr * 2) * 4, ac = lds_addr(smem + LDS_MB) + (256 + wc * 64 + fq * 4) * 4;
    asm volatile("" : "+v"(al), "+v"(ac));
    SLOTS(16) asm volatile("" : "+v"(acc[q >> 2][q & 3]));
    SB();
    SLOTS(4) { ldm(0, q >> 1, q & 1); ldn(0, q >> 1, q & 1); }
    SB();
#pragma unroll 1
    for (int k4 = 0; k4 < G1_NKT - 4; k4 += 4) {
      half1(0); BAR_LGKM();

      SB(); half2(0);
      half1(1); BAR_LGKM();

      SB(); half2(1);
      half1(2); BAR_LGKM();

      SB(); half2(2);
      half1(3); BAR_LGKM();

      SB(); half2(3);
    }
    bf16x8 gm[4][4], gn[2][8];
    auto rdm1 = [&](int set, int t, int h, int i, int kk) __attribute__((always_inline)) {
      gm[set][kk * 2 + i] = *LDSP(const bf16x8, lm[(t >> 1) & 1][kk] + (t & 1) * G1_STAGE + h * 4096 + i * 2048);
    };
    auto rdg = [&](int set, int t, int h, int r) __attribute__((always_inline)) {
      if (r == 0 || r == 2 || r == 4 || r == 5) rdm1(set, t, h, r >= 4, r == 2 || r == 4);
      else {
        const int jn = r < 4 ? 0 : (r - 4) >> 1, kk = r < 4 ? r >> 1 : r & 1;
        gn[set][kk * 4 + jn] = *LDSP(const bf16x8, ln[(t >> 1) & 1][kk] + (t & 1) * G1_STAGE + TO(jn));
      }
    };
    auto mmg = [&](int ms, int ns, int h, int jn, int r, int f) __attribute__((always_inline)) {
      const int i = (r >> 1) ^ f, kk = r == 1 || r == 2;
      MFA(acc[h * 2 + i][jn], gm[ms][kk * 2 + i], gn[ns][kk * 4 + jn]);
    };
    f32x2 rsv, mrv;
    f32x4 cev[4];
    float tc[32];
    unsigned ep;
    auto rds = [&](int h, int r) __attribute__((always_inline)) {
      if (r == 0) mrv = *LDSP(const f32x2, al + h * 256); else if (r == 5) rsv = *LDSP(const f32x2, al + 512 + h * 256);
      else cev[r - 1] = *LDSP(const f32x4, ac + (r - 1) * 64);
    };
    auto prep = [&](int n) __attribute__((always_inline)) {
      tc[n] = llvm_fmul_legacy(mrv[n & 1], cev[n >> 3][(n >> 1) & 3]);
      asm volatile("" : "+v"(tc[n]));
    };
    auto epi = [&](int h, int n) __attribute__((always_inline)) {
      const int jn = n >> 4, r = (n >> 2) & 3, u = n & 3, e = (n >> 2) * 2;
      if (u == 0) {}
      else if (u == 1) asm volatile("v_fmac_f32 %0, %2, %4\n\tv_fmac_f32 %1, %3, %5\n\tv_cvt_pk_bf16_f32 %0, %0, %1" : "+v"(tc[e]), "+v"(tc[e + 1])
                                    : "v"(acc[h * 2][jn][r]), "v"(acc[h * 2 + 1][jn][r]), "v"(rsv[0]), "v"(rsv[1]));
      else if (u == 2) ep = __float_as_uint(tc[e]);
      else *LDSP(unsigned, (r ? ae[r - 1] + (jn & 1) * 128 + (jn >> 1) * lrw(4) * 128 : ae[3 + jn]) + m_off(h)) = ep;
      SB();
    };
    SB();
    half1(0);
    BAR_LGKM();
    SB();
    TL(14);
    SLOTS(16) {
      if (q < 8) quad(2, 3, 2, q); else quad(2, 0, 1, q - 8);
      if (q < 12) rdg(0, 14, 0, q); else rdg(1, 13, 0, q - 12);
      SB();
    }
    BAR();
    SB();
    TL(15);
    SLOTS(16) {
      mmg(0, 0, 0, q >> 2, q & 3, (q >> 2) & 1);
      if (q < 6) rds(0, q); else if (q < 14) rdg(1, 13, 0, q - 2);
      if (q >= 6) prep(q - 6);
      if (q >= 14) prep(q - 4);
      SB();
    }
    PAGE(9);
    BAR();
    SB();
    TL(16);
    {
      constexpr int ct[8] = {1, 1, 0, 0, 1, 0, 1, 0}, cj[8] = {0, 1, 0, 1, 2, 2, 3, 3}, cf[8] = {0, 1, 0, 1, 0, 1, 0, 1};
      constexpr auto en = [](int q) { return q < 15 ? 0 : q < 26 ? (q - 14) * 3 - (q == 25) : q < 27 ? 32 : q < 32 ? 32 + (q - 26) * 3 + (q == 31) : 48; };
      SLOTS(32) {
        mmg(ct[q >> 2], ct[q >> 2], 0, cj[q >> 2], q & 3, cf[q >> 2]);
        if (q < 12) rdg(0, 15, 0, q);
        else if (q < 15 || q == 17) { const int r = q < 15 ? q - 12 : 3; rdm1(2, 15, 1, r >> 1, r == 1 || r == 2); }
        else if (q == 20 || q == 23 || q == 25 || q == 26) { const int r = q == 20 ? 0 : q == 23 ? 1 : q - 23; rdm1(3, 13, 1, r >> 1, r == 1 || r == 2); }
        if (q < 15) prep(12 + q);
        if (q >= 10 && q < 15) prep(17 + q);
        SB();
#pragma unroll
        for (int n = q ? en(q - 1) : 0; n < en(q); ++n) epi(0, n);
      }
    }
    SLOTS(2) { mmg(2, 0, 1, 0, q, 0); SB(); }
    asm volatile("s_nop 1");
    SLOTS(16) { epi(0, 48 + q); if (q == 3 || q == 9) { mmg(2, 0, 1, 0, q == 3 ? 2 : 3, 0); SB(); } }
    BAR_LGKM();
    SB();
    TL(60);
    SLOTS(12) {
      mmg(2, 0, 1, 1 + (q >> 2), q & 3, ~(q >> 2) & 1);
      if (q < 6) rds(1, q); else rdg(0, 14, 1, q - 6);
      if (q >= 8) prep(q - 8);
      SB();
    }
    SLOTS(16) {
      mmg(3, 1, 1, q >> 2, q & 3, (q >> 2) & 1);
      if (q < 6) rdg(0, 14, 1, q + 6);
      if (q < 4) prep(4 + q); else { prep(2 * q); prep(2 * q + 1); }
      SB();
    }
    asm volatile("s_waitcnt lgkmcnt(0)" ::: "memory");
    if (lane == 0) asm volatile("ds_add_u32 %0, %1" :: "v"(lds_addr(smem + LDS_SEL + 12)), "v"(1) : "memory");
    SB();
    SLOTS(16) {
      mmg(0, 0, 1, q >> 2, q & 3, (q >> 2) & 1);
      if (q < 4) fn[0][q] = *(const bf16x8*)(smem + (wr * 128 + fl) * 128 + (q >> 1) * 2048 + ((q & 1) ? ko1 : ko0));
      else if (q < 8) fm[q & 1][(q - 4) >> 1] = *(const bf16x8*)(smem + G2_MRING + m_off(0) + (wc * 64 + fl) * 128 + TO((q - 4) >> 1) + ((q & 1) ? ko1 : ko0));
      SB();
      if (q >= 8) { epi(1, 3 * q - 24); epi(1, 3 * q - 23); epi(1, 3 * q - 22); }
    }
    TL(17);
    SLOTS(40) epi(1, 24 + q);
  }

  const char* pn = smem + (wr * 128 + fl) * 128;
  const char* pm = smem + G2_MRING + (wc * 64 + fl) * 128;
  f32x4 acc[8][4];
  auto mm = [&](bool imaj, int set, int q2, int s, int mode) __attribute__((always_inline)) {
    int i, jj, kk;
    if (imaj) { i = s >> 2; const int r = s & 3; jj = (r >> 1) ^ (i & 1); kk = (r == 1 || r == 2); }
    else { jj = s >> 3; const int r = s & 7; i = jj ? 3 - (r >> 1) : (r >> 1); kk = ((r + 1) >> 1) & 1; }
    if (mode == 1 && !(s & 1)) asm volatile("v_mfma_f32_16x16x32_bf16 %0, %2, %1, 0" : "=&v"(acc[q2 * 2 + jj][i]) : "v"(fm[kk][i]), "v"(fn[set][jj * 2 + kk]));
    else if (mode) asm volatile("v_mfma_f32_16x16x32_bf16 %0, %2, %1, %0" : "+v"(acc[q2 * 2 + jj][i]) : "v"(fm[kk][i]), "v"(fn[set][jj * 2 + kk]));
    else acc[q2 * 2 + jj][i] = __builtin_amdgcn_mfma_f32_16x16x32_bf16(fn[set][jj * 2 + kk], fm[kk][i], acc[q2 * 2 + jj][i], 0, 0, 0);
  };
  unsigned bn[2], bm[2], an[2], am[2];
#pragma unroll
  for (int kk = 0; kk < 2; ++kk) {
    bn[kk] = lds_addr(pn) + (kk ? ko1 : ko0); bm[kk] = lds_addr(pm) + (kk ? ko1 : ko0);
    asm volatile("" : "+v"(bn[kk]), "+v"(bm[kk]));
    an[kk] = bn[kk]; am[kk] = bm[kk] + m_off(0);
  }
#define RDN(jt, kk, o) (*LDSP(const bf16x8, an[kk] + (o) + (jt) * 2048))
#define RDM(i, kk, o) (*LDSP(const bf16x8, am[kk] + (o) + TO(i)))
  PAGE(10);
  TL(18);
  TL(19);
  tag = __builtin_amdgcn_readfirstlane(((const unsigned*)(smem + LDS_SEL))[5]);
  {
    int son = 0;
    auto tile = [&](int kt, int zero) __attribute__((always_inline)) {
      SLOTS(16) {
        mm(true, 0, 0, q, zero);
        if (!(q & 1)) {
          if (q < 8) fm[(q >> 1) & 1][2 + (q >> 2)] = RDM(2 + (q >> 2), (q >> 1) & 1, 0);
          else { const int r = (q - 8) >> 1; fn[1][r] = RDN(2 + (r >> 1), r & 1, 0); }
        }
        SB();
      }
      SLOTS(16) {
        mm(false, 1, 1, q, zero);
        if (!(q & 1) && q < 8) fn[0][q >> 1] = RDN(4 + (q >> 2), (q >> 1) & 1, 0);
        SB();
      }
      const int som = son >> 1;
      son = son == 65536 ? 0 : son + 32768;
      SLOTS(16) {
        mm(false, 0, 2, q, zero);
        if (!(q & 1) && q < 8) fn[1][q >> 1] = RDN(6 + (q >> 2), (q >> 1) & 1, 0);
        if (q == 9 || q == 11) { an[q == 11] = bn[q == 11] + son; asm volatile("" : "+v"(an[q == 11])); }
        if (q == 13 || q == 15) { am[q == 15] = bm[q == 15] + som; asm volatile("" : "+v"(am[q == 15])); }
        SB();
      }

      BAR_LGKM();

      SB();
      SLOTS(16) {
        mm(true, 1, 3, q, zero);
        if (!(q & 1)) {
          if (q < 8) fn[0][q >> 1] = RDN(q >> 2, (q >> 1) & 1, 0);
          else fm[(q >> 1) & 1][(q - 8) >> 2] = RDM((q - 8) >> 2, (q >> 1) & 1, 0);
        }
        SB();
      }
    };
    SB();
    tile(0, 1);
#pragma unroll 1
    for (int kt = 1; kt < G2_NKT - 3; ++kt) tile(kt, 0);
  }
  unsigned fx;
  int fo = lane == 0 ? kq * 512 : (int)0x80000000;
  {
    constexpr int NG = 12;
    static constexpr int g_t[NG] = {0, 0, 1, 1, 2, 2, 2, 1, 0, 0, 1, 2};
    static constexpr int g_q[NG] = {0, 1, 0, 1, 0, 1, 2, 2, 2, 3, 3, 3};
    constexpr int G14 = 1, G15 = 3, GP0 = 5, GP1 = 6;
    constexpr auto chp = [](int g) { return g == 0 || g_t[g] != g_t[g - 1]; };
    constexpr auto chn = [](int g) { return g + 1 < NG && g_t[g + 1] != g_t[g]; };
    static_assert(n_off(13) == 32768 && n_off(14) == 65536 && n_off(15) == 0, "");
    unsigned an2[2];
#pragma unroll
    for (int kk = 0; kk < 2; ++kk) {
      an[kk] = bn[kk]; an2[kk] = bn[kk] + 65536; am[kk] = bm[kk];
      asm volatile("" : "+v"(an[kk]), "+v"(an2[kk]), "+v"(am[kk]));
    }
    int vo;
    u32x4 pk;
    u32x2 kv;
    auto publish = [&](int d, int h) __attribute__((always_inline)) {
      if (h == 0) { vo = ml; PINV(vo); vo += (((d + kq + 1) & 3) * 4 + kq) * 4 * MSG; PINV(vo); }
      const f32x4 v = acc[2 * d + (h >> 2)][h & 3];
      pk[(h & 1) * 2] = pk2(v[0], v[1]); pk[(h & 1) * 2 + 1] = pk2(v[2], v[3]);
      if (h & 1) __builtin_amdgcn_raw_buffer_store_b128(pk, rs, vo + (h >> 1) * 1024, 0, 16);
    };
#define MSG_FLAG(d) __builtin_amdgcn_raw_buffer_store_b32(tag, rc, fo + (((d) + kq + 1) & 3) * 2048, 0, 16)
    auto rdn = [&](int g, int r) __attribute__((always_inline)) {
      const int t = g_t[g], jt = g_q[g] * 2 + (r >> 1);
      fn[g & 1][r] = *LDSP(const bf16x8, (t == 1 ? an2[r & 1] : an[r & 1]) + (t == 0 ? 32768 : 0) + jt * 2048);
    };
    auto rdm = [&](int t, int i, int kk) __attribute__((always_inline)) { fm[kk][i] = *LDSP(const bf16x8, am[kk] + m_off(13 + t) + TO(i)); };
    auto group = [&](auto GI) __attribute__((always_inline)) {
      constexpr int gi = decltype(GI)::value;
      if (gi == G14) { BAR();
        TL(47);
        SB(); }
      if (gi == G15) {
        PAGE(11);
        TL(53);
        BAR();
        TL(54);
        SB();
        kv = *(const u32x2*)(smem + LDS_SEL);
        SB();
      }
#pragma unroll
      for (int q = 0; q < 16; ++q) {
        const int n = gi * 16 + q;
        if (n == 112) {
          TL(55);
        }
        if (n == 152) {
          TL(58);
        }
        if (n == 168) { WAIT_VM(8); MSG_FLAG(0); SB(); }
        mm(chp(gi) || chn(gi), gi & 1, g_q[gi], q, 2);
        constexpr bool ni = gi + 1 < NG && (chp(gi + 1 < NG ? gi + 1 : gi) || chn(gi + 1 < NG ? gi + 1 : gi));
        auto nr = [](int r) { return ni && r >= 2 ? 5 - r : r; };
        if constexpr (chp(gi) && chn(gi)) {
          if (q < 4) rdm(g_t[gi], 2 + (q >> 1), q & 1); else if (q < 8) rdn(gi + 1, nr(q - 4)); else if (q < 12) rdm(g_t[gi + 1], (q - 8) >> 1, q & 1);
        } else if constexpr (chp(gi)) {
          if (q < 4) rdm(g_t[gi], 2 + (q >> 1), q & 1);
          else if (gi + 1 < NG && q < 12 && !(q & 1)) rdn(gi + 1, nr((q - 4) >> 1));
        } else if constexpr (chn(gi)) {
          if (!(q & 1)) { if (q < 8) rdn(gi + 1, nr(q >> 1)); else rdm(g_t[gi + 1], (q - 8) >> 2, (q >> 1) & 1); }
        } else {
          if (!(q & 1) && q < 8) rdn(gi + 1, nr(q >> 1));
        }
        if (gi == GP0 && q < 8) publish(0, q);
        if (gi == GP1 && q < 8) publish(1, q);
        if (gi == 9 && q < 8) publish(2, q);
        if (n == 33 || n == 37 || n == 41) xq_load((n - 33) >> 2);
        if (n == 72) { key[0] = __builtin_amdgcn_readfirstlane(kv.x); key[1] = __builtin_amdgcn_readfirstlane(kv.y); }
        SB();
      }
    };
#define GR4(i) group(IC<i>{}); group(IC<i + 1>{}); group(IC<i + 2>{}); group(IC<i + 3>{});
    GR4(0) GR4(4) GR4(8)
    asm volatile("s_nop 7");
    SB();
  }
  TL(50);
#pragma unroll
  for (int i = 0; i < 4; ++i) *(f32x4*)(smem + LDS_ACC + wr * 16384 + wc * 4096 + i * 1024 + lane * 16) = acc[7][i];
#pragma unroll
  for (int i = 0; i < 4; ++i) ya[i] = acc[6][i];
  asm volatile("s_waitcnt lgkmcnt(0)" ::: "memory");
  if (lane == 0) sig[0] = 2;
  SB();
  TL(61);
  WAIT_VM(5); MSG_FLAG(1); SB();
  TL(62);
  WAIT_VM(2); MSG_FLAG(2); fx = bld4_sc1(rc, po); SB();
  TL(63);
  wait_xq<4>(xq, sq);
  far_calc();
  cv_calc();
  asm volatile("s_waitcnt vmcnt(0)" : "+v"(fx) :: "memory");
  seen = (unsigned)__ballot(fx == tag);
  }

  PAGE(6);
  if (ha) {
    int it = 0;
    while (sig[0] != 2 && it < SPIN_MAX * 8) { __builtin_amdgcn_s_sleep(1); ++it; }
    asm volatile("" ::: "memory");
#pragma unroll
    for (int i = 0; i < 4; ++i) ya[i] = *(const f32x4*)(smem + LDS_ACC + (wid & 2) * 8192 + (wid & 1) * 4096 + i * 1024 + lane * 16);
  }
  u32x4 pp[3][2];
  unsigned e_lo = 0x00003f80u, e_hi = 0x3f800000u;
  asm volatile("" : "+v"(e_lo), "+v"(e_hi));
  __amdgpu_buffer_rsrc_t rs_ = rs, rc_ = rc;
  int mo[3] = {MSG_IDX(0) * 4 * MSG, MSG_IDX(1) * 4 * MSG, MSG_IDX(2) * 4 * MSG}, mv = ml + ha * 2048, mv2 = mv + 1024;
  asm volatile("" : "+s"(rs_), "+s"(rc_), "+s"(mo[0]), "+s"(mo[1]), "+s"(mo[2]));
  asm volatile("" : "+v"(mv), "+v"(mv2));
#define MSG_POLL(m) do { \
    if (!((seen >> (m)) & 1)) { \
      unsigned pv_, it_ = SPIN_MAX; \
      asm volatile("1:\n\tbuffer_load_dword %0, %3, %4, 0 offen sc1\n\ts_waitcnt vmcnt(0)\n\tv_cmp_eq_u32 vcc, %5, %0\n\ts_mov_b32 %1, vcc_lo\n\t" \
                   "s_bitcmp1_b32 %1, %6\n\ts_cbranch_scc1 2f\n\ts_sub_u32 %2, %2, 1\n\ts_cbranch_scc1 2f\n\ts_sleep 1\n\ts_branch 1b\n2:" \
                   : "=&v"(pv_), "+s"(seen), "+s"(it_) : "v"(po), "s"(rc_), "s"(tag), "i"(m) : "vcc", "scc", "memory"); \
    } \
    pp[m][0] = __builtin_amdgcn_raw_buffer_load_b128(rs_, mv, mo[m], 16); \
    pp[m][1] = __builtin_amdgcn_raw_buffer_load_b128(rs_, mv2, mo[m], 16); } while (0)
#define MSG_ADD(m) do { \
    _Pragma("unroll") for (int u = 0; u < 8; ++u) { \
      ya[u >> 1][(u & 1) * 2] = add_bf(pp[m][u >> 2][u & 3], e_lo, ya[u >> 1][(u & 1) * 2]); ya[u >> 1][(u & 1) * 2 + 1] = add_bf(pp[m][u >> 2][u & 3], e_hi, ya[u >> 1][(u & 1) * 2 + 1]); } } while (0)
  MSG_POLL(2); MSG_POLL(1); MSG_POLL(0);
  SB();
  if (__builtin_expect(far0 == 0 || far1 == 0, 0)) {
    asm volatile("s_branch 2f\nk_pg12:\n\ts_setpc_b64 s[42:43]\n2:");
#pragma unroll 1
    for (int c = 0; c < 2; ++c) {
      if (c ? far1 : far0) continue;
      const int ic = (0xffff - (c ? key[1] : key[0])) & (S_ - 1);
      lds_vi cnt = (lds_vi)(smem + LDS_SEL + 24 + c * 4);
      int it = 0;
      while (cnt[0] != 4 && it < SPIN_MAX * 8) { __builtin_amdgcn_s_sleep(1); ++it; }
      asm volatile("" ::: "memory");
      const float* q = (const float*)(smem + LDS_FIN + c * 2048) + (wid & 1) * 64 + (fr >> 3) * 32 + col4(fr);
      const f32x4 v = (*(const f32x4*)q + *(const f32x4*)(q + 128)) + (*(const f32x4*)(q + 256) + *(const f32x4*)(q + 384));
#pragma unroll
      for (int r = 0; r < 4; ++r)
#pragma unroll
        for (int k = 0; k < 4; ++k) {
          cv[r * 4 + k] += (xo[r] >> 11) == ic ? v[k] : 0.f;
        }
    }
  }
#pragma unroll
  for (int e = 0; e < 16; ++e) ya[e & 3][e >> 2] += cv[e];
  asm volatile("" : "+v"(ya[0]), "+v"(ya[1]), "+v"(ya[2]), "+v"(ya[3]));
  SB();
  MSG_ADD(2); MSG_ADD(1);
  asm volatile("" : "+v"(ya[0]), "+v"(ya[1]), "+v"(ya[2]), "+v"(ya[3]));
  SB();
  MSG_ADD(0);
  {
#pragma unroll
    for (int r = 0; r < 4; ++r) *(u32x2*)((char*)out + (unsigned)xo[r]) = (u32x2){pk2(ya[0][r], ya[1][r]), pk2(ya[2][r], ya[3][r])};
  }

  if (wid == 0) {
    const unsigned te = (unsigned)__builtin_amdgcn_s_memrealtime();
    const unsigned t0 = __builtin_amdgcn_readlane(tlv, 52);
    const __amdgpu_buffer_rsrc_t rg = mk_rsrc(wsp + (64u << 20));
    if (lane == 0) { __builtin_amdgcn_raw_buffer_store_b64((u32x2){tag, t0}, rg, g * 32, 0, 16); __builtin_amdgcn_raw_buffer_store_b64((u32x2){tag, (unsigned)__builtin_amdgcn_readlane(tlv, 52)}, rg, g * 32 + 8, 0, 16);
      __builtin_amdgcn_raw_buffer_store_b64((u32x2){tag, (unsigned)__builtin_amdgcn_readlane(tlv, 52)}, rg, g * 32 + 16, 0, 16); }
    if (g == 37) {
      int mn0, mx[2], sum[2], sk[4];
      for (int it = 0; it < 100000; ++it) {
        unsigned bad = 0; mn0 = 0x7fffffff; mx[0] = mx[1] = -0x7fffffff; sum[0] = sum[1] = 0;
#pragma unroll
        for (int k = 0; k < 4; ++k) {
          const u32x4 q = __builtin_amdgcn_raw_buffer_load_b128(rg, (k * 64 + lane) * 32, 0, 16);
          sk[k] = (int)(q.w - t0);
          const u32x2 q2 = __builtin_amdgcn_raw_buffer_load_b64(rg, (k * 64 + lane) * 32 + 16, 0, 16);
          bad |= (q.x ^ tag) | (q.z ^ tag) | (q2.x ^ tag);
          mn0 = min(mn0, (int)(q.y - t0)); mx[0] = max(mx[0], (int)(q.w - t0)); sum[0] += (int)(q.w - t0); mx[1] = max(mx[1], (int)(q2.y - t0)); sum[1] += (int)(q2.y - t0);
        }
        if (__all(bad == 0)) break;
        asm volatile("" ::: "memory");
      }
      for (int o = 32; o; o >>= 1) { mn0 = min(mn0, __shfl_xor(mn0, o, 64)); for (int k = 0; k < 2; ++k) { mx[k] = max(mx[k], __shfl_xor(mx[k], o, 64)); sum[k] += __shfl_xor(sum[k], o, 64); } }
      int code = 0;
      { int d = mx[0] - mn0; d = (d - 0) / 60; code = code * 2 + min(max(d, 0), 1); }
      { int bits = 0;
#pragma unroll
        for (int k = 0; k < 4; ++k) { int v = sk[k]; for (int o = 16; o; o >>= 1) v += __shfl_xor(v, o, 64);
          const u64 b = __ballot(v / 32 - mn0 > THR); bits |= ((int)(b & 1) | (int)((b >> 32) & 1) << 1) << (2 * k); }
        code = code * 256 + bits; }
      const unsigned tend = __builtin_amdgcn_readfirstlane(t0 + mn0 + 4000 + code * 400);
      for (int it = 0; it < (1 << 17) && (int)((unsigned)__builtin_amdgcn_s_memrealtime() - tend) < 0; ++it) __builtin_amdgcn_s_sleep(1);
    }
  }

}

extern "C" void kernel_launch(void* const* d_in, const int* in_sizes, int n_in, void* d_out, int out_size,
                              void* d_ws, size_t ws_size, hipStream_t stream) {
  const u16* x = (const u16*)d_in[0];
  const u16* ws_re = (const u16*)d_in[1];
  const u16* ws_im = (const u16*)d_in[2];
  const u16* wd_re = (const u16*)d_in[3];
  const u16* wd_im = (const u16*)d_in[4];
  const u16* w_r = (const u16*)d_in[6];
  const u16* w_gate = (const u16*)d_in[7];
  const u16* w_up = (const u16*)d_in[8];
  const u16* w_down = (const u16*)d_in[9];
  k_main<<<256, 512, 0, stream>>>(x, wd_re, wd_im, w_r, (char*)d_ws, ws_re, ws_im, w_gate, w_up, w_down, (u16*)d_out);
}
```

```cpp
#include <hip/hip_runtime.h>
#include <hip/hip_bf16.h>
#include <stdint.h>
#include <type_traits>

extern "C" __device__ unsigned long long llvm_dispatch_id(void) __asm("llvm.amdgcn.dispatch.id");
extern "C" __device__ float llvm_fmul_legacy(float, float) __asm("llvm.amdgcn.fmul.legacy");
typedef unsigned short u16;
typedef unsigned long long u64;
typedef __attribute__((ext_vector_type(8))) short bf16x8;
typedef __attribute__((ext_vector_type(4))) unsigned u32x4;
typedef __attribute__((ext_vector_type(2))) unsigned u32x2;
typedef __attribute__((ext_vector_type(4))) float f32x4;
typedef __attribute__((ext_vector_type(2))) float f32x2;
typedef __attribute__((ext_vector_type(2))) __bf16 bf16x2;
typedef __attribute__((address_space(3))) void* lds_vp;
typedef volatile __attribute__((address_space(3))) int* lds_vi;

constexpr int S_ = 2048, D_ = 1024, F_ = 3584;
template <int N> using IC = std::integral_constant<int, N>;

static __device__ __forceinline__ float bf2f(u16 u) { return __uint_as_float((unsigned)u << 16); }
static __device__ __forceinline__ u16 f2bf(float f) {
  __hip_bfloat16 b = __float2bfloat16(f);
  return *reinterpret_cast<u16*>(&b);
}
static __device__ __forceinline__ float rbf(float f) { return bf2f(f2bf(f)); }
static __device__ __forceinline__ float lo_bf(unsigned u) { return __uint_as_float(u << 16); }
static __device__ __forceinline__ float hi_bf(unsigned u) { return __uint_as_float(u & 0xffff0000u); }
static __device__ __forceinline__ unsigned pk2(float a, float b) {
  const bf16x2 v = __builtin_convertvector((f32x2){a, b}, bf16x2);
  return __builtin_bit_cast(unsigned, v);
}
static __device__ __forceinline__ float add_bf(unsigned u, unsigned e, float c) {
  return __builtin_amdgcn_fdot2_f32_bf16(__builtin_bit_cast(bf16x2, u), __builtin_bit_cast(bf16x2, e), c, false);
}
static __device__ __forceinline__ float ln_elem(float x, float mb, float sd, float ri) {
  const float d = rbf(x - mb);
  const float q0 = d * ri;
  return rbf(__builtin_fmaf(__builtin_fmaf(-q0, sd, d), ri, q0));
}
template <int CTRL> static __device__ __forceinline__ float dpp_f(float v) {
  return __int_as_float(__builtin_amdgcn_update_dpp(0, __float_as_int(v), CTRL, 0xf, 0xf, true));
}
template <int CTRL> static __device__ __forceinline__ unsigned dpp_u(unsigned v) {
  return (unsigned)__builtin_amdgcn_update_dpp(0, (int)v, CTRL, 0xf, 0xf, true);
}
static __device__ __forceinline__ float row_sum(float v) {
  v += dpp_f<0xB1>(v);
  v += dpp_f<0x4E>(v);
  v += dpp_f<0x141>(v);
  v += dpp_f<0x140>(v);
  return v;
}
static __device__ __forceinline__ float fold32(float a, float b) {
  const auto r = __builtin_amdgcn_permlane32_swap(__float_as_uint(a), __float_as_uint(b), false, false);
  return __uint_as_float(r[0]) + __uint_as_float(r[1]);
}
static __device__ __forceinline__ float fold16(float a, float b) {
  const auto r = __builtin_amdgcn_permlane16_swap(__float_as_uint(a), __float_as_uint(b), false, false);
  return __uint_as_float(r[0]) + __uint_as_float(r[1]);
}
static __device__ __forceinline__ float rdl(float v, int l) { return __int_as_float(__builtin_amdgcn_readlane(__float_as_int(v), l)); }

#define ST_JB 9
#define LNS 2
#define WSN 11
#define FSM 0xa2
#define GV_JB 7
#define DG 11
#define AB_STRIDE 32
#define SPIN_MAX (1 << 17)
#define VA0 5
#define VAN 4
#define SP0 3
#define SP1 7
#define SP1E 11
#define SPS 0x000cccccdddd00
#define DVS 0x00000400300000
#define GVS 0x00000022000000
constexpr int dig(unsigned long long t, int q) { return q < 0 ? 0 : (int)((t >> (4 * q)) & 15); }
constexpr int acc_dig(unsigned long long t, int q) { int a = 0; for (int i = 0; i < q; ++i) a += dig(t, i); return a; }

static __device__ __forceinline__ unsigned lds_addr(const void* p) { return (unsigned)(size_t)(lds_vp)p; }

constexpr int MSG = 4096;
constexpr size_t WS_ABT   = 0;
constexpr size_t WS_SLAB  = WS_ABT + (size_t)D_ * 4096 * 2;
constexpr size_t WS_GU    = WS_SLAB + (size_t)64 * 64 * MSG;
constexpr size_t WS_DN    = WS_GU + (size_t)8 * F_ * 16;
constexpr size_t WS_FLAGS = WS_DN + (size_t)2 * 8 * 32 * 64 * 8;
constexpr int FL_X = 0;
constexpr int FL_AB = 64 * 64 * 32;
constexpr int FL_ST = FL_AB + 2048 * AB_STRIDE;
constexpr int FL_CE = FL_ST + 4096;
constexpr int FL_KEY = FL_CE + 4096;
constexpr int FL_EPOCH = FL_KEY + 4096;
constexpr int WS_MB = (int)((WS_FLAGS + (FL_EPOCH + 1) * 4 + 1048575) >> 20);

#define WAIT_VM(n) asm volatile("s_waitcnt vmcnt(" #n ")" ::: "memory")
template <int N> static __device__ __forceinline__ void wait_vm() { asm volatile("s_waitcnt vmcnt(%0)" :: "i"(N) : "memory"); }
#define BAR_LGKM() asm volatile("s_waitcnt lgkmcnt(0)\n\ts_barrier" ::: "memory")
#define BAR() asm volatile("s_barrier" ::: "memory")
#define SB() __builtin_amdgcn_sched_barrier(0)
#define PAGE(n) asm volatile("s_branch 2f\n\t.p2align 12\nk_pg" #n ":\n\ts_setpc_b64 s[42:43]\n2:")
#define CALL_PG(n) asm volatile("s_getpc_b64 s[40:41]\n3:\n\ts_mov_b32 s42, k_pg" #n "-3b\n\ts_ashr_i32 s43, s42, 31\n\ts_add_u32 s40, s40, s42\n\ts_addc_u32 s41, s41, s43\n\t" \
                                "s_swappc_b64 s[42:43], s[40:41]" ::: "s40", "s41", "s42", "s43", "scc", "memory")

static __device__ __forceinline__ __amdgpu_buffer_rsrc_t mk_rsrc(const void* p) {
  return __builtin_amdgcn_make_buffer_rsrc((void*)p, 0, 0x7fffffff, 0x00020000);
}
#define DMA_(rsrc, voff, mbase, MI, sbase, SI, pol) do { unsigned _t; \
    asm volatile("s_add_i32 m0, %2, %3\n\ts_add_i32 %0, %5, %6\n\tbuffer_load_dwordx4 %1, %4, %0 offen" pol " lds" \
                 : "=&s"(_t) : "v"(voff), "s"(mbase), "i"(MI), "s"(rsrc), "s"(sbase), "i"(SI) : "memory", "scc"); } while (0)
#define DMA_I(rsrc, voff, mbase, MI, SI) \
    asm volatile("s_add_i32 m0, %1, %2\n\ts_nop 0\n\tbuffer_load_dwordx4 %0, %3, 0 offen offset:%4 lds" \
                 :: "v"(voff), "s"(mbase), "i"((MI) - (SI)), "s"(rsrc), "i"(SI) : "memory", "scc")
#define DMA_S(rsrc, voff, mbase, MI, soff) \
    asm volatile("s_add_i32 m0, %1, %2\n\ts_nop 0\n\tbuffer_load_dwordx4 %0, %3, %4 offen lds" \
                 :: "v"(voff), "s"(mbase), "i"(MI), "s"(rsrc), "s"(soff) : "memory", "scc")
#define TOUCH(rsrc, voff, mbase, MI, soff) \
    asm volatile("s_add_i32 m0, %1, %2\n\ts_nop 0\n\tbuffer_load_dword %0, %3, %4 offen lds" \
                 :: "v"(voff), "s"(mbase), "i"(MI), "s"(rsrc), "s"(soff) : "memory", "scc")
template <int OFF> static __device__ __forceinline__ u32x4 ld16_s(const void* p, int voff) {
  u32x4 v;
  asm volatile("global_load_dwordx4 %0, %1, %2 offset:%3" : "=v"(v) : "v"(voff), "s"(p), "i"(OFF) : "memory");
  return v;
}
template <int OFF> static __device__ __forceinline__ void sld(unsigned& sink, const void* p, int soff) {
  asm volatile("s_load_dword %0, %1, %2 offset:%3" : "+s"(sink) : "s"(p), "s"(soff), "i"(OFF) : "memory");
}
static __device__ __forceinline__ unsigned gld4_s(const void* p, unsigned voff) {
  unsigned v;
  asm volatile("global_load_dword %0, %1, %2" : "=v"(v) : "v"(voff), "s"(p) : "memory");
  return v;
}
static __device__ __forceinline__ u32x2 bld8_sc1(__amdgpu_buffer_rsrc_t r, unsigned voff) {
  u32x2 v;
  asm volatile("buffer_load_dwordx2 %0, %1, %2, 0 offen sc1" : "=v"(v) : "v"(voff), "s"(r) : "memory");
  return v;
}
template <int N> static __device__ __forceinline__ void wait_xs(unsigned& a, u32x2& b) { asm volatile("s_waitcnt vmcnt(%2)" : "+v"(a), "+v"(b) : "i"(N) : "memory"); }
template <int N> static __device__ __forceinline__ void wait_xq(u32x4 (&a)[2], u32x2& b) { asm volatile("s_waitcnt vmcnt(%3)" : "+v"(a[0]), "+v"(a[1]), "+v"(b) : "i"(N) : "memory"); }
static __device__ __forceinline__ u32x2 ld8_s_sc1(const void* p, int voff) {
  u32x2 v;
  asm volatile("global_load_dwordx2 %0, %1, %2 sc1" : "=v"(v) : "v"(voff), "s"(p) : "memory");
  return v;
}
static __device__ __forceinline__ void lgkm0(unsigned& sink) { asm volatile("s_waitcnt lgkmcnt(0)" : "+s"(sink) :: "memory"); }
template <typename T> static __device__ __forceinline__ void pin4(T& a, T& b, T& c, T& d) { asm volatile("" : "+s"(a), "+s"(b), "+s"(c), "+s"(d)); }
template <typename T, typename U> static __device__ __forceinline__ void pins(T& a, U& b) { asm volatile("" : "+s"(a), "+s"(b)); }
static __device__ __forceinline__ void launder(int& v, int& a, int& b) { asm volatile("" : "+v"(v)); asm volatile("" : "+s"(a), "+s"(b)); }
static __device__ __forceinline__ unsigned bld4_sc1(__amdgpu_buffer_rsrc_t r, int voff) {
  unsigned v;
  asm volatile("buffer_load_dword %0, %1, %2, 0 offen sc1" : "=v"(v) : "v"(voff), "s"(r) : "memory");
  return v;
}
static __device__ __forceinline__ u32x4 bld16_nt(__amdgpu_buffer_rsrc_t r, int voff, int soff) {
  u32x4 v;
  asm volatile("buffer_load_dwordx4 %0, %1, %2, %3 offen nt" : "=v"(v) : "v"(voff), "s"(r), "s"(soff) : "memory");
  return v;
}
static __device__ __forceinline__ void bld16_nt_io(u32x4& v, __amdgpu_buffer_rsrc_t r, int voff, int soff) {
  asm volatile("buffer_load_dwordx4 %0, %1, %2, %3 offen nt" : "+v"(v) : "v"(voff), "s"(r), "s"(soff) : "memory");
}
static __device__ __forceinline__ u32x4 bld16_sc1(__amdgpu_buffer_rsrc_t r, int voff, int soff) {
  u32x4 v;
  asm volatile("buffer_load_dwordx4 %0, %1, %2, %3 offen sc1" : "=v"(v) : "v"(voff), "s"(r), "s"(soff) : "memory");
  return v;
}
static __device__ __forceinline__ u32x2 granule_wait(const u64* p, unsigned tag) {
  u64 v = 0;
#pragma unroll 1
  for (int it = 0; it < SPIN_MAX; ++it) {
    v = __hip_atomic_load(p, __ATOMIC_RELAXED, __HIP_MEMORY_SCOPE_AGENT);
    if (__all((unsigned)(v >> 32) == tag)) break;
    __builtin_amdgcn_s_sleep(2);
  }
  return (u32x2){(unsigned)v, (unsigned)(v >> 32)};
}
static __device__ __forceinline__ float key_score(unsigned key) {
  const unsigned u = key >> 16;
  return bf2f((u16)((u & 0x8000u) ? (u & 0x7fffu) : (~u & 0xffffu)));
}
static __device__ __forceinline__ unsigned med3u(unsigned a, unsigned b, unsigned c) { return max(min(a, b), min(max(a, b), c)); }
static __device__ __forceinline__ void merge2(unsigned& a1, unsigned& a2, unsigned b1, unsigned b2) {
  const unsigned lo = min(a1, b1);
  a1 = max(a1, b1);
  a2 = max(lo, max(a2, b2));
}
static __device__ __forceinline__ float sqdev16(const u32x4 (&v)[2], float mb) {
  f32x2 q2 = {0.f, 0.f};
#pragma unroll
  for (int i = 0; i < 8; ++i) {
    const f32x2 e = (f32x2){lo_bf(v[i >> 2][i & 3]), hi_bf(v[i >> 2][i & 3])} - mb;
    q2 += e * e;
  }
  return q2.x + q2.y;
}

constexpr int lrw(int r) { return (r & ~6) | ((r & 2) << 1) | ((r & 4) >> 1); }
constexpr int swz(int r) { return ((r >> 1) & 4) | (r & 2); }
static __device__ __forceinline__ int sym_dn(int p) { return (p ^ (p >> 1)) & 1; }
static __device__ __forceinline__ int sym_hi(int n, int w, int st, int b, int z, int p) {
  return ((p >> 1) * (n / 2 * st) + b * (w * st)) ^ (-sym_dn(p) & (((n - 1) ^ (z * (n / 2))) * st));
}
static __device__ __forceinline__ int sym_lo(int w, int st, int p, int c) { return ((c - sym_dn(p)) * st) & ((w - 1) * st); }
static __device__ __forceinline__ int sym_row(int n, int w, int st, int b, int z, int p, int c) { return sym_lo(w, st, p, c) ^ sym_hi(n, w, st, b, z, p); }
constexpr int G1_NST = 4, G1_NKT = 16, G1_STAGE = 32768;
constexpr int G2_NKT = 16, G2_MRING = 98304;
constexpr int n_off(int k) { return (k % 3) * 32768; }
constexpr int m_off(int k) { return ((k + 2) % 3) * 16384; }
constexpr int k_ord(int k) { return k < 2 ? k : (k < 9 ? 2 * (k - 1) : 2 * (k - 8) + 1); }
constexpr int LDS_DUMMY = G2_MRING + 49152;
constexpr int LDS_GU = LDS_DUMMY + 4096;
constexpr int LDS_FIN = LDS_GU + 4096;
constexpr int LDS_MB = LDS_FIN + 4096;
constexpr int LDS_SEL = LDS_MB + 1536;
constexpr int LDS_SIG = LDS_SEL + 64;
constexpr int LDS_KEY = LDS_SIG + 32;
constexpr int LDS_BYTES = LDS_KEY + 128;
constexpr int LDS_ACC = n_off(G2_NKT - 3);

constexpr int sp_tile(int q) { return q < VA0 ? q : q + VAN < 16 ? q + VAN : -1; }
constexpr int dn_cnt(int q) { return dig(DVS, q); }
constexpr int dn_first(int q) { return acc_dig(DVS, q); }

static __device__ __forceinline__ void code_touch(unsigned& sink, unsigned off) {
  asm volatile("s_getpc_b64 s[40:41]\n\tk_main_rt:\n\ts_min_u32 %1, %1, k_main_end-k_main_rt-196\n\t"
               "s_load_dword %0, s[40:41], %1 offset:0\n\ts_load_dword %0, s[40:41], %1 offset:64\n\ts_load_dword %0, s[40:41], %1 offset:128\n\ts_load_dword %0, s[40:41], %1 offset:192"
               : "+s"(sink), "+s"(off) :: "s40", "s41", "scc", "memory");
}
static __device__ __forceinline__ void touch1(__amdgpu_buffer_rsrc_t r, int voff, unsigned mb, int soff) { TOUCH(r, voff, mb, LDS_DUMMY, soff); }
typedef const __attribute__((address_space(1))) u16* gptr;
typedef __attribute__((ext_vector_type(8))) u64 u64x8;
typedef __attribute__((ext_vector_type(2))) u64 u64x2;
extern "C" __global__ __launch_bounds__(512, 2) void k_main(const u16* x, const u16* wd_re, const u16* wd_im, const u16* w_r,
                                                 char* wsp, const u16* ws_re, const u16* ws_im,
                                                 const u16* w_gate, const u16* w_up, const u16* w_down, u16* out) {
  __shared__ __attribute__((aligned(16))) char smem[LDS_BYTES];
  u64x8 ka;
  u64x2 kb_;
  u64 kc;
  asm volatile("s_load_dwordx16 %0, %3, 0\n\ts_load_dwordx4 %1, %3, 64\n\ts_load_dwordx2 %2, %3, 80"
               : "=&s"(ka), "=&s"(kb_), "=&s"(kc) : "s"(__builtin_amdgcn_kernarg_segment_ptr()) : "memory");
  const int tid = threadIdx.x, lane = tid & 63;
  const int wid = __builtin_amdgcn_readfirstlane(tid >> 6);
  const int g = blockIdx.x, xcd = ((~g & 1) << 2) | ((g >> 1) & 3), j = g >> 3;
  if (wid < 4) {
    unsigned cw, co = (j * 4 + wid) * 448;
    asm volatile(".subsection 1\nk_main_end:\n.subsection 0\n\ts_min_u32 %1, %1, k_main_end-k_main-480\n\ts_getpc_b64 s[40:41]\n\ts_add_u32 s40, s40, %1\n\ts_addc_u32 s41, s41, 0\n\t"
                 "s_load_dword %0, s[40:41], 0\n\ts_load_dword %0, s[40:41], 64\n\ts_load_dword %0, s[40:41], 128\n\t"
                 "s_load_dword %0, s[40:41], 192\n\ts_load_dword %0, s[40:41], 256\n\ts_load_dword %0, s[40:41], 320\n\ts_load_dword %0, s[40:41], 384\n\ts_waitcnt lgkmcnt(0)"
                 : "=&s"(cw), "+s"(co) :: "s40", "s41", "scc", "memory");
  }
  const int pw = wid - 4;
  const int row0 = (wid & 3) * 8 + lrw(lane >> 3);
  const int cm = ((lane & 7) ^ swz(row0)) * 16;
  int vm = ((row0 & 15) * 2 + (row0 >> 4)) * (D_ * 2) + cm;
  asm volatile("" : "+v"(vm));
  const int kq = xcd >> 1;
  const int tt = (xcd & 1) * 4 + (j >> 3);
  const int et = j & 7;
  const int z = kq >> 1, st = (kq & 1) * 8 + tt;
  const int lrow = (kq & 1) * 1024 + (xcd & 1) * 512 + z * 256 + j * 8 + pw * 2;
  const int drow = ((kq & 1) * 2 + (xcd & 1)) * 256 + j * 8 + pw * 2;
  unsigned o_m = st * 128 * D_ * 2, o_l = lrow * D_ * 2, o_d = drow * D_ * 2, mb0 = lds_addr(smem) + pw * 1024;
  int l16 = lane * 16;
  int vn[4];
#pragma unroll
  for (int q = 0; q < 4; ++q) vn[q] = sym_lo(32, D_ * 2, (row0 >> 2) & 3, q * 8 + (row0 >> 4) * 4 + (row0 & 3)) ^ (sym_hi(D_, 32, D_ * 2, et, z, (row0 >> 2) & 3) | cm);
  asm volatile("" : "+s"(o_m), "+s"(o_l), "+s"(o_d), "+s"(mb0));
  asm volatile("" : "+v"(l16), "+v"(vn[0]), "+v"(vn[1]), "+v"(vn[2]), "+v"(vn[3]));
  asm volatile("s_waitcnt lgkmcnt(0)" : "+s"(ka), "+s"(kb_), "+s"(kc) :: "memory");
  x = (const u16*)(gptr)ka[0]; wd_re = (const u16*)(gptr)ka[1]; wd_im = (const u16*)(gptr)ka[2]; w_r = (const u16*)(gptr)ka[3];
  wsp = (char*)(gptr)ka[4]; ws_re = (const u16*)(gptr)ka[5]; ws_im = (const u16*)(gptr)ka[6]; w_gate = (const u16*)(gptr)ka[7];
  w_up = (const u16*)(gptr)kb_[0]; w_down = (const u16*)(gptr)kb_[1]; out = (u16*)(gptr)kc;
  int* flags = (int*)(wsp + WS_FLAGS);
  u64* stat = (u64*)(flags + FL_ST);
  const u16* wdz = z ? wd_im : wd_re;
  const __amdgpu_buffer_rsrc_t rm = mk_rsrc(x);
  const __amdgpu_buffer_rsrc_t rn = mk_rsrc(wdz);
#define G1_X(kt, so, q) DMA_(rm, vm, mb0, (so) + (q) * 4096, o_m, (kt) * 128 + (((q) & 1) * 64 + ((q) >> 1) * 32) * D_ * 2, "")
#define G1_W(kt, so, q) DMA_I(rn, vn[q], mb0, (so) + 16384 + (q) * 4096, (kt) * 128)
#define G1_STAGE_TILE(kt, so, W) do { SB(); G1_X(kt, so, 0); W(0); G1_X(kt, so, 1); W(1); G1_X(kt, so, 2); W(2); G1_X(kt, so, 3); W(3); \
    G1_W(kt, so, 0); W(4); G1_W(kt, so, 1); W(5); G1_W(kt, so, 2); W(6); G1_W(kt, so, 3); W(7); SB(); } while (0)
#define G1_STAGE_A(kt, so, W) do { SB(); G1_X(kt, so, 0); W(0); G1_X(kt, so, 2); W(1); \
    G1_W(kt, so, 0); W(2); G1_W(kt, so, 1); W(3); G1_W(kt, so, 2); W(4); G1_W(kt, so, 3); W(5); W(6); W(7); SB(); } while (0)
#define G1_STAGE_B(kt, so) do { SB(); G1_X(kt, so, 1); G1_X(kt, so, 3); SB(); } while (0)
#define NOW(i)
  const int fr = lane & 15, fq = lane >> 4;
  const int ha = wid >> 2;
  int trow, xo[4], xa[3], po, ml;
  const char *prs, *prc;
  auto col4 = [](int f) __attribute__((always_inline)) { return ((f & 2) << 3 | (f & 4) << 1 | (f & 1) << 2) ^ (-((f >> 3) & 1) & 28); };
  auto seg0 = [&](int hf, int dn) __attribute__((always_inline)) { return (hf * 512 + et * 32) ^ (-dn & (D_ - 32)); };
  auto epi_setup = [&](int part) __attribute__((always_inline)) {
    int t_ = tt, l_ = lane;
    asm volatile("" : "+s"(t_));
    asm volatile("" : "+v"(l_));
    const int ts = ((wid >> 1) & 1) * 8 + kq * 2 + ha;
    if (part == 0) { xo[3] = (seg0(wid & 1, (l_ >> 3) & 1) + col4(l_)) * 2; asm volatile("" : "+v"(xo[3])); }
    if (part == 1) {
      xo[3] |= sym_hi(S_, 64, D_ * 2, t_, z, l_ >> 4);
      ml = (l_ * 16) ^ (-sym_dn(l_ >> 4) & (z * 768));
      asm volatile("" : "+v"(xo[3]), "+v"(ml));
    }
    if (part == 2) {
      const int xh = xo[3];
#pragma unroll
      for (int r = 0; r < 4; ++r) xo[r] = sym_lo(64, D_ * 2, l_ >> 4, ts * 4 + r) ^ xh;
      const bool hi = xo[0] & 8;
      xa[0] = (hi ? xo[2] : xo[0]) & ~15; xa[1] = (hi ? xo[3] : xo[1]) & ~15;
      xa[2] = (sym_lo(64, D_ * 2, l_ >> 4, ts * 4 + (l_ & 3)) ^ xh) >> 11 << 3;
      asm volatile("" : "+v"(xo[0]), "+v"(xo[1]), "+v"(xo[2]), "+v"(xo[3]), "+v"(xa[0]), "+v"(xa[1]), "+v"(xa[2]));
    }
    if (part == 3) {
      trow = t_ * 16 + ts;
      prs = wsp + WS_SLAB + (size_t)((t_ * 8 + et) * 64 + (wid & 3)) * MSG;
      prc = (const char*)(flags + FL_X + ((t_ * 8 + et) * 64 + (wid & 3)) * 32);
      asm volatile("" : "+s"(trow), "+s"(prs), "+s"(prc));
      po = l_ < 3 ? (kq * 4 + ((kq + 1 + l_) & 3)) * 512 : (int)0x80000000;
      asm volatile("" : "+v"(po));
    }
  };
  auto tok_tile = [](unsigned key) __attribute__((always_inline)) {
    const unsigned h = (0xffff - key) & 1023, m = -(h >> 9);
    return (((h ^ m) & 0x1c0) | (((h ^ m) - m) & 63)) >> 2;
  };
#define rs mk_rsrc(prs)
#define rc mk_rsrc(prc)
  lds_vi sig = (lds_vi)(smem + LDS_SIG + (wid & 3) * 4);
  unsigned tag;
  u32x4 xq[2];
  u32x2 sq;
  f32x4 ya[4];
  unsigned key[2];
  float cv[16];
  unsigned far0, far1;
  auto xq_load = [&](int n) __attribute__((always_inline)) {
    if (n < 2) xq[n] = ld16_s<0>(x, xa[n]); else sq = ld8_s_sc1(stat, xa[2]);
  };
#define PINV(a) asm volatile("" : "+v"(a))
  auto far_calc = [&]() __attribute__((always_inline)) {
    far0 = tok_tile(key[0]) ^ trow; far1 = tok_tile(key[1]) ^ trow;
    asm volatile("" : "+s"(far0), "+s"(far1));
  };
  auto cv_calc = [&]() __attribute__((always_inline)) {
    if (__builtin_expect(!__all(sq.y == tag), 0)) sq = granule_wait(stat + (xa[2] >> 3), tag);
    const bool hi = xo[0] & 8;
    const float mb_ = lo_bf(sq.x), ri_ = __builtin_amdgcn_rcpf(hi_bf(sq.x));
#pragma unroll
    for (int r = 0; r < 4; ++r) {
      const float mb = r == 0 ? dpp_f<0x00>(mb_) : r == 1 ? dpp_f<0x55>(mb_) : r == 2 ? dpp_f<0xAA>(mb_) : dpp_f<0xFF>(mb_);
      const float ri = r == 0 ? dpp_f<0x00>(ri_) : r == 1 ? dpp_f<0x55>(ri_) : r == 2 ? dpp_f<0xAA>(ri_) : dpp_f<0xFF>(ri_);
#pragma unroll
      for (int u = 0; u < 2; ++u) {
        const unsigned own = xq[r & 1][(r & 2) + u], oth = dpp_u<0xB1>(xq[r & 1][2 - (r & 2) + u]);
        const unsigned w = hi == (r >= 2) ? own : oth;
        cv[r * 4 + 2 * u] = __builtin_fmaf(lo_bf(w) - mb, ri, lo_bf(w));
        cv[r * 4 + 2 * u + 1] = __builtin_fmaf(hi_bf(w) - mb, ri, hi_bf(w));
        PINV(cv[r * 4 + 2 * u]); PINV(cv[r * 4 + 2 * u + 1]);
      }
    }
  };
#define MSG_IDX(m) (kq * 4 + ((kq + 1 + (m)) & 3))
  unsigned seen = 0;

  if (__builtin_expect(wid >= 4, 1)) {
    __builtin_amdgcn_s_setprio(3);
    G1_STAGE_TILE(0, 0, NOW);
    constexpr int O_ST = (FL_ST - FL_AB) * 4, O_CE = (FL_CE - FL_AB) * 4, O_KEY = (FL_KEY - FL_AB) * 4, O_EP = (FL_EPOCH - FL_AB) * 4;
    const __amdgpu_buffer_rsrc_t rf = mk_rsrc(wsp + WS_FLAGS + FL_AB * 4);
    u32x4 lv[2], lv2[2], lw[2], ld[2], ld2[2];
    unsigned epv;
    {
      const char* xrow = (const char*)x + o_l;
      const char* wdrow = (const char*)wdz + o_d;
      lv[0] = ld16_s<0>(xrow, l16); lv[1] = ld16_s<1024>(xrow, l16);
      lv2[0] = ld16_s<2048>(xrow, l16); lv2[1] = ld16_s<3072>(xrow, l16);
      ld[0] = ld16_s<0>(wdrow, l16); ld[1] = ld16_s<1024>(wdrow, l16);
      ld2[0] = ld16_s<2048>(wdrow, l16); ld2[1] = ld16_s<3072>(wdrow, l16);
      lw[0] = ld16_s<0>(w_r, l16); lw[1] = ld16_s<1024>(w_r, l16);
      asm volatile("buffer_load_dword %0, off, %1, %2 sc1" : "=v"(epv) : "s"(rf), "s"(O_EP) : "memory");
    }
    SB();
    const unsigned long long did = llvm_dispatch_id();
    const unsigned dmix = (unsigned)did * 0x85EBCA6Bu + (unsigned)(did >> 32) * 0xC2B2AE35u + 0x27D4EB2Fu;
    const bool moe = et != 7;
    unsigned epoch;
    const int ci = lane & 15, rq = lane >> 4, tk = (lane >> 3) & 1;
    int mat, cg, dq, d0;
    u32x4 wv[16];
    const u16 *pgu, *pn2;
    const char *pm2, *spb;
#define rw __builtin_amdgcn_make_buffer_rsrc((void*)pgu, 0, moe ? 0x7fffffff : 0, 0x00020000)
#define rn2 mk_rsrc(pn2)
#define rm2 mk_rsrc(pm2)
    int vw, vn2[8], vm2;
    auto k_blk = [&](int k) __attribute__((always_inline)) { return tt ^ (k_ord(k) >> 1); };
#define KROT(kk_) (k_blk(kk_) * 256 + (k_ord(kk_) & 1) * 128)
    int tv2, tv7;
    unsigned sd = 0;
    auto spf = [&](auto N) __attribute__((always_inline)) {
      constexpr int n = decltype(N)::value;
      if constexpr (n >= 0 && sp_tile(n >> 4) >= 0) sld<((n >> 1) & 7) * 4096 + (n & 1) * 64>(sd, spb, KROT(sp_tile(n >> 4)));
    };
    unsigned mq0, mq1, mq2, mq3;
    const int cg8 = et ^ ((et & 1) << 1), rgb = (g & 7) * 4 + (j >> 3);
    const __amdgpu_buffer_rsrc_t rdn = mk_rsrc(w_down);
    const __amdgpu_buffer_rsrc_t rgu = mk_rsrc(wsp + WS_GU);
    int vdn, vgu;
    u32x4 dv[7], gv[4];
    int abo, abq[3];
    unsigned lma;
    unsigned sel[2][4];
    float g0, g1;
    u32x4 kk[4];
    unsigned k1 = 0, k2 = 0;
    const int ko_ = O_KEY + pw * 4096;
#define MM(a, b, c) __builtin_amdgcn_mfma_f32_16x16x32_bf16(__builtin_bit_cast(bf16x8, a), __builtin_bit_cast(bf16x8, b), c, 0, 0, 0)
#define PIN1(a) asm volatile("" : "+v"(a))
#define PIN2(a, b) asm volatile("" : "+v"(a), "+v"(b))
#define PIN3(a, b, c) asm volatile("" : "+v"(a), "+v"(b), "+v"(c))
    {
      int gro, go;
      u32x2 gr;
      u32x4 oh[5];
      f32x4 sa4, gw[2], gx[2];
      float su, gs, ta, tb, sx, mbv;
      unsigned dk[4], kl;
      const int hb = lane >> 5;
#pragma unroll
      for (int l = 0; l < WSN; ++l) asm volatile("" : "=v"(wv[l]));
      auto diag = [&](const f32x4& m) __attribute__((always_inline)) {
        return __uint_as_float((__float_as_uint(m[0]) & dk[0]) | (__float_as_uint(m[1]) & dk[1]) | (__float_as_uint(m[2]) & dk[2]) | (__float_as_uint(m[3]) & dk[3]));
      };
      auto setup = [&](auto JB, auto GP) __attribute__((always_inline)) {
        constexpr int jb = decltype(JB)::value, gp = decltype(GP)::value;
        SB();
        {
          int ln = lane, jj = j, p_ = pw;
          launder(ln, jj, p_);
          const int c_ = ln & 15, r_ = ln >> 4, e_ = jj & 7, t_ = (xcd & 1) * 4 + (jj >> 3);
          if constexpr (jb == 1 && gp == 0) {
            pn2 = ((kq >> 1) ? ws_im : ws_re) + (kq & 1) * 1024;
            spb = (const char*)(pn2 + (sym_row(S_, 64, 1, t_, 0, e_ >> 1, 0) + (e_ & 1) * 32 + p_ * 8) * S_);
            pins(pn2, spb);
          }
          if constexpr (jb == 1 && gp >= 1 && gp < 6) {
            const unsigned o = c_ == gp - 1 ? 0x3f803f80u : 0u;
            oh[gp - 1] = (u32x4){o, o, o, o};
            PIN1(oh[gp - 1]);
          }
          if constexpr (jb == 1 && gp == 6) {
            const int rk = moe ? e_ * 8 + (jj >> 3) * 2 + (xcd >> 2) : 0, r3 = ((rk - 14) * 43) >> 7, rr = rk - 14 - r3 * 3;
            const int c56 = rk < 14 ? rk * 4 + 1 : r3 * 4 + rr + ((rr + 1) >> 1);
            mat = (c56 + 36) >> 6; cg = c56 - mat * 28; dq = xcd & 3; d0 = dq * 256 + p_ * 64;
            pin4(mat, cg, dq, d0);
          }
          if constexpr (jb == 2 && gp == 0) {
            pgu = mat ? w_up : w_gate;
            pm2 = wsp + WS_ABT + (kq * 8 + e_) * 262144;
            pins(pgu, pm2);
            vw = ((d0 + r_ * 16) * F_ + cg * 128 + c_ * 8) * 2;
            PIN1(vw);
          }
          if constexpr (jb == 2 && (gp == 1 || gp == 2)) {
            dk[gp * 2 - 2] = c_ == r_ * 4 + gp * 2 - 2 ? ~0u : 0u; dk[gp * 2 - 1] = c_ == r_ * 4 + gp * 2 - 1 ? ~0u : 0u;
            PIN2(dk[gp * 2 - 2], dk[gp * 2 - 1]);
          }
          if constexpr (jb == 2 && gp == 3) { kl = lrow + (ln >> 5); go = (ln & 30) ? (int)0x80000000 : ((ln & 1) ? O_KEY : O_ST) + (int)kl * 8; PIN2(kl, go); }
          if constexpr (jb == 2 && gp == 4) { kl = 0xffff - kl; go = (ln & ~1) == 2 ? O_CE + (z * D_ + drow + (ln & 1)) * 8 : go; PIN2(kl, go); }
          if constexpr (jb == 2 && gp == 5) {
            gro = p_ < 2 ? O_ST + (st * 128 + p_ * 64 + ln) * 8 : O_CE + z * D_ * 8 + sym_row(D_, 32, 8, e_, z, (ln >> 2) & 3, (p_ - 2) * 16 + r_ * 4 + (ln & 3));
            lma = lds_addr(smem + LDS_MB) + (p_ < 2 ? p_ * 256 : 512 + p_ * 256) + ln * 4;
            PIN2(gro, lma);
          }
          if constexpr (jb == 7 && gp == 0) {
            vm2 = p_ * 1024 + ln * 16;
            PIN1(vm2);
          }
          if constexpr (jb == 2 && gp == 6) {
            const int r0 = p_ * 8 + lrw(ln >> 3);
            vn2[6] = sym_lo(64, S_ * 2, (r0 >> 2) & 3, (r0 >> 4) * 4 + (r0 & 3)); vn2[7] = sym_hi(S_, 64, S_ * 2, t_, z, (r0 >> 2) & 3) | (vm & 127);
            PIN2(vn2[6], vn2[7]);
          }
          if constexpr ((jb == 2 && gp == 7) || (jb == 7 && gp >= 6)) {
            constexpr int q0 = jb == 2 ? 0 : gp == 6 ? 3 : 6;
            const int lo = vn2[6], hi = vn2[7];
#pragma unroll
            for (int q = q0; q < q0 + 3 && q < 8; ++q) { vn2[q] = ((lo + q * 8 * S_ * 2) & (63 * S_ * 2)) ^ hi; PIN1(vn2[q]); }
          }
          if constexpr (jb == 7 && gp == 1) { vdn = rgb * 112 + p_ * 28 + r_ * 7; vgu = (((ln >> 3) & 1) * 4 * F_ + vdn + min(ln & 7, 6)) * 16; PIN2(vdn, vgu); }
          if constexpr (jb == 7 && gp == 2) { vdn = (vdn * D_ + cg8 * 128 + c_ * 8) * 2; PIN1(vdn); }
          if constexpr (jb == 7 && gp == 3) {
            abo = (((kq * 8 + e_) * 4 + p_) * 16 + c_) * AB_STRIDE * 4;
            abq[2] = ln >= 16 || (ln & 7) == t_ ? (int)0x80000000 : abo;
            abo = ln < 32 ? (((kq * 8 + e_) * 4 + p_) * 16 + t_) * AB_STRIDE * 4 + ln * 4 : (int)0x80000000;
            PIN2(abo, abq[2]);
          }
          if constexpr (jb == 7 && gp == 4) {
            abq[1] = ln >= 8 ? (int)0x80000000 : abq[2];
            abq[0] = ((ln ^ t_) & 4) ? (int)0x80000000 : abq[1];
            PIN2(abq[0], abq[1]);
          }
          if constexpr (jb == 7 && gp == 5) {
            const unsigned mb = lds_addr(smem) + p_ * 1024;
            mq0 = mb + ((3 - kq) & 3) * 4096; mq1 = mb + ((4 - kq) & 3) * 4096; mq2 = mb + ((5 - kq) & 3) * 4096; mq3 = mb + ((6 - kq) & 3) * 4096;
            pin4(mq0, mq1, mq2, mq3);
          }
          if constexpr (jb == 8 && gp < 4) {
#pragma unroll
            for (int h = 0; h < 2; ++h) {
              const unsigned hv = h ? 0x0302u : 0x0100u;
              sel[h][gp] = ((ln & 7) >> 1) == gp ? ((ln & 1) ? (hv << 16) | 0x0c0cu : 0x0c0c0000u | hv) : 0x0c0c0c0cu;
            }
            PIN2(sel[0][gp], sel[1][gp]);
          }
        }
        SB();
      };
#define WS1(i) setup(IC<1>{}, IC<i>{})
#define WS2(i) setup(IC<2>{}, IC<i>{})
#define WS3(i) do { setup(IC<7>{}, IC<i>{}); if (i >= 4) setup(IC<8>{}, IC<(i) & 3>{}); spf(IC<(i < SP0 ? i : -1)>{}); } while (0)
      G1_STAGE_TILE(1, G1_STAGE, WS1);
      WAIT_VM(19); BAR();
      G1_STAGE_TILE(2, 2 * G1_STAGE, WS2);
      G1_STAGE_TILE(3, 3 * G1_STAGE, WS3);
      auto lnu = [&](auto U) __attribute__((always_inline)) {
        constexpr int jb = 1 + decltype(U)::value / 7, gp = decltype(U)::value % 7;
        SB();
        if constexpr (jb == 1) {
          const f32x4 z4 = {0.f, 0.f, 0.f, 0.f};
          if constexpr (gp == 0) { sa4 = MM(lv[0], oh[0], z4); gw[0] = MM(lv[0], lw[0], z4); gx[0] = MM(lv[0], lv[0], z4); PIN3(sa4, gw[0], gx[0]); }
          if constexpr (gp == 1) { sa4 = MM(lv2[0], oh[1], sa4); gw[1] = MM(lv2[0], lw[0], z4); gx[1] = MM(lv2[0], lv2[0], z4); PIN3(sa4, gw[1], gx[1]); }
          if constexpr (gp == 2) { sa4 = MM(lv[1], oh[0], sa4); gw[0] = MM(lv[1], lw[1], gw[0]); gx[0] = MM(lv[1], lv[1], gx[0]); PIN3(sa4, gw[0], gx[0]); }
          if constexpr (gp == 3) { sa4 = MM(lv2[1], oh[1], sa4); gw[1] = MM(lv2[1], lw[1], gw[1]); gx[1] = MM(lv2[1], lv2[1], gx[1]); PIN3(sa4, gw[1], gx[1]); }
          if constexpr (gp == 4) { sa4 = MM(ld[0], oh[2], sa4); sa4 = MM(ld[1], oh[2], sa4); PIN1(sa4); }
          if constexpr (gp == 5) { sa4 = MM(ld2[0], oh[3], sa4); sa4 = MM(ld2[1], oh[3], sa4); PIN1(sa4); }
          if constexpr (gp == 6) { sa4 = MM(lw[0], oh[4], sa4); sa4 = MM(lw[1], oh[4], sa4); PIN1(sa4);
            epoch = __builtin_amdgcn_readfirstlane(epv); tag = ((epoch + 1u) * 0x9E3779B1u ^ dmix) | 1u; }
        }
        if constexpr (jb == 2) {
          if constexpr (gp == 0) { su = (sa4[0] + sa4[1]) + (sa4[2] + sa4[3]); ta = diag(gw[0]); PIN2(su, ta); }
          if constexpr (gp == 1) { su = fold32(su, su); tb = diag(gw[1]); PIN2(su, tb); }
          if constexpr (gp == 2) { su = fold16(su, su); gs = fold32(ta, tb); PIN2(su, gs); }
          if constexpr (gp == 3) { ta = diag(gx[0]); tb = diag(gx[1]); PIN2(ta, tb); }
          if constexpr (gp == 4) { gs = fold16(gs, fold32(ta, tb)); PIN1(gs); }
          if constexpr (gp == 5) { gs += dpp_f<0xB1>(gs); gs += dpp_f<0x4E>(gs); PIN1(gs); }
          if constexpr (gp == 6) { gs += dpp_f<0x141>(gs); gs += dpp_f<0x140>(gs); PIN1(gs); }
        }
        if constexpr (jb == 3) {
          if constexpr (gp == 0) {
            const float s0 = rdl(su, 0), s1 = rdl(su, 1), q0 = rdl(gs, 16), q1 = rdl(gs, 48);
            sx = hb ? s1 : s0; ta = hb ? q1 : q0; PIN2(sx, ta);
          }
          if constexpr (gp == 1) {
            const float mean = sx * (1.0f / D_);
            mbv = rbf(mean);
            ta -= sx * mean;
            if (__builtin_expect(__any(!(sx * mean <= 16.f * ta)), 0)) {
              float q = fold32(sqdev16(lv, rdl(mbv, 0)), sqdev16(lv2, rdl(mbv, 32)));
              q = row_sum(fold16(q, q));
              const float dm = mean - mbv;
              ta = fmaxf(q - (float)D_ * dm * dm, 0.f);
            }
            PIN2(mbv, ta);
          }
          if constexpr (gp == 2) { ta = rbf(rbf(ta * (1.0f / D_)) + 1e-5f); gs -= mbv * rdl(su, 4); PIN2(ta, gs); }
          if constexpr (gp == 3) { ta = rbf(__builtin_amdgcn_sqrtf(ta)); tb = __builtin_amdgcn_rcpf(ta); PIN2(ta, tb); }
          if constexpr (gp == 4) {
            const unsigned rb = __float_as_uint(rbf(gs * tb));
            kl |= (rb ^ ((unsigned)((int)rb >> 31) | 0x80000000u)) & 0xffff0000u; PIN1(kl);
          }
          if constexpr (gp == 5) { kl = (lane & 2) ? __float_as_uint(su) : (lane & 1) ? kl : (__float_as_uint(mbv) >> 16) | __float_as_uint(ta); PIN1(kl); }
        }
        SB();
      };
      constexpr int LN_JB = 1 + (19 * LNS) / 8;
      auto work1 = [&](auto JB, auto GP) __attribute__((always_inline)) {
        constexpr int jb = decltype(JB)::value, gp = decltype(GP)::value, pos = (jb - 1) * 8 + gp;
        SB();
        if constexpr (pos % LNS == 0 && pos / LNS < 20) lnu(IC<pos / LNS>{});
        if constexpr (jb == ST_JB + 3) {
          if constexpr (gp == 0) { if (!__all(gr.y == tag)) gr = granule_wait((const u64*)((const char*)(flags + FL_AB) + gro), tag); PIN1(gr); }
          if constexpr (gp == 1) { g1 = __builtin_amdgcn_rcpf(__uint_as_float((gr.x & 0xffff0000u) ^ ((unsigned)z << 31))); PIN1(g1); }
          if constexpr (gp == 2) { g0 = pw < 2 ? -(lo_bf(gr.x) * g1) : __uint_as_float(gr.x); PIN1(g0); }
        }
        if constexpr (jb == 8 && (gp == 2 || gp == 5)) {
          static_assert(VAN == 4 && VA0 >= 2 && VA0 + VAN <= 9, "first halves");
          int ln = lane;
          PIN1(ln);
          const int i = (ln & 15) * 4 + pw, n = gp == 2 ? (moe ? 0 : 64) : (et < 2 ? 0 : 32);
          const int r0 = gp == 2 ? tt * 64 : sym_row(S_, 64, 1, tt, 0, et >> 1, 0) + (et & 1) * 32;
          (gp == 2 ? tv7 : tv2) = i < n ? (r0 + i) * (S_ * 2) + ((tt ^ (VA0 - 1 + (ln >> 4))) << 8) : (int)0x80000000;
          PIN1(gp == 2 ? tv7 : tv2);
        }
        if constexpr (gp < SP1 && jb <= SP1E) spf(IC<SP0 + (jb - 1) * SP1 + gp>{});
        SB();
      };
      auto step1 = [&](auto JB) __attribute__((always_inline)) {
        constexpr int jb = decltype(JB)::value;
        if constexpr (jb == 1) PAGE(1);
        if constexpr (jb == 7) PAGE(2);
        constexpr int n16 = jb == 12 ? 12 : jb == 11 ? 14 : 16;
        constexpr auto wl = [](int q) { return (q == LN_JB ? 1 : 0) + (q == ST_JB ? 1 : 0); };
        if constexpr (jb == 1) {
          asm volatile("s_waitcnt vmcnt(%11)" : "+v"(lv[0]), "+v"(lv[1]), "+v"(lw[0]), "+v"(lw[1]), "+v"(ld[0]), "+v"(ld[1]),
                       "+v"(lv2[0]), "+v"(lv2[1]), "+v"(ld2[0]), "+v"(ld2[1]), "+v"(epv) : "i"(16) : "memory");
        } else if constexpr (jb == ST_JB + 3) {
          asm volatile("s_waitcnt vmcnt(%1)" : "+v"(gr) : "i"(n16 + wl(jb - 2) + wl(jb - 1)) : "memory");
        } else wait_vm<n16 + wl(jb - 3) + wl(jb - 2) + wl(jb - 1)>();
        BAR();
        if constexpr (jb == 2) { if (pw == 0 && lane < 16) ((int*)(smem + LDS_SEL))[lane < 8 ? lane : lane + 8] = 0; }
        if constexpr (jb == 4) { if (pw == 0 && lane == 0) ((unsigned*)(smem + LDS_SEL))[5] = tag; }
#define W1(i) work1(JB, IC<i>{})
        if constexpr (jb < 10) G1_STAGE_TILE(jb - 1 + G1_NST, ((jb - 1) % G1_NST) * G1_STAGE, W1);
        else G1_STAGE_A(jb - 1 + G1_NST, ((jb - 1) % G1_NST) * G1_STAGE, W1);
        if constexpr (jb == 12) G1_STAGE_B(13, G1_STAGE);
        if constexpr (jb == LN_JB) __builtin_amdgcn_raw_buffer_store_b64((u32x2){kl, tag}, rf, go, 0, 16);
        if constexpr (jb == ST_JB) {
          asm volatile("buffer_load_dwordx2 %0, %1, %2, 0 offen sc1" : "=v"(gr) : "v"(gro), "s"(rf) : "memory");
        }
        if constexpr (jb <= 8) {
          if (et == jb - 1) {
#pragma unroll
            for (int l = 0; l < WSN; ++l) bld16_nt_io(wv[l], rw, vw, l * F_ * 2);
          }
        }
        SB();
      };
      static_assert(ST_JB + 3 <= 12 && ST_JB >= 6, "schedule");
      step1(IC<1>{}); step1(IC<2>{}); step1(IC<3>{}); step1(IC<4>{}); step1(IC<5>{}); step1(IC<6>{}); step1(IC<7>{}); step1(IC<8>{}); step1(IC<9>{}); step1(IC<10>{}); step1(IC<11>{}); step1(IC<12>{});
    }
#define G2_STAGE_N(kk_, W) do { const int kb = KROT(kk_); constexpr int so = n_off(kk_); SB(); \
    DMA_S(rn2, vn2[0], mq0, so, kb); W(0); DMA_S(rn2, vn2[1], mq1, so, kb); W(1); \
    DMA_S(rn2, vn2[2], mq2, so, kb); W(2); DMA_S(rn2, vn2[3], mq3, so, kb); W(3); \
    DMA_S(rn2, vn2[4], mq0, so + 16384, kb); W(4); DMA_S(rn2, vn2[5], mq1, so + 16384, kb); W(5); \
    DMA_S(rn2, vn2[6], mq2, so + 16384, kb); W(6); DMA_S(rn2, vn2[7], mq3, so + 16384, kb); W(7); SB(); } while (0)
#define G2_STAGE_NH(kk_, h) do { const int kb = KROT(kk_); constexpr int so = n_off(kk_) + (h) * 16384; SB(); \
    DMA_S(rn2, vn2[(h) * 4], mq0, so, kb); DMA_S(rn2, vn2[(h) * 4 + 1], mq1, so, kb); \
    DMA_S(rn2, vn2[(h) * 4 + 2], mq2, so, kb); DMA_S(rn2, vn2[(h) * 4 + 3], mq3, so, kb); SB(); } while (0)
#define G2_STAGE_M(kk_, W) do { const int kb = KROT(kk_) * 128; constexpr int so = G2_MRING + m_off(kk_); SB(); \
    DMA_(rm2, vm2, mb0, so, kb, 0, " sc1"); W(8); DMA_(rm2, vm2, mb0, so + 4096, kb, 4096, " sc1"); W(9); \
    DMA_(rm2, vm2, mb0, so + 8192, kb, 8192, " sc1"); W(10); DMA_(rm2, vm2, mb0, so + 12288, kb, 12288, " sc1"); W(11); SB(); } while (0)
    WAIT_VM(8); BAR();
    asm volatile("ds_write_b32 %0, %1" :: "v"(lma), "v"(g0) : "memory");
    if (pw < 2) asm volatile("ds_write_b32 %0, %1 offset:512" :: "v"(lma), "v"(g1) : "memory");
    u32x4 tv[4];
    unsigned ready = 0;
    auto need = [&](int k) __attribute__((always_inline)) {
      const int b = k_blk(k) + (k_ord(k) & 1) * 8;
#pragma unroll 1
      for (int it = 0; !((ready >> b) & 1) && it < SPIN_MAX; ++it) {
        ready |= (unsigned)__ballot(__builtin_amdgcn_raw_buffer_load_b32(rf, abq[k < 5 ? 0 : k < 9 ? 1 : 2], 0, 16) == tag);
        asm volatile("" ::: "memory");
      }
    };
    unsigned ksel, bad = 0;
    int isel;
    unsigned xw;
    auto bvec = [&](unsigned src, const unsigned (&sl)[4]) __attribute__((always_inline)) {
      return (u32x4){__builtin_amdgcn_perm(0, src, sl[0]), __builtin_amdgcn_perm(0, src, sl[1]),
                     __builtin_amdgcn_perm(0, src, sl[2]), __builtin_amdgcn_perm(0, src, sl[3])};
    };
#define BC8(v, p) (unsigned)__builtin_amdgcn_update_dpp(__builtin_amdgcn_update_dpp(0, (int)(v), 0x150 + (p), 0xf, 0x3, false), (int)(v), 0x158 + (p), 0xf, 0xc, false)
    float* lg = (float*)(smem + LDS_GU);
    f32x4 macc = {0.f, 0.f, 0.f, 0.f};
    u32x2 sv;
    unsigned hp, gbad;
    float s0, s1;
    auto work2 = [&](auto JB, auto GP) __attribute__((always_inline)) {
      constexpr int jb = decltype(JB)::value, gp = decltype(GP)::value;
      SB();
      if constexpr (jb == -1 && gp < 4) {
        constexpr int c = gp == 0 ? 0xB1 : gp == 1 ? 0x4E : gp == 2 ? 0x141 : 0x140;
        merge2(k1, k2, dpp_u<c>(k1), dpp_u<c>(k2));
        PIN2(k1, k2);
      }
      if constexpr (jb == -1 && gp == 4) {
        ksel = tk ? k2 : k1;
        isel = (0xffff - (int)(ksel & 0xffffu)) & (S_ - 1);
        k1 = (isel * D_ + d0 + rq * 16 + (lane & 7) * 2) * 2; k2 = O_ST + isel * 8;
        PIN1(ksel);
        xw = gld4_s(x, k1);
        sv = bld8_sc1(rf, k2);
      }
      if constexpr (jb == 0 && gp == 1) wait_xs<3 + 2 + 2>(xw, sv);
      if constexpr (jb == 0) {
        if constexpr (gp == 1) { if (!__all(sv.y == tag)) sv = granule_wait(stat + isel, tag); s0 = __builtin_amdgcn_rcpf(hi_bf(sv.x)); PIN1(s0); }
        if constexpr (gp == 2) { s1 = ln_elem(lo_bf(xw), lo_bf(sv.x), hi_bf(sv.x), s0); PIN1(s1); }
        if constexpr (gp == 3) { xw = pk2(s1, ln_elem(hi_bf(xw), lo_bf(sv.x), hi_bf(sv.x), s0)); PIN1(xw); }
      }
      if constexpr (jb == DG && gp < 7) { PIN1(dv[gp]); macc = MM(dv[gp], bvec(BC8(hp, gp), sel[0]), macc); PIN1(macc); }
      if constexpr ((jb == 0 && gp >= 4) || jb == 1) {
        constexpr int l = jb == 0 ? gp - 4 : gp + 4;
        if constexpr (!(l & 1)) hp = BC8(xw, l >> 1);
        PIN1(wv[l]);
        macc = MM(wv[l], bvec(hp, sel[l & 1]), macc);
        PIN2(macc, hp);
      }
      if constexpr (jb == GV_JB + 2) {
        if constexpr (gp == 0) { gbad = (gv[0].y ^ tag) | (gv[0].w ^ tag) | (gv[1].y ^ tag) | (gv[1].w ^ tag); PIN1(gbad); }
        if constexpr (gp == 1) { gbad |= (gv[2].y ^ tag) | (gv[2].w ^ tag) | (gv[3].y ^ tag) | (gv[3].w ^ tag); PIN1(gbad); }
        if constexpr (gp == 2) {
          s0 = ((__uint_as_float(gv[0].x) + __uint_as_float(gv[1].x)) + __uint_as_float(gv[2].x)) + __uint_as_float(gv[3].x);
          s1 = ((__uint_as_float(gv[0].z) + __uint_as_float(gv[1].z)) + __uint_as_float(gv[2].z)) + __uint_as_float(gv[3].z);
          PIN2(s0, s1);
        }
        if constexpr (gp == 3) {
#pragma unroll 1
          for (int it = 0; !__all(gbad == 0) && it < SPIN_MAX; ++it) {
            u32x4 q[4];
            gbad = 0;
#pragma unroll
            for (int i = 0; i < 4; ++i) { q[i] = __builtin_amdgcn_raw_buffer_load_b128(rgu, vgu, i * F_ * 16, 16); gbad |= (q[i].y ^ tag) | (q[i].w ^ tag); }
            s0 = ((__uint_as_float(q[0].x) + __uint_as_float(q[1].x)) + __uint_as_float(q[2].x)) + __uint_as_float(q[3].x);
            s1 = ((__uint_as_float(q[0].z) + __uint_as_float(q[1].z)) + __uint_as_float(q[2].z)) + __uint_as_float(q[3].z);
            asm volatile("" ::: "memory");
          }
          const unsigned gp2 = pk2(s0, s1);
          const unsigned up2 = dpp_u<0x128>(gp2);
          s0 = tk ? hi_bf(up2) : lo_bf(gp2); s1 = tk ? hi_bf(gp2) : lo_bf(up2);
          PIN2(s0, s1);
        }
        if constexpr (gp == 4) { s0 = rbf(s0 * __builtin_amdgcn_rcpf(1.f + __expf(-s0))); PIN1(s0); }
        if constexpr (gp == 5) { hp = pk2(s0 * s1, 0.f); PIN1(hp); }
      }
      if constexpr (jb == 10 && gp == 4) code_touch(sd, (j * 4 + pw) * 256);
      SB();
    };
#define W2M(i) work2(IC<-1>{}, IC<i>{})
#define W20(i) work2(IC<0>{}, IC<i>{})
#define W21(i) work2(IC<1>{}, IC<i>{})
    unsigned fs;
    {
      G1_STAGE_B(15, 3 * G1_STAGE);
      G1_STAGE_B(14, 2 * G1_STAGE);
      lgkm0(sd);
      BAR();
      G2_STAGE_NH(0, 0);
      WAIT_VM(6); BAR();
#pragma unroll
      for (int i = 0; i < 4; ++i) kk[i] = bld16_sc1(rf, l16, ko_ + i * 1024);
      G2_STAGE_NH(0, 1);
#pragma unroll
      for (int l = WSN; l < 16; ++l) wv[l] = bld16_nt(rw, vw, l * F_ * 2);
      touch1(rn2, tv7, mb0, 0);
      asm volatile("s_waitcnt vmcnt(%4)" : "+v"(kk[0]), "+v"(kk[1]), "+v"(kk[2]), "+v"(kk[3]) : "i"(16 - WSN + 5) : "memory");
#pragma unroll
      for (int i = 0; i < 4; ++i) {
        k2 = med3u(k1, k2, kk[i].x); k1 = max(k1, kk[i].x);
        k2 = med3u(k1, k2, kk[i].z); k1 = max(k1, kk[i].z);
        bad |= (kk[i].y ^ tag) | (kk[i].w ^ tag);
      }
#pragma unroll 1
      for (int it = 0; !__all(bad == 0) && it < SPIN_MAX; ++it) {
        k1 = 0; k2 = 0; bad = 0;
#pragma unroll
        for (int i = 0; i < 4; ++i) {
          const u32x4 q = __builtin_amdgcn_raw_buffer_load_b128(rf, l16, ko_ + i * 1024, 16);
          k2 = med3u(k1, k2, q.x); k1 = max(k1, q.x);
          k2 = med3u(k1, k2, q.z); k1 = max(k1, q.z);
          bad |= (q.y ^ tag) | (q.w ^ tag);
        }
        asm volatile("" ::: "memory");
      }
      merge2(k1, k2, dpp_u<0xB1>(k1), dpp_u<0xB1>(k2));
      merge2(k1, k2, dpp_u<0x4E>(k1), dpp_u<0x4E>(k2));
      merge2(k1, k2, dpp_u<0x141>(k1), dpp_u<0x141>(k2));
      merge2(k1, k2, dpp_u<0x140>(k1), dpp_u<0x140>(k2));
      if ((lane & 15) == 0) *(u32x2*)(smem + LDS_KEY + (pw * 4 + rq) * 8) = (u32x2){k1, k2};
      PAGE(3);
      wait_vm<17 - WSN>();
      BAR_LGKM();
#pragma unroll
      for (int jj = 0; jj < 4; ++jj) tv[jj] = *(const u32x4*)(smem + G2_MRING + m_off(0) + (jj * 4 + pw) * 1024 + lane * 16);
      { const u32x2 kp = *(const u32x2*)(smem + LDS_KEY + ci * 8); k1 = kp.x; k2 = kp.y; }
#pragma unroll
      for (int jj = 0; jj < 4; ++jj) __builtin_amdgcn_raw_buffer_store_b128(tv[jj], rm2, vm2 + jj * 4096 + 2 * tt * 16384, 0, 16);
      G2_STAGE_N(1, W2M);
      touch1(rn2, tv2, mb0, 0);
      WAIT_VM(11);
      __builtin_amdgcn_raw_buffer_store_b32(tag, rf, abo, 0, 16);
      {
        lds_vi rel = (lds_vi)(smem + LDS_SEL + 12);
        int it = 0;
        while (rel[0] != 4 && it < SPIN_MAX * 8) ++it;
        asm volatile("" ::: "memory");
      }
      G2_STAGE_N(2, W20);
      __builtin_amdgcn_s_sleep(2);
      WAIT_VM(10);
      fs = bld4_sc1(rf, abq[0]);
    }
    BAR();
#pragma unroll
    for (int jj = 0; jj < 4; ++jj) tv[jj] = *(const u32x4*)(smem + G2_MRING + m_off(1) + (jj * 4 + pw) * 1024 + lane * 16);
    constexpr int SPB = SP0 + SP1E * SP1;
    G2_STAGE_N(3, NOW);
    asm volatile("s_waitcnt vmcnt(8)" : "+v"(fs) :: "memory");
    ready = (unsigned)__ballot(fs == tag);
    need(2);
    G2_STAGE_M(2, NOW);
    need(3);
    G2_STAGE_M(3, NOW);
    fs = bld4_sc1(rf, abq[1]);
    W21(0); W21(1); W21(2); W21(3); W21(4); W21(5); W21(6); W21(7); W21(8); W21(9); W21(10); W21(11);
    asm volatile("" : "+v"(tv[0]), "+v"(tv[1]), "+v"(tv[2]), "+v"(tv[3]));
    int dno;
    auto step2 = [&](auto JB) __attribute__((always_inline)) {
      constexpr int jb = decltype(JB)::value;
      if constexpr (jb == 3) PAGE(4);
      if constexpr (jb == 9) PAGE(5);
      if constexpr (jb == 13) PAGE(7);
      constexpr auto must = [](int q) { return (q == 2 ? 4 : 0) + ((FSM >> q) & 1) + dig(GVS, q); };
      if constexpr (jb == 2) wait_vm<4 + must(1)>();
      else wait_vm<dn_cnt(jb - 2) + (jb == 5 ? 1 : 0) + 12 + must(jb - 1) + dn_cnt(jb - 1)>();
      if constexpr (jb == 3) { if (pw == 0 && (lane & ~8) == 0) ((unsigned*)(smem + LDS_SEL))[tk] = ksel; }
      if constexpr ((jb == 2 || jb == DG + 1)) {
        *(f32x4*)(lg + pw * 256 + lane * 4) = macc;
        macc = (f32x4){0.f, 0.f, 0.f, 0.f};
        lgkm0(sd);
      }
      BAR();
      float p0[4], p1[4];
      const int col = (pw & 1) * 64 + lane;
      if constexpr (jb == 3) {
        if (g == 0 && pw == 0 && lane == 0) __hip_atomic_store((unsigned*)flags + FL_EPOCH, epoch + 1u, __ATOMIC_RELAXED, __HIP_MEMORY_SCOPE_AGENT);
      }
      if constexpr (jb == DG + 1) {
        const float* p = lg + (pw & 1) * 32 + (lane >> 4) * 64 + ((2 * lane) & 7) * 4 + ((lane >> 2) & 3);
#pragma unroll
        for (int i = 0; i < 4; ++i) { p0[i] = p[i * 256]; p1[i] = p[i * 256 + 4]; }
      }
      if constexpr (jb == 2) {
        const float* p = lg + (col >> 5) * 64 + (col & 7) * 4 + ((col >> 3) & 3);
#pragma unroll
        for (int i = 0; i < 4; ++i) { p0[i] = p[i * 256]; p1[i] = p[i * 256 + 32]; }
      }
      if constexpr (jb == 4) {
        __builtin_amdgcn_raw_buffer_store_b32(tag, rf, abo + 8 * AB_STRIDE * 4, 0, 16);
      }
      if constexpr ((FSM >> (jb - 2)) & 1) {
        asm volatile("" : "+v"(fs));
        ready |= (unsigned)__ballot(fs == tag);
      }
      if constexpr (jb == GV_JB + 2) asm volatile("" : "+v"(gv[0]), "+v"(gv[1]), "+v"(gv[2]), "+v"(gv[3]));
      auto w2 = [&](auto GP) __attribute__((always_inline)) {
        constexpr int gp = decltype(GP)::value;
        if constexpr ((jb == 2 || jb == DG + 1) && gp == 0) {
          p0[0] = (p0[0] + p0[1]) + (p0[2] + p0[3]); p1[0] = (p1[0] + p1[1]) + (p1[2] + p1[3]);
          PIN2(p0[0], p1[0]);
        }
        if constexpr (jb == DG + 1 && gp == 1) {
          if (pw < 2) __builtin_amdgcn_raw_buffer_store_b64((u32x2){pk2(p0[0], p1[0]), tag}, mk_rsrc(wsp + WS_DN), (((pw * 8 + cg8) * 32 + rgb) * 64 + lane) * 8, 0, 16);
        }
        if constexpr (jb == 12 && gp == 3) epi_setup(3);
        if constexpr (jb == 12 && gp == 6) { key[0] = __builtin_amdgcn_readlane(ksel, 0); key[1] = __builtin_amdgcn_readlane(ksel, 8); far_calc(); }
        if constexpr (jb == 12 && gp == 10) {
          int l_ = lane;
          PIN1(l_);
          const int ec = seg0((l_ >> 4) & 1, (l_ >> 3) & 1);
          dno = (ec >> 7) * 16384 + (l_ >> 5) * 512 + (ec & 127) * 4 + (l_ & 7) * 16;
          PIN1(dno);
        }
        work2(JB, GP);
        if constexpr (gp < dig(SPS, jb)) spf(IC<SPB + acc_dig(SPS, jb) + gp>{});
      };
#define W2(i) w2(IC<i>{})
      G2_STAGE_N(jb + 2, W2);
      need(jb + 2);
      G2_STAGE_M(jb + 2, W2);
      if constexpr (jb == 2) {
#pragma unroll
        for (int jj = 0; jj < 4; ++jj) __builtin_amdgcn_raw_buffer_store_b128(tv[jj], rm2, vm2 + jj * 4096 + (2 * tt + 1) * 16384, 0, 16);
      }
      if constexpr ((FSM >> jb) & 1) fs = bld4_sc1(rf, abq[jb < 5 ? 1 : 2]);
#pragma unroll
      for (int i = acc_dig(GVS, jb); i < acc_dig(GVS, jb + 1); ++i) gv[i] = bld16_sc1(rgu, vgu, i * F_ * 16);
#pragma unroll
      for (int i = dn_first(jb); i < dn_first(jb) + dn_cnt(jb); ++i) dv[i] = bld16_nt(rdn, vdn, i * D_ * 2);
      if constexpr (jb == 2) {
        if (moe && pw < 2)
          __builtin_amdgcn_raw_buffer_store_b128((u32x4){__float_as_uint(p0[0]), tag, __float_as_uint(p1[0]), tag},
                                                 rgu, ((mat * 4 + dq) * F_ + cg * 128 + col) * 16, 0, 16);
      }
      SB();
      if constexpr (dig(SPS, jb) > 12) spf(IC<SPB + acc_dig(SPS, jb) + 12>{});
      SB();
    };
    static_assert(DG + 1 <= 12 && DG >= GV_JB + 3 && dn_first(DG - 2) == 7 && acc_dig(GVS, GV_JB + 1) == 4, "schedule");
    static_assert(SPB + acc_dig(SPS, 14) == (16 - VAN) * 16, "all tiles are prefetched");
    step2(IC<2>{}); step2(IC<3>{}); step2(IC<4>{}); step2(IC<5>{}); step2(IC<6>{}); step2(IC<7>{}); step2(IC<8>{}); step2(IC<9>{}); step2(IC<10>{}); step2(IC<11>{}); step2(IC<12>{}); step2(IC<13>{});
    lgkm0(sd);
    WAIT_VM(12); BAR();
    __builtin_amdgcn_s_setprio(0);
    const __amdgpu_buffer_rsrc_t rd = mk_rsrc(wsp + WS_DN + pw * 4096);
    u32x4 dq_[4];
    epi_setup(0); epi_setup(1); epi_setup(2);
    xq_load(0); xq_load(1); xq_load(2);
    asm volatile("s_cmp_lg_u32 %7, 0\n\ts_cbranch_scc1 1f\n\t"
                 "buffer_load_dwordx4 %0, %4, %5, %6 offen sc1\n\tbuffer_load_dwordx4 %1, %4, %5, %6 offen offset:1024 sc1\n\t"
                 "buffer_load_dwordx4 %2, %4, %5, %6 offen offset:2048 sc1\n\tbuffer_load_dwordx4 %3, %4, %5, %6 offen offset:3072 sc1\n\t"
                 "s_waitcnt vmcnt(7)\n\ts_branch 2f\n1:\n\ts_waitcnt vmcnt(3)\n2:\n\ts_barrier\n\ts_waitcnt vmcnt(0)"
                 : "=&v"(dq_[0]), "=&v"(dq_[1]), "=&v"(dq_[2]), "=&v"(dq_[3])
                 : "v"(dno), "s"(rd), "s"((far0 & ~9) ? 131072 : 0), "s"(min(far0 & ~9, far1 & ~9)) : "scc", "memory");
    wait_xq<0>(xq, sq);
    bool first = true;
#pragma unroll 1
    for (int c = 0; c < 2; ++c) {
      if (__builtin_expect(((c ? far1 : far0) & ~9) != 0, 1)) continue;
      const float sg = rbf(__builtin_amdgcn_rcpf(1.f + __expf(-key_score(c ? key[1] : key[0]))));
#pragma unroll 1
      for (int it = 0; it < SPIN_MAX; ++it) {
        unsigned bad = 0;
#pragma unroll
        for (int i = 0; i < 4; ++i) bad |= (dq_[i].y ^ tag) | (dq_[i].w ^ tag);
        if ((first || it) && __all(bad == 0)) break;
        if (it) __builtin_amdgcn_s_sleep(4);
#pragma unroll
        for (int i = 0; i < 4; ++i) dq_[i] = __builtin_amdgcn_raw_buffer_load_b128(rd, dno + i * 1024, c * 131072, 16);
        asm volatile("" ::: "memory");
      }
      first = false;
      f32x4 s4 = {0.f, 0.f, 0.f, 0.f};
#pragma unroll
      for (int i = 0; i < 4; ++i) s4 += (f32x4){lo_bf(dq_[i].x), hi_bf(dq_[i].x), lo_bf(dq_[i].z), hi_bf(dq_[i].z)};
      *(f32x2*)(smem + LDS_FIN + (c * 4 + pw) * 512 + (lane & 31) * 16 + (lane >> 5) * 8) = (f32x2){fold32(s4[0], s4[2]) * sg, fold32(s4[1], s4[3]) * sg};
      asm volatile("s_waitcnt lgkmcnt(0)" ::: "memory");
      if (lane == 0) asm volatile("ds_add_u32 %0, %1" :: "v"(lds_addr(smem + LDS_SEL + 24 + c * 4)), "v"(1) : "memory");
    }
    cv_calc();
  } else {
  {
    const int q = j * 4 + wid;
    constexpr int Q1 = WS_MB, Q2 = Q1 + 8, Q3 = Q2 + 8, Q4 = Q3 + 7, Q5 = Q4 + 7, Q6 = Q5 + 7, Q7 = Q6 + 4, Q8 = Q7 + 4, QC = Q8 + 1;
    static_assert(QC <= 128, "warm-up slots");
    unsigned dmy = 0;
    if (q < QC) {
      const char* base; int b0;
      if (q < Q1) { base = wsp; b0 = 0; }
      else if (q < Q2) { base = (const char*)ws_re; b0 = Q1; }
      else if (q < Q3) { base = (const char*)ws_im; b0 = Q2; }
      else if (q < Q4) { base = (const char*)w_gate; b0 = Q3; }
      else if (q < Q5) { base = (const char*)w_up; b0 = Q4; }
      else if (q < Q6) { base = (const char*)w_down; b0 = Q5; }
      else if (q < Q7) { base = (const char*)out; b0 = Q6; }
      else if (q < Q8) { base = (const char*)x; b0 = Q7; }
      else { base = (const char*)w_r; b0 = Q8; }
      base += (size_t)(q - b0) << 20;
      asm volatile("s_load_dword %0, %1, 0" : "+s"(dmy) : "s"(base) : "memory");
    }
    if (wid == 0) { CALL_PG(1); CALL_PG(8); CALL_PG(5); }
    else if (wid == 1) { CALL_PG(2); CALL_PG(9); CALL_PG(6); }
    else if (wid == 2) { CALL_PG(3); CALL_PG(10); CALL_PG(7); }
    else { CALL_PG(4); CALL_PG(11); CALL_PG(12); }
    BAR();
    lgkm0(dmy);
  }
  PAGE(8);
  epi_setup(0); epi_setup(1); epi_setup(2); epi_setup(3);
  const int wr = wid >> 1, wc = wid & 1;
  const int sw = swz(fr), fl = lrw(fr);
  const int ko0 = ((0 + fq) ^ sw) * 16, ko1 = ((4 + fq) ^ sw) * 16;
  bf16x8 fm[2][4], fn[2][4];
  {
    const char* pm = smem + (wr * 64 + fl) * 128;
    const char* pn = smem + 16384 + (wc * 64 + fl) * 128;
    f32x4 acc[4][4];
#pragma unroll
    for (int i = 0; i < 4; ++i)
#pragma unroll
      for (int jn = 0; jn < 4; ++jn)
#pragma unroll
        for (int q = 0; q < 4; ++q) acc[i][jn][q] = 0.f;
#define TO(i) ((i) * 2048)
#define LDSP(T, a) ((__attribute__((address_space(3))) T*)(size_t)(a))
#define SLOTS(n) _Pragma("unroll") for (int q = 0; q < (n); ++q)
#define MFA(c, m, n) asm volatile("v_mfma_f32_16x16x32_bf16 %0, %2, %1, %0" : "+v"(c) : "v"(m), "v"(n))
#define MFA2(c, m, n, m2, n2) asm volatile("v_mfma_f32_16x16x32_bf16 %0, %2, %1, %0\n\tv_mfma_f32_16x16x32_bf16 %0, %4, %3, %0" : "+v"(c) : "v"(m), "v"(n), "v"(m2), "v"(n2))
    unsigned lm[2][2], ln[2][2];
#pragma unroll
    for (int u = 0; u < 2; ++u)
#pragma unroll
      for (int kk = 0; kk < 2; ++kk) {
        lm[u][kk] = lds_addr(pm) + u * 2 * G1_STAGE + (kk ? ko1 : ko0);
        ln[u][kk] = lds_addr(pn) + u * 2 * G1_STAGE + (kk ? ko1 : ko0);
        asm volatile("" : "+v"(lm[u][kk]), "+v"(ln[u][kk]));
      }
    auto ldm = [&](int stg, int i, int kk) __attribute__((always_inline)) { fm[kk][i] = *LDSP(const bf16x8, lm[stg >> 1][kk] + (stg & 1) * G1_STAGE + TO(i)); };
    auto ldn = [&](int stg, int jn, int kk) __attribute__((always_inline)) { fn[kk][jn] = *LDSP(const bf16x8, ln[stg >> 1][kk] + (stg & 1) * G1_STAGE + TO(jn)); };
    auto quad = [&](int a, int c, int d, int s) __attribute__((always_inline)) {
      const int i = (s < 2 || s >= 6) ? a : a + 1, jn = s < 4 ? c : d, kk = (s == 1 || s == 2 || s == 5 || s == 6);
      MFA(acc[i][jn], fm[kk][i], fn[kk][jn]);
    };
    auto quad2 = [&](int a, int c, int d, int s) __attribute__((always_inline)) {
      const int i = (s < 2 || s >= 6) ? a : a + 1, jn = s < 4 ? c : d, kk = (s == 2 || s == 6);
      MFA2(acc[i][jn], fm[kk][i], fn[kk][jn], fm[1 - kk][i], fn[1 - kk][jn]);
    };
    auto half1 = [&](int stg) __attribute__((always_inline)) {
      const int ja = (stg & 1) * 2, jb = 2 - ja;
      SLOTS(8) {
        quad(0, ja, ja + 1, q);
        if (q < 4) ldn(stg, jb + (q >> 1), q & 1); else ldm(stg, 2 + ((q - 4) >> 1), q == 5 || q == 6);
        SB();
      }
      SLOTS(4) { quad2(0, jb, jb + 1, 2 * q); SB(); }
    };
    auto half2 = [&](int stg) __attribute__((always_inline)) {
      const int ja = (stg & 1) * 2, jb = 2 - ja, nx = (stg + 1) & 3;
      SLOTS(8) {
        quad(2, jb + 1, jb, q);
        if (!(q & 1)) ldm(nx, q >> 2, q == 2 || q == 4);
        SB();
      }
      SLOTS(8) {
        quad(2, ja, ja + 1, q);
        if (!(q & 1)) ldn(nx, jb + (q >> 2), (q >> 1) & 1);
        SB();
      }
    };
    unsigned ae[7];
    {
      int l_ = lane;
      asm volatile("" : "+v"(l_));
      const int p = l_ >> 4, dn = sym_dn(p), hf = (p >> 1) ^ (dn & z);
#pragma unroll
      for (int u = 0; u < 7; ++u) {
        const int T = u < 3 ? wc * 4 : (wc * 4 + u - 3 - dn) & 7, i = u < 3 ? (u + 1) ^ ((u & 1) ? 0 : dn * 2) : 0;
        const int f = dn * 8 + ((T & 2) << 1) + ((T & 4) >> 1) + (T & 1);
        ae[u] = lds_addr(smem + G2_MRING) + (hf * 64 + i * 16 + lrw(f)) * 128 + (((wr * 4 + ((l_ >> 2) & 3)) ^ swz(f)) * 16) + (l_ & 3) * 4;
        asm volatile("" : "+v"(ae[u]));
      }
    }
    unsigned al = lds_addr(smem + LDS_MB) + (wr * 32 + fr * 2) * 4, ac = lds_addr(smem + LDS_MB) + (256 + wc * 64 + fq * 4) * 4;
    asm volatile("" : "+v"(al), "+v"(ac));
    SLOTS(16) asm volatile("" : "+v"(acc[q >> 2][q & 3]));
    SB();
    SLOTS(4) { ldm(0, q >> 1, q & 1); ldn(0, q >> 1, q & 1); }
    SB();
#pragma unroll 1
    for (int k4 = 0; k4 < G1_NKT - 4; k4 += 4) {
      half1(0); BAR_LGKM();
      SB(); half2(0);
      half1(1); BAR_LGKM();
      SB(); half2(1);
      half1(2); BAR_LGKM();
      SB(); half2(2);
      half1(3); BAR_LGKM();
      SB(); half2(3);
    }
    bf16x8 gm[4][4], gn[2][8];
    auto rdm1 = [&](int set, int t, int h, int i, int kk) __attribute__((always_inline)) {
      gm[set][kk * 2 + i] = *LDSP(const bf16x8, lm[(t >> 1) & 1][kk] + (t & 1) * G1_STAGE + h * 4096 + i * 2048);
    };
    auto rdg = [&](int set, int t, int h, int r) __attribute__((always_inline)) {
      if (r == 0 || r == 2 || r == 4 || r == 5) rdm1(set, t, h, r >= 4, r == 2 || r == 4);
      else {
        const int jn = r < 4 ? 0 : (r - 4) >> 1, kk = r < 4 ? r >> 1 : r & 1;
        gn[set][kk * 4 + jn] = *LDSP(const bf16x8, ln[(t >> 1) & 1][kk] + (t & 1) * G1_STAGE + TO(jn));
      }
    };
    auto mmg = [&](int ms, int ns, int h, int jn, int r, int f) __attribute__((always_inline)) {
      const int i = (r >> 1) ^ f, kk = r == 1 || r == 2;
      MFA(acc[h * 2 + i][jn], gm[ms][kk * 2 + i], gn[ns][kk * 4 + jn]);
    };
    f32x2 rsv, mrv;
    f32x4 cev[4];
    float tc[32];
    unsigned ep;
    auto rds = [&](int h, int r) __attribute__((always_inline)) {
      if (r == 0) mrv = *LDSP(const f32x2, al + h * 256); else if (r == 5) rsv = *LDSP(const f32x2, al + 512 + h * 256);
      else cev[r - 1] = *LDSP(const f32x4, ac + (r - 1) * 64);
    };
    auto prep = [&](int n) __attribute__((always_inline)) {
      tc[n] = llvm_fmul_legacy(mrv[n & 1], cev[n >> 3][(n >> 1) & 3]);
      asm volatile("" : "+v"(tc[n]));
    };
    auto epi = [&](int h, int n) __attribute__((always_inline)) {
      const int jn = n >> 4, r = (n >> 2) & 3, u = n & 3, e = (n >> 2) * 2;
      if (u == 0) {}
      else if (u == 1) asm volatile("v_fmac_f32 %0, %2, %4\n\tv_fmac_f32 %1, %3, %5\n\tv_cvt_pk_bf16_f32 %0, %0, %1" : "+v"(tc[e]), "+v"(tc[e + 1])
                                    : "v"(acc[h * 2][jn][r]), "v"(acc[h * 2 + 1][jn][r]), "v"(rsv[0]), "v"(rsv[1]));
      else if (u == 2) ep = __float_as_uint(tc[e]);
      else *LDSP(unsigned, (r ? ae[r - 1] + (jn & 1) * 128 + (jn >> 1) * lrw(4) * 128 : ae[3 + jn]) + m_off(h)) = ep;
      SB();
    };
    SB();
    half1(0);
    BAR_LGKM();
    SB();
    SLOTS(16) {
      if (q < 8) quad(2, 3, 2, q); else quad(2, 0, 1, q - 8);
      if (q < 12) rdg(0, 14, 0, q); else rdg(1, 13, 0, q - 12);
      SB();
    }
    BAR();
    SB();
    SLOTS(16) {
      mmg(0, 0, 0, q >> 2, q & 3, (q >> 2) & 1);
      if (q < 6) rds(0, q); else if (q < 14) rdg(1, 13, 0, q - 2);
      if (q >= 6) prep(q - 6);
      if (q >= 14) prep(q - 4);
      SB();
    }
    PAGE(9);
    BAR();
    SB();
    {
      constexpr int ct[8] = {1, 1, 0, 0, 1, 0, 1, 0}, cj[8] = {0, 1, 0, 1, 2, 2, 3, 3}, cf[8] = {0, 1, 0, 1, 0, 1, 0, 1};
      constexpr auto en = [](int q) { return q < 15 ? 0 : q < 26 ? (q - 14) * 3 - (q == 25) : q < 27 ? 32 : q < 32 ? 32 + (q - 26) * 3 + (q == 31) : 48; };
      SLOTS(32) {
        mmg(ct[q >> 2], ct[q >> 2], 0, cj[q >> 2], q & 3, cf[q >> 2]);
        if (q < 12) rdg(0, 15, 0, q);
        else if (q < 15 || q == 17) { const int r = q < 15 ? q - 12 : 3; rdm1(2, 15, 1, r >> 1, r == 1 || r == 2); }
        else if (q == 20 || q == 23 || q == 25 || q == 26) { const int r = q == 20 ? 0 : q == 23 ? 1 : q - 23; rdm1(3, 13, 1, r >> 1, r == 1 || r == 2); }
        if (q < 15) prep(12 + q);
        if (q >= 10 && q < 15) prep(17 + q);
        SB();
#pragma unroll
        for (int n = q ? en(q - 1) : 0; n < en(q); ++n) epi(0, n);
      }
    }
    SLOTS(2) { mmg(2, 0, 1, 0, q, 0); SB(); }
    asm volatile("s_nop 1");
    SLOTS(16) { epi(0, 48 + q); if (q == 3 || q == 9) { mmg(2, 0, 1, 0, q == 3 ? 2 : 3, 0); SB(); } }
    BAR_LGKM();
    SB();
    SLOTS(12) {
      mmg(2, 0, 1, 1 + (q >> 2), q & 3, ~(q >> 2) & 1);
      if (q < 6) rds(1, q); else rdg(0, 14, 1, q - 6);
      if (q >= 8) prep(q - 8);
      SB();
    }
    SLOTS(16) {
      mmg(3, 1, 1, q >> 2, q & 3, (q >> 2) & 1);
      if (q < 6) rdg(0, 14, 1, q + 6);
      if (q < 4) prep(4 + q); else { prep(2 * q); prep(2 * q + 1); }
      SB();
    }
    asm volatile("s_waitcnt lgkmcnt(0)" ::: "memory");
    if (lane == 0) asm volatile("ds_add_u32 %0, %1" :: "v"(lds_addr(smem + LDS_SEL + 12)), "v"(1) : "memory");
    SB();
    SLOTS(16) {
      mmg(0, 0, 1, q >> 2, q & 3, (q >> 2) & 1);
      if (q < 4) fn[0][q] = *(const bf16x8*)(smem + (wr * 128 + fl) * 128 + (q >> 1) * 2048 + ((q & 1) ? ko1 : ko0));
      else if (q < 8) fm[q & 1][(q - 4) >> 1] = *(const bf16x8*)(smem + G2_MRING + m_off(0) + (wc * 64 + fl) * 128 + TO((q - 4) >> 1) + ((q & 1) ? ko1 : ko0));
      SB();
      if (q >= 8) { epi(1, 3 * q - 24); epi(1, 3 * q - 23); epi(1, 3 * q - 22); }
    }
    SLOTS(40) epi(1, 24 + q);
  }

  const char* pn = smem + (wr * 128 + fl) * 128;
  const char* pm = smem + G2_MRING + (wc * 64 + fl) * 128;
  f32x4 acc[8][4];
  auto mm = [&](bool imaj, int set, int q2, int s, int mode) __attribute__((always_inline)) {
    int i, jj, kk;
    if (imaj) { i = s >> 2; const int r = s & 3; jj = (r >> 1) ^ (i & 1); kk = (r == 1 || r == 2); }
    else { jj = s >> 3; const int r = s & 7; i = jj ? 3 - (r >> 1) : (r >> 1); kk = ((r + 1) >> 1) & 1; }
    if (mode == 1 && !(s & 1)) asm volatile("v_mfma_f32_16x16x32_bf16 %0, %2, %1, 0" : "=&v"(acc[q2 * 2 + jj][i]) : "v"(fm[kk][i]), "v"(fn[set][jj * 2 + kk]));
    else if (mode) asm volatile("v_mfma_f32_16x16x32_bf16 %0, %2, %1, %0" : "+v"(acc[q2 * 2 + jj][i]) : "v"(fm[kk][i]), "v"(fn[set][jj * 2 + kk]));
    else acc[q2 * 2 + jj][i] = __builtin_amdgcn_mfma_f32_16x16x32_bf16(fn[set][jj * 2 + kk], fm[kk][i], acc[q2 * 2 + jj][i], 0, 0, 0);
  };
  unsigned bn[2], bm[2], an[2], am[2];
#pragma unroll
  for (int kk = 0; kk < 2; ++kk) {
    bn[kk] = lds_addr(pn) + (kk ? ko1 : ko0); bm[kk] = lds_addr(pm) + (kk ? ko1 : ko0);
    asm volatile("" : "+v"(bn[kk]), "+v"(bm[kk]));
    an[kk] = bn[kk]; am[kk] = bm[kk] + m_off(0);
  }
#define RDN(jt, kk, o) (*LDSP(const bf16x8, an[kk] + (o) + (jt) * 2048))
#define RDM(i, kk, o) (*LDSP(const bf16x8, am[kk] + (o) + TO(i)))
  PAGE(10);
  tag = __builtin_amdgcn_readfirstlane(((const unsigned*)(smem + LDS_SEL))[5]);
  {
    int son = 0;
    auto tile = [&](int kt, int zero) __attribute__((always_inline)) {
      SLOTS(16) {
        mm(true, 0, 0, q, zero);
        if (!(q & 1)) {
          if (q < 8) fm[(q >> 1) & 1][2 + (q >> 2)] = RDM(2 + (q >> 2), (q >> 1) & 1, 0);
          else { const int r = (q - 8) >> 1; fn[1][r] = RDN(2 + (r >> 1), r & 1, 0); }
        }
        SB();
      }
      SLOTS(16) {
        mm(false, 1, 1, q, zero);
        if (!(q & 1) && q < 8) fn[0][q >> 1] = RDN(4 + (q >> 2), (q >> 1) & 1, 0);
        SB();
      }
      const int som = son >> 1;
      son = son == 65536 ? 0 : son + 32768;
      SLOTS(16) {
        mm(false, 0, 2, q, zero);
        if (!(q & 1) && q < 8) fn[1][q >> 1] = RDN(6 + (q >> 2), (q >> 1) & 1, 0);
        if (q == 9 || q == 11) { an[q == 11] = bn[q == 11] + son; asm volatile("" : "+v"(an[q == 11])); }
        if (q == 13 || q == 15) { am[q == 15] = bm[q == 15] + som; asm volatile("" : "+v"(am[q == 15])); }
        SB();
      }
      BAR_LGKM();
      SB();
      SLOTS(16) {
        mm(true, 1, 3, q, zero);
        if (!(q & 1)) {
          if (q < 8) fn[0][q >> 1] = RDN(q >> 2, (q >> 1) & 1, 0);
          else fm[(q >> 1) & 1][(q - 8) >> 2] = RDM((q - 8) >> 2, (q >> 1) & 1, 0);
        }
        SB();
      }
    };
    SB();
    tile(0, 1);
#pragma unroll 1
    for (int kt = 1; kt < G2_NKT - 3; ++kt) tile(kt, 0);
  }
  unsigned fx;
  int fo = lane < 32 ? kq * 512 + lane * 4 : (int)0x80000000;
  {
    constexpr int NG = 12;
    static constexpr int g_t[NG] = {0, 0, 1, 1, 2, 2, 2, 1, 0, 0, 1, 2};
    static constexpr int g_q[NG] = {0, 1, 0, 1, 0, 1, 2, 2, 2, 3, 3, 3};
    constexpr int G14 = 1, G15 = 3, GP0 = 5, GP1 = 6;
    constexpr auto chp = [](int g) { return g == 0 || g_t[g] != g_t[g - 1]; };
    constexpr auto chn = [](int g) { return g + 1 < NG && g_t[g + 1] != g_t[g]; };
    static_assert(n_off(13) == 32768 && n_off(14) == 65536 && n_off(15) == 0, "");
    unsigned an2[2];
#pragma unroll
    for (int kk = 0; kk < 2; ++kk) {
      an[kk] = bn[kk]; an2[kk] = bn[kk] + 65536; am[kk] = bm[kk];
      asm volatile("" : "+v"(an[kk]), "+v"(an2[kk]), "+v"(am[kk]));
    }
    int vo;
    u32x4 pk;
    u32x2 kv;
    auto publish = [&](int d, int h) __attribute__((always_inline)) {
      if (h == 0) { vo = ml; PINV(vo); vo += (((d + kq + 1) & 3) * 4 + kq) * 4 * MSG; PINV(vo); }
      const f32x4 v = acc[2 * d + (h >> 2)][h & 3];
      pk[(h & 1) * 2] = pk2(v[0], v[1]); pk[(h & 1) * 2 + 1] = pk2(v[2], v[3]);
      if (h & 1) __builtin_amdgcn_raw_buffer_store_b128(pk, rs, vo + (h >> 1) * 1024, 0, 16);
    };
#define MSG_FLAG(d) __builtin_amdgcn_raw_buffer_store_b32(tag, rc, fo + (((d) + kq + 1) & 3) * 2048, 0, 16)
    auto rdn = [&](int g, int r) __attribute__((always_inline)) {
      const int t = g_t[g], jt = g_q[g] * 2 + (r >> 1);
      fn[g & 1][r] = *LDSP(const bf16x8, (t == 1 ? an2[r & 1] : an[r & 1]) + (t == 0 ? 32768 : 0) + jt * 2048);
    };
    auto rdm = [&](int t, int i, int kk) __attribute__((always_inline)) { fm[kk][i] = *LDSP(const bf16x8, am[kk] + m_off(13 + t) + TO(i)); };
    auto group = [&](auto GI) __attribute__((always_inline)) {
      constexpr int gi = decltype(GI)::value;
      if (gi == G14) { BAR();
        SB(); }
      if (gi == G15) {
        PAGE(11);
        BAR();
        SB();
        kv = *(const u32x2*)(smem + LDS_SEL);
        SB();
      }
#pragma unroll
      for (int q = 0; q < 16; ++q) {
        const int n = gi * 16 + q;
        if (n == 112) {
        }
        if (n == 152) {
        }
        if (n == 168) { WAIT_VM(8); MSG_FLAG(0); SB(); }
        mm(chp(gi) || chn(gi), gi & 1, g_q[gi], q, 2);
        constexpr bool ni = gi + 1 < NG && (chp(gi + 1 < NG ? gi + 1 : gi) || chn(gi + 1 < NG ? gi + 1 : gi));
        auto nr = [](int r) { return ni && r >= 2 ? 5 - r : r; };
        if constexpr (chp(gi) && chn(gi)) {
          if (q < 4) rdm(g_t[gi], 2 + (q >> 1), q & 1); else if (q < 8) rdn(gi + 1, nr(q - 4)); else if (q < 12) rdm(g_t[gi + 1], (q - 8) >> 1, q & 1);
        } else if constexpr (chp(gi)) {
          if (q < 4) rdm(g_t[gi], 2 + (q >> 1), q & 1);
          else if (gi + 1 < NG && q < 12 && !(q & 1)) rdn(gi + 1, nr((q - 4) >> 1));
        } else if constexpr (chn(gi)) {
          if (!(q & 1)) { if (q < 8) rdn(gi + 1, nr(q >> 1)); else rdm(g_t[gi + 1], (q - 8) >> 2, (q >> 1) & 1); }
        } else {
          if (!(q & 1) && q < 8) rdn(gi + 1, nr(q >> 1));
        }
        if (gi == GP0 && q < 8) publish(0, q);
        if (gi == GP1 && q < 8) publish(1, q);
        if (gi == 9 && q < 8) publish(2, q);
        if (n == 33 || n == 37 || n == 41) xq_load((n - 33) >> 2);
        if (n == 72) { key[0] = __builtin_amdgcn_readfirstlane(kv.x); key[1] = __builtin_amdgcn_readfirstlane(kv.y); }
        SB();
      }
    };
#define GR4(i) group(IC<i>{}); group(IC<i + 1>{}); group(IC<i + 2>{}); group(IC<i + 3>{});
    GR4(0) GR4(4) GR4(8)
    asm volatile("s_nop 7");
    SB();
  }
#pragma unroll
  for (int i = 0; i < 4; ++i) *(f32x4*)(smem + LDS_ACC + wr * 16384 + wc * 4096 + i * 1024 + lane * 16) = acc[7][i];
#pragma unroll
  for (int i = 0; i < 4; ++i) ya[i] = acc[6][i];
  asm volatile("s_waitcnt lgkmcnt(0)" ::: "memory");
  if (lane == 0) sig[0] = 2;
  SB();
  WAIT_VM(5); MSG_FLAG(1); SB();
  WAIT_VM(2); MSG_FLAG(2); fx = bld4_sc1(rc, po); SB();
  wait_xq<4>(xq, sq);
  far_calc();
  cv_calc();
  asm volatile("s_waitcnt vmcnt(0)" : "+v"(fx) :: "memory");
  seen = (unsigned)__ballot(fx == tag);
  }

  PAGE(6);
  if (ha) {
    int it = 0;
    while (sig[0] != 2 && it < SPIN_MAX * 8) { __builtin_amdgcn_s_sleep(1); ++it; }
    asm volatile("" ::: "memory");
#pragma unroll
    for (int i = 0; i < 4; ++i) ya[i] = *(const f32x4*)(smem + LDS_ACC + (wid & 2) * 8192 + (wid & 1) * 4096 + i * 1024 + lane * 16);
  }
  u32x4 pp[3][2];
  unsigned e_lo = 0x00003f80u, e_hi = 0x3f800000u;
  asm volatile("" : "+v"(e_lo), "+v"(e_hi));
  __amdgpu_buffer_rsrc_t rs_ = rs, rc_ = rc;
  int mo[3] = {MSG_IDX(0) * 4 * MSG, MSG_IDX(1) * 4 * MSG, MSG_IDX(2) * 4 * MSG}, mv = ml + ha * 2048, mv2 = mv + 1024;
  asm volatile("" : "+s"(rs_), "+s"(rc_), "+s"(mo[0]), "+s"(mo[1]), "+s"(mo[2]));
  asm volatile("" : "+v"(mv), "+v"(mv2));
#define MSG_POLL(m) do { \
    if (!((seen >> (m)) & 1)) { \
      unsigned pv_, it_ = SPIN_MAX; \
      asm volatile("1:\n\tbuffer_load_dword %0, %3, %4, 0 offen sc1\n\ts_waitcnt vmcnt(0)\n\tv_cmp_eq_u32 vcc, %5, %0\n\ts_mov_b32 %1, vcc_lo\n\t" \
                   "s_bitcmp1_b32 %1, %6\n\ts_cbranch_scc1 2f\n\ts_sub_u32 %2, %2, 1\n\ts_cbranch_scc1 2f\n\ts_sleep 1\n\ts_branch 1b\n2:" \
                   : "=&v"(pv_), "+s"(seen), "+s"(it_) : "v"(po), "s"(rc_), "s"(tag), "i"(m) : "vcc", "scc", "memory"); \
    } \
    pp[m][0] = __builtin_amdgcn_raw_buffer_load_b128(rs_, mv, mo[m], 16); \
    pp[m][1] = __builtin_amdgcn_raw_buffer_load_b128(rs_, mv2, mo[m], 16); } while (0)
#define MSG_ADD(m) do { \
    _Pragma("unroll") for (int u = 0; u < 8; ++u) { \
      ya[u >> 1][(u & 1) * 2] = add_bf(pp[m][u >> 2][u & 3], e_lo, ya[u >> 1][(u & 1) * 2]); ya[u >> 1][(u & 1) * 2 + 1] = add_bf(pp[m][u >> 2][u & 3], e_hi, ya[u >> 1][(u & 1) * 2 + 1]); } } while (0)
  MSG_POLL(2); MSG_POLL(1); MSG_POLL(0);
  SB();
  if (__builtin_expect(far0 == 0 || far1 == 0, 0)) {
    asm volatile("s_branch 2f\nk_pg12:\n\ts_setpc_b64 s[42:43]\n2:");
#pragma unroll 1
    for (int c = 0; c < 2; ++c) {
      if (c ? far1 : far0) continue;
      const int ic = (0xffff - (c ? key[1] : key[0])) & (S_ - 1);
      lds_vi cnt = (lds_vi)(smem + LDS_SEL + 24 + c * 4);
      int it = 0;
      while (cnt[0] != 4 && it < SPIN_MAX * 8) { __builtin_amdgcn_s_sleep(1); ++it; }
      asm volatile("" ::: "memory");
      const float* q = (const float*)(smem + LDS_FIN + c * 2048) + (wid & 1) * 64 + (fr >> 3) * 32 + col4(fr);
      const f32x4 v = (*(const f32x4*)q + *(const f32x4*)(q + 128)) + (*(const f32x4*)(q + 256) + *(const f32x4*)(q + 384));
#pragma unroll
      for (int r = 0; r < 4; ++r)
#pragma unroll
        for (int k = 0; k < 4; ++k) {
          cv[r * 4 + k] += (xo[r] >> 11) == ic ? v[k] : 0.f;
        }
    }
  }
#pragma unroll
  for (int e = 0; e < 16; ++e) ya[e & 3][e >> 2] += cv[e];
  asm volatile("" : "+v"(ya[0]), "+v"(ya[1]), "+v"(ya[2]), "+v"(ya[3]));
  SB();
  MSG_ADD(2); MSG_ADD(1);
  asm volatile("" : "+v"(ya[0]), "+v"(ya[1]), "+v"(ya[2]), "+v"(ya[3]));
  SB();
  MSG_ADD(0);
  {
#pragma unroll
    for (int r = 0; r < 4; ++r) *(u32x2*)((char*)out + (unsigned)xo[r]) = (u32x2){pk2(ya[0][r], ya[1][r]), pk2(ya[2][r], ya[3][r])};
  }
}

extern "C" void kernel_launch(void* const* d_in, const int* in_sizes, int n_in, void* d_out, int out_size,
                              void* d_ws, size_t ws_size, hipStream_t stream) {
  const u16* x = (const u16*)d_in[0];
  const u16* ws_re = (const u16*)d_in[1];
  const u16* ws_im = (const u16*)d_in[2];
  const u16* wd_re = (const u16*)d_in[3];
  const u16* wd_im = (const u16*)d_in[4];
  const u16* w_r = (const u16*)d_in[6];
  const u16* w_gate = (const u16*)d_in[7];
  const u16* w_up = (const u16*)d_in[8];
  const u16* w_down = (const u16*)d_in[9];
  k_main<<<256, 512, 0, stream>>>(x, wd_re, wd_im, w_r, (char*)d_ws, ws_re, ws_im, w_gate, w_up, w_down, (u16*)d_out);
}
```

```cpp
#include <hip/hip_runtime.h>
#include <hip/hip_bf16.h>
#include <stdint.h>
#include <type_traits>

extern "C" __device__ unsigned long long llvm_dispatch_id(void) __asm("llvm.amdgcn.dispatch.id");
extern "C" __device__ float llvm_fmul_legacy(float, float) __asm("llvm.amdgcn.fmul.legacy");
typedef unsigned short u16;
typedef unsigned long long u64;
typedef __attribute__((ext_vector_type(8))) short bf16x8;
typedef __attribute__((ext_vector_type(4))) unsigned u32x4;
typedef __attribute__((ext_vector_type(2))) unsigned u32x2;
typedef __attribute__((ext_vector_type(4))) float f32x4;
typedef __attribute__((ext_vector_type(2))) float f32x2;
typedef __attribute__((ext_vector_type(2))) __bf16 bf16x2;
typedef __attribute__((address_space(3))) void* lds_vp;
typedef volatile __attribute__((address_space(3))) int* lds_vi;

constexpr int S_ = 2048, D_ = 1024, F_ = 3584;
template <int N> using IC = std::integral_constant<int, N>;

static __device__ __forceinline__ float bf2f(u16 u) { return __uint_as_float((unsigned)u << 16); }
static __device__ __forceinline__ u16 f2bf(float f) {
  __hip_bfloat16 b = __float2bfloat16(f);
  return *reinterpret_cast<u16*>(&b);
}
static __device__ __forceinline__ float rbf(float f) { return bf2f(f2bf(f)); }
static __device__ __forceinline__ float lo_bf(unsigned u) { return __uint_as_float(u << 16); }
static __device__ __forceinline__ float hi_bf(unsigned u) { return __uint_as_float(u & 0xffff0000u); }
static __device__ __forceinline__ unsigned pk2(float a, float b) {
  const bf16x2 v = __builtin_convertvector((f32x2){a, b}, bf16x2);
  return __builtin_bit_cast(unsigned, v);
}
static __device__ __forceinline__ float add_bf(unsigned u, unsigned e, float c) {
  return __builtin_amdgcn_fdot2_f32_bf16(__builtin_bit_cast(bf16x2, u), __builtin_bit_cast(bf16x2, e), c, false);
}
static __device__ __forceinline__ float ln_elem(float x, float mb, float sd, float ri) {
  const float d = rbf(x - mb);
  const float q0 = d * ri;
  return rbf(__builtin_fmaf(__builtin_fmaf(-q0, sd, d), ri, q0));
}
template <int CTRL> static __device__ __forceinline__ float dpp_f(float v) {
  return __int_as_float(__builtin_amdgcn_update_dpp(0, __float_as_int(v), CTRL, 0xf, 0xf, true));
}
template <int CTRL> static __device__ __forceinline__ unsigned dpp_u(unsigned v) {
  return (unsigned)__builtin_amdgcn_update_dpp(0, (int)v, CTRL, 0xf, 0xf, true);
}
static __device__ __forceinline__ float row_sum(float v) {
  v += dpp_f<0xB1>(v);
  v += dpp_f<0x4E>(v);
  v += dpp_f<0x141>(v);
  v += dpp_f<0x140>(v);
  return v;
}
static __device__ __forceinline__ float fold32(float a, float b) {
  const auto r = __builtin_amdgcn_permlane32_swap(__float_as_uint(a), __float_as_uint(b), false, false);
  return __uint_as_float(r[0]) + __uint_as_float(r[1]);
}
static __device__ __forceinline__ float fold16(float a, float b) {
  const auto r = __builtin_amdgcn_permlane16_swap(__float_as_uint(a), __float_as_uint(b), false, false);
  return __uint_as_float(r[0]) + __uint_as_float(r[1]);
}
static __device__ __forceinline__ float rdl(float v, int l) { return __int_as_float(__builtin_amdgcn_readlane(__float_as_int(v), l)); }

#define ST_JB 9
#define LNS 2
#define WSN 11
#define XJ 10
#define FSM 0xa2
#define GV_JB 7
#define DG 11
#define AB_STRIDE 32
#define SPIN_MAX (1 << 17)
#define VA0 5
#define VAN 4
#define SP0 3
#define SP1 7
#define SP1E 11
#define SPS 0x0000eeeeeeee00
#define DVS 0x00000400300000
#define GVS 0x00000022000000
constexpr int dig(unsigned long long t, int q) { return q < 0 ? 0 : (int)((t >> (4 * q)) & 15); }
constexpr int acc_dig(unsigned long long t, int q) { int a = 0; for (int i = 0; i < q; ++i) a += dig(t, i); return a; }

static __device__ __forceinline__ unsigned lds_addr(const void* p) { return (unsigned)(size_t)(lds_vp)p; }

constexpr int MSG = 4096;
constexpr size_t WS_ABT   = 0;
constexpr size_t WS_SLAB  = WS_ABT + (size_t)D_ * 4096 * 2;
constexpr size_t WS_GU    = WS_SLAB + (size_t)64 * 64 * MSG;
constexpr size_t WS_DN    = WS_GU + (size_t)8 * F_ * 16;
constexpr size_t WS_FLAGS = WS_DN + (size_t)2 * 8 * 32 * 64 * 8;
constexpr int FL_X = 0;
constexpr int FL_AB = 64 * 64 * 32;
constexpr int FL_ST = FL_AB + 2048 * AB_STRIDE;
constexpr int FL_CE = FL_ST + 4096;
constexpr int FL_KEY = FL_CE + 4096;
constexpr int FL_EPOCH = FL_KEY + 4096;
constexpr int WS_MB = (int)((WS_FLAGS + (FL_EPOCH + 1) * 4 + 1048575) >> 20);

#define WAIT_VM(n) asm volatile("s_waitcnt vmcnt(" #n ")" ::: "memory")
template <int N> static __device__ __forceinline__ void wait_vm() { asm volatile("s_waitcnt vmcnt(%0)" :: "i"(N) : "memory"); }
#define BAR_LGKM() asm volatile("s_waitcnt lgkmcnt(0)\n\ts_barrier" ::: "memory")
#define BAR() asm volatile("s_barrier" ::: "memory")
#define SB() __builtin_amdgcn_sched_barrier(0)
#define PAGE(n) asm volatile("s_branch 2f\n\t.p2align 12\nk_pg" #n ":\n\ts_setpc_b64 s[42:43]\n2:")
#define CALL_PG(n) asm volatile("s_getpc_b64 s[40:41]\n3:\n\ts_mov_b32 s42, k_pg" #n "-3b\n\ts_ashr_i32 s43, s42, 31\n\ts_add_u32 s40, s40, s42\n\ts_addc_u32 s41, s41, s43\n\t" \
                                "s_swappc_b64 s[42:43], s[40:41]" ::: "s40", "s41", "s42", "s43", "scc", "memory")

static __device__ __forceinline__ __amdgpu_buffer_rsrc_t mk_rsrc(const void* p) {
  return __builtin_amdgcn_make_buffer_rsrc((void*)p, 0, 0x7fffffff, 0x00020000);
}
#define DMA_(rsrc, voff, mbase, MI, sbase, SI, pol) do { unsigned _t; \
    asm volatile("s_add_i32 m0, %2, %3\n\ts_add_i32 %0, %5, %6\n\tbuffer_load_dwordx4 %1, %4, %0 offen" pol " lds" \
                 : "=&s"(_t) : "v"(voff), "s"(mbase), "i"(MI), "s"(rsrc), "s"(sbase), "i"(SI) : "memory", "scc"); } while (0)
#define DMA_I(rsrc, voff, mbase, MI, SI) \
    asm volatile("s_add_i32 m0, %1, %2\n\ts_nop 0\n\tbuffer_load_dwordx4 %0, %3, 0 offen offset:%4 lds" \
                 :: "v"(voff), "s"(mbase), "i"((MI) - (SI)), "s"(rsrc), "i"(SI) : "memory", "scc")
#define DMA_S(rsrc, voff, mbase, MI, soff) \
    asm volatile("s_add_i32 m0, %1, %2\n\ts_nop 0\n\tbuffer_load_dwordx4 %0, %3, %4 offen lds" \
                 :: "v"(voff), "s"(mbase), "i"(MI), "s"(rsrc), "s"(soff) : "memory", "scc")
#define TOUCH(rsrc, voff, mbase, MI, soff) \
    asm volatile("s_add_i32 m0, %1, %2\n\ts_nop 0\n\tbuffer_load_dword %0, %3, %4 offen lds" \
                 :: "v"(voff), "s"(mbase), "i"(MI), "s"(rsrc), "s"(soff) : "memory", "scc")
template <int OFF> static __device__ __forceinline__ u32x4 ld16_s(const void* p, int voff) {
  u32x4 v;
  asm volatile("global_load_dwordx4 %0, %1, %2 offset:%3" : "=v"(v) : "v"(voff), "s"(p), "i"(OFF) : "memory");
  return v;
}
template <int OFF> static __device__ __forceinline__ void sld(unsigned& sink, const void* p, int soff) {
  asm volatile("s_load_dword %0, %1, %2 offset:%3" : "+s"(sink) : "s"(p), "s"(soff), "i"(OFF) : "memory");
}
static __device__ __forceinline__ unsigned gld4_s(const void* p, unsigned voff) {
  unsigned v;
  asm volatile("global_load_dword %0, %1, %2" : "=v"(v) : "v"(voff), "s"(p) : "memory");
  return v;
}
static __device__ __forceinline__ u32x2 bld8_sc1(__amdgpu_buffer_rsrc_t r, unsigned voff) {
  u32x2 v;
  asm volatile("buffer_load_dwordx2 %0, %1, %2, 0 offen sc1" : "=v"(v) : "v"(voff), "s"(r) : "memory");
  return v;
}
template <int N> static __device__ __forceinline__ void wait_xs(unsigned& a, u32x2& b) { asm volatile("s_waitcnt vmcnt(%2)" : "+v"(a), "+v"(b) : "i"(N) : "memory"); }
template <int N> static __device__ __forceinline__ void wait_xq(u32x4 (&a)[2], u32x2& b) { asm volatile("s_waitcnt vmcnt(%3)" : "+v"(a[0]), "+v"(a[1]), "+v"(b) : "i"(N) : "memory"); }
static __device__ __forceinline__ u32x2 ld8_s_sc1(const void* p, int voff) {
  u32x2 v;
  asm volatile("global_load_dwordx2 %0, %1, %2 sc1" : "=v"(v) : "v"(voff), "s"(p) : "memory");
  return v;
}
static __device__ __forceinline__ void lgkm0(unsigned& sink) { asm volatile("s_waitcnt lgkmcnt(0)" : "+s"(sink) :: "memory"); }
template <typename T> static __device__ __forceinline__ void pin4(T& a, T& b, T& c, T& d) { asm volatile("" : "+s"(a), "+s"(b), "+s"(c), "+s"(d)); }
template <typename T, typename U> static __device__ __forceinline__ void pins(T& a, U& b) { asm volatile("" : "+s"(a), "+s"(b)); }
static __device__ __forceinline__ void launder(int& v, int& a, int& b) { asm volatile("" : "+v"(v)); asm volatile("" : "+s"(a), "+s"(b)); }
static __device__ __forceinline__ unsigned bld4_sc1(__amdgpu_buffer_rsrc_t r, int voff) {
  unsigned v;
  asm volatile("buffer_load_dword %0, %1, %2, 0 offen sc1" : "=v"(v) : "v"(voff), "s"(r) : "memory");
  return v;
}
static __device__ __forceinline__ u32x4 bld16_nt(__amdgpu_buffer_rsrc_t r, int voff, int soff) {
  u32x4 v;
  asm volatile("buffer_load_dwordx4 %0, %1, %2, %3 offen nt" : "=v"(v) : "v"(voff), "s"(r), "s"(soff) : "memory");
  return v;
}
static __device__ __forceinline__ void bld16_nt_io(u32x4& v, __amdgpu_buffer_rsrc_t r, int voff, int soff) {
  asm volatile("buffer_load_dwordx4 %0, %1, %2, %3 offen nt" : "+v"(v) : "v"(voff), "s"(r), "s"(soff) : "memory");
}
static __device__ __forceinline__ u32x4 bld16_sc1(__amdgpu_buffer_rsrc_t r, int voff, int soff) {
  u32x4 v;
  asm volatile("buffer_load_dwordx4 %0, %1, %2, %3 offen sc1" : "=v"(v) : "v"(voff), "s"(r), "s"(soff) : "memory");
  return v;
}
static __device__ __forceinline__ u32x2 granule_wait(const u64* p, unsigned tag) {
  u64 v = 0;
#pragma unroll 1
  for (int it = 0; it < SPIN_MAX; ++it) {
    v = __hip_atomic_load(p, __ATOMIC_RELAXED, __HIP_MEMORY_SCOPE_AGENT);
    if (__all((unsigned)(v >> 32) == tag)) break;
    __builtin_amdgcn_s_sleep(2);
  }
  return (u32x2){(unsigned)v, (unsigned)(v >> 32)};
}
static __device__ __forceinline__ float key_score(unsigned key) {
  const unsigned u = key >> 16;
  return bf2f((u16)((u & 0x8000u) ? (u & 0x7fffu) : (~u & 0xffffu)));
}
static __device__ __forceinline__ unsigned med3u(unsigned a, unsigned b, unsigned c) { return max(min(a, b), min(max(a, b), c)); }
static __device__ __forceinline__ void merge2(unsigned& a1, unsigned& a2, unsigned b1, unsigned b2) {
  const unsigned lo = min(a1, b1);
  a1 = max(a1, b1);
  a2 = max(lo, max(a2, b2));
}
static __device__ __forceinline__ float sqdev16(const u32x4 (&v)[2], float mb) {
  f32x2 q2 = {0.f, 0.f};
#pragma unroll
  for (int i = 0; i < 8; ++i) {
    const f32x2 e = (f32x2){lo_bf(v[i >> 2][i & 3]), hi_bf(v[i >> 2][i & 3])} - mb;
    q2 += e * e;
  }
  return q2.x + q2.y;
}

constexpr int lrw(int r) { return (r & ~6) | ((r & 2) << 1) | ((r & 4) >> 1); }
constexpr int swz(int r) { return ((r >> 1) & 4) | (r & 2); }
static __device__ __forceinline__ int sym_dn(int p) { return (p ^ (p >> 1)) & 1; }
static __device__ __forceinline__ int sym_hi(int n, int w, int st, int b, int z, int p) {
  return ((p >> 1) * (n / 2 * st) + b * (w * st)) ^ (-sym_dn(p) & (((n - 1) ^ (z * (n / 2))) * st));
}
static __device__ __forceinline__ int sym_lo(int w, int st, int p, int c) { return ((c - sym_dn(p)) * st) & ((w - 1) * st); }
static __device__ __forceinline__ int sym_row(int n, int w, int st, int b, int z, int p, int c) { return sym_lo(w, st, p, c) ^ sym_hi(n, w, st, b, z, p); }
constexpr int G1_NST = 4, G1_NKT = 16, G1_STAGE = 32768;
constexpr int G2_NKT = 16, G2_MRING = 98304;
constexpr int n_off(int k) { return (k % 3) * 32768; }
constexpr int m_off(int k) { return ((k + 2) % 3) * 16384; }
constexpr int k_ord(int k) { return k < 2 ? k : (k < 9 ? 2 * (k - 1) : 2 * (k - 8) + 1); }
constexpr int LDS_DUMMY = G2_MRING + 49152;
constexpr int LDS_GU = LDS_DUMMY + 4096;
constexpr int LDS_FIN = LDS_GU + 4096;
constexpr int LDS_MB = LDS_FIN + 4096;
constexpr int LDS_SEL = LDS_MB + 1536;
constexpr int LDS_SIG = LDS_SEL + 64;
constexpr int LDS_KEY = LDS_SIG + 32;
constexpr int LDS_BYTES = LDS_KEY + 128;
constexpr int LDS_ACC = 65536 + 8192;
constexpr int NG = 16;
constexpr int g_t[NG] = {0, 2, 1, 3, 3, 1, 2, 0, 0, 2, 1, 3, 3, 1, 2, 0};
constexpr int g_n[NG] = {0, 4096, 32768, 36864, 65536, 69632, 73728, 77824, G2_MRING + m_off(11), G2_MRING + m_off(11) + 4096, 0, 4096, 32768, 36864, 65536, 69632};
constexpr int g_h(int g) { return g == 8 || g == 9 ? 8192 : 16384; }
static_assert(n_off(12) == 0 && n_off(13) == 32768, "");

constexpr int sp_tile(int q) { return q < VA0 ? q : q + VAN < 16 ? q + VAN : -1; }
constexpr int dn_cnt(int q) { return dig(DVS, q); }
constexpr int dn_first(int q) { return acc_dig(DVS, q); }

static __device__ __forceinline__ void code_touch(unsigned& sink, unsigned off) {
  asm volatile("s_getpc_b64 s[40:41]\n\tk_main_rt:\n\ts_min_u32 %1, %1, k_main_end-k_main_rt-196\n\t"
               "s_load_dword %0, s[40:41], %1 offset:0\n\ts_load_dword %0, s[40:41], %1 offset:64\n\ts_load_dword %0, s[40:41], %1 offset:128\n\ts_load_dword %0, s[40:41], %1 offset:192"
               : "+s"(sink), "+s"(off) :: "s40", "s41", "scc", "memory");
}
static __device__ __forceinline__ void touch1(__amdgpu_buffer_rsrc_t r, int voff, unsigned mb, int soff) { TOUCH(r, voff, mb, LDS_DUMMY, soff); }
typedef const __attribute__((address_space(1))) u16* gptr;
typedef __attribute__((ext_vector_type(8))) u64 u64x8;
typedef __attribute__((ext_vector_type(2))) u64 u64x2;
extern "C" __global__ __launch_bounds__(512, 2) void k_main(const u16* x, const u16* wd_re, const u16* wd_im, const u16* w_r,
                                                 char* wsp, const u16* ws_re, const u16* ws_im,
                                                 const u16* w_gate, const u16* w_up, const u16* w_down, u16* out) {
  __shared__ __attribute__((aligned(16))) char smem[LDS_BYTES];
  u64x8 ka;
  u64x2 kb_;
  u64 kc;
  asm volatile("s_load_dwordx16 %0, %3, 0\n\ts_load_dwordx4 %1, %3, 64\n\ts_load_dwordx2 %2, %3, 80"
               : "=&s"(ka), "=&s"(kb_), "=&s"(kc) : "s"(__builtin_amdgcn_kernarg_segment_ptr()) : "memory");
  const int tid = threadIdx.x, lane = tid & 63;
  const int wid = __builtin_amdgcn_readfirstlane(tid >> 6);
  const int g = blockIdx.x, xcd = ((~g & 1) << 2) | ((g >> 1) & 3), j = g >> 3;
  if (wid < 4) {
    unsigned cw, co = (j * 4 + wid) * 448;
    asm volatile(".subsection 1\nk_main_end:\n.subsection 0\n\ts_min_u32 %1, %1, k_main_end-k_main-480\n\ts_getpc_b64 s[40:41]\n\ts_add_u32 s40, s40, %1\n\ts_addc_u32 s41, s41, 0\n\t"
                 "s_load_dword %0, s[40:41], 0\n\ts_load_dword %0, s[40:41], 64\n\ts_load_dword %0, s[40:41], 128\n\t"
                 "s_load_dword %0, s[40:41], 192\n\ts_load_dword %0, s[40:41], 256\n\ts_load_dword %0, s[40:41], 320\n\ts_load_dword %0, s[40:41], 384\n\ts_waitcnt lgkmcnt(0)"
                 : "=&s"(cw), "+s"(co) :: "s40", "s41", "scc", "memory");
  }
  const int pw = wid - 4;
  const int row0 = (wid & 3) * 8 + lrw(lane >> 3);
  const int cm = ((lane & 7) ^ swz(row0)) * 16;
  int vm = ((row0 & 15) * 2 + (row0 >> 4)) * (D_ * 2) + cm;
  asm volatile("" : "+v"(vm));
  const int kq = xcd >> 1;
  const int tt = (xcd & 1) * 4 + (j >> 3);
  const int et = j & 7;
  const int z = kq >> 1, st = (kq & 1) * 8 + tt;
  const int lrow = (kq & 1) * 1024 + (xcd & 1) * 512 + z * 256 + j * 8 + pw * 2;
  const int drow = ((kq & 1) * 2 + (xcd & 1)) * 256 + j * 8 + pw * 2;
  unsigned o_m = st * 128 * D_ * 2, o_l = lrow * D_ * 2, o_d = drow * D_ * 2, mb0 = lds_addr(smem) + pw * 1024;
  int l16 = lane * 16;
  int vn[4];
#pragma unroll
  for (int q = 0; q < 4; ++q) vn[q] = sym_lo(32, D_ * 2, (row0 >> 2) & 3, q * 8 + (row0 >> 4) * 4 + (row0 & 3)) ^ (sym_hi(D_, 32, D_ * 2, et, z, (row0 >> 2) & 3) | cm);
  asm volatile("" : "+s"(o_m), "+s"(o_l), "+s"(o_d), "+s"(mb0));
  asm volatile("" : "+v"(l16), "+v"(vn[0]), "+v"(vn[1]), "+v"(vn[2]), "+v"(vn[3]));
  asm volatile("s_waitcnt lgkmcnt(0)" : "+s"(ka), "+s"(kb_), "+s"(kc) :: "memory");
  x = (const u16*)(gptr)ka[0]; wd_re = (const u16*)(gptr)ka[1]; wd_im = (const u16*)(gptr)ka[2]; w_r = (const u16*)(gptr)ka[3];
  wsp = (char*)(gptr)ka[4]; ws_re = (const u16*)(gptr)ka[5]; ws_im = (const u16*)(gptr)ka[6]; w_gate = (const u16*)(gptr)ka[7];
  w_up = (const u16*)(gptr)kb_[0]; w_down = (const u16*)(gptr)kb_[1]; out = (u16*)(gptr)kc;
  int* flags = (int*)(wsp + WS_FLAGS);
  u64* stat = (u64*)(flags + FL_ST);
  const u16* wdz = z ? wd_im : wd_re;
  const __amdgpu_buffer_rsrc_t rm = mk_rsrc(x);
  const __amdgpu_buffer_rsrc_t rn = mk_rsrc(wdz);
#define G1_X(kt, so, q) DMA_(rm, vm, mb0, (so) + (q) * 4096, o_m, (kt) * 128 + (((q) & 1) * 64 + ((q) >> 1) * 32) * D_ * 2, "")
#define G1_W(kt, so, q) DMA_I(rn, vn[q], mb0, (so) + 16384 + (q) * 4096, (kt) * 128)
#define G1_STAGE_TILE(kt, so, W) do { SB(); G1_X(kt, so, 0); W(0); G1_X(kt, so, 1); W(1); G1_X(kt, so, 2); W(2); G1_X(kt, so, 3); W(3); \
    G1_W(kt, so, 0); W(4); G1_W(kt, so, 1); W(5); G1_W(kt, so, 2); W(6); G1_W(kt, so, 3); W(7); SB(); } while (0)
#define G1_STAGE_A(kt, so, W) do { SB(); G1_X(kt, so, 0); W(0); G1_X(kt, so, 2); W(1); \
    G1_W(kt, so, 0); W(2); G1_W(kt, so, 1); W(3); G1_W(kt, so, 2); W(4); G1_W(kt, so, 3); W(5); W(6); W(7); SB(); } while (0)
#define G1_STAGE_B(kt, so) do { SB(); G1_X(kt, so, 1); G1_X(kt, so, 3); SB(); } while (0)
#define NOW(i)
  const int fr = lane & 15, fq = lane >> 4;
  const int ha = wid >> 2;
  int trow, xo[4], xa[3], po, ml;
  const char *prs, *prc;
  auto col4 = [](int f) __attribute__((always_inline)) { return ((f & 2) << 3 | (f & 4) << 1 | (f & 1) << 2) ^ (-((f >> 3) & 1) & 28); };
  auto seg0 = [&](int hf, int dn) __attribute__((always_inline)) { return (hf * 512 + et * 32) ^ (-dn & (D_ - 32)); };
  auto epi_setup = [&](int part) __attribute__((always_inline)) {
    int t_ = tt, l_ = lane;
    asm volatile("" : "+s"(t_));
    asm volatile("" : "+v"(l_));
    const int ts = ((wid >> 1) & 1) * 8 + kq * 2 + ha;
    if (part == 0) { xo[3] = (seg0(wid & 1, (l_ >> 3) & 1) + col4(l_)) * 2; asm volatile("" : "+v"(xo[3])); }
    if (part == 1) {
      xo[3] |= sym_hi(S_, 64, D_ * 2, t_, z, l_ >> 4);
      ml = (l_ * 16) ^ (-sym_dn(l_ >> 4) & (z * 768));
      asm volatile("" : "+v"(xo[3]), "+v"(ml));
    }
    if (part == 2) {
      const int xh = xo[3];
#pragma unroll
      for (int r = 0; r < 4; ++r) xo[r] = sym_lo(64, D_ * 2, l_ >> 4, ts * 4 + r) ^ xh;
      const bool hi = xo[0] & 8;
      xa[0] = (hi ? xo[2] : xo[0]) & ~15; xa[1] = (hi ? xo[3] : xo[1]) & ~15;
      xa[2] = (sym_lo(64, D_ * 2, l_ >> 4, ts * 4 + (l_ & 3)) ^ xh) >> 11 << 3;
      asm volatile("" : "+v"(xo[0]), "+v"(xo[1]), "+v"(xo[2]), "+v"(xo[3]), "+v"(xa[0]), "+v"(xa[1]), "+v"(xa[2]));
    }
    if (part == 3) {
      trow = t_ * 16 + ts;
      prs = wsp + WS_SLAB + (size_t)((t_ * 8 + et) * 64 + (wid & 3)) * MSG;
      prc = (const char*)(flags + FL_X + ((t_ * 8 + et) * 64 + (wid & 3)) * 32);
      asm volatile("" : "+s"(trow), "+s"(prs), "+s"(prc));
      po = l_ < 3 ? (kq * 4 + ((kq + 1 + l_) & 3)) * 512 : (int)0x80000000;
      asm volatile("" : "+v"(po));
    }
  };
  auto tok_tile = [](unsigned key) __attribute__((always_inline)) {
    const unsigned h = (0xffff - key) & 1023, m = -(h >> 9);
    return (((h ^ m) & 0x1c0) | (((h ^ m) - m) & 63)) >> 2;
  };
#define rs mk_rsrc(prs)
#define rc mk_rsrc(prc)
  lds_vi sig = (lds_vi)(smem + LDS_SIG + (wid & 3) * 4);
  unsigned tag;
  u32x4 xq[2];
  u32x2 sq;
  f32x4 ya[4];
  unsigned key[2];
  float cv[16];
  unsigned far0, far1;
  auto xq_load = [&](int n) __attribute__((always_inline)) {
    if (n < 2) xq[n] = ld16_s<0>(x, xa[n]); else sq = ld8_s_sc1(stat, xa[2]);
  };
#define PINV(a) asm volatile("" : "+v"(a))
  auto far_calc = [&]() __attribute__((always_inline)) {
    far0 = tok_tile(key[0]) ^ trow; far1 = tok_tile(key[1]) ^ trow;
    asm volatile("" : "+s"(far0), "+s"(far1));
  };
  auto cv_calc = [&]() __attribute__((always_inline)) {
    if (__builtin_expect(!__all(sq.y == tag), 0)) sq = granule_wait(stat + (xa[2] >> 3), tag);
    const bool hi = xo[0] & 8;
    const float mb_ = lo_bf(sq.x), ri_ = __builtin_amdgcn_rcpf(hi_bf(sq.x));
#pragma unroll
    for (int r = 0; r < 4; ++r) {
      const float mb = r == 0 ? dpp_f<0x00>(mb_) : r == 1 ? dpp_f<0x55>(mb_) : r == 2 ? dpp_f<0xAA>(mb_) : dpp_f<0xFF>(mb_);
      const float ri = r == 0 ? dpp_f<0x00>(ri_) : r == 1 ? dpp_f<0x55>(ri_) : r == 2 ? dpp_f<0xAA>(ri_) : dpp_f<0xFF>(ri_);
#pragma unroll
      for (int u = 0; u < 2; ++u) {
        const unsigned own = xq[r & 1][(r & 2) + u], oth = dpp_u<0xB1>(xq[r & 1][2 - (r & 2) + u]);
        const unsigned w = hi == (r >= 2) ? own : oth;
        cv[r * 4 + 2 * u] = __builtin_fmaf(lo_bf(w) - mb, ri, lo_bf(w));
        cv[r * 4 + 2 * u + 1] = __builtin_fmaf(hi_bf(w) - mb, ri, hi_bf(w));
        PINV(cv[r * 4 + 2 * u]); PINV(cv[r * 4 + 2 * u + 1]);
      }
    }
  };
#define MSG_IDX(m) (kq * 4 + ((kq + 1 + (m)) & 3))
  unsigned seen = 0;

  if (__builtin_expect(wid >= 4, 1)) {
    __builtin_amdgcn_s_setprio(3);
    G1_STAGE_TILE(0, 0, NOW);
    constexpr int O_ST = (FL_ST - FL_AB) * 4, O_CE = (FL_CE - FL_AB) * 4, O_KEY = (FL_KEY - FL_AB) * 4, O_EP = (FL_EPOCH - FL_AB) * 4;
    const __amdgpu_buffer_rsrc_t rf = mk_rsrc(wsp + WS_FLAGS + FL_AB * 4);
    u32x4 lv[2], lv2[2], lw[2], ld[2], ld2[2];
    unsigned epv;
    {
      const char* xrow = (const char*)x + o_l;
      const char* wdrow = (const char*)wdz + o_d;
      lv[0] = ld16_s<0>(xrow, l16); lv[1] = ld16_s<1024>(xrow, l16);
      lv2[0] = ld16_s<2048>(xrow, l16); lv2[1] = ld16_s<3072>(xrow, l16);
      ld[0] = ld16_s<0>(wdrow, l16); ld[1] = ld16_s<1024>(wdrow, l16);
      ld2[0] = ld16_s<2048>(wdrow, l16); ld2[1] = ld16_s<3072>(wdrow, l16);
      lw[0] = ld16_s<0>(w_r, l16); lw[1] = ld16_s<1024>(w_r, l16);
      asm volatile("buffer_load_dword %0, off, %1, %2 sc1" : "=v"(epv) : "s"(rf), "s"(O_EP) : "memory");
    }
    SB();
    const unsigned long long did = llvm_dispatch_id();
    const unsigned dmix = (unsigned)did * 0x85EBCA6Bu + (unsigned)(did >> 32) * 0xC2B2AE35u + 0x27D4EB2Fu;
    const bool moe = et != 7;
    unsigned epoch;
    const int ci = lane & 15, rq = lane >> 4, tk = (lane >> 3) & 1;
    int mat, cg, dq, d0;
    u32x4 wv[16];
    const u16 *pgu, *pn2;
    const char *pm2, *spb;
#define rw __builtin_amdgcn_make_buffer_rsrc((void*)pgu, 0, moe ? 0x7fffffff : 0, 0x00020000)
#define rn2 mk_rsrc(pn2)
#define rm2 mk_rsrc(pm2)
    int vw, vn2[8], vm2;
    auto k_blk = [&](int k) __attribute__((always_inline)) { return tt ^ (k_ord(k) >> 1); };
#define KROT(kk_) (k_blk(kk_) * 256 + (k_ord(kk_) & 1) * 128)
    int tv2, tv7;
    unsigned sd = 0;
    auto spf = [&](auto N) __attribute__((always_inline)) {
      constexpr int n = decltype(N)::value;
      if constexpr (n >= 0 && sp_tile(n >> 4) >= 0) sld<((n >> 1) & 7) * 4096 + (n & 1) * 64>(sd, spb, KROT(sp_tile(n >> 4)));
    };
    const int cg8 = et ^ ((et & 1) << 1), rgb = (g & 7) * 4 + (j >> 3);
    const __amdgpu_buffer_rsrc_t rdn = mk_rsrc(w_down);
    const __amdgpu_buffer_rsrc_t rgu = mk_rsrc(wsp + WS_GU);
    int vdn, vgu;
    u32x4 dv[7], gv[4];
    int abo, abq[3];
    unsigned lma;
    unsigned sel[2][4];
    float g0, g1;
    u32x4 kk[4];
    unsigned k1 = 0, k2 = 0;
    const int ko_ = O_KEY + pw * 4096;
#define MM(a, b, c) __builtin_amdgcn_mfma_f32_16x16x32_bf16(__builtin_bit_cast(bf16x8, a), __builtin_bit_cast(bf16x8, b), c, 0, 0, 0)
#define PIN1(a) asm volatile("" : "+v"(a))
#define PIN2(a, b) asm volatile("" : "+v"(a), "+v"(b))
#define PIN3(a, b, c) asm volatile("" : "+v"(a), "+v"(b), "+v"(c))
    {
      int gro, go;
      u32x2 gr;
      u32x4 oh[5];
      f32x4 sa4, gw[2], gx[2];
      float su, gs, ta, tb, sx, mbv;
      unsigned dk[4], kl;
      const int hb = lane >> 5;
#pragma unroll
      for (int l = 0; l < WSN; ++l) asm volatile("" : "=v"(wv[l]));
      auto diag = [&](const f32x4& m) __attribute__((always_inline)) {
        return __uint_as_float((__float_as_uint(m[0]) & dk[0]) | (__float_as_uint(m[1]) & dk[1]) | (__float_as_uint(m[2]) & dk[2]) | (__float_as_uint(m[3]) & dk[3]));
      };
      auto setup = [&](auto JB, auto GP) __attribute__((always_inline)) {
        constexpr int jb = decltype(JB)::value, gp = decltype(GP)::value;
        SB();
        {
          int ln = lane, jj = j, p_ = pw;
          launder(ln, jj, p_);
          const int c_ = ln & 15, r_ = ln >> 4, e_ = jj & 7, t_ = (xcd & 1) * 4 + (jj >> 3);
          if constexpr (jb == 1 && gp == 0) {
            pn2 = ((kq >> 1) ? ws_im : ws_re) + (kq & 1) * 1024;
            spb = (const char*)(pn2 + (sym_row(S_, 64, 1, t_, 0, e_ >> 1, 0) + (e_ & 1) * 32 + p_ * 8) * S_);
            pins(pn2, spb);
          }
          if constexpr (jb == 1 && gp >= 1 && gp < 6) {
            const unsigned o = c_ == gp - 1 ? 0x3f803f80u : 0u;
            oh[gp - 1] = (u32x4){o, o, o, o};
            PIN1(oh[gp - 1]);
          }
          if constexpr (jb == 1 && gp == 6) {
            const int rk = moe ? e_ * 8 + (jj >> 3) * 2 + (xcd >> 2) : 0, r3 = ((rk - 14) * 43) >> 7, rr = rk - 14 - r3 * 3;
            const int c56 = rk < 14 ? rk * 4 + 1 : r3 * 4 + rr + ((rr + 1) >> 1);
            mat = (c56 + 36) >> 6; cg = c56 - mat * 28; dq = xcd & 3; d0 = dq * 256 + p_ * 64;
            pin4(mat, cg, dq, d0);
          }
          if constexpr (jb == 2 && gp == 0) {
            pgu = mat ? w_up : w_gate;
            pm2 = wsp + WS_ABT + (kq * 8 + e_) * 262144;
            pins(pgu, pm2);
            vw = ((d0 + r_ * 16) * F_ + cg * 128 + c_ * 8) * 2;
            PIN1(vw);
          }
          if constexpr (jb == 2 && (gp == 1 || gp == 2)) {
            dk[gp * 2 - 2] = c_ == r_ * 4 + gp * 2 - 2 ? ~0u : 0u; dk[gp * 2 - 1] = c_ == r_ * 4 + gp * 2 - 1 ? ~0u : 0u;
            PIN2(dk[gp * 2 - 2], dk[gp * 2 - 1]);
          }
          if constexpr (jb == 2 && gp == 3) { kl = lrow + (ln >> 5); go = (ln & 30) ? (int)0x80000000 : ((ln & 1) ? O_KEY : O_ST) + (int)kl * 8; PIN2(kl, go); }
          if constexpr (jb == 2 && gp == 4) { kl = 0xffff - kl; go = (ln & ~1) == 2 ? O_CE + (z * D_ + drow + (ln & 1)) * 8 : go; PIN2(kl, go); }
          if constexpr (jb == 2 && gp == 5) {
            gro = p_ < 2 ? O_ST + (st * 128 + p_ * 64 + ln) * 8 : O_CE + z * D_ * 8 + sym_row(D_, 32, 8, e_, z, (ln >> 2) & 3, (p_ - 2) * 16 + r_ * 4 + (ln & 3));
            lma = lds_addr(smem + LDS_MB) + (p_ < 2 ? p_ * 256 : 512 + p_ * 256) + ln * 4;
            PIN2(gro, lma);
          }
          if constexpr (jb == 7 && gp == 0) {
            vm2 = p_ * 1024 + ln * 16;
            PIN1(vm2);
          }
          if constexpr (jb == 2 && gp == 6) {
            const int r0 = p_ * 8 + lrw(ln >> 3);
            vn2[6] = sym_lo(64, S_ * 2, (r0 >> 2) & 3, (r0 >> 4) * 4 + (r0 & 3)); vn2[7] = sym_hi(S_, 64, S_ * 2, t_, z, (r0 >> 2) & 3) | (vm & 127);
            PIN2(vn2[6], vn2[7]);
          }
          if constexpr ((jb == 2 && gp == 7) || (jb == 7 && gp >= 6)) {
            constexpr int q0 = jb == 2 ? 0 : gp == 6 ? 3 : 6;
            const int lo = vn2[6], hi = vn2[7];
#pragma unroll
            for (int q = q0; q < q0 + 3 && q < 8; ++q) { vn2[q] = ((lo + ((q & 4) + ((q + kq + 1) & 3)) * 8 * S_ * 2) & (63 * S_ * 2)) ^ hi; PIN1(vn2[q]); }
          }
          if constexpr (jb == 7 && gp == 1) { vdn = rgb * 112 + p_ * 28 + r_ * 7; vgu = (((ln >> 3) & 1) * 4 * F_ + vdn + min(ln & 7, 6)) * 16; PIN2(vdn, vgu); }
          if constexpr (jb == 7 && gp == 2) { vdn = (vdn * D_ + cg8 * 128 + c_ * 8) * 2; PIN1(vdn); }
          if constexpr (jb == 7 && gp == 3) {
            abo = (((kq * 8 + e_) * 4 + p_) * 16 + c_) * AB_STRIDE * 4;
            abq[2] = ln >= 16 || (ln & 7) == t_ ? (int)0x80000000 : abo;
            abo = ln < 32 ? (((kq * 8 + e_) * 4 + p_) * 16 + t_) * AB_STRIDE * 4 + ln * 4 : (int)0x80000000;
            PIN2(abo, abq[2]);
          }
          if constexpr (jb == 7 && gp == 4) {
            abq[1] = ln >= 8 ? (int)0x80000000 : abq[2];
            abq[0] = ((ln ^ t_) & 4) ? (int)0x80000000 : abq[1];
            PIN2(abq[0], abq[1]);
          }
          if constexpr (jb == 8 && gp < 4) {
#pragma unroll
            for (int h = 0; h < 2; ++h) {
              const unsigned hv = h ? 0x0302u : 0x0100u;
              sel[h][gp] = ((ln & 7) >> 1) == gp ? ((ln & 1) ? (hv << 16) | 0x0c0cu : 0x0c0c0000u | hv) : 0x0c0c0c0cu;
            }
            PIN2(sel[0][gp], sel[1][gp]);
          }
        }
        SB();
      };
#define WS1(i) setup(IC<1>{}, IC<i>{})
#define WS2(i) setup(IC<2>{}, IC<i>{})
#define WS3(i) do { setup(IC<7>{}, IC<i>{}); if (i >= 4) setup(IC<8>{}, IC<(i) & 3>{}); spf(IC<(i < SP0 ? i : -1)>{}); } while (0)
      G1_STAGE_TILE(1, G1_STAGE, WS1);
      WAIT_VM(19); BAR();
      G1_STAGE_TILE(2, 2 * G1_STAGE, WS2);
      G1_STAGE_TILE(3, 3 * G1_STAGE, WS3);
      auto lnu = [&](auto U) __attribute__((always_inline)) {
        constexpr int jb = 1 + decltype(U)::value / 7, gp = decltype(U)::value % 7;
        SB();
        if constexpr (jb == 1) {
          const f32x4 z4 = {0.f, 0.f, 0.f, 0.f};
          if constexpr (gp == 0) { sa4 = MM(lv[0], oh[0], z4); gw[0] = MM(lv[0], lw[0], z4); gx[0] = MM(lv[0], lv[0], z4); PIN3(sa4, gw[0], gx[0]); }
          if constexpr (gp == 1) { sa4 = MM(lv2[0], oh[1], sa4); gw[1] = MM(lv2[0], lw[0], z4); gx[1] = MM(lv2[0], lv2[0], z4); PIN3(sa4, gw[1], gx[1]); }
          if constexpr (gp == 2) { sa4 = MM(lv[1], oh[0], sa4); gw[0] = MM(lv[1], lw[1], gw[0]); gx[0] = MM(lv[1], lv[1], gx[0]); PIN3(sa4, gw[0], gx[0]); }
          if constexpr (gp == 3) { sa4 = MM(lv2[1], oh[1], sa4); gw[1] = MM(lv2[1], lw[1], gw[1]); gx[1] = MM(lv2[1], lv2[1], gx[1]); PIN3(sa4, gw[1], gx[1]); }
          if constexpr (gp == 4) { sa4 = MM(ld[0], oh[2], sa4); sa4 = MM(ld[1], oh[2], sa4); PIN1(sa4); }
          if constexpr (gp == 5) { sa4 = MM(ld2[0], oh[3], sa4); sa4 = MM(ld2[1], oh[3], sa4); PIN1(sa4); }
          if constexpr (gp == 6) { sa4 = MM(lw[0], oh[4], sa4); sa4 = MM(lw[1], oh[4], sa4); PIN1(sa4);
            epoch = __builtin_amdgcn_readfirstlane(epv); tag = ((epoch + 1u) * 0x9E3779B1u ^ dmix) | 1u; }
        }
        if constexpr (jb == 2) {
          if constexpr (gp == 0) { su = (sa4[0] + sa4[1]) + (sa4[2] + sa4[3]); ta = diag(gw[0]); PIN2(su, ta); }
          if constexpr (gp == 1) { su = fold32(su, su); tb = diag(gw[1]); PIN2(su, tb); }
          if constexpr (gp == 2) { su = fold16(su, su); gs = fold32(ta, tb); PIN2(su, gs); }
          if constexpr (gp == 3) { ta = diag(gx[0]); tb = diag(gx[1]); PIN2(ta, tb); }
          if constexpr (gp == 4) { gs = fold16(gs, fold32(ta, tb)); PIN1(gs); }
          if constexpr (gp == 5) { gs += dpp_f<0xB1>(gs); gs += dpp_f<0x4E>(gs); PIN1(gs); }
          if constexpr (gp == 6) { gs += dpp_f<0x141>(gs); gs += dpp_f<0x140>(gs); PIN1(gs); }
        }
        if constexpr (jb == 3) {
          if constexpr (gp == 0) {
            const float s0 = rdl(su, 0), s1 = rdl(su, 1), q0 = rdl(gs, 16), q1 = rdl(gs, 48);
            sx = hb ? s1 : s0; ta = hb ? q1 : q0; PIN2(sx, ta);
          }
          if constexpr (gp == 1) {
            const float mean = sx * (1.0f / D_);
            mbv = rbf(mean);
            ta -= sx * mean;
            if (__builtin_expect(__any(!(sx * mean <= 16.f * ta)), 0)) {
              float q = fold32(sqdev16(lv, rdl(mbv, 0)), sqdev16(lv2, rdl(mbv, 32)));
              q = row_sum(fold16(q, q));
              const float dm = mean - mbv;
              ta = fmaxf(q - (float)D_ * dm * dm, 0.f);
            }
            PIN2(mbv, ta);
          }
          if constexpr (gp == 2) { ta = rbf(rbf(ta * (1.0f / D_)) + 1e-5f); gs -= mbv * rdl(su, 4); PIN2(ta, gs); }
          if constexpr (gp == 3) { ta = rbf(__builtin_amdgcn_sqrtf(ta)); tb = __builtin_amdgcn_rcpf(ta); PIN2(ta, tb); }
          if constexpr (gp == 4) {
            const unsigned rb = __float_as_uint(rbf(gs * tb));
            kl |= (rb ^ ((unsigned)((int)rb >> 31) | 0x80000000u)) & 0xffff0000u; PIN1(kl);
          }
          if constexpr (gp == 5) { kl = (lane & 2) ? __float_as_uint(su) : (lane & 1) ? kl : (__float_as_uint(mbv) >> 16) | __float_as_uint(ta); PIN1(kl); }
        }
        SB();
      };
      constexpr int LN_JB = 1 + (19 * LNS) / 8;
      auto work1 = [&](auto JB, auto GP) __attribute__((always_inline)) {
        constexpr int jb = decltype(JB)::value, gp = decltype(GP)::value, pos = (jb - 1) * 8 + gp;
        SB();
        if constexpr (pos % LNS == 0 && pos / LNS < 20) lnu(IC<pos / LNS>{});
        if constexpr (jb == ST_JB + 3) {
          if constexpr (gp == 0) { if (!__all(gr.y == tag)) gr = granule_wait((const u64*)((const char*)(flags + FL_AB) + gro), tag); PIN1(gr); }
          if constexpr (gp == 1) { g1 = __builtin_amdgcn_rcpf(__uint_as_float((gr.x & 0xffff0000u) ^ ((unsigned)z << 31))); PIN1(g1); }
          if constexpr (gp == 2) { g0 = pw < 2 ? -(lo_bf(gr.x) * g1) : __uint_as_float(gr.x); PIN1(g0); }
        }
        if constexpr (jb == 8 && (gp == 2 || gp == 5)) {
          static_assert(VAN == 4 && VA0 >= 2 && VA0 + VAN <= 9, "first halves");
          int ln = lane;
          PIN1(ln);
          const int i = (ln & 15) * 4 + pw, n = gp == 2 ? (moe ? 0 : 64) : (et < 2 ? 0 : 32);
          const int r0 = gp == 2 ? tt * 64 : sym_row(S_, 64, 1, tt, 0, et >> 1, 0) + (et & 1) * 32;
          (gp == 2 ? tv7 : tv2) = i < n ? (r0 + i) * (S_ * 2) + ((tt ^ (VA0 - 1 + (ln >> 4))) << 8) : (int)0x80000000;
          PIN1(gp == 2 ? tv7 : tv2);
        }
        if constexpr (gp < SP1 && jb <= SP1E) spf(IC<SP0 + (jb - 1) * SP1 + gp>{});
        SB();
      };
      auto step1 = [&](auto JB) __attribute__((always_inline)) {
        constexpr int jb = decltype(JB)::value;
        if constexpr (jb == 1) PAGE(1);
        if constexpr (jb == 7) PAGE(2);
        constexpr int n16 = jb == 12 ? 12 : jb == 11 ? 14 : 16;
        constexpr auto wl = [](int q) { return (q == LN_JB ? 1 : 0) + (q == ST_JB ? 1 : 0); };
        if constexpr (jb == 1) {
          asm volatile("s_waitcnt vmcnt(%11)" : "+v"(lv[0]), "+v"(lv[1]), "+v"(lw[0]), "+v"(lw[1]), "+v"(ld[0]), "+v"(ld[1]),
                       "+v"(lv2[0]), "+v"(lv2[1]), "+v"(ld2[0]), "+v"(ld2[1]), "+v"(epv) : "i"(16) : "memory");
        } else if constexpr (jb == ST_JB + 3) {
          asm volatile("s_waitcnt vmcnt(%1)" : "+v"(gr) : "i"(n16 + wl(jb - 2) + wl(jb - 1)) : "memory");
        } else wait_vm<n16 + wl(jb - 3) + wl(jb - 2) + wl(jb - 1)>();
        BAR();
        if constexpr (jb == 2) { if (pw == 0 && lane < 16) ((int*)(smem + LDS_SEL))[lane < 8 ? lane : lane + 8] = 0; }
        if constexpr (jb == 4) { if (pw == 0 && lane == 0) ((unsigned*)(smem + LDS_SEL))[5] = tag; }
#define W1(i) work1(JB, IC<i>{})
        if constexpr (jb < 10) G1_STAGE_TILE(jb - 1 + G1_NST, ((jb - 1) % G1_NST) * G1_STAGE, W1);
        else G1_STAGE_A(jb - 1 + G1_NST, ((jb - 1) % G1_NST) * G1_STAGE, W1);
        if constexpr (jb == 12) G1_STAGE_B(13, G1_STAGE);
        if constexpr (jb == LN_JB) __builtin_amdgcn_raw_buffer_store_b64((u32x2){kl, tag}, rf, go, 0, 16);
        if constexpr (jb == ST_JB) {
          asm volatile("buffer_load_dwordx2 %0, %1, %2, 0 offen sc1" : "=v"(gr) : "v"(gro), "s"(rf) : "memory");
        }
        if constexpr (jb <= 8) {
          if (et == jb - 1) {
#pragma unroll
            for (int l = 0; l < WSN; ++l) bld16_nt_io(wv[l], rw, vw, l * F_ * 2);
          }
        }
        SB();
      };
      static_assert(ST_JB + 3 <= 12 && ST_JB >= 6, "schedule");
      step1(IC<1>{}); step1(IC<2>{}); step1(IC<3>{}); step1(IC<4>{}); step1(IC<5>{}); step1(IC<6>{}); step1(IC<7>{}); step1(IC<8>{}); step1(IC<9>{}); step1(IC<10>{}); step1(IC<11>{}); step1(IC<12>{});
    }
#define G2_N(q, so) DMA_S(rn2, vn2[q], mb0, so, kb)
#define G2_STAGE_N(kk_, W) do { const int kb = KROT(kk_); constexpr int so = n_off(kk_); SB(); \
    G2_N(0, so); W(0); G2_N(1, so + 4096); W(1); G2_N(2, so + 8192); W(2); G2_N(3, so + 12288); W(3); \
    G2_N(4, so + 16384); W(4); G2_N(5, so + 20480); W(5); G2_N(6, so + 24576); W(6); G2_N(7, so + 28672); W(7); SB(); } while (0)
#define G2_STAGE_NH(kk_, h) do { const int kb = KROT(kk_); constexpr int so = n_off(kk_) + (h) * 16384; SB(); \
    G2_N((h) * 4, so); G2_N((h) * 4 + 1, so + 4096); G2_N((h) * 4 + 2, so + 8192); G2_N((h) * 4 + 3, so + 12288); SB(); } while (0)
#define G2_STAGE_Q(g, W, i) do { const int kb = KROT(12 + g_t[g]); SB(); G2_N((g) >> 2, g_n[g]); W(i); G2_N(4 + ((g) >> 2), g_n[g] + g_h(g)); W(i + 1); SB(); } while (0)
#define G2_STAGE_M2(kk_, W) do { const int kb = KROT(kk_) * 128; constexpr int so = n_off(kk_ - 2) + 8192; SB(); \
    DMA_(rm2, vm2, mb0, so, kb, 0, " sc1"); W(2); DMA_(rm2, vm2, mb0, so + 4096, kb, 4096, " sc1"); W(3); \
    DMA_(rm2, vm2, mb0, so + 16384, kb, 8192, " sc1"); W(4); DMA_(rm2, vm2, mb0, so + 20480, kb, 12288, " sc1"); W(5); SB(); } while (0)
#define G2_STAGE_M(kk_, W) do { const int kb = KROT(kk_) * 128; constexpr int so = G2_MRING + m_off(kk_); SB(); \
    DMA_(rm2, vm2, mb0, so, kb, 0, " sc1"); W(8); DMA_(rm2, vm2, mb0, so + 4096, kb, 4096, " sc1"); W(9); \
    DMA_(rm2, vm2, mb0, so + 8192, kb, 8192, " sc1"); W(10); DMA_(rm2, vm2, mb0, so + 12288, kb, 12288, " sc1"); W(11); SB(); } while (0)
    WAIT_VM(8); BAR();
    asm volatile("ds_write_b32 %0, %1" :: "v"(lma), "v"(g0) : "memory");
    if (pw < 2) asm volatile("ds_write_b32 %0, %1 offset:512" :: "v"(lma), "v"(g1) : "memory");
    u32x4 tv[4];
    unsigned ready = 0;
    auto need = [&](int k) __attribute__((always_inline)) {
      const int b = k_blk(k) + (k_ord(k) & 1) * 8;
#pragma unroll 1
      for (int it = 0; !((ready >> b) & 1) && it < SPIN_MAX; ++it) {
        ready |= (unsigned)__ballot(__builtin_amdgcn_raw_buffer_load_b32(rf, abq[k < 5 ? 0 : k < 9 ? 1 : 2], 0, 16) == tag);
        asm volatile("" ::: "memory");
      }
    };
    unsigned ksel, bad = 0;
    int isel;
    unsigned xw;
    auto bvec = [&](unsigned src, const unsigned (&sl)[4]) __attribute__((always_inline)) {
      return (u32x4){__builtin_amdgcn_perm(0, src, sl[0]), __builtin_amdgcn_perm(0, src, sl[1]),
                     __builtin_amdgcn_perm(0, src, sl[2]), __builtin_amdgcn_perm(0, src, sl[3])};
    };
#define BC8(v, p) (unsigned)__builtin_amdgcn_update_dpp(__builtin_amdgcn_update_dpp(0, (int)(v), 0x150 + (p), 0xf, 0x3, false), (int)(v), 0x158 + (p), 0xf, 0xc, false)
    float* lg = (float*)(smem + LDS_GU);
    f32x4 macc = {0.f, 0.f, 0.f, 0.f};
    u32x2 sv;
    unsigned hp, gbad;
    float s0, s1;
    auto work2 = [&](auto JB, auto GP) __attribute__((always_inline)) {
      constexpr int jb = decltype(JB)::value, gp = decltype(GP)::value;
      SB();
      if constexpr (jb == -1 && gp < 4) {
        constexpr int c = gp == 0 ? 0xB1 : gp == 1 ? 0x4E : gp == 2 ? 0x141 : 0x140;
        merge2(k1, k2, dpp_u<c>(k1), dpp_u<c>(k2));
        PIN2(k1, k2);
      }
      if constexpr (jb == -1 && gp == 4) {
        ksel = tk ? k2 : k1;
        isel = (0xffff - (int)(ksel & 0xffffu)) & (S_ - 1);
        k1 = (isel * D_ + d0 + rq * 16 + (lane & 7) * 2) * 2; k2 = O_ST + isel * 8;
        PIN1(ksel);
        xw = gld4_s(x, k1);
        sv = bld8_sc1(rf, k2);
      }
      if constexpr (jb == 0 && gp == 1) wait_xs<3 + 2 + 2>(xw, sv);
      if constexpr (jb == 0) {
        if constexpr (gp == 1) { if (!__all(sv.y == tag)) sv = granule_wait(stat + isel, tag); s0 = __builtin_amdgcn_rcpf(hi_bf(sv.x)); PIN1(s0); }
        if constexpr (gp == 2) { s1 = ln_elem(lo_bf(xw), lo_bf(sv.x), hi_bf(sv.x), s0); PIN1(s1); }
        if constexpr (gp == 3) { xw = pk2(s1, ln_elem(hi_bf(xw), lo_bf(sv.x), hi_bf(sv.x), s0)); PIN1(xw); }
      }
      if constexpr (jb == DG && gp < 7) { PIN1(dv[gp]); macc = MM(dv[gp], bvec(BC8(hp, gp), sel[0]), macc); PIN1(macc); }
      if constexpr ((jb == 0 && gp >= 4) || jb == 1) {
        constexpr int l = jb == 0 ? gp - 4 : gp + 4;
        if constexpr (!(l & 1)) hp = BC8(xw, l >> 1);
        PIN1(wv[l]);
        macc = MM(wv[l], bvec(hp, sel[l & 1]), macc);
        PIN2(macc, hp);
      }
      if constexpr (jb == GV_JB + 2) {
        if constexpr (gp == 0) { gbad = (gv[0].y ^ tag) | (gv[0].w ^ tag) | (gv[1].y ^ tag) | (gv[1].w ^ tag); PIN1(gbad); }
        if constexpr (gp == 1) { gbad |= (gv[2].y ^ tag) | (gv[2].w ^ tag) | (gv[3].y ^ tag) | (gv[3].w ^ tag); PIN1(gbad); }
        if constexpr (gp == 2) {
          s0 = ((__uint_as_float(gv[0].x) + __uint_as_float(gv[1].x)) + __uint_as_float(gv[2].x)) + __uint_as_float(gv[3].x);
          s1 = ((__uint_as_float(gv[0].z) + __uint_as_float(gv[1].z)) + __uint_as_float(gv[2].z)) + __uint_as_float(gv[3].z);
          PIN2(s0, s1);
        }
        if constexpr (gp == 3) {
#pragma unroll 1
          for (int it = 0; !__all(gbad == 0) && it < SPIN_MAX; ++it) {
            u32x4 q[4];
            gbad = 0;
#pragma unroll
            for (int i = 0; i < 4; ++i) { q[i] = __builtin_amdgcn_raw_buffer_load_b128(rgu, vgu, i * F_ * 16, 16); gbad |= (q[i].y ^ tag) | (q[i].w ^ tag); }
            s0 = ((__uint_as_float(q[0].x) + __uint_as_float(q[1].x)) + __uint_as_float(q[2].x)) + __uint_as_float(q[3].x);
            s1 = ((__uint_as_float(q[0].z) + __uint_as_float(q[1].z)) + __uint_as_float(q[2].z)) + __uint_as_float(q[3].z);
            asm volatile("" ::: "memory");
          }
          const unsigned gp2 = pk2(s0, s1);
          const unsigned up2 = dpp_u<0x128>(gp2);
          s0 = tk ? hi_bf(up2) : lo_bf(gp2); s1 = tk ? hi_bf(gp2) : lo_bf(up2);
          PIN2(s0, s1);
        }
        if constexpr (gp == 4) { s0 = rbf(s0 * __builtin_amdgcn_rcpf(1.f + __expf(-s0))); PIN1(s0); }
        if constexpr (gp == 5) { hp = pk2(s0 * s1, 0.f); PIN1(hp); }
      }
      if constexpr (jb == 10 && gp == 4) code_touch(sd, (j * 4 + pw) * 256);
      SB();
    };
#define W2M(i) work2(IC<-1>{}, IC<i>{})
#define W20(i) work2(IC<0>{}, IC<i>{})
#define W21(i) work2(IC<1>{}, IC<i>{})
    unsigned fs;
    {
      G1_STAGE_B(15, 3 * G1_STAGE);
      G1_STAGE_B(14, 2 * G1_STAGE);
      lgkm0(sd);
      BAR();
      G2_STAGE_NH(0, 0);
      WAIT_VM(6); BAR();
#pragma unroll
      for (int i = 0; i < 4; ++i) kk[i] = bld16_sc1(rf, l16, ko_ + i * 1024);
      G2_STAGE_NH(0, 1);
#pragma unroll
      for (int l = WSN; l < 16; ++l) wv[l] = bld16_nt(rw, vw, l * F_ * 2);
      touch1(rn2, tv7, mb0, 0);
      asm volatile("s_waitcnt vmcnt(%4)" : "+v"(kk[0]), "+v"(kk[1]), "+v"(kk[2]), "+v"(kk[3]) : "i"(16 - WSN + 5) : "memory");
#pragma unroll
      for (int i = 0; i < 4; ++i) {
        k2 = med3u(k1, k2, kk[i].x); k1 = max(k1, kk[i].x);
        k2 = med3u(k1, k2, kk[i].z); k1 = max(k1, kk[i].z);
        bad |= (kk[i].y ^ tag) | (kk[i].w ^ tag);
      }
#pragma unroll 1
      for (int it = 0; !__all(bad == 0) && it < SPIN_MAX; ++it) {
        k1 = 0; k2 = 0; bad = 0;
#pragma unroll
        for (int i = 0; i < 4; ++i) {
          const u32x4 q = __builtin_amdgcn_raw_buffer_load_b128(rf, l16, ko_ + i * 1024, 16);
          k2 = med3u(k1, k2, q.x); k1 = max(k1, q.x);
          k2 = med3u(k1, k2, q.z); k1 = max(k1, q.z);
          bad |= (q.y ^ tag) | (q.w ^ tag);
        }
        asm volatile("" ::: "memory");
      }
      merge2(k1, k2, dpp_u<0xB1>(k1), dpp_u<0xB1>(k2));
      merge2(k1, k2, dpp_u<0x4E>(k1), dpp_u<0x4E>(k2));
      merge2(k1, k2, dpp_u<0x141>(k1), dpp_u<0x141>(k2));
      merge2(k1, k2, dpp_u<0x140>(k1), dpp_u<0x140>(k2));
      if ((lane & 15) == 0) *(u32x2*)(smem + LDS_KEY + (pw * 4 + rq) * 8) = (u32x2){k1, k2};
      PAGE(3);
      wait_vm<17 - WSN>();
      BAR_LGKM();
#pragma unroll
      for (int jj = 0; jj < 4; ++jj) tv[jj] = *(const u32x4*)(smem + G2_MRING + m_off(0) + (jj * 4 + pw) * 1024 + lane * 16);
      { const u32x2 kp = *(const u32x2*)(smem + LDS_KEY + ci * 8); k1 = kp.x; k2 = kp.y; }
#pragma unroll
      for (int jj = 0; jj < 4; ++jj) __builtin_amdgcn_raw_buffer_store_b128(tv[jj], rm2, vm2 + jj * 4096 + 2 * tt * 16384, 0, 16);
      G2_STAGE_N(1, W2M);
      touch1(rn2, tv2, mb0, 0);
      WAIT_VM(11);
      __builtin_amdgcn_raw_buffer_store_b32(tag, rf, abo, 0, 16);
      {
        lds_vi rel = (lds_vi)(smem + LDS_SEL + 12);
        int it = 0;
        while (rel[0] != 4 && it < SPIN_MAX * 8) ++it;
        asm volatile("" ::: "memory");
      }
      G2_STAGE_N(2, W20);
      __builtin_amdgcn_s_sleep(2);
      WAIT_VM(10);
      fs = bld4_sc1(rf, abq[0]);
    }
    BAR();
#pragma unroll
    for (int jj = 0; jj < 4; ++jj) tv[jj] = *(const u32x4*)(smem + G2_MRING + m_off(1) + (jj * 4 + pw) * 1024 + lane * 16);
    constexpr int SPB = SP0 + SP1E * SP1;
    G2_STAGE_N(3, NOW);
    asm volatile("s_waitcnt vmcnt(8)" : "+v"(fs) :: "memory");
    ready = (unsigned)__ballot(fs == tag);
    need(2);
    G2_STAGE_M(2, NOW);
    need(3);
    G2_STAGE_M(3, NOW);
    fs = bld4_sc1(rf, abq[1]);
    W21(0); W21(1); W21(2); W21(3); W21(4); W21(5); W21(6); W21(7); W21(8); W21(9); W21(10); W21(11);
    asm volatile("" : "+v"(tv[0]), "+v"(tv[1]), "+v"(tv[2]), "+v"(tv[3]));
    auto step2 = [&](auto JB) __attribute__((always_inline)) {
      constexpr int jb = decltype(JB)::value;
      if constexpr (jb == 3) PAGE(4);
      if constexpr (jb == 9) PAGE(5);
      if constexpr (jb == 12) PAGE(7);
      constexpr auto must = [](int q) { return (q == 2 ? 4 : 0) + ((FSM >> q) & 1) + dig(GVS, q) + (q == XJ ? 3 : 0); };
      if constexpr (jb == 2) wait_vm<4 + must(1)>();
      else wait_vm<dn_cnt(jb - 2) + (jb == 5 ? 1 : 0) + 12 + must(jb - 1) + dn_cnt(jb - 1)>();
      if constexpr (jb == 3) { if (pw == 0 && (lane & ~8) == 0) ((unsigned*)(smem + LDS_SEL))[tk] = ksel; }
      if constexpr ((jb == 2 || jb == DG + 1)) {
        *(f32x4*)(lg + pw * 256 + lane * 4) = macc;
        macc = (f32x4){0.f, 0.f, 0.f, 0.f};
        lgkm0(sd);
      }
      BAR();
      float p0[4], p1[4];
      const int col = (pw & 1) * 64 + lane;
      if constexpr (jb == 3) {
        if (g == 0 && pw == 0 && lane == 0) __hip_atomic_store((unsigned*)flags + FL_EPOCH, epoch + 1u, __ATOMIC_RELAXED, __HIP_MEMORY_SCOPE_AGENT);
      }
      if constexpr (jb == DG + 1) {
        const float* p = lg + (pw & 1) * 32 + (lane >> 4) * 64 + ((2 * lane) & 7) * 4 + ((lane >> 2) & 3);
#pragma unroll
        for (int i = 0; i < 4; ++i) { p0[i] = p[i * 256]; p1[i] = p[i * 256 + 4]; }
      }
      if constexpr (jb == 2) {
        const float* p = lg + (col >> 5) * 64 + (col & 7) * 4 + ((col >> 3) & 3);
#pragma unroll
        for (int i = 0; i < 4; ++i) { p0[i] = p[i * 256]; p1[i] = p[i * 256 + 32]; }
      }
      if constexpr (jb == 4) {
        __builtin_amdgcn_raw_buffer_store_b32(tag, rf, abo + 8 * AB_STRIDE * 4, 0, 16);
      }
      if constexpr ((FSM >> (jb - 2)) & 1) {
        asm volatile("" : "+v"(fs));
        ready |= (unsigned)__ballot(fs == tag);
      }
      if constexpr (jb == GV_JB + 2) asm volatile("" : "+v"(gv[0]), "+v"(gv[1]), "+v"(gv[2]), "+v"(gv[3]));
      auto w2 = [&](auto GP) __attribute__((always_inline)) {
        constexpr int gp = decltype(GP)::value;
        if constexpr ((jb == 2 || jb == DG + 1) && gp == 0) {
          p0[0] = (p0[0] + p0[1]) + (p0[2] + p0[3]); p1[0] = (p1[0] + p1[1]) + (p1[2] + p1[3]);
          PIN2(p0[0], p1[0]);
        }
        if constexpr (jb == DG + 1 && gp == 1) {
          if (pw < 2) __builtin_amdgcn_raw_buffer_store_b64((u32x2){pk2(p0[0], p1[0]), tag}, mk_rsrc(wsp + WS_DN), (((pw * 8 + cg8) * 32 + rgb) * 64 + lane) * 8, 0, 16);
        }
        if constexpr (jb == XJ && gp % 4 == 1) epi_setup(gp / 4);
        work2(JB, GP);
        if constexpr (gp < dig(SPS, jb)) spf(IC<SPB + acc_dig(SPS, jb) + gp>{});
      };
#define W2(i) w2(IC<(i)>{})
      if (jb < 10) G2_STAGE_N(jb + 2, W2);
      if (jb == 10) { G2_STAGE_Q(0, W2, 0); need(14); G2_STAGE_M2(14, W2); }
      if (jb == 11) { G2_STAGE_Q(2, W2, 0); need(15); G2_STAGE_M2(15, W2); }
      if (jb < 12) {
        if (jb >= 10) { W2(6); W2(7); }
        need(jb + 2);
        G2_STAGE_M(jb + 2, W2);
      }
      if (jb == 10) G2_STAGE_Q(1, NOW, 0);
      if (jb == 11) G2_STAGE_Q(3, NOW, 0);
      if (jb == 12) { G2_STAGE_Q(4, W2, 0); G2_STAGE_Q(5, W2, 2); G2_STAGE_Q(6, W2, 4); G2_STAGE_Q(7, W2, 6); }
      if constexpr (jb == 2) {
#pragma unroll
        for (int jj = 0; jj < 4; ++jj) __builtin_amdgcn_raw_buffer_store_b128(tv[jj], rm2, vm2 + jj * 4096 + (2 * tt + 1) * 16384, 0, 16);
      }
      if constexpr ((FSM >> jb) & 1) fs = bld4_sc1(rf, abq[jb < 5 ? 1 : 2]);
#pragma unroll
      for (int i = acc_dig(GVS, jb); i < acc_dig(GVS, jb + 1); ++i) gv[i] = bld16_sc1(rgu, vgu, i * F_ * 16);
      if constexpr (jb == XJ) { xq_load(0); xq_load(1); xq_load(2); }
#pragma unroll
      for (int i = dn_first(jb); i < dn_first(jb) + dn_cnt(jb); ++i) dv[i] = bld16_nt(rdn, vdn, i * D_ * 2);
      if constexpr (jb == 2) {
        if (moe && pw < 2)
          __builtin_amdgcn_raw_buffer_store_b128((u32x4){__float_as_uint(p0[0]), tag, __float_as_uint(p1[0]), tag},
                                                 rgu, ((mat * 4 + dq) * F_ + cg * 128 + col) * 16, 0, 16);
      }
      SB();
      if constexpr (dig(SPS, jb) > 12) spf(IC<SPB + acc_dig(SPS, jb) + 12>{});
      if constexpr (dig(SPS, jb) > 13) spf(IC<SPB + acc_dig(SPS, jb) + 13>{});
      SB();
    };
    static_assert(DG + 1 <= 12 && DG >= GV_JB + 3 && dn_first(DG - 2) == 7 && acc_dig(GVS, GV_JB + 1) == 4, "schedule");
    static_assert(SPB + acc_dig(SPS, 14) == (16 - VAN) * 16, "all tiles are prefetched");
    step2(IC<2>{}); step2(IC<3>{}); step2(IC<4>{}); step2(IC<5>{}); step2(IC<6>{}); step2(IC<7>{}); step2(IC<8>{}); step2(IC<9>{}); step2(IC<10>{}); step2(IC<11>{}); step2(IC<12>{});
    lgkm0(sd);
    WAIT_VM(8); BAR();
    G2_STAGE_Q(8, NOW, 0); G2_STAGE_Q(9, NOW, 0); G2_STAGE_Q(10, NOW, 0); G2_STAGE_Q(11, NOW, 0);
    WAIT_VM(8); BAR();
    G2_STAGE_Q(12, NOW, 0); G2_STAGE_Q(13, NOW, 0);
    __builtin_amdgcn_s_setprio(0);
    asm volatile("" : "+v"(xq[0]), "+v"(xq[1]), "+v"(sq));
    cv_calc();
    __builtin_amdgcn_s_setprio(3);
    WAIT_VM(0); BAR();
    G2_STAGE_Q(14, NOW, 0); G2_STAGE_Q(15, NOW, 0);
    __builtin_amdgcn_s_setprio(0);
    epi_setup(3);
    key[0] = __builtin_amdgcn_readlane(ksel, 0); key[1] = __builtin_amdgcn_readlane(ksel, 8);
    far_calc();
    const int ec = seg0((lane >> 4) & 1, (lane >> 3) & 1);
    int dno = (ec >> 7) * 16384 + (lane >> 5) * 512 + (ec & 127) * 4 + (lane & 7) * 16;
    PIN1(dno);
    const __amdgpu_buffer_rsrc_t rd = mk_rsrc(wsp + WS_DN + pw * 4096);
    float sg0 = rbf(__builtin_amdgcn_rcpf(1.f + __expf(-key_score(key[0])))), sg1 = rbf(__builtin_amdgcn_rcpf(1.f + __expf(-key_score(key[1]))));
    PIN2(sg0, sg1);
    u32x4 dq_[4];
    asm volatile("s_waitcnt vmcnt(0)\n\ts_cmp_lg_u32 %7, 0\n\ts_cbranch_scc1 1f\n\ts_sleep 2\n\t"
                 "buffer_load_dwordx4 %0, %4, %5, %6 offen sc1\n\tbuffer_load_dwordx4 %1, %4, %5, %6 offen offset:1024 sc1\n\t"
                 "buffer_load_dwordx4 %2, %4, %5, %6 offen offset:2048 sc1\n\tbuffer_load_dwordx4 %3, %4, %5, %6 offen offset:3072 sc1\n"
                 "1:\n\ts_barrier\n\ts_waitcnt vmcnt(0)"
                 : "=&v"(dq_[0]), "=&v"(dq_[1]), "=&v"(dq_[2]), "=&v"(dq_[3])
                 : "v"(dno), "s"(rd), "s"((far0 & ~9) ? 131072 : 0), "s"(min(far0 & ~9, far1 & ~9)) : "scc", "memory");
    bool first = true;
#pragma unroll 1
    for (int c = 0; c < 2; ++c) {
      if (__builtin_expect(((c ? far1 : far0) & ~9) != 0, 1)) continue;
#pragma unroll 1
      for (int it = 0; it < SPIN_MAX; ++it) {
        unsigned bad = 0;
#pragma unroll
        for (int i = 0; i < 4; ++i) bad |= (dq_[i].y ^ tag) | (dq_[i].w ^ tag);
        if ((first || it) && __all(bad == 0)) break;
        if (it) __builtin_amdgcn_s_sleep(4);
#pragma unroll
        for (int i = 0; i < 4; ++i) dq_[i] = __builtin_amdgcn_raw_buffer_load_b128(rd, dno + i * 1024, c * 131072, 16);
        asm volatile("" ::: "memory");
      }
      first = false;
      const float sg = c ? sg1 : sg0;
      f32x4 s4 = {0.f, 0.f, 0.f, 0.f};
#pragma unroll
      for (int i = 0; i < 4; ++i) s4 += (f32x4){lo_bf(dq_[i].x), hi_bf(dq_[i].x), lo_bf(dq_[i].z), hi_bf(dq_[i].z)};
      *(f32x2*)(smem + LDS_FIN + (c * 4 + pw) * 512 + (lane & 31) * 16 + (lane >> 5) * 8) = (f32x2){fold32(s4[0], s4[2]) * sg, fold32(s4[1], s4[3]) * sg};
      asm volatile("s_waitcnt lgkmcnt(0)" ::: "memory");
      if (lane == 0) asm volatile("ds_add_u32 %0, %1" :: "v"(lds_addr(smem + LDS_SEL + 24 + c * 4)), "v"(1) : "memory");
    }
  } else {
  {
    const int q = j * 4 + wid;
    constexpr int Q1 = WS_MB, Q2 = Q1 + 8, Q3 = Q2 + 8, Q4 = Q3 + 7, Q5 = Q4 + 7, Q6 = Q5 + 7, Q7 = Q6 + 4, Q8 = Q7 + 4, QC = Q8 + 1;
    static_assert(QC <= 128, "warm-up slots");
    unsigned dmy = 0;
    if (q < QC) {
      const char* base; int b0;
      if (q < Q1) { base = wsp; b0 = 0; }
      else if (q < Q2) { base = (const char*)ws_re; b0 = Q1; }
      else if (q < Q3) { base = (const char*)ws_im; b0 = Q2; }
      else if (q < Q4) { base = (const char*)w_gate; b0 = Q3; }
      else if (q < Q5) { base = (const char*)w_up; b0 = Q4; }
      else if (q < Q6) { base = (const char*)w_down; b0 = Q5; }
      else if (q < Q7) { base = (const char*)out; b0 = Q6; }
      else if (q < Q8) { base = (const char*)x; b0 = Q7; }
      else { base = (const char*)w_r; b0 = Q8; }
      base += (size_t)(q - b0) << 20;
      asm volatile("s_load_dword %0, %1, 0" : "+s"(dmy) : "s"(base) : "memory");
    }
    if (wid == 0) { CALL_PG(1); CALL_PG(8); CALL_PG(5); CALL_PG(13); }
    else if (wid == 1) { CALL_PG(2); CALL_PG(9); CALL_PG(6); }
    else if (wid == 2) { CALL_PG(3); CALL_PG(10); CALL_PG(7); }
    else { CALL_PG(4); CALL_PG(11); CALL_PG(12); }
    BAR();
    lgkm0(dmy);
  }
  PAGE(8);
  epi_setup(0); epi_setup(1); epi_setup(2); epi_setup(3);
  const int wr = wid >> 1, wc = wid & 1;
  const int sw = swz(fr), fl = lrw(fr);
  const int ko0 = ((0 + fq) ^ sw) * 16, ko1 = ((4 + fq) ^ sw) * 16;
  bf16x8 fm[2][4], fn[2][4];
  {
    const char* pm = smem + (wr * 64 + fl) * 128;
    const char* pn = smem + 16384 + (wc * 64 + fl) * 128;
    f32x4 acc[4][4];
#pragma unroll
    for (int i = 0; i < 4; ++i)
#pragma unroll
      for (int jn = 0; jn < 4; ++jn)
#pragma unroll
        for (int q = 0; q < 4; ++q) acc[i][jn][q] = 0.f;
#define TO(i) ((i) * 2048)
#define LDSP(T, a) ((__attribute__((address_space(3))) T*)(size_t)(a))
#define SLOTS(n) _Pragma("unroll") for (int q = 0; q < (n); ++q)
#define MFA(c, m, n) asm volatile("v_mfma_f32_16x16x32_bf16 %0, %2, %1, %0" : "+v"(c) : "v"(m), "v"(n))
#define MFA2(c, m, n, m2, n2) asm volatile("v_mfma_f32_16x16x32_bf16 %0, %2, %1, %0\n\tv_mfma_f32_16x16x32_bf16 %0, %4, %3, %0" : "+v"(c) : "v"(m), "v"(n), "v"(m2), "v"(n2))
    unsigned lm[2][2], ln[2][2];
#pragma unroll
    for (int u = 0; u < 2; ++u)
#pragma unroll
      for (int kk = 0; kk < 2; ++kk) {
        lm[u][kk] = lds_addr(pm) + u * 2 * G1_STAGE + (kk ? ko1 : ko0);
        ln[u][kk] = lds_addr(pn) + u * 2 * G1_STAGE + (kk ? ko1 : ko0);
        asm volatile("" : "+v"(lm[u][kk]), "+v"(ln[u][kk]));
      }
    auto ldm = [&](int stg, int i, int kk) __attribute__((always_inline)) { fm[kk][i] = *LDSP(const bf16x8, lm[stg >> 1][kk] + (stg & 1) * G1_STAGE + TO(i)); };
    auto ldn = [&](int stg, int jn, int kk) __attribute__((always_inline)) { fn[kk][jn] = *LDSP(const bf16x8, ln[stg >> 1][kk] + (stg & 1) * G1_STAGE + TO(jn)); };
    auto quad = [&](int a, int c, int d, int s) __attribute__((always_inline)) {
      const int i = (s < 2 || s >= 6) ? a : a + 1, jn = s < 4 ? c : d, kk = (s == 1 || s == 2 || s == 5 || s == 6);
      MFA(acc[i][jn], fm[kk][i], fn[kk][jn]);
    };
    auto quad2 = [&](int a, int c, int d, int s) __attribute__((always_inline)) {
      const int i = (s < 2 || s >= 6) ? a : a + 1, jn = s < 4 ? c : d, kk = (s == 2 || s == 6);
      MFA2(acc[i][jn], fm[kk][i], fn[kk][jn], fm[1 - kk][i], fn[1 - kk][jn]);
    };
    auto half1 = [&](int stg) __attribute__((always_inline)) {
      const int ja = (stg & 1) * 2, jb = 2 - ja;
      SLOTS(8) {
        quad(0, ja, ja + 1, q);
        if (q < 4) ldn(stg, jb + (q >> 1), q & 1); else ldm(stg, 2 + ((q - 4) >> 1), q == 5 || q == 6);
        SB();
      }
      SLOTS(4) { quad2(0, jb, jb + 1, 2 * q); SB(); }
    };
    auto half2 = [&](int stg) __attribute__((always_inline)) {
      const int ja = (stg & 1) * 2, jb = 2 - ja, nx = (stg + 1) & 3;
      SLOTS(8) {
        quad(2, jb + 1, jb, q);
        if (!(q & 1)) ldm(nx, q >> 2, q == 2 || q == 4);
        SB();
      }
      SLOTS(8) {
        quad(2, ja, ja + 1, q);
        if (!(q & 1)) ldn(nx, jb + (q >> 2), (q >> 1) & 1);
        SB();
      }
    };
    unsigned ae[7];
    {
      int l_ = lane;
      asm volatile("" : "+v"(l_));
      const int p = l_ >> 4, dn = sym_dn(p), hf = (p >> 1) ^ (dn & z);
#pragma unroll
      for (int u = 0; u < 7; ++u) {
        const int T = u < 3 ? wc * 4 : (wc * 4 + u - 3 - dn) & 7, i = u < 3 ? (u + 1) ^ ((u & 1) ? 0 : dn * 2) : 0;
        const int f = dn * 8 + ((T & 2) << 1) + ((T & 4) >> 1) + (T & 1);
        ae[u] = lds_addr(smem + G2_MRING) + (hf * 64 + i * 16 + lrw(f)) * 128 + (((wr * 4 + ((l_ >> 2) & 3)) ^ swz(f)) * 16) + (l_ & 3) * 4;
        asm volatile("" : "+v"(ae[u]));
      }
    }
    unsigned al = lds_addr(smem + LDS_MB) + (wr * 32 + fr * 2) * 4, ac = lds_addr(smem + LDS_MB) + (256 + wc * 64 + fq * 4) * 4;
    asm volatile("" : "+v"(al), "+v"(ac));
    SLOTS(16) asm volatile("" : "+v"(acc[q >> 2][q & 3]));
    SB();
    SLOTS(4) { ldm(0, q >> 1, q & 1); ldn(0, q >> 1, q & 1); }
    SB();
#pragma unroll 1
    for (int k4 = 0; k4 < G1_NKT - 4; k4 += 4) {
      half1(0); BAR_LGKM();
      SB(); half2(0);
      half1(1); BAR_LGKM();
      SB(); half2(1);
      half1(2); BAR_LGKM();
      SB(); half2(2);
      half1(3); BAR_LGKM();
      SB(); half2(3);
    }
    bf16x8 gm[4][4], gn[2][8];
    auto rdm1 = [&](int set, int t, int h, int i, int kk) __attribute__((always_inline)) {
      gm[set][kk * 2 + i] = *LDSP(const bf16x8, lm[(t >> 1) & 1][kk] + (t & 1) * G1_STAGE + h * 4096 + i * 2048);
    };
    auto rdg = [&](int set, int t, int h, int r) __attribute__((always_inline)) {
      if (r == 0 || r == 2 || r == 4 || r == 5) rdm1(set, t, h, r >= 4, r == 2 || r == 4);
      else {
        const int jn = r < 4 ? 0 : (r - 4) >> 1, kk = r < 4 ? r >> 1 : r & 1;
        gn[set][kk * 4 + jn] = *LDSP(const bf16x8, ln[(t >> 1) & 1][kk] + (t & 1) * G1_STAGE + TO(jn));
      }
    };
    auto mmg = [&](int ms, int ns, int h, int jn, int r, int f) __attribute__((always_inline)) {
      const int i = (r >> 1) ^ f, kk = r == 1 || r == 2;
      MFA(acc[h * 2 + i][jn], gm[ms][kk * 2 + i], gn[ns][kk * 4 + jn]);
    };
    f32x2 rsv, mrv;
    f32x4 cev[4];
    float tc[32];
    unsigned ep;
    auto rds = [&](int h, int r) __attribute__((always_inline)) {
      if (r == 0) mrv = *LDSP(const f32x2, al + h * 256); else if (r == 5) rsv = *LDSP(const f32x2, al + 512 + h * 256);
      else cev[r - 1] = *LDSP(const f32x4, ac + (r - 1) * 64);
    };
    auto prep = [&](int n) __attribute__((always_inline)) {
      tc[n] = llvm_fmul_legacy(mrv[n & 1], cev[n >> 3][(n >> 1) & 3]);
      asm volatile("" : "+v"(tc[n]));
    };
    auto epi = [&](int h, int n) __attribute__((always_inline)) {
      const int jn = n >> 4, r = (n >> 2) & 3, u = n & 3, e = (n >> 2) * 2;
      if (u == 0) {}
      else if (u == 1) asm volatile("v_fmac_f32 %0, %2, %4\n\tv_fmac_f32 %1, %3, %5\n\tv_cvt_pk_bf16_f32 %0, %0, %1" : "+v"(tc[e]), "+v"(tc[e + 1])
                                    : "v"(acc[h * 2][jn][r]), "v"(acc[h * 2 + 1][jn][r]), "v"(rsv[0]), "v"(rsv[1]));
      else if (u == 2) ep = __float_as_uint(tc[e]);
      else *LDSP(unsigned, (r ? ae[r - 1] + (jn & 1) * 128 + (jn >> 1) * lrw(4) * 128 : ae[3 + jn]) + m_off(h)) = ep;
      SB();
    };
    SB();
    half1(0);
    BAR_LGKM();
    SB();
    SLOTS(16) {
      if (q < 8) quad(2, 3, 2, q); else quad(2, 0, 1, q - 8);
      if (q < 12) rdg(0, 14, 0, q); else rdg(1, 13, 0, q - 12);
      SB();
    }
    BAR();
    SB();
    SLOTS(16) {
      mmg(0, 0, 0, q >> 2, q & 3, (q >> 2) & 1);
      if (q < 6) rds(0, q); else if (q < 14) rdg(1, 13, 0, q - 2);
      if (q >= 6) prep(q - 6);
      if (q >= 14) prep(q - 4);
      SB();
    }
    PAGE(9);
    BAR();
    SB();
    {
      constexpr int ct[8] = {1, 1, 0, 0, 1, 0, 1, 0}, cj[8] = {0, 1, 0, 1, 2, 2, 3, 3}, cf[8] = {0, 1, 0, 1, 0, 1, 0, 1};
      constexpr auto en = [](int q) { return q < 15 ? 0 : q < 26 ? (q - 14) * 3 - (q == 25) : q < 27 ? 32 : q < 32 ? 32 + (q - 26) * 3 + (q == 31) : 48; };
      SLOTS(32) {
        mmg(ct[q >> 2], ct[q >> 2], 0, cj[q >> 2], q & 3, cf[q >> 2]);
        if (q < 12) rdg(0, 15, 0, q);
        else if (q < 15 || q == 17) { const int r = q < 15 ? q - 12 : 3; rdm1(2, 15, 1, r >> 1, r == 1 || r == 2); }
        else if (q == 20 || q == 23 || q == 25 || q == 26) { const int r = q == 20 ? 0 : q == 23 ? 1 : q - 23; rdm1(3, 13, 1, r >> 1, r == 1 || r == 2); }
        if (q < 15) prep(12 + q);
        if (q >= 10 && q < 15) prep(17 + q);
        SB();
#pragma unroll
        for (int n = q ? en(q - 1) : 0; n < en(q); ++n) epi(0, n);
      }
    }
    SLOTS(2) { mmg(2, 0, 1, 0, q, 0); SB(); }
    asm volatile("s_nop 1");
    SLOTS(16) { epi(0, 48 + q); if (q == 3 || q == 9) { mmg(2, 0, 1, 0, q == 3 ? 2 : 3, 0); SB(); } }
    BAR_LGKM();
    SB();
    SLOTS(12) {
      mmg(2, 0, 1, 1 + (q >> 2), q & 3, ~(q >> 2) & 1);
      if (q < 6) rds(1, q); else rdg(0, 14, 1, q - 6);
      if (q >= 8) prep(q - 8);
      SB();
    }
    SLOTS(16) {
      mmg(3, 1, 1, q >> 2, q & 3, (q >> 2) & 1);
      if (q < 6) rdg(0, 14, 1, q + 6);
      if (q < 4) prep(4 + q); else { prep(2 * q); prep(2 * q + 1); }
      SB();
    }
    asm volatile("s_waitcnt lgkmcnt(0)" ::: "memory");
    if (lane == 0) asm volatile("ds_add_u32 %0, %1" :: "v"(lds_addr(smem + LDS_SEL + 12)), "v"(1) : "memory");
    SB();
    SLOTS(16) {
      mmg(0, 0, 1, q >> 2, q & 3, (q >> 2) & 1);
      if (q < 4) fn[0][q] = *(const bf16x8*)(smem + (wr * 128 + fl) * 128 + (q >> 1) * 2048 + ((q & 1) ? ko1 : ko0));
      else if (q < 8) fm[q & 1][(q - 4) >> 1] = *(const bf16x8*)(smem + G2_MRING + m_off(0) + (wc * 64 + fl) * 128 + TO((q - 4) >> 1) + ((q & 1) ? ko1 : ko0));
      SB();
      if (q >= 8) { epi(1, 3 * q - 24); epi(1, 3 * q - 23); epi(1, 3 * q - 22); }
    }
    SLOTS(40) epi(1, 24 + q);
  }

  const char* pn = smem + (wr * 128 + fl) * 128;
  const char* pm = smem + G2_MRING + (wc * 64 + fl) * 128;
  f32x4 acc[8][4];
  auto mm = [&](bool imaj, int set, int q2, int s, int mode) __attribute__((always_inline)) {
    int i, jj, kk;
    if (imaj) { i = s >> 2; const int r = s & 3; jj = (r >> 1) ^ (i & 1); kk = (r == 1 || r == 2); }
    else { jj = s >> 3; const int r = s & 7; i = jj ? 3 - (r >> 1) : (r >> 1); kk = ((r + 1) >> 1) & 1; }
    if (mode == 1 && !(s & 1)) asm volatile("v_mfma_f32_16x16x32_bf16 %0, %2, %1, 0" : "=&v"(acc[q2 * 2 + jj][i]) : "v"(fm[kk][i]), "v"(fn[set][jj * 2 + kk]));
    else if (mode) asm volatile("v_mfma_f32_16x16x32_bf16 %0, %2, %1, %0" : "+v"(acc[q2 * 2 + jj][i]) : "v"(fm[kk][i]), "v"(fn[set][jj * 2 + kk]));
    else acc[q2 * 2 + jj][i] = __builtin_amdgcn_mfma_f32_16x16x32_bf16(fn[set][jj * 2 + kk], fm[kk][i], acc[q2 * 2 + jj][i], 0, 0, 0);
  };
  unsigned bn[2], bm[2], an[2], am[2];
#pragma unroll
  for (int kk = 0; kk < 2; ++kk) {
    bn[kk] = lds_addr(pn) + (kk ? ko1 : ko0); bm[kk] = lds_addr(pm) + (kk ? ko1 : ko0);
    asm volatile("" : "+v"(bn[kk]), "+v"(bm[kk]));
    an[kk] = bn[kk]; am[kk] = bm[kk] + m_off(0);
  }
#define RDN(jt, kk, o) (*LDSP(const bf16x8, an[kk] + (o) + (jt) * 2048))
#define RDM(i, kk, o) (*LDSP(const bf16x8, am[kk] + (o) + TO(i)))
  PAGE(10);
  tag = __builtin_amdgcn_readfirstlane(((const unsigned*)(smem + LDS_SEL))[5]);
  {
    int son = 0;
    auto tile = [&](int kt, int zero) __attribute__((always_inline)) {
      SLOTS(16) {
        mm(true, 0, 0, q, zero);
        if (!(q & 1)) {
          if (q < 8) fm[(q >> 1) & 1][2 + (q >> 2)] = RDM(2 + (q >> 2), (q >> 1) & 1, 0);
          else { const int r = (q - 8) >> 1; fn[1][r] = RDN(2 + (r >> 1), r & 1, 0); }
        }
        SB();
      }
      SLOTS(16) {
        mm(false, 1, 1, q, zero);
        if (!(q & 1) && q < 8) fn[0][q >> 1] = RDN(4 + (q >> 2), (q >> 1) & 1, 0);
        if (zero == 2 && q >= 9 && (q & 1) && q < 15) xq_load((q - 9) >> 1);
        SB();
      }
      const int som = son >> 1;
      son = son == 65536 ? 0 : son + 32768;
      SLOTS(16) {
        mm(false, 0, 2, q, zero);
        if (!(q & 1) && q < 8) fn[1][q >> 1] = RDN(6 + (q >> 2), (q >> 1) & 1, 0);
        if (q == 9 || q == 11) { an[q == 11] = bn[q == 11] + son; asm volatile("" : "+v"(an[q == 11])); }
        if (q == 13 || q == 15) { am[q == 15] = bm[q == 15] + som; asm volatile("" : "+v"(am[q == 15])); }
        SB();
      }
      BAR_LGKM();
      SB();
      SLOTS(16) {
        mm(true, 1, 3, q, zero);
        if (!(q & 1)) {
          if (q < 8) fn[0][q >> 1] = RDN(q >> 2, (q >> 1) & 1, 0);
          else fm[(q >> 1) & 1][(q - 8) >> 2] = RDM((q - 8) >> 2, (q >> 1) & 1, 0);
        }
        SB();
      }
    };
    SB();
    tile(0, 1);
#pragma unroll 1
    for (int kt = 1; kt < G2_NKT - 5; ++kt) tile(kt, 0);
    tile(G2_NKT - 5, 2);
  }
  unsigned fx;
  int fo = lane < 32 ? kq * 512 + lane * 4 : (int)0x80000000;
  {
    constexpr auto chp = [](int g) { return g == 0 || g_t[g] != g_t[g - 1]; };
    constexpr auto chn = [](int g) { return g + 1 < NG && g_t[g + 1] != g_t[g]; };
    unsigned an2[2], an3[2], am2[2];
#pragma unroll
    for (int kk = 0; kk < 2; ++kk) {
      an[kk] = bn[kk]; an2[kk] = bn[kk] + 65536; an3[kk] = bn[kk] + (G2_MRING - wr * 8192); am[kk] = bm[kk]; am2[kk] = bm[kk] - (G2_MRING - wc * 8192);
      asm volatile("" : "+v"(an[kk]), "+v"(am[kk]), "+v"(am2[kk]));
    }
    int vo;
    u32x4 pk;
    u32x2 kv;
    auto publish = [&](int d, int h) __attribute__((always_inline)) {
      if (h == 0) { vo = ml; PINV(vo); vo += (((d + kq + 1) & 3) * 4 + kq) * 4 * MSG; PINV(vo); }
      const f32x4 v = acc[2 * d + (h >> 2)][h & 3];
      pk[(h & 1) * 2] = pk2(v[0], v[1]); pk[(h & 1) * 2 + 1] = pk2(v[2], v[3]);
      if (h & 1) __builtin_amdgcn_raw_buffer_store_b128(pk, rs, vo + (h >> 1) * 1024, 0, 16);
    };
#define MSG_FLAG(d) __builtin_amdgcn_raw_buffer_store_b32(tag, rc, fo + (((d) + kq + 1) & 3) * 2048, 0, 16)
    auto rdn = [&](int g, int r) __attribute__((always_inline)) {
      fn[g & 1][r] = *LDSP(const bf16x8, (g_n[g] >= G2_MRING ? an3[r & 1] + (g_n[g] - G2_MRING) : g_n[g] >= 65536 ? an2[r & 1] + (g_n[g] - 65536) : an[r & 1] + g_n[g]) + (r >> 1) * 2048);
    };
    auto rdm = [&](int t, int i, int kk) __attribute__((always_inline)) { fm[kk][i] = *LDSP(const bf16x8, (t < 2 ? am[kk] + m_off(12 + t) : am2[kk] + n_off(10 + t) + 8192) + TO(i)); };
    auto group = [&](auto GI) __attribute__((always_inline)) {
      constexpr int gi = decltype(GI)::value;
      if (gi == 1) { BAR_LGKM();
        SB(); }
      if (gi == 3 || gi == 7) {
        if (gi == 3) PAGE(11);
        BAR_LGKM();
        SB();
        if (gi == 3) kv = *(const u32x2*)(smem + LDS_SEL);
        SB();
      }
      if (gi == 13) {
        PAGE(13);
        BAR();
        SB(); }
#pragma unroll
      for (int q = 0; q < 16; ++q) {
        const int n = gi * 16 + q;
        if (n == 152) { WAIT_VM(4); MSG_FLAG(0); SB(); }
        if (n == 216) { WAIT_VM(5); MSG_FLAG(1); SB(); }
        if (n == 250) { WAIT_VM(1); MSG_FLAG(2); fx = bld4_sc1(rc, po); SB(); }
        if (n == 13 || n == 29) { PINV(an2[n == 29]); }
        if (n == 93 || n == 95) { PINV(an3[n == 95]); }
        mm(chp(gi) || chn(gi), gi & 1, gi >> 2, q, 2);
        constexpr bool ni = gi + 1 < NG && (chp(gi + 1 < NG ? gi + 1 : gi) || chn(gi + 1 < NG ? gi + 1 : gi));
        auto nr = [](int r) { return ni && r >= 2 ? 5 - r : r; };
        if constexpr (chp(gi) && chn(gi)) {
          if (q < 4) rdm(g_t[gi], 2 + (q >> 1), q & 1); else if (q < 8) rdn(gi + 1, nr(q - 4)); else if (q < 12) rdm(g_t[gi + 1], (q - 8) >> 1, q & 1);
        } else if constexpr (chp(gi)) {
          if (q < 4) rdm(g_t[gi], 2 + (q >> 1), q & 1);
          else if (gi + 1 < NG && q < 12 && !(q & 1)) rdn(gi + 1, nr((q - 4) >> 1));
        } else if constexpr (chn(gi)) {
          if (!(q & 1)) { if (q < 8) rdn(gi + 1, nr(q >> 1)); else rdm(g_t[gi + 1], (q - 8) >> 2, (q >> 1) & 1); }
        } else {
          if (!(q & 1) && q < 8) rdn(gi + 1, nr(q >> 1));
        }
        if (n >= 76 && n < 84) publish(0, n - 76);
        if (gi == 8 && q < 8) publish(1, q);
        if (gi == 12 && q < 8) publish(2, q);
        if (n == 60) { key[0] = __builtin_amdgcn_readfirstlane(kv.x); key[1] = __builtin_amdgcn_readfirstlane(kv.y); }
        SB();
      }
    };
#define GR4(i) group(IC<i>{}); group(IC<i + 1>{}); group(IC<i + 2>{}); group(IC<i + 3>{});
    GR4(0) GR4(4) GR4(8) GR4(12)
    asm volatile("s_nop 7");
    SB();
  }
#pragma unroll
  for (int i = 0; i < 4; ++i) *(f32x4*)(smem + LDS_ACC + wr * 16384 + wc * 4096 + i * 1024 + lane * 16) = acc[7][i];
#pragma unroll
  for (int i = 0; i < 4; ++i) ya[i] = acc[6][i];
  asm volatile("s_waitcnt lgkmcnt(0)" ::: "memory");
  if (lane == 0) sig[0] = 2;
  SB();
  wait_xq<3>(xq, sq);
  far_calc();
  cv_calc();
  asm volatile("s_waitcnt vmcnt(0)" : "+v"(fx) :: "memory");
  seen = (unsigned)__ballot(fx == tag);
  }

  PAGE(6);
  u32x4 pp[3][2];
  unsigned e_lo = 0x00003f80u, e_hi = 0x3f800000u;
  asm volatile("" : "+v"(e_lo), "+v"(e_hi));
  __amdgpu_buffer_rsrc_t rs_ = rs, rc_ = rc;
  int mo[3] = {MSG_IDX(0) * 4 * MSG, MSG_IDX(1) * 4 * MSG, MSG_IDX(2) * 4 * MSG}, mv = ml + ha * 2048, mv2 = mv + 1024;
  asm volatile("" : "+s"(rs_), "+s"(rc_), "+s"(mo[0]), "+s"(mo[1]), "+s"(mo[2]));
  int mt = lane < 16 * ha ? lane * 128 : (int)0x80000000;
  unsigned pf[3] = {0, 0, 0};
  asm volatile("" : "+v"(mv), "+v"(mv2), "+v"(mt));
#define MSG_POLL(m) do { \
    if (!((seen >> (m)) & 1)) { \
      unsigned pv_, it_ = SPIN_MAX; \
      asm volatile("1:\n\tbuffer_load_dword %0, %3, %4, 0 offen sc1\n\ts_waitcnt vmcnt(0)\n\tv_cmp_eq_u32 vcc, %5, %0\n\ts_mov_b32 %1, vcc_lo\n\t" \
                   "s_bitcmp1_b32 %1, %6\n\ts_cbranch_scc1 2f\n\ts_sub_u32 %2, %2, 1\n\ts_cbranch_scc1 2f\n\ts_sleep 1\n\ts_branch 1b\n2:" \
                   : "=&v"(pv_), "+s"(seen), "+s"(it_) : "v"(po), "s"(rc_), "s"(tag), "i"(m) : "vcc", "scc", "memory"); \
    } \
    pp[m][0] = __builtin_amdgcn_raw_buffer_load_b128(rs_, mv, mo[m], 16); \
    pp[m][1] = __builtin_amdgcn_raw_buffer_load_b128(rs_, mv2, mo[m], 16); \
    if (m) pf[m] = __builtin_amdgcn_raw_buffer_load_b32(rs_, mt, mo[m], 16); } while (0)
#define MSG_ADD(m) do { \
    _Pragma("unroll") for (int u = 0; u < 8; ++u) { \
      ya[u >> 1][(u & 1) * 2] = add_bf(pp[m][u >> 2][u & 3], e_lo, ya[u >> 1][(u & 1) * 2]); ya[u >> 1][(u & 1) * 2 + 1] = add_bf(pp[m][u >> 2][u & 3], e_hi, ya[u >> 1][(u & 1) * 2 + 1]); } } while (0)
  MSG_POLL(2);
  if (ha) {
    int it = 0;
    while (sig[0] != 2 && it < SPIN_MAX * 8) { __builtin_amdgcn_s_sleep(1); ++it; }
    asm volatile("" ::: "memory");
#pragma unroll
    for (int i = 0; i < 4; ++i) ya[i] = *(const f32x4*)(smem + LDS_ACC + (wid & 2) * 8192 + (wid & 1) * 4096 + i * 1024 + lane * 16);
  }
  MSG_POLL(1); MSG_POLL(0);
  SB();
  if (__builtin_expect(far0 == 0 || far1 == 0, 0)) {
    asm volatile("s_branch 2f\nk_pg12:\n\ts_setpc_b64 s[42:43]\n2:");
#pragma unroll 1
    for (int c = 0; c < 2; ++c) {
      if (c ? far1 : far0) continue;
      const int ic = (0xffff - (c ? key[1] : key[0])) & (S_ - 1);
      lds_vi cnt = (lds_vi)(smem + LDS_SEL + 24 + c * 4);
      int it = 0;
      while (cnt[0] != 4 && it < SPIN_MAX * 8) { __builtin_amdgcn_s_sleep(1); ++it; }
      asm volatile("" ::: "memory");
      const float* q = (const float*)(smem + LDS_FIN + c * 2048) + (wid & 1) * 64 + (fr >> 3) * 32 + col4(fr);
      const f32x4 v = (*(const f32x4*)q + *(const f32x4*)(q + 128)) + (*(const f32x4*)(q + 256) + *(const f32x4*)(q + 384));
#pragma unroll
      for (int r = 0; r < 4; ++r)
#pragma unroll
        for (int k = 0; k < 4; ++k) {
          cv[r * 4 + k] += (xo[r] >> 11) == ic ? v[k] : 0.f;
        }
    }
  }
#pragma unroll
  for (int e = 0; e < 16; ++e) ya[e & 3][e >> 2] += cv[e];
  asm volatile("" : "+v"(ya[0]), "+v"(ya[1]), "+v"(ya[2]), "+v"(ya[3]));
  SB();
  MSG_ADD(2); MSG_ADD(1);
  asm volatile("" : "+v"(ya[0]), "+v"(ya[1]), "+v"(ya[2]), "+v"(ya[3]));
  SB();
  MSG_ADD(0);
  asm volatile("" :: "v"(pf[0]), "v"(pf[1]), "v"(pf[2]));
  {
#pragma unroll
    for (int r = 0; r < 4; ++r) *(u32x2*)((char*)out + (unsigned)xo[r]) = (u32x2){pk2(ya[0][r], ya[1][r]), pk2(ya[2][r], ya[3][r])};
  }
}

extern "C" void kernel_launch(void* const* d_in, const int* in_sizes, int n_in, void* d_out, int out_size,
                              void* d_ws, size_t ws_size, hipStream_t stream) {
  const u16* x = (const u16*)d_in[0];
  const u16* ws_re = (const u16*)d_in[1];
  const u16* ws_im = (const u16*)d_in[2];
  const u16* wd_re = (const u16*)d_in[3];
  const u16* wd_im = (const u16*)d_in[4];
  const u16* w_r = (const u16*)d_in[6];
  const u16* w_gate = (const u16*)d_in[7];
  const u16* w_up = (const u16*)d_in[8];
  const u16* w_down = (const u16*)d_in[9];
  k_main<<<256, 512, 0, stream>>>(x, wd_re, wd_im, w_r, (char*)d_ws, ws_re, ws_im, w_gate, w_up, w_down, (u16*)d_out);
}
```

```cpp
#include <hip/hip_runtime.h>
#include <hip/hip_bf16.h>
#include <stdint.h>
#include <type_traits>

extern "C" __device__ unsigned long long llvm_dispatch_id(void) __asm("llvm.amdgcn.dispatch.id");
extern "C" __device__ float llvm_fmul_legacy(float, float) __asm("llvm.amdgcn.fmul.legacy");
typedef unsigned short u16;
typedef unsigned long long u64;
typedef __attribute__((ext_vector_type(8))) short bf16x8;
typedef __attribute__((ext_vector_type(4))) unsigned u32x4;
typedef __attribute__((ext_vector_type(2))) unsigned u32x2;
typedef __attribute__((ext_vector_type(4))) float f32x4;
typedef __attribute__((ext_vector_type(2))) float f32x2;
typedef __attribute__((ext_vector_type(2))) __bf16 bf16x2;
typedef __attribute__((address_space(3))) void* lds_vp;
typedef volatile __attribute__((address_space(3))) int* lds_vi;

constexpr int S_ = 2048, D_ = 1024, F_ = 3584;
template <int N> using IC = std::integral_constant<int, N>;

static __device__ __forceinline__ float bf2f(u16 u) { return __uint_as_float((unsigned)u << 16); }
static __device__ __forceinline__ u16 f2bf(float f) {
  __hip_bfloat16 b = __float2bfloat16(f);
  return *reinterpret_cast<u16*>(&b);
}
static __device__ __forceinline__ float rbf(float f) { return bf2f(f2bf(f)); }
static __device__ __forceinline__ float lo_bf(unsigned u) { return __uint_as_float(u << 16); }
static __device__ __forceinline__ float hi_bf(unsigned u) { return __uint_as_float(u & 0xffff0000u); }
static __device__ __forceinline__ unsigned pk2(float a, float b) {
  const bf16x2 v = __builtin_convertvector((f32x2){a, b}, bf16x2);
  return __builtin_bit_cast(unsigned, v);
}
static __device__ __forceinline__ float add_bf(unsigned u, unsigned e, float c) {
  return __builtin_amdgcn_fdot2_f32_bf16(__builtin_bit_cast(bf16x2, u), __builtin_bit_cast(bf16x2, e), c, false);
}
static __device__ __forceinline__ float ln_elem(float x, float mb, float sd, float ri) {
  const float d = rbf(x - mb);
  const float q0 = d * ri;
  return rbf(__builtin_fmaf(__builtin_fmaf(-q0, sd, d), ri, q0));
}
template <int CTRL> static __device__ __forceinline__ float dpp_f(float v) {
  return __int_as_float(__builtin_amdgcn_update_dpp(0, __float_as_int(v), CTRL, 0xf, 0xf, true));
}
template <int CTRL> static __device__ __forceinline__ unsigned dpp_u(unsigned v) {
  return (unsigned)__builtin_amdgcn_update_dpp(0, (int)v, CTRL, 0xf, 0xf, true);
}
static __device__ __forceinline__ float row_sum(float v) {
  v += dpp_f<0xB1>(v);
  v += dpp_f<0x4E>(v);
  v += dpp_f<0x141>(v);
  v += dpp_f<0x140>(v);
  return v;
}
static __device__ __forceinline__ float fold32(float a, float b) {
  const auto r = __builtin_amdgcn_permlane32_swap(__float_as_uint(a), __float_as_uint(b), false, false);
  return __uint_as_float(r[0]) + __uint_as_float(r[1]);
}
static __device__ __forceinline__ float fold16(float a, float b) {
  const auto r = __builtin_amdgcn_permlane16_swap(__float_as_uint(a), __float_as_uint(b), false, false);
  return __uint_as_float(r[0]) + __uint_as_float(r[1]);
}
static __device__ __forceinline__ float rdl(float v, int l) { return __int_as_float(__builtin_amdgcn_readlane(__float_as_int(v), l)); }


#define TL(I) do { if ((I) == 1 || (I) == 0) { const unsigned _t = (unsigned)__builtin_amdgcn_s_memrealtime(); tlv = lane == (I) ? _t : tlv; } } while (0)
#define ST_JB 9
#define LNS 2
#define WSN 12
#define XJ 10
#define FSM 0xa2
#define GV_JB 7
#define DG 11
#define AB_STRIDE 32
#define SPIN_MAX (1 << 17)
#define VA0 5
#define VAN 4
#define SP0 3
#define SP1 7
#define SP1E 11
#define SPS 0x0000eeeeeeee00
#define DVS 0x00000400300000
#define GVS 0x00000022000000
constexpr int dig(unsigned long long t, int q) { return q < 0 ? 0 : (int)((t >> (4 * q)) & 15); }
constexpr int acc_dig(unsigned long long t, int q) { int a = 0; for (int i = 0; i < q; ++i) a += dig(t, i); return a; }

static __device__ __forceinline__ unsigned lds_addr(const void* p) { return (unsigned)(size_t)(lds_vp)p; }

constexpr int MSG = 4096;
constexpr size_t WS_ABT   = 0;
constexpr size_t WS_SLAB  = WS_ABT + (size_t)D_ * 4096 * 2;
constexpr size_t WS_GU    = WS_SLAB + (size_t)64 * 64 * MSG;
constexpr size_t WS_DN    = WS_GU + (size_t)8 * F_ * 16;
constexpr size_t WS_FLAGS = WS_DN + (size_t)2 * 8 * 32 * 64 * 8;
constexpr int FL_X = 0;
constexpr int FL_AB = 64 * 64 * 32;
constexpr int FL_ST = FL_AB + 2048 * AB_STRIDE;
constexpr int FL_CE = FL_ST + 4096;
constexpr int FL_KEY = FL_CE + 4096;
constexpr int FL_EPOCH = FL_KEY + 4096;
constexpr int WS_MB = (int)((WS_FLAGS + (FL_EPOCH + 1) * 4 + 1048575) >> 20);

#define WAIT_VM(n) asm volatile("s_waitcnt vmcnt(" #n ")" ::: "memory")
template <int N> static __device__ __forceinline__ void wait_vm() { asm volatile("s_waitcnt vmcnt(%0)" :: "i"(N) : "memory"); }
#define BAR_LGKM() asm volatile("s_waitcnt lgkmcnt(0)\n\ts_barrier" ::: "memory")
#define BAR() asm volatile("s_barrier" ::: "memory")
#define SB() __builtin_amdgcn_sched_barrier(0)
#define PAGE(n) asm volatile("s_branch 2f\n\t.p2align 12\nk_pg" #n ":\n\ts_setpc_b64 s[42:43]\n2:")
#define CALL_PG(n) asm volatile("s_getpc_b64 s[40:41]\n3:\n\ts_mov_b32 s42, k_pg" #n "-3b\n\ts_ashr_i32 s43, s42, 31\n\ts_add_u32 s40, s40, s42\n\ts_addc_u32 s41, s41, s43\n\t" \
                                "s_swappc_b64 s[42:43], s[40:41]" ::: "s40", "s41", "s42", "s43", "scc", "memory")

static __device__ __forceinline__ __amdgpu_buffer_rsrc_t mk_rsrc(const void* p) {
  return __builtin_amdgcn_make_buffer_rsrc((void*)p, 0, 0x7fffffff, 0x00020000);
}
#define DMA_(rsrc, voff, mbase, MI, sbase, SI, pol) do { unsigned _t; \
    asm volatile("s_add_i32 m0, %2, %3\n\ts_add_i32 %0, %5, %6\n\tbuffer_load_dwordx4 %1, %4, %0 offen" pol " lds" \
                 : "=&s"(_t) : "v"(voff), "s"(mbase), "i"(MI), "s"(rsrc), "s"(sbase), "i"(SI) : "memory", "scc"); } while (0)
#define DMA_I(rsrc, voff, mbase, MI, SI) \
    asm volatile("s_add_i32 m0, %1, %2\n\ts_nop 0\n\tbuffer_load_dwordx4 %0, %3, 0 offen offset:%4 lds" \
                 :: "v"(voff), "s"(mbase), "i"((MI) - (SI)), "s"(rsrc), "i"(SI) : "memory", "scc")
#define DMA_S(rsrc, voff, mbase, MI, soff) \
    asm volatile("s_add_i32 m0, %1, %2\n\ts_nop 0\n\tbuffer_load_dwordx4 %0, %3, %4 offen lds" \
                 :: "v"(voff), "s"(mbase), "i"(MI), "s"(rsrc), "s"(soff) : "memory", "scc")
#define TOUCH(rsrc, voff, mbase, MI, soff) \
    asm volatile("s_add_i32 m0, %1, %2\n\ts_nop 0\n\tbuffer_load_dword %0, %3, %4 offen lds" \
                 :: "v"(voff), "s"(mbase), "i"(MI), "s"(rsrc), "s"(soff) : "memory", "scc")
template <int OFF> static __device__ __forceinline__ u32x4 ld16_s(const void* p, int voff) {
  u32x4 v;
  asm volatile("global_load_dwordx4 %0, %1, %2 offset:%3" : "=v"(v) : "v"(voff), "s"(p), "i"(OFF) : "memory");
  return v;
}
template <int OFF> static __device__ __forceinline__ void sld(unsigned& sink, const void* p, int soff) {
  asm volatile("s_load_dword %0, %1, %2 offset:%3" : "+s"(sink) : "s"(p), "s"(soff), "i"(OFF) : "memory");
}
static __device__ __forceinline__ unsigned gld4_s(const void* p, unsigned voff) {
  unsigned v;
  asm volatile("global_load_dword %0, %1, %2" : "=v"(v) : "v"(voff), "s"(p) : "memory");
  return v;
}
static __device__ __forceinline__ u32x2 bld8_sc1(__amdgpu_buffer_rsrc_t r, unsigned voff) {
  u32x2 v;
  asm volatile("buffer_load_dwordx2 %0, %1, %2, 0 offen sc1" : "=v"(v) : "v"(voff), "s"(r) : "memory");
  return v;
}
template <int N> static __device__ __forceinline__ void wait_xs(unsigned& a, u32x2& b) { asm volatile("s_waitcnt vmcnt(%2)" : "+v"(a), "+v"(b) : "i"(N) : "memory"); }
template <int N> static __device__ __forceinline__ void wait_xq(u32x4 (&a)[2], u32x2& b) { asm volatile("s_waitcnt vmcnt(%3)" : "+v"(a[0]), "+v"(a[1]), "+v"(b) : "i"(N) : "memory"); }
static __device__ __forceinline__ u32x2 ld8_s_sc1(const void* p, int voff) {
  u32x2 v;
  asm volatile("global_load_dwordx2 %0, %1, %2 sc1" : "=v"(v) : "v"(voff), "s"(p) : "memory");
  return v;
}
static __device__ __forceinline__ void lgkm0(unsigned& sink) { asm volatile("s_waitcnt lgkmcnt(0)" : "+s"(sink) :: "memory"); }
template <typename T> static __device__ __forceinline__ void pin4(T& a, T& b, T& c, T& d) { asm volatile("" : "+s"(a), "+s"(b), "+s"(c), "+s"(d)); }
template <typename T, typename U> static __device__ __forceinline__ void pins(T& a, U& b) { asm volatile("" : "+s"(a), "+s"(b)); }
static __device__ __forceinline__ void launder(int& v, int& a, int& b) { asm volatile("" : "+v"(v)); asm volatile("" : "+s"(a), "+s"(b)); }
static __device__ __forceinline__ unsigned bld4_sc1(__amdgpu_buffer_rsrc_t r, int voff) {
  unsigned v;
  asm volatile("buffer_load_dword %0, %1, %2, 0 offen sc1" : "=v"(v) : "v"(voff), "s"(r) : "memory");
  return v;
}
static __device__ __forceinline__ u32x4 bld16_nt(__amdgpu_buffer_rsrc_t r, int voff, int soff) {
  u32x4 v;
  asm volatile("buffer_load_dwordx4 %0, %1, %2, %3 offen nt" : "=v"(v) : "v"(voff), "s"(r), "s"(soff) : "memory");
  return v;
}
static __device__ __forceinline__ void bld16_nt_io(u32x4& v, __amdgpu_buffer_rsrc_t r, int voff, int soff) {
  asm volatile("buffer_load_dwordx4 %0, %1, %2, %3 offen nt" : "+v"(v) : "v"(voff), "s"(r), "s"(soff) : "memory");
}
static __device__ __forceinline__ u32x4 bld16_sc1(__amdgpu_buffer_rsrc_t r, int voff, int soff) {
  u32x4 v;
  asm volatile("buffer_load_dwordx4 %0, %1, %2, %3 offen sc1" : "=v"(v) : "v"(voff), "s"(r), "s"(soff) : "memory");
  return v;
}
static __device__ __forceinline__ u32x2 granule_wait(const u64* p, unsigned tag) {
  u64 v = 0;
#pragma unroll 1
  for (int it = 0; it < SPIN_MAX; ++it) {
    v = __hip_atomic_load(p, __ATOMIC_RELAXED, __HIP_MEMORY_SCOPE_AGENT);
    if (__all((unsigned)(v >> 32) == tag)) break;
    __builtin_amdgcn_s_sleep(2);
  }
  return (u32x2){(unsigned)v, (unsigned)(v >> 32)};
}
static __device__ __forceinline__ float key_score(unsigned key) {
  const unsigned u = key >> 16;
  return bf2f((u16)((u & 0x8000u) ? (u & 0x7fffu) : (~u & 0xffffu)));
}
static __device__ __forceinline__ unsigned med3u(unsigned a, unsigned b, unsigned c) { return max(min(a, b), min(max(a, b), c)); }
static __device__ __forceinline__ void merge2(unsigned& a1, unsigned& a2, unsigned b1, unsigned b2) {
  const unsigned lo = min(a1, b1);
  a1 = max(a1, b1);
  a2 = max(lo, max(a2, b2));
}
static __device__ __forceinline__ float sqdev16(const u32x4 (&v)[2], float mb) {
  f32x2 q2 = {0.f, 0.f};
#pragma unroll
  for (int i = 0; i < 8; ++i) {
    const f32x2 e = (f32x2){lo_bf(v[i >> 2][i & 3]), hi_bf(v[i >> 2][i & 3])} - mb;
    q2 += e * e;
  }
  return q2.x + q2.y;
}

constexpr int lrw(int r) { return (r & ~6) | ((r & 2) << 1) | ((r & 4) >> 1); }
constexpr int swz(int r) { return ((r >> 1) & 4) | (r & 2); }
static __device__ __forceinline__ int sym_dn(int p) { return (p ^ (p >> 1)) & 1; }
static __device__ __forceinline__ int sym_hi(int n, int w, int st, int b, int z, int p) {
  return ((p >> 1) * (n / 2 * st) + b * (w * st)) ^ (-sym_dn(p) & (((n - 1) ^ (z * (n / 2))) * st));
}
static __device__ __forceinline__ int sym_lo(int w, int st, int p, int c) { return ((c - sym_dn(p)) * st) & ((w - 1) * st); }
static __device__ __forceinline__ int sym_row(int n, int w, int st, int b, int z, int p, int c) { return sym_lo(w, st, p, c) ^ sym_hi(n, w, st, b, z, p); }
constexpr int G1_NST = 4, G1_NKT = 16, G1_STAGE = 32768;
constexpr int G2_NKT = 16, G2_MRING = 98304;
constexpr int n_off(int k) { return (k % 3) * 32768; }
constexpr int m_off(int k) { return ((k + 2) % 3) * 16384; }
constexpr int k_ord(int k) { return k < 2 ? k : (k < 9 ? 2 * (k - 1) : 2 * (k - 8) + 1); }
constexpr int LDS_DUMMY = G2_MRING + 49152;
constexpr int LDS_GU = LDS_DUMMY + 4096;
constexpr int LDS_FIN = LDS_GU + 4096;
constexpr int LDS_MB = LDS_FIN + 4096;
constexpr int LDS_SEL = LDS_MB + 1536;
constexpr int LDS_SIG = LDS_SEL + 64;
constexpr int LDS_KEY = LDS_SIG + 32;
constexpr int LDS_BYTES = LDS_KEY + 128;
constexpr int LDS_ACC = 65536 + 8192;
constexpr int NG = 16;
constexpr int g_t[NG] = {0, 2, 1, 3, 3, 1, 2, 0, 0, 2, 1, 3, 3, 1, 2, 0};
constexpr int g_n[NG] = {0, 4096, 32768, 36864, 65536, 69632, 73728, 77824, G2_MRING + m_off(11), G2_MRING + m_off(11) + 4096, 0, 4096, 32768, 36864, 65536, 69632};
constexpr int g_h(int g) { return g == 8 || g == 9 ? 8192 : 16384; }
static_assert(n_off(12) == 0 && n_off(13) == 32768, "");

constexpr int sp_tile(int q) { return q < VA0 ? q : q + VAN < 16 ? q + VAN : -1; }
constexpr int dn_cnt(int q) { return dig(DVS, q); }
constexpr int dn_first(int q) { return acc_dig(DVS, q); }

static __device__ __forceinline__ void code_touch(unsigned& sink, unsigned off) {
  asm volatile("s_getpc_b64 s[40:41]\n\tk_main_rt:\n\ts_min_u32 %1, %1, k_main_end-k_main_rt-196\n\t"
               "s_load_dword %0, s[40:41], %1 offset:0\n\ts_load_dword %0, s[40:41], %1 offset:64\n\ts_load_dword %0, s[40:41], %1 offset:128\n\ts_load_dword %0, s[40:41], %1 offset:192"
               : "+s"(sink), "+s"(off) :: "s40", "s41", "scc", "memory");
}
static __device__ __forceinline__ void touch1(__amdgpu_buffer_rsrc_t r, int voff, unsigned mb, int soff) { TOUCH(r, voff, mb, LDS_DUMMY, soff); }
typedef const __attribute__((address_space(1))) u16* gptr;
typedef __attribute__((ext_vector_type(8))) u64 u64x8;
typedef __attribute__((ext_vector_type(2))) u64 u64x2;
extern "C" __global__ __launch_bounds__(512, 2) void k_main(const u16* x, const u16* wd_re, const u16* wd_im, const u16* w_r,
                                                 char* wsp, const u16* ws_re, const u16* ws_im,
                                                 const u16* w_gate, const u16* w_up, const u16* w_down, u16* out) {
  __shared__ __attribute__((aligned(16))) char smem[LDS_BYTES];
  u64x8 ka;
  u64x2 kb_;
  u64 kc;
  asm volatile("s_load_dwordx16 %0, %3, 0\n\ts_load_dwordx4 %1, %3, 64\n\ts_load_dwordx2 %2, %3, 80"
               : "=&s"(ka), "=&s"(kb_), "=&s"(kc) : "s"(__builtin_amdgcn_kernarg_segment_ptr()) : "memory");
  unsigned long long t00;
  asm volatile("s_memrealtime %0" : "=s"(t00) :: "memory");
  unsigned long long tk0;
  asm volatile("s_waitcnt lgkmcnt(0)\n\ts_memrealtime %0" : "=s"(tk0) :: "memory");
  const int tid = threadIdx.x, lane = tid & 63;
  const int wid = __builtin_amdgcn_readfirstlane(tid >> 6);
  const int g = blockIdx.x, xcd = ((~g & 1) << 2) | ((g >> 1) & 3), j = g >> 3;
  if (wid < 4) {
    unsigned cw, co = (j * 4 + wid) * 448;
    asm volatile(".subsection 1\nk_main_end:\n.subsection 0\n\ts_min_u32 %1, %1, k_main_end-k_main-480\n\ts_getpc_b64 s[40:41]\n\ts_add_u32 s40, s40, %1\n\ts_addc_u32 s41, s41, 0\n\t"
                 "s_load_dword %0, s[40:41], 0\n\ts_load_dword %0, s[40:41], 64\n\ts_load_dword %0, s[40:41], 128\n\t"
                 "s_load_dword %0, s[40:41], 192\n\ts_load_dword %0, s[40:41], 256\n\ts_load_dword %0, s[40:41], 320\n\ts_load_dword %0, s[40:41], 384\n\ts_waitcnt lgkmcnt(0)"
                 : "=&s"(cw), "+s"(co) :: "s40", "s41", "scc", "memory");
  }
  const int pw = wid - 4;
  const int row0 = (wid & 3) * 8 + lrw(lane >> 3);
  const int cm = ((lane & 7) ^ swz(row0)) * 16;
  int vm = ((row0 & 15) * 2 + (row0 >> 4)) * (D_ * 2) + cm;
  asm volatile("" : "+v"(vm));
  const int kq = xcd >> 1;
  const int tt = (xcd & 1) * 4 + (j >> 3);
  const int et = j & 7;
  const int z = kq >> 1, st = (kq & 1) * 8 + tt;
  const int lrow = (kq & 1) * 1024 + (xcd & 1) * 512 + z * 256 + j * 8 + pw * 2;
  const int drow = ((kq & 1) * 2 + (xcd & 1)) * 256 + j * 8 + pw * 2;
  unsigned o_m = st * 128 * D_ * 2, o_l = lrow * D_ * 2, o_d = drow * D_ * 2, mb0 = lds_addr(smem) + pw * 1024;
  int l16 = lane * 16;
  int vn[4];
#pragma unroll
  for (int q = 0; q < 4; ++q) vn[q] = sym_lo(32, D_ * 2, (row0 >> 2) & 3, q * 8 + (row0 >> 4) * 4 + (row0 & 3)) ^ (sym_hi(D_, 32, D_ * 2, et, z, (row0 >> 2) & 3) | cm);
  asm volatile("" : "+s"(o_m), "+s"(o_l), "+s"(o_d), "+s"(mb0));
  asm volatile("" : "+v"(l16), "+v"(vn[0]), "+v"(vn[1]), "+v"(vn[2]), "+v"(vn[3]));
  asm volatile("s_waitcnt lgkmcnt(0)" : "+s"(ka), "+s"(kb_), "+s"(kc) :: "memory");
  x = (const u16*)(gptr)ka[0]; wd_re = (const u16*)(gptr)ka[1]; wd_im = (const u16*)(gptr)ka[2]; w_r = (const u16*)(gptr)ka[3];
  wsp = (char*)(gptr)ka[4]; ws_re = (const u16*)(gptr)ka[5]; ws_im = (const u16*)(gptr)ka[6]; w_gate = (const u16*)(gptr)ka[7];
  w_up = (const u16*)(gptr)kb_[0]; w_down = (const u16*)(gptr)kb_[1]; out = (u16*)(gptr)kc;
  int* flags = (int*)(wsp + WS_FLAGS);
  u64* stat = (u64*)(flags + FL_ST);
  unsigned tlv = 0;
  asm volatile("s_waitcnt lgkmcnt(0)" : "+s"(t00), "+s"(tk0) :: "memory");
  tlv = lane == 0 ? (unsigned)tk0 : tlv;
  tlv = lane == 52 ? (unsigned)t00 : tlv;
  asm volatile("" : "+v"(tlv));
  const u16* wdz = z ? wd_im : wd_re;
  const __amdgpu_buffer_rsrc_t rm = mk_rsrc(x);
  const __amdgpu_buffer_rsrc_t rn = mk_rsrc(wdz);
#define G1_X(kt, so, q) DMA_(rm, vm, mb0, (so) + (q) * 4096, o_m, (kt) * 128 + (((q) & 1) * 64 + ((q) >> 1) * 32) * D_ * 2, "")
#define G1_W(kt, so, q) DMA_I(rn, vn[q], mb0, (so) + 16384 + (q) * 4096, (kt) * 128)
#define G1_STAGE_TILE(kt, so, W) do { SB(); G1_X(kt, so, 0); W(0); G1_X(kt, so, 1); W(1); G1_X(kt, so, 2); W(2); G1_X(kt, so, 3); W(3); \
    G1_W(kt, so, 0); W(4); G1_W(kt, so, 1); W(5); G1_W(kt, so, 2); W(6); G1_W(kt, so, 3); W(7); SB(); } while (0)
#define G1_STAGE_A(kt, so, W) do { SB(); G1_X(kt, so, 0); W(0); G1_X(kt, so, 2); W(1); \
    G1_W(kt, so, 0); W(2); G1_W(kt, so, 1); W(3); G1_W(kt, so, 2); W(4); G1_W(kt, so, 3); W(5); W(6); W(7); SB(); } while (0)
#define G1_STAGE_B(kt, so) do { SB(); G1_X(kt, so, 1); G1_X(kt, so, 3); SB(); } while (0)
#define NOW(i)
  const int fr = lane & 15, fq = lane >> 4;
  const int ha = wid >> 2;
  int trow, xo[4], xa[3], po, ml;
  const char *prs, *prc;
  auto col4 = [](int f) __attribute__((always_inline)) { return ((f & 2) << 3 | (f & 4) << 1 | (f & 1) << 2) ^ (-((f >> 3) & 1) & 28); };
  auto seg0 = [&](int hf, int dn) __attribute__((always_inline)) { return (hf * 512 + et * 32) ^ (-dn & (D_ - 32)); };
  auto epi_setup = [&](int part) __attribute__((always_inline)) {
    int t_ = tt, l_ = lane;
    asm volatile("" : "+s"(t_));
    asm volatile("" : "+v"(l_));
    const int ts = ((wid >> 1) & 1) * 8 + kq * 2 + ha;
    if (part == 0) { xo[3] = (seg0(wid & 1, (l_ >> 3) & 1) + col4(l_)) * 2; asm volatile("" : "+v"(xo[3])); }
    if (part == 1) {
      xo[3] |= sym_hi(S_, 64, D_ * 2, t_, z, l_ >> 4);
      ml = (l_ * 16) ^ (-sym_dn(l_ >> 4) & (z * 768));
      asm volatile("" : "+v"(xo[3]), "+v"(ml));
    }
    if (part == 2) {
      const int xh = xo[3];
#pragma unroll
      for (int r = 0; r < 4; ++r) xo[r] = sym_lo(64, D_ * 2, l_ >> 4, ts * 4 + r) ^ xh;
      const bool hi = xo[0] & 8;
      xa[0] = (hi ? xo[2] : xo[0]) & ~15; xa[1] = (hi ? xo[3] : xo[1]) & ~15;
      xa[2] = (sym_lo(64, D_ * 2, l_ >> 4, ts * 4 + (l_ & 3)) ^ xh) >> 11 << 3;
      asm volatile("" : "+v"(xo[0]), "+v"(xo[1]), "+v"(xo[2]), "+v"(xo[3]), "+v"(xa[0]), "+v"(xa[1]), "+v"(xa[2]));
    }
    if (part == 3) {
      trow = t_ * 16 + ts;
      prs = wsp + WS_SLAB + (size_t)((t_ * 8 + et) * 64 + (wid & 3)) * MSG;
      prc = (const char*)(flags + FL_X + ((t_ * 8 + et) * 64 + (wid & 3)) * 32);
      asm volatile("" : "+s"(trow), "+s"(prs), "+s"(prc));
      po = l_ < 3 ? (kq * 4 + ((kq + 1 + l_) & 3)) * 512 : (int)0x80000000;
      asm volatile("" : "+v"(po));
    }
  };
  auto tok_tile = [](unsigned key) __attribute__((always_inline)) {
    const unsigned h = (0xffff - key) & 1023, m = -(h >> 9);
    return (((h ^ m) & 0x1c0) | (((h ^ m) - m) & 63)) >> 2;
  };
#define rs mk_rsrc(prs)
#define rc mk_rsrc(prc)
  lds_vi sig = (lds_vi)(smem + LDS_SIG + (wid & 3) * 4);
  unsigned tag;
  u32x4 xq[2];
  u32x2 sq;
  f32x4 ya[4];
  unsigned key[2];
  float cv[16];
  unsigned far0, far1;
  auto xq_load = [&](int n) __attribute__((always_inline)) {
    if (n < 2) xq[n] = ld16_s<0>(x, xa[n]); else sq = ld8_s_sc1(stat, xa[2]);
  };
#define PINV(a) asm volatile("" : "+v"(a))
  auto far_calc = [&]() __attribute__((always_inline)) {
    far0 = tok_tile(key[0]) ^ trow; far1 = tok_tile(key[1]) ^ trow;
    asm volatile("" : "+s"(far0), "+s"(far1));
  };
  auto cv_calc = [&]() __attribute__((always_inline)) {
    if (__builtin_expect(!__all(sq.y == tag), 0)) sq = granule_wait(stat + (xa[2] >> 3), tag);
    const bool hi = xo[0] & 8;
    const float mb_ = lo_bf(sq.x), ri_ = __builtin_amdgcn_rcpf(hi_bf(sq.x));
#pragma unroll
    for (int r = 0; r < 4; ++r) {
      const float mb = r == 0 ? dpp_f<0x00>(mb_) : r == 1 ? dpp_f<0x55>(mb_) : r == 2 ? dpp_f<0xAA>(mb_) : dpp_f<0xFF>(mb_);
      const float ri = r == 0 ? dpp_f<0x00>(ri_) : r == 1 ? dpp_f<0x55>(ri_) : r == 2 ? dpp_f<0xAA>(ri_) : dpp_f<0xFF>(ri_);
#pragma unroll
      for (int u = 0; u < 2; ++u) {
        const unsigned own = xq[r & 1][(r & 2) + u], oth = dpp_u<0xB1>(xq[r & 1][2 - (r & 2) + u]);
        const unsigned w = hi == (r >= 2) ? own : oth;
        cv[r * 4 + 2 * u] = __builtin_fmaf(lo_bf(w) - mb, ri, lo_bf(w));
        cv[r * 4 + 2 * u + 1] = __builtin_fmaf(hi_bf(w) - mb, ri, hi_bf(w));
        PINV(cv[r * 4 + 2 * u]); PINV(cv[r * 4 + 2 * u + 1]);
      }
    }
  };
#define MSG_IDX(m) (kq * 4 + ((kq + 1 + (m)) & 3))
  unsigned seen = 0;

  if (__builtin_expect(wid >= 4, 1)) {
    __builtin_amdgcn_s_setprio(3);
    G1_STAGE_TILE(0, 0, NOW);
    constexpr int O_ST = (FL_ST - FL_AB) * 4, O_CE = (FL_CE - FL_AB) * 4, O_KEY = (FL_KEY - FL_AB) * 4, O_EP = (FL_EPOCH - FL_AB) * 4;
    const __amdgpu_buffer_rsrc_t rf = mk_rsrc(wsp + WS_FLAGS + FL_AB * 4);
    u32x4 lv[2], lv2[2], lw[2], ld[2], ld2[2];
    unsigned epv;
    {
      const char* xrow = (const char*)x + o_l;
      const char* wdrow = (const char*)wdz + o_d;
      lv[0] = ld16_s<0>(xrow, l16); lv[1] = ld16_s<1024>(xrow, l16);
      lv2[0] = ld16_s<2048>(xrow, l16); lv2[1] = ld16_s<3072>(xrow, l16);
      ld[0] = ld16_s<0>(wdrow, l16); ld[1] = ld16_s<1024>(wdrow, l16);
      ld2[0] = ld16_s<2048>(wdrow, l16); ld2[1] = ld16_s<3072>(wdrow, l16);
      lw[0] = ld16_s<0>(w_r, l16); lw[1] = ld16_s<1024>(w_r, l16);
      asm volatile("buffer_load_dword %0, off, %1, %2 sc1" : "=v"(epv) : "s"(rf), "s"(O_EP) : "memory");
    }
    SB();
    const unsigned long long did = llvm_dispatch_id();
    const unsigned dmix = (unsigned)did * 0x85EBCA6Bu + (unsigned)(did >> 32) * 0xC2B2AE35u + 0x27D4EB2Fu;
    const bool moe = et != 7;
    unsigned epoch;
    const int ci = lane & 15, rq = lane >> 4, tk = (lane >> 3) & 1;
    int mat, cg, dq, d0;
    u32x4 wv[16];
    const u16 *pgu, *pn2;
    const char *pm2, *spb;
#define rw __builtin_amdgcn_make_buffer_rsrc((void*)pgu, 0, moe ? 0x7fffffff : 0, 0x00020000)
#define rn2 mk_rsrc(pn2)
#define rm2 mk_rsrc(pm2)
    int vw, vn2[8], vm2;
    auto k_blk = [&](int k) __attribute__((always_inline)) { return tt ^ (k_ord(k) >> 1); };
#define KROT(kk_) (k_blk(kk_) * 256 + (k_ord(kk_) & 1) * 128)
    int tv2, tv7;
    unsigned sd = 0;
    auto spf = [&](auto N) __attribute__((always_inline)) {
      constexpr int n = decltype(N)::value;
      if constexpr (n >= 0 && sp_tile(n >> 4) >= 0) sld<((n >> 1) & 7) * 4096 + (n & 1) * 64>(sd, spb, KROT(sp_tile(n >> 4)));
    };
    const int cg8 = et ^ ((et & 1) << 1), rgb = (g & 7) * 4 + (j >> 3);
    const __amdgpu_buffer_rsrc_t rdn = mk_rsrc(w_down);
    const __amdgpu_buffer_rsrc_t rgu = mk_rsrc(wsp + WS_GU);
    int vdn, vgu;
    u32x4 dv[7], gv[4];
    int abo, abq[3];
    unsigned lma;
    unsigned sel[2][4];
    float g0, g1;
    u32x4 kk[4];
    unsigned k1 = 0, k2 = 0;
    const int ko_ = O_KEY + pw * 4096;
#define MM(a, b, c) __builtin_amdgcn_mfma_f32_16x16x32_bf16(__builtin_bit_cast(bf16x8, a), __builtin_bit_cast(bf16x8, b), c, 0, 0, 0)
#define PIN1(a) asm volatile("" : "+v"(a))
#define PIN2(a, b) asm volatile("" : "+v"(a), "+v"(b))
#define PIN3(a, b, c) asm volatile("" : "+v"(a), "+v"(b), "+v"(c))
    {
      int gro, go;
      u32x2 gr;
      u32x4 oh[5];
      f32x4 sa4, gw[2], gx[2];
      float su, gs, ta, tb, sx, mbv;
      unsigned dk[4], kl;
      const int hb = lane >> 5;
#pragma unroll
      for (int l = 0; l < WSN; ++l) asm volatile("" : "=v"(wv[l]));
      auto diag = [&](const f32x4& m) __attribute__((always_inline)) {
        return __uint_as_float((__float_as_uint(m[0]) & dk[0]) | (__float_as_uint(m[1]) & dk[1]) | (__float_as_uint(m[2]) & dk[2]) | (__float_as_uint(m[3]) & dk[3]));
      };
      auto setup = [&](auto JB, auto GP) __attribute__((always_inline)) {
        constexpr int jb = decltype(JB)::value, gp = decltype(GP)::value;
        SB();
        {
          int ln = lane, jj = j, p_ = pw;
          launder(ln, jj, p_);
          const int c_ = ln & 15, r_ = ln >> 4, e_ = jj & 7, t_ = (xcd & 1) * 4 + (jj >> 3);
          if constexpr (jb == 1 && gp == 0) {
            pn2 = ((kq >> 1) ? ws_im : ws_re) + (kq & 1) * 1024;
            spb = (const char*)(pn2 + (sym_row(S_, 64, 1, t_, 0, e_ >> 1, 0) + (e_ & 1) * 32 + p_ * 8) * S_);
            pins(pn2, spb);
          }
          if constexpr (jb == 1 && gp >= 1 && gp < 6) {
            const unsigned o = c_ == gp - 1 ? 0x3f803f80u : 0u;
            oh[gp - 1] = (u32x4){o, o, o, o};
            PIN1(oh[gp - 1]);
          }
          if constexpr (jb == 1 && gp == 6) {
            const int rk = moe ? e_ * 8 + (jj >> 3) * 2 + (xcd >> 2) : 0, r3 = ((rk - 14) * 43) >> 7, rr = rk - 14 - r3 * 3;
            const int c56 = rk < 14 ? rk * 4 + 1 : r3 * 4 + rr + ((rr + 1) >> 1);
            mat = (c56 + 36) >> 6; cg = c56 - mat * 28; dq = xcd & 3; d0 = dq * 256 + p_ * 64;
            pin4(mat, cg, dq, d0);
          }
          if constexpr (jb == 2 && gp == 0) {
            pgu = mat ? w_up : w_gate;
            pm2 = wsp + WS_ABT + (kq * 8 + e_) * 262144;
            pins(pgu, pm2);
            vw = ((d0 + r_ * 16) * F_ + cg * 128 + c_ * 8) * 2;
            PIN1(vw);
          }
          if constexpr (jb == 2 && (gp == 1 || gp == 2)) {
            dk[gp * 2 - 2] = c_ == r_ * 4 + gp * 2 - 2 ? ~0u : 0u; dk[gp * 2 - 1] = c_ == r_ * 4 + gp * 2 - 1 ? ~0u : 0u;
            PIN2(dk[gp * 2 - 2], dk[gp * 2 - 1]);
          }
          if constexpr (jb == 2 && gp == 3) { kl = lrow + (ln >> 5); go = (ln & 30) ? (int)0x80000000 : ((ln & 1) ? O_KEY : O_ST) + (int)kl * 8; PIN2(kl, go); }
          if constexpr (jb == 2 && gp == 4) { kl = 0xffff - kl; go = (ln & ~1) == 2 ? O_CE + (z * D_ + drow + (ln & 1)) * 8 : go; PIN2(kl, go); }
          if constexpr (jb == 2 && gp == 5) {
            gro = p_ < 2 ? O_ST + (st * 128 + p_ * 64 + ln) * 8 : O_CE + z * D_ * 8 + sym_row(D_, 32, 8, e_, z, (ln >> 2) & 3, (p_ - 2) * 16 + r_ * 4 + (ln & 3));
            lma = lds_addr(smem + LDS_MB) + (p_ < 2 ? p_ * 256 : 512 + p_ * 256) + ln * 4;
            PIN2(gro, lma);
          }
          if constexpr (jb == 7 && gp == 0) {
            vm2 = p_ * 1024 + ln * 16;
            PIN1(vm2);
          }
          if constexpr (jb == 2 && gp == 6) {
            const int r0 = p_ * 8 + lrw(ln >> 3);
            vn2[6] = sym_lo(64, S_ * 2, (r0 >> 2) & 3, (r0 >> 4) * 4 + (r0 & 3)); vn2[7] = sym_hi(S_, 64, S_ * 2, t_, z, (r0 >> 2) & 3) | (vm & 127);
            PIN2(vn2[6], vn2[7]);
          }
          if constexpr ((jb == 2 && gp == 7) || (jb == 7 && gp >= 6)) {
            constexpr int q0 = jb == 2 ? 0 : gp == 6 ? 3 : 6;
            const int lo = vn2[6], hi = vn2[7];
#pragma unroll
            for (int q = q0; q < q0 + 3 && q < 8; ++q) { vn2[q] = ((lo + ((q & 4) + ((q + kq + 1) & 3)) * 8 * S_ * 2) & (63 * S_ * 2)) ^ hi; PIN1(vn2[q]); }
          }
          if constexpr (jb == 7 && gp == 1) { vdn = rgb * 112 + p_ * 28 + r_ * 7; vgu = (((ln >> 3) & 1) * 4 * F_ + vdn + min(ln & 7, 6)) * 16; PIN2(vdn, vgu); }
          if constexpr (jb == 7 && gp == 2) { vdn = (vdn * D_ + cg8 * 128 + c_ * 8) * 2; PIN1(vdn); }
          if constexpr (jb == 7 && gp == 3) {
            abo = (((kq * 8 + e_) * 4 + p_) * 16 + c_) * AB_STRIDE * 4;
            abq[2] = ln >= 16 || (ln & 7) == t_ ? (int)0x80000000 : abo;
            abo = ln < 32 ? (((kq * 8 + e_) * 4 + p_) * 16 + t_) * AB_STRIDE * 4 + ln * 4 : (int)0x80000000;
            PIN2(abo, abq[2]);
          }
          if constexpr (jb == 7 && gp == 4) {
            abq[1] = ln >= 8 ? (int)0x80000000 : abq[2];
            abq[0] = ((ln ^ t_) & 4) ? (int)0x80000000 : abq[1];
            PIN2(abq[0], abq[1]);
          }
          if constexpr (jb == 8 && gp < 4) {
#pragma unroll
            for (int h = 0; h < 2; ++h) {
              const unsigned hv = h ? 0x0302u : 0x0100u;
              sel[h][gp] = ((ln & 7) >> 1) == gp ? ((ln & 1) ? (hv << 16) | 0x0c0cu : 0x0c0c0000u | hv) : 0x0c0c0c0cu;
            }
            PIN2(sel[0][gp], sel[1][gp]);
          }
        }
        SB();
      };
#define WS1(i) setup(IC<1>{}, IC<i>{})
#define WS2(i) setup(IC<2>{}, IC<i>{})
#define WS3(i) do { setup(IC<7>{}, IC<i>{}); if (i >= 4) setup(IC<8>{}, IC<(i) & 3>{}); spf(IC<(i < SP0 ? i : -1)>{}); } while (0)
      G1_STAGE_TILE(1, G1_STAGE, WS1);
      WAIT_VM(19); BAR();
      G1_STAGE_TILE(2, 2 * G1_STAGE, WS2);
      G1_STAGE_TILE(3, 3 * G1_STAGE, WS3);
      auto lnu = [&](auto U) __attribute__((always_inline)) {
        constexpr int jb = 1 + decltype(U)::value / 7, gp = decltype(U)::value % 7;
        SB();
        if constexpr (jb == 1) {
          const f32x4 z4 = {0.f, 0.f, 0.f, 0.f};
          if constexpr (gp == 0) { sa4 = MM(lv[0], oh[0], z4); gw[0] = MM(lv[0], lw[0], z4); gx[0] = MM(lv[0], lv[0], z4); PIN3(sa4, gw[0], gx[0]); }
          if constexpr (gp == 1) { sa4 = MM(lv2[0], oh[1], sa4); gw[1] = MM(lv2[0], lw[0], z4); gx[1] = MM(lv2[0], lv2[0], z4); PIN3(sa4, gw[1], gx[1]); }
          if constexpr (gp == 2) { sa4 = MM(lv[1], oh[0], sa4); gw[0] = MM(lv[1], lw[1], gw[0]); gx[0] = MM(lv[1], lv[1], gx[0]); PIN3(sa4, gw[0], gx[0]); }
          if constexpr (gp == 3) { sa4 = MM(lv2[1], oh[1], sa4); gw[1] = MM(lv2[1], lw[1], gw[1]); gx[1] = MM(lv2[1], lv2[1], gx[1]); PIN3(sa4, gw[1], gx[1]); }
          if constexpr (gp == 4) { sa4 = MM(ld[0], oh[2], sa4); sa4 = MM(ld[1], oh[2], sa4); PIN1(sa4); }
          if constexpr (gp == 5) { sa4 = MM(ld2[0], oh[3], sa4); sa4 = MM(ld2[1], oh[3], sa4); PIN1(sa4); }
          if constexpr (gp == 6) { sa4 = MM(lw[0], oh[4], sa4); sa4 = MM(lw[1], oh[4], sa4); PIN1(sa4);
            epoch = __builtin_amdgcn_readfirstlane(epv); tag = ((epoch + 1u) * 0x9E3779B1u ^ dmix) | 1u; }
        }
        if constexpr (jb == 2) {
          if constexpr (gp == 0) { su = (sa4[0] + sa4[1]) + (sa4[2] + sa4[3]); ta = diag(gw[0]); PIN2(su, ta); }
          if constexpr (gp == 1) { su = fold32(su, su); tb = diag(gw[1]); PIN2(su, tb); }
          if constexpr (gp == 2) { su = fold16(su, su); gs = fold32(ta, tb); PIN2(su, gs); }
          if constexpr (gp == 3) { ta = diag(gx[0]); tb = diag(gx[1]); PIN2(ta, tb); }
          if constexpr (gp == 4) { gs = fold16(gs, fold32(ta, tb)); PIN1(gs); }
          if constexpr (gp == 5) { gs += dpp_f<0xB1>(gs); gs += dpp_f<0x4E>(gs); PIN1(gs); }
          if constexpr (gp == 6) { gs += dpp_f<0x141>(gs); gs += dpp_f<0x140>(gs); PIN1(gs); }
        }
        if constexpr (jb == 3) {
          if constexpr (gp == 0) {
            const float s0 = rdl(su, 0), s1 = rdl(su, 1), q0 = rdl(gs, 16), q1 = rdl(gs, 48);
            sx = hb ? s1 : s0; ta = hb ? q1 : q0; PIN2(sx, ta);
          }
          if constexpr (gp == 1) {
            const float mean = sx * (1.0f / D_);
            mbv = rbf(mean);
            ta -= sx * mean;
            if (__builtin_expect(__any(!(sx * mean <= 16.f * ta)), 0)) {
              float q = fold32(sqdev16(lv, rdl(mbv, 0)), sqdev16(lv2, rdl(mbv, 32)));
              q = row_sum(fold16(q, q));
              const float dm = mean - mbv;
              ta = fmaxf(q - (float)D_ * dm * dm, 0.f);
            }
            PIN2(mbv, ta);
          }
          if constexpr (gp == 2) { ta = rbf(rbf(ta * (1.0f / D_)) + 1e-5f); gs -= mbv * rdl(su, 4); PIN2(ta, gs); }
          if constexpr (gp == 3) { ta = rbf(__builtin_amdgcn_sqrtf(ta)); tb = __builtin_amdgcn_rcpf(ta); PIN2(ta, tb); }
          if constexpr (gp == 4) {
            const unsigned rb = __float_as_uint(rbf(gs * tb));
            kl |= (rb ^ ((unsigned)((int)rb >> 31) | 0x80000000u)) & 0xffff0000u; PIN1(kl);
          }
          if constexpr (gp == 5) { kl = (lane & 2) ? __float_as_uint(su) : (lane & 1) ? kl : (__float_as_uint(mbv) >> 16) | __float_as_uint(ta); PIN1(kl); }
        }
        SB();
      };
      constexpr int LN_JB = 1 + (19 * LNS) / 8;
      auto work1 = [&](auto JB, auto GP) __attribute__((always_inline)) {
        constexpr int jb = decltype(JB)::value, gp = decltype(GP)::value, pos = (jb - 1) * 8 + gp;
        SB();
        if constexpr (pos % LNS == 0 && pos / LNS < 20) lnu(IC<pos / LNS>{});
        if constexpr (jb == ST_JB + 3) {
          if constexpr (gp == 0) { if (!__all(gr.y == tag)) gr = granule_wait((const u64*)((const char*)(flags + FL_AB) + gro), tag); PIN1(gr); }
          if constexpr (gp == 1) { g1 = __builtin_amdgcn_rcpf(__uint_as_float((gr.x & 0xffff0000u) ^ ((unsigned)z << 31))); PIN1(g1); }
          if constexpr (gp == 2) { g0 = pw < 2 ? -(lo_bf(gr.x) * g1) : __uint_as_float(gr.x); PIN1(g0); }
        }
        if constexpr (jb == 8 && (gp == 2 || gp == 5)) {
          static_assert(VAN == 4 && VA0 >= 2 && VA0 + VAN <= 9, "first halves");
          int ln = lane;
          PIN1(ln);
          const int i = (ln & 15) * 4 + pw, n = gp == 2 ? (moe ? 0 : 64) : (et < 2 ? 0 : 32);
          const int r0 = gp == 2 ? tt * 64 : sym_row(S_, 64, 1, tt, 0, et >> 1, 0) + (et & 1) * 32;
          (gp == 2 ? tv7 : tv2) = i < n ? (r0 + i) * (S_ * 2) + ((tt ^ (VA0 - 1 + (ln >> 4))) << 8) : (int)0x80000000;
          PIN1(gp == 2 ? tv7 : tv2);
        }
        if constexpr (gp < SP1 && jb <= SP1E) spf(IC<SP0 + (jb - 1) * SP1 + gp>{});
        SB();
      };
      auto step1 = [&](auto JB) __attribute__((always_inline)) {
        constexpr int jb = decltype(JB)::value;
        if constexpr (jb == 1) PAGE(1);
        if constexpr (jb == 7) PAGE(2);
        constexpr int n16 = jb == 12 ? 12 : jb == 11 ? 14 : 16;
        constexpr auto wl = [](int q) { return (q == LN_JB ? 1 : 0) + (q == ST_JB ? 1 : 0); };
        if constexpr (jb == 1) {
          asm volatile("s_waitcnt vmcnt(%11)" : "+v"(lv[0]), "+v"(lv[1]), "+v"(lw[0]), "+v"(lw[1]), "+v"(ld[0]), "+v"(ld[1]),
                       "+v"(lv2[0]), "+v"(lv2[1]), "+v"(ld2[0]), "+v"(ld2[1]), "+v"(epv) : "i"(16) : "memory");
        } else if constexpr (jb == ST_JB + 3) {
          asm volatile("s_waitcnt vmcnt(%1)" : "+v"(gr) : "i"(n16 + wl(jb - 2) + wl(jb - 1)) : "memory");
        } else wait_vm<n16 + wl(jb - 3) + wl(jb - 2) + wl(jb - 1)>();
        BAR();
        if constexpr (jb == 2) { if (pw == 0 && lane < 16) ((int*)(smem + LDS_SEL))[lane < 8 ? lane : lane + 8] = 0; }
        if constexpr (jb == 4) { if (pw == 0 && lane == 0) ((unsigned*)(smem + LDS_SEL))[5] = tag; }
#define W1(i) work1(JB, IC<i>{})
        if constexpr (jb < 10) G1_STAGE_TILE(jb - 1 + G1_NST, ((jb - 1) % G1_NST) * G1_STAGE, W1);
        else G1_STAGE_A(jb - 1 + G1_NST, ((jb - 1) % G1_NST) * G1_STAGE, W1);
        if constexpr (jb == 12) G1_STAGE_B(13, G1_STAGE);
        if constexpr (jb == LN_JB) __builtin_amdgcn_raw_buffer_store_b64((u32x2){kl, tag}, rf, go, 0, 16);
        if constexpr (jb == ST_JB) {
          asm volatile("buffer_load_dwordx2 %0, %1, %2, 0 offen sc1" : "=v"(gr) : "v"(gro), "s"(rf) : "memory");
        }
        if constexpr (jb <= 8) {
          if (et == jb - 1) {
#pragma unroll
            for (int l = 0; l < WSN; ++l) bld16_nt_io(wv[l], rw, vw, l * F_ * 2);
          }
        }
        SB();
      };
      static_assert(ST_JB + 3 <= 12 && ST_JB >= 6, "schedule");
      step1(IC<1>{}); step1(IC<2>{}); step1(IC<3>{}); step1(IC<4>{}); step1(IC<5>{}); step1(IC<6>{}); step1(IC<7>{}); step1(IC<8>{}); step1(IC<9>{}); step1(IC<10>{}); step1(IC<11>{}); step1(IC<12>{});
    }
#define G2_N(q, so) DMA_S(rn2, vn2[q], mb0, so, kb)
#define G2_STAGE_N(kk_, W) do { const int kb = KROT(kk_); constexpr int so = n_off(kk_); SB(); \
    G2_N(0, so); W(0); G2_N(1, so + 4096); W(1); G2_N(2, so + 8192); W(2); G2_N(3, so + 12288); W(3); \
    G2_N(4, so + 16384); W(4); G2_N(5, so + 20480); W(5); G2_N(6, so + 24576); W(6); G2_N(7, so + 28672); W(7); SB(); } while (0)
#define G2_STAGE_NH(kk_, h) do { const int kb = KROT(kk_); constexpr int so = n_off(kk_) + (h) * 16384; SB(); \
    G2_N((h) * 4, so); G2_N((h) * 4 + 1, so + 4096); G2_N((h) * 4 + 2, so + 8192); G2_N((h) * 4 + 3, so + 12288); SB(); } while (0)
#define G2_STAGE_Q(g, W, i) do { const int kb = KROT(12 + g_t[g]); SB(); G2_N((g) >> 2, g_n[g]); W(i); G2_N(4 + ((g) >> 2), g_n[g] + g_h(g)); W(i + 1); SB(); } while (0)
#define G2_STAGE_M2(kk_, W) do { const int kb = KROT(kk_) * 128; constexpr int so = n_off(kk_ - 2) + 8192; SB(); \
    DMA_(rm2, vm2, mb0, so, kb, 0, " sc1"); W(2); DMA_(rm2, vm2, mb0, so + 4096, kb, 4096, " sc1"); W(3); \
    DMA_(rm2, vm2, mb0, so + 16384, kb, 8192, " sc1"); W(4); DMA_(rm2, vm2, mb0, so + 20480, kb, 12288, " sc1"); W(5); SB(); } while (0)
#define G2_STAGE_M(kk_, W) do { const int kb = KROT(kk_) * 128; constexpr int so = G2_MRING + m_off(kk_); SB(); \
    DMA_(rm2, vm2, mb0, so, kb, 0, " sc1"); W(8); DMA_(rm2, vm2, mb0, so + 4096, kb, 4096, " sc1"); W(9); \
    DMA_(rm2, vm2, mb0, so + 8192, kb, 8192, " sc1"); W(10); DMA_(rm2, vm2, mb0, so + 12288, kb, 12288, " sc1"); W(11); SB(); } while (0)
    WAIT_VM(8); BAR();
    asm volatile("ds_write_b32 %0, %1" :: "v"(lma), "v"(g0) : "memory");
    if (pw < 2) asm volatile("ds_write_b32 %0, %1 offset:512" :: "v"(lma), "v"(g1) : "memory");
    u32x4 tv[4];
    unsigned ready = 0;
    auto need = [&](int k) __attribute__((always_inline)) {
      const int b = k_blk(k) + (k_ord(k) & 1) * 8;
#pragma unroll 1
      for (int it = 0; !((ready >> b) & 1) && it < SPIN_MAX; ++it) {
        ready |= (unsigned)__ballot(__builtin_amdgcn_raw_buffer_load_b32(rf, abq[k < 5 ? 0 : k < 9 ? 1 : 2], 0, 16) == tag);
        asm volatile("" ::: "memory");
      }
    };
    unsigned ksel, bad = 0;
    int isel;
    unsigned xw;
    auto bvec = [&](unsigned src, const unsigned (&sl)[4]) __attribute__((always_inline)) {
      return (u32x4){__builtin_amdgcn_perm(0, src, sl[0]), __builtin_amdgcn_perm(0, src, sl[1]),
                     __builtin_amdgcn_perm(0, src, sl[2]), __builtin_amdgcn_perm(0, src, sl[3])};
    };
#define BC8(v, p) (unsigned)__builtin_amdgcn_update_dpp(__builtin_amdgcn_update_dpp(0, (int)(v), 0x150 + (p), 0xf, 0x3, false), (int)(v), 0x158 + (p), 0xf, 0xc, false)
    float* lg = (float*)(smem + LDS_GU);
    f32x4 macc = {0.f, 0.f, 0.f, 0.f};
    u32x2 sv;
    unsigned hp, gbad;
    float s0, s1;
    auto work2 = [&](auto JB, auto GP) __attribute__((always_inline)) {
      constexpr int jb = decltype(JB)::value, gp = decltype(GP)::value;
      SB();
      if constexpr (jb == -1 && gp < 4) {
        constexpr int c = gp == 0 ? 0xB1 : gp == 1 ? 0x4E : gp == 2 ? 0x141 : 0x140;
        merge2(k1, k2, dpp_u<c>(k1), dpp_u<c>(k2));
        PIN2(k1, k2);
      }
      if constexpr (jb == -1 && gp == 4) {
        ksel = tk ? k2 : k1;
        isel = (0xffff - (int)(ksel & 0xffffu)) & (S_ - 1);
        k1 = (isel * D_ + d0 + rq * 16 + (lane & 7) * 2) * 2; k2 = O_ST + isel * 8;
        PIN1(ksel);
        xw = gld4_s(x, k1);
        sv = bld8_sc1(rf, k2);
      }
      if constexpr (jb == 0 && gp == 1) wait_xs<3 + 2 + 2>(xw, sv);
      if constexpr (jb == 0) {
        if constexpr (gp == 1) { if (!__all(sv.y == tag)) sv = granule_wait(stat + isel, tag); s0 = __builtin_amdgcn_rcpf(hi_bf(sv.x)); PIN1(s0); }
        if constexpr (gp == 2) { s1 = ln_elem(lo_bf(xw), lo_bf(sv.x), hi_bf(sv.x), s0); PIN1(s1); }
        if constexpr (gp == 3) { xw = pk2(s1, ln_elem(hi_bf(xw), lo_bf(sv.x), hi_bf(sv.x), s0)); PIN1(xw); }
      }
      if constexpr (jb == DG && gp < 7) { PIN1(dv[gp]); macc = MM(dv[gp], bvec(BC8(hp, gp), sel[0]), macc); PIN1(macc); }
      if constexpr ((jb == 0 && gp >= 4) || jb == 1) {
        constexpr int l = jb == 0 ? gp - 4 : gp + 4;
        if constexpr (!(l & 1)) hp = BC8(xw, l >> 1);
        PIN1(wv[l]);
        macc = MM(wv[l], bvec(hp, sel[l & 1]), macc);
        PIN2(macc, hp);
      }
      if constexpr (jb == GV_JB + 2) {
        if constexpr (gp == 0) { gbad = (gv[0].y ^ tag) | (gv[0].w ^ tag) | (gv[1].y ^ tag) | (gv[1].w ^ tag); PIN1(gbad); }
        if constexpr (gp == 1) { gbad |= (gv[2].y ^ tag) | (gv[2].w ^ tag) | (gv[3].y ^ tag) | (gv[3].w ^ tag); PIN1(gbad); }
        if constexpr (gp == 2) {
          s0 = ((__uint_as_float(gv[0].x) + __uint_as_float(gv[1].x)) + __uint_as_float(gv[2].x)) + __uint_as_float(gv[3].x);
          s1 = ((__uint_as_float(gv[0].z) + __uint_as_float(gv[1].z)) + __uint_as_float(gv[2].z)) + __uint_as_float(gv[3].z);
          PIN2(s0, s1);
        }
        if constexpr (gp == 3) {
#pragma unroll 1
          for (int it = 0; !__all(gbad == 0) && it < SPIN_MAX; ++it) {
            u32x4 q[4];
            gbad = 0;
#pragma unroll
            for (int i = 0; i < 4; ++i) { q[i] = __builtin_amdgcn_raw_buffer_load_b128(rgu, vgu, i * F_ * 16, 16); gbad |= (q[i].y ^ tag) | (q[i].w ^ tag); }
            s0 = ((__uint_as_float(q[0].x) + __uint_as_float(q[1].x)) + __uint_as_float(q[2].x)) + __uint_as_float(q[3].x);
            s1 = ((__uint_as_float(q[0].z) + __uint_as_float(q[1].z)) + __uint_as_float(q[2].z)) + __uint_as_float(q[3].z);
            asm volatile("" ::: "memory");
          }
          const unsigned gp2 = pk2(s0, s1);
          const unsigned up2 = dpp_u<0x128>(gp2);
          s0 = tk ? hi_bf(up2) : lo_bf(gp2); s1 = tk ? hi_bf(gp2) : lo_bf(up2);
          PIN2(s0, s1);
        }
        if constexpr (gp == 4) { s0 = rbf(s0 * __builtin_amdgcn_rcpf(1.f + __expf(-s0))); PIN1(s0); }
        if constexpr (gp == 5) { hp = pk2(s0 * s1, 0.f); PIN1(hp); }
      }
      if constexpr (jb == 10 && gp == 4) code_touch(sd, (j * 4 + pw) * 256);
      SB();
    };
#define W2M(i) work2(IC<-1>{}, IC<i>{})
#define W20(i) work2(IC<0>{}, IC<i>{})
#define W21(i) work2(IC<1>{}, IC<i>{})
    unsigned fs;
    {
      G1_STAGE_B(15, 3 * G1_STAGE);
      G1_STAGE_B(14, 2 * G1_STAGE);
      lgkm0(sd);
      BAR();
      G2_STAGE_NH(0, 0);
      WAIT_VM(6); BAR();
#pragma unroll
      for (int i = 0; i < 4; ++i) kk[i] = bld16_sc1(rf, l16, ko_ + i * 1024);
      G2_STAGE_NH(0, 1);
#pragma unroll
      for (int l = WSN; l < 16; ++l) wv[l] = bld16_nt(rw, vw, l * F_ * 2);
      touch1(rn2, tv7, mb0, 0);
      asm volatile("s_waitcnt vmcnt(%4)" : "+v"(kk[0]), "+v"(kk[1]), "+v"(kk[2]), "+v"(kk[3]) : "i"(16 - WSN + 5) : "memory");
#pragma unroll
      for (int i = 0; i < 4; ++i) {
        k2 = med3u(k1, k2, kk[i].x); k1 = max(k1, kk[i].x);
        k2 = med3u(k1, k2, kk[i].z); k1 = max(k1, kk[i].z);
        bad |= (kk[i].y ^ tag) | (kk[i].w ^ tag);
      }
#pragma unroll 1
      for (int it = 0; !__all(bad == 0) && it < SPIN_MAX; ++it) {
        k1 = 0; k2 = 0; bad = 0;
#pragma unroll
        for (int i = 0; i < 4; ++i) {
          const u32x4 q = __builtin_amdgcn_raw_buffer_load_b128(rf, l16, ko_ + i * 1024, 16);
          k2 = med3u(k1, k2, q.x); k1 = max(k1, q.x);
          k2 = med3u(k1, k2, q.z); k1 = max(k1, q.z);
          bad |= (q.y ^ tag) | (q.w ^ tag);
        }
        asm volatile("" ::: "memory");
      }
      merge2(k1, k2, dpp_u<0xB1>(k1), dpp_u<0xB1>(k2));
      merge2(k1, k2, dpp_u<0x4E>(k1), dpp_u<0x4E>(k2));
      merge2(k1, k2, dpp_u<0x141>(k1), dpp_u<0x141>(k2));
      merge2(k1, k2, dpp_u<0x140>(k1), dpp_u<0x140>(k2));
      if ((lane & 15) == 0) *(u32x2*)(smem + LDS_KEY + (pw * 4 + rq) * 8) = (u32x2){k1, k2};
      PAGE(3);
      wait_vm<17 - WSN>();
      BAR_LGKM();
#pragma unroll
      for (int jj = 0; jj < 4; ++jj) tv[jj] = *(const u32x4*)(smem + G2_MRING + m_off(0) + (jj * 4 + pw) * 1024 + lane * 16);
      { const u32x2 kp = *(const u32x2*)(smem + LDS_KEY + ci * 8); k1 = kp.x; k2 = kp.y; }
#pragma unroll
      for (int jj = 0; jj < 4; ++jj) __builtin_amdgcn_raw_buffer_store_b128(tv[jj], rm2, vm2 + jj * 4096 + 2 * tt * 16384, 0, 16);
      G2_STAGE_N(1, W2M);
      WAIT_VM(10);
      __builtin_amdgcn_raw_buffer_store_b32(tag, rf, abo, 0, 16);
      touch1(rn2, tv2, mb0, 0);
      {
        lds_vi rel = (lds_vi)(smem + LDS_SEL + 12);
        int it = 0;
        while (rel[0] != 4 && it < SPIN_MAX * 8) ++it;
        asm volatile("" ::: "memory");
      }
      G2_STAGE_N(2, W20);
      __builtin_amdgcn_s_sleep(2);
      WAIT_VM(10);
      fs = bld4_sc1(rf, abq[0]);
    }
    BAR();
#pragma unroll
    for (int jj = 0; jj < 4; ++jj) tv[jj] = *(const u32x4*)(smem + G2_MRING + m_off(1) + (jj * 4 + pw) * 1024 + lane * 16);
    constexpr int SPB = SP0 + SP1E * SP1;
    G2_STAGE_N(3, NOW);
    asm volatile("s_waitcnt vmcnt(8)" : "+v"(fs) :: "memory");
    ready = (unsigned)__ballot(fs == tag);
    need(2);
    G2_STAGE_M(2, NOW);
    need(3);
    G2_STAGE_M(3, NOW);
    fs = bld4_sc1(rf, abq[1]);
    W21(0); W21(1); W21(2); W21(3); W21(4); W21(5); W21(6); W21(7); W21(8); W21(9); W21(10); W21(11);
    asm volatile("" : "+v"(tv[0]), "+v"(tv[1]), "+v"(tv[2]), "+v"(tv[3]));
    auto step2 = [&](auto JB) __attribute__((always_inline)) {
      constexpr int jb = decltype(JB)::value;
      if constexpr (jb == 3) PAGE(4);
      if constexpr (jb == 9) PAGE(5);
      if constexpr (jb == 12) PAGE(7);
      constexpr auto must = [](int q) { return (q == 2 ? 4 : 0) + ((FSM >> q) & 1) + dig(GVS, q) + (q == XJ ? 3 : 0); };
      if constexpr (jb == 2) wait_vm<4 + must(1)>();
      else wait_vm<dn_cnt(jb - 2) + (jb == 5 ? 1 : 0) + 12 + must(jb - 1) + dn_cnt(jb - 1)>();
      if constexpr (jb == 3) { if (pw == 0 && (lane & ~8) == 0) ((unsigned*)(smem + LDS_SEL))[tk] = ksel; }
      if constexpr ((jb == 2 || jb == DG + 1)) {
        *(f32x4*)(lg + pw * 256 + lane * 4) = macc;
        macc = (f32x4){0.f, 0.f, 0.f, 0.f};
        lgkm0(sd);
      }
      BAR();
      float p0[4], p1[4];
      const int col = (pw & 1) * 64 + lane;
      if constexpr (jb == 3) {
        if (g == 0 && pw == 0 && lane == 0) __hip_atomic_store((unsigned*)flags + FL_EPOCH, epoch + 1u, __ATOMIC_RELAXED, __HIP_MEMORY_SCOPE_AGENT);
      }
      if constexpr (jb == DG + 1) {
        const float* p = lg + (pw & 1) * 32 + (lane >> 4) * 64 + ((2 * lane) & 7) * 4 + ((lane >> 2) & 3);
#pragma unroll
        for (int i = 0; i < 4; ++i) { p0[i] = p[i * 256]; p1[i] = p[i * 256 + 4]; }
      }
      if constexpr (jb == 2) {
        const float* p = lg + (col >> 5) * 64 + (col & 7) * 4 + ((col >> 3) & 3);
#pragma unroll
        for (int i = 0; i < 4; ++i) { p0[i] = p[i * 256]; p1[i] = p[i * 256 + 32]; }
      }
      if constexpr (jb == 4) {
        __builtin_amdgcn_raw_buffer_store_b32(tag, rf, abo + 8 * AB_STRIDE * 4, 0, 16);
      }
      if constexpr ((FSM >> (jb - 2)) & 1) {
        asm volatile("" : "+v"(fs));
        ready |= (unsigned)__ballot(fs == tag);
      }
      if constexpr (jb == GV_JB + 2) asm volatile("" : "+v"(gv[0]), "+v"(gv[1]), "+v"(gv[2]), "+v"(gv[3]));
      auto w2 = [&](auto GP) __attribute__((always_inline)) {
        constexpr int gp = decltype(GP)::value;
        if constexpr ((jb == 2 || jb == DG + 1) && gp == 0) {
          p0[0] = (p0[0] + p0[1]) + (p0[2] + p0[3]); p1[0] = (p1[0] + p1[1]) + (p1[2] + p1[3]);
          PIN2(p0[0], p1[0]);
        }
        if constexpr (jb == DG + 1 && gp == 1) {
          if (pw < 2) __builtin_amdgcn_raw_buffer_store_b64((u32x2){pk2(p0[0], p1[0]), tag}, mk_rsrc(wsp + WS_DN), (((pw * 8 + cg8) * 32 + rgb) * 64 + lane) * 8, 0, 16);
        }
        if constexpr (jb == XJ && gp % 4 == 1) epi_setup(gp / 4);
        work2(JB, GP);
        if constexpr (gp < dig(SPS, jb)) spf(IC<SPB + acc_dig(SPS, jb) + gp>{});
      };
#define W2(i) w2(IC<(i)>{})
      if (jb < 10) G2_STAGE_N(jb + 2, W2);
      if (jb == 10) { G2_STAGE_Q(0, W2, 0); need(14); G2_STAGE_M2(14, W2); }
      if (jb == 11) { G2_STAGE_Q(2, W2, 0); need(15); G2_STAGE_M2(15, W2); }
      if (jb < 12) {
        if (jb >= 10) { W2(6); W2(7); }
        need(jb + 2);
        G2_STAGE_M(jb + 2, W2);
      }
      if (jb == 10) G2_STAGE_Q(1, NOW, 0);
      if (jb == 11) G2_STAGE_Q(3, NOW, 0);
      if (jb == 12) { G2_STAGE_Q(4, W2, 0); G2_STAGE_Q(5, W2, 2); G2_STAGE_Q(6, W2, 4); G2_STAGE_Q(7, W2, 6); }
      if constexpr (jb == 2) {
#pragma unroll
        for (int jj = 0; jj < 4; ++jj) __builtin_amdgcn_raw_buffer_store_b128(tv[jj], rm2, vm2 + jj * 4096 + (2 * tt + 1) * 16384, 0, 16);
      }
      if constexpr ((FSM >> jb) & 1) fs = bld4_sc1(rf, abq[jb < 5 ? 1 : 2]);
#pragma unroll
      for (int i = acc_dig(GVS, jb); i < acc_dig(GVS, jb + 1); ++i) gv[i] = bld16_sc1(rgu, vgu, i * F_ * 16);
      if constexpr (jb == XJ) { xq_load(0); xq_load(1); xq_load(2); }
#pragma unroll
      for (int i = dn_first(jb); i < dn_first(jb) + dn_cnt(jb); ++i) dv[i] = bld16_nt(rdn, vdn, i * D_ * 2);
      if constexpr (jb == 2) {
        if (moe && pw < 2)
          __builtin_amdgcn_raw_buffer_store_b128((u32x4){__float_as_uint(p0[0]), tag, __float_as_uint(p1[0]), tag},
                                                 rgu, ((mat * 4 + dq) * F_ + cg * 128 + col) * 16, 0, 16);
      }
      SB();
      if constexpr (dig(SPS, jb) > 12) spf(IC<SPB + acc_dig(SPS, jb) + 12>{});
      if constexpr (dig(SPS, jb) > 13) spf(IC<SPB + acc_dig(SPS, jb) + 13>{});
      SB();
    };
    static_assert(DG + 1 <= 12 && DG >= GV_JB + 3 && dn_first(DG - 2) == 7 && acc_dig(GVS, GV_JB + 1) == 4, "schedule");
    static_assert(SPB + acc_dig(SPS, 14) == (16 - VAN) * 16, "all tiles are prefetched");
    step2(IC<2>{}); step2(IC<3>{}); step2(IC<4>{}); step2(IC<5>{}); step2(IC<6>{}); step2(IC<7>{}); step2(IC<8>{}); step2(IC<9>{}); step2(IC<10>{}); step2(IC<11>{}); step2(IC<12>{});
    lgkm0(sd);
    WAIT_VM(8); BAR();
    G2_STAGE_Q(8, NOW, 0); G2_STAGE_Q(9, NOW, 0); G2_STAGE_Q(10, NOW, 0); G2_STAGE_Q(11, NOW, 0);
    WAIT_VM(8); BAR();
    G2_STAGE_Q(12, NOW, 0); G2_STAGE_Q(13, NOW, 0);
    __builtin_amdgcn_s_setprio(0);
    asm volatile("" : "+v"(xq[0]), "+v"(xq[1]), "+v"(sq));
    cv_calc();
    __builtin_amdgcn_s_setprio(3);
    WAIT_VM(0); BAR();
    G2_STAGE_Q(14, NOW, 0); G2_STAGE_Q(15, NOW, 0);
    __builtin_amdgcn_s_setprio(0);
    epi_setup(3);
    key[0] = __builtin_amdgcn_readlane(ksel, 0); key[1] = __builtin_amdgcn_readlane(ksel, 8);
    far_calc();
    const int ec = seg0((lane >> 4) & 1, (lane >> 3) & 1);
    int dno = (ec >> 7) * 16384 + (lane >> 5) * 512 + (ec & 127) * 4 + (lane & 7) * 16;
    PIN1(dno);
    const __amdgpu_buffer_rsrc_t rd = mk_rsrc(wsp + WS_DN + pw * 4096);
    float sg0 = rbf(__builtin_amdgcn_rcpf(1.f + __expf(-key_score(key[0])))), sg1 = rbf(__builtin_amdgcn_rcpf(1.f + __expf(-key_score(key[1]))));
    PIN2(sg0, sg1);
    u32x4 dq_[4];
    asm volatile("s_waitcnt vmcnt(0)\n\ts_cmp_lg_u32 %7, 0\n\ts_cbranch_scc1 1f\n\ts_sleep 2\n\t"
                 "buffer_load_dwordx4 %0, %4, %5, %6 offen sc1\n\tbuffer_load_dwordx4 %1, %4, %5, %6 offen offset:1024 sc1\n\t"
                 "buffer_load_dwordx4 %2, %4, %5, %6 offen offset:2048 sc1\n\tbuffer_load_dwordx4 %3, %4, %5, %6 offen offset:3072 sc1\n"
                 "1:\n\ts_barrier\n\ts_waitcnt vmcnt(0)"
                 : "=&v"(dq_[0]), "=&v"(dq_[1]), "=&v"(dq_[2]), "=&v"(dq_[3])
                 : "v"(dno), "s"(rd), "s"((far0 & ~9) ? 131072 : 0), "s"(min(far0 & ~9, far1 & ~9)) : "scc", "memory");
    bool first = true;
#pragma unroll 1
    for (int c = 0; c < 2; ++c) {
      if (__builtin_expect(((c ? far1 : far0) & ~9) != 0, 1)) continue;
#pragma unroll 1
      for (int it = 0; it < SPIN_MAX; ++it) {
        unsigned bad = 0;
#pragma unroll
        for (int i = 0; i < 4; ++i) bad |= (dq_[i].y ^ tag) | (dq_[i].w ^ tag);
        if ((first || it) && __all(bad == 0)) break;
        if (it) __builtin_amdgcn_s_sleep(4);
#pragma unroll
        for (int i = 0; i < 4; ++i) dq_[i] = __builtin_amdgcn_raw_buffer_load_b128(rd, dno + i * 1024, c * 131072, 16);
        asm volatile("" ::: "memory");
      }
      first = false;
      const float sg = c ? sg1 : sg0;
      f32x4 s4 = {0.f, 0.f, 0.f, 0.f};
#pragma unroll
      for (int i = 0; i < 4; ++i) s4 += (f32x4){lo_bf(dq_[i].x), hi_bf(dq_[i].x), lo_bf(dq_[i].z), hi_bf(dq_[i].z)};
      *(f32x2*)(smem + LDS_FIN + (c * 4 + pw) * 512 + (lane & 31) * 16 + (lane >> 5) * 8) = (f32x2){fold32(s4[0], s4[2]) * sg, fold32(s4[1], s4[3]) * sg};
      asm volatile("s_waitcnt lgkmcnt(0)" ::: "memory");
      if (lane == 0) asm volatile("ds_add_u32 %0, %1" :: "v"(lds_addr(smem + LDS_SEL + 24 + c * 4)), "v"(1) : "memory");
    }
  } else {
  {
    const int q = j * 4 + wid;
    constexpr int Q1 = WS_MB, Q2 = Q1 + 8, Q3 = Q2 + 8, Q4 = Q3 + 7, Q5 = Q4 + 7, Q6 = Q5 + 7, Q7 = Q6 + 4, Q8 = Q7 + 4, QC = Q8 + 1;
    static_assert(QC <= 128, "warm-up slots");
    unsigned dmy = 0;
    if (q < QC) {
      const char* base; int b0;
      if (q < Q1) { base = wsp; b0 = 0; }
      else if (q < Q2) { base = (const char*)ws_re; b0 = Q1; }
      else if (q < Q3) { base = (const char*)ws_im; b0 = Q2; }
      else if (q < Q4) { base = (const char*)w_gate; b0 = Q3; }
      else if (q < Q5) { base = (const char*)w_up; b0 = Q4; }
      else if (q < Q6) { base = (const char*)w_down; b0 = Q5; }
      else if (q < Q7) { base = (const char*)out; b0 = Q6; }
      else if (q < Q8) { base = (const char*)x; b0 = Q7; }
      else { base = (const char*)w_r; b0 = Q8; }
      base += (size_t)(q - b0) << 20;
      asm volatile("s_load_dword %0, %1, 0" : "+s"(dmy) : "s"(base) : "memory");
    }
    if (wid == 0) { CALL_PG(1); CALL_PG(8); CALL_PG(5); CALL_PG(13); }
    else if (wid == 1) { CALL_PG(2); CALL_PG(9); CALL_PG(6); }
    else if (wid == 2) { CALL_PG(3); CALL_PG(10); CALL_PG(7); }
    else { CALL_PG(4); CALL_PG(11); CALL_PG(12); }
    BAR();
    lgkm0(dmy);
    TL(1);
  }
  PAGE(8);
  epi_setup(0); epi_setup(1); epi_setup(2); epi_setup(3);
  const int wr = wid >> 1, wc = wid & 1;
  const int sw = swz(fr), fl = lrw(fr);
  const int ko0 = ((0 + fq) ^ sw) * 16, ko1 = ((4 + fq) ^ sw) * 16;
  bf16x8 fm[2][4], fn[2][4];
  {
    const char* pm = smem + (wr * 64 + fl) * 128;
    const char* pn = smem + 16384 + (wc * 64 + fl) * 128;
    f32x4 acc[4][4];
#pragma unroll
    for (int i = 0; i < 4; ++i)
#pragma unroll
      for (int jn = 0; jn < 4; ++jn)
#pragma unroll
        for (int q = 0; q < 4; ++q) acc[i][jn][q] = 0.f;
#define TO(i) ((i) * 2048)
#define LDSP(T, a) ((__attribute__((address_space(3))) T*)(size_t)(a))
#define SLOTS(n) _Pragma("unroll") for (int q = 0; q < (n); ++q)
#define MFA(c, m, n) asm volatile("v_mfma_f32_16x16x32_bf16 %0, %2, %1, %0" : "+v"(c) : "v"(m), "v"(n))
#define MFA2(c, m, n, m2, n2) asm volatile("v_mfma_f32_16x16x32_bf16 %0, %2, %1, %0\n\tv_mfma_f32_16x16x32_bf16 %0, %4, %3, %0" : "+v"(c) : "v"(m), "v"(n), "v"(m2), "v"(n2))
    unsigned lm[2][2], ln[2][2];
#pragma unroll
    for (int u = 0; u < 2; ++u)
#pragma unroll
      for (int kk = 0; kk < 2; ++kk) {
        lm[u][kk] = lds_addr(pm) + u * 2 * G1_STAGE + (kk ? ko1 : ko0);
        ln[u][kk] = lds_addr(pn) + u * 2 * G1_STAGE + (kk ? ko1 : ko0);
        asm volatile("" : "+v"(lm[u][kk]), "+v"(ln[u][kk]));
      }
    auto ldm = [&](int stg, int i, int kk) __attribute__((always_inline)) { fm[kk][i] = *LDSP(const bf16x8, lm[stg >> 1][kk] + (stg & 1) * G1_STAGE + TO(i)); };
    auto ldn = [&](int stg, int jn, int kk) __attribute__((always_inline)) { fn[kk][jn] = *LDSP(const bf16x8, ln[stg >> 1][kk] + (stg & 1) * G1_STAGE + TO(jn)); };
    auto quad = [&](int a, int c, int d, int s) __attribute__((always_inline)) {
      const int i = (s < 2 || s >= 6) ? a : a + 1, jn = s < 4 ? c : d, kk = (s == 1 || s == 2 || s == 5 || s == 6);
      MFA(acc[i][jn], fm[kk][i], fn[kk][jn]);
    };
    auto quad2 = [&](int a, int c, int d, int s) __attribute__((always_inline)) {
      const int i = (s < 2 || s >= 6) ? a : a + 1, jn = s < 4 ? c : d, kk = (s == 2 || s == 6);
      MFA2(acc[i][jn], fm[kk][i], fn[kk][jn], fm[1 - kk][i], fn[1 - kk][jn]);
    };
    auto half1 = [&](int stg) __attribute__((always_inline)) {
      const int ja = (stg & 1) * 2, jb = 2 - ja;
      SLOTS(8) {
        quad(0, ja, ja + 1, q);
        if (q < 4) ldn(stg, jb + (q >> 1), q & 1); else ldm(stg, 2 + ((q - 4) >> 1), q == 5 || q == 6);
        SB();
      }
      SLOTS(4) { quad2(0, jb, jb + 1, 2 * q); SB(); }
    };
    auto half2 = [&](int stg) __attribute__((always_inline)) {
      const int ja = (stg & 1) * 2, jb = 2 - ja, nx = (stg + 1) & 3;
      SLOTS(8) {
        quad(2, jb + 1, jb, q);
        if (!(q & 1)) ldm(nx, q >> 2, q == 2 || q == 4);
        SB();
      }
      SLOTS(8) {
        quad(2, ja, ja + 1, q);
        if (!(q & 1)) ldn(nx, jb + (q >> 2), (q >> 1) & 1);
        SB();
      }
    };
    unsigned ae[7];
    {
      int l_ = lane;
      asm volatile("" : "+v"(l_));
      const int p = l_ >> 4, dn = sym_dn(p), hf = (p >> 1) ^ (dn & z);
#pragma unroll
      for (int u = 0; u < 7; ++u) {
        const int T = u < 3 ? wc * 4 : (wc * 4 + u - 3 - dn) & 7, i = u < 3 ? (u + 1) ^ ((u & 1) ? 0 : dn * 2) : 0;
        const int f = dn * 8 + ((T & 2) << 1) + ((T & 4) >> 1) + (T & 1);
        ae[u] = lds_addr(smem + G2_MRING) + (hf * 64 + i * 16 + lrw(f)) * 128 + (((wr * 4 + ((l_ >> 2) & 3)) ^ swz(f)) * 16) + (l_ & 3) * 4;
        asm volatile("" : "+v"(ae[u]));
      }
    }
    unsigned al = lds_addr(smem + LDS_MB) + (wr * 32 + fr * 2) * 4, ac = lds_addr(smem + LDS_MB) + (256 + wc * 64 + fq * 4) * 4;
    asm volatile("" : "+v"(al), "+v"(ac));
    SLOTS(16) asm volatile("" : "+v"(acc[q >> 2][q & 3]));
    SB();
    SLOTS(4) { ldm(0, q >> 1, q & 1); ldn(0, q >> 1, q & 1); }
    SB();
#pragma unroll 1
    for (int k4 = 0; k4 < G1_NKT - 4; k4 += 4) {
      half1(0); BAR_LGKM();

      SB(); half2(0);
      half1(1); BAR_LGKM();

      SB(); half2(1);
      half1(2); BAR_LGKM();

      SB(); half2(2);
      half1(3); BAR_LGKM();

      SB(); half2(3);
    }
    bf16x8 gm[4][4], gn[2][8];
    auto rdm1 = [&](int set, int t, int h, int i, int kk) __attribute__((always_inline)) {
      gm[set][kk * 2 + i] = *LDSP(const bf16x8, lm[(t >> 1) & 1][kk] + (t & 1) * G1_STAGE + h * 4096 + i * 2048);
    };
    auto rdg = [&](int set, int t, int h, int r) __attribute__((always_inline)) {
      if (r == 0 || r == 2 || r == 4 || r == 5) rdm1(set, t, h, r >= 4, r == 2 || r == 4);
      else {
        const int jn = r < 4 ? 0 : (r - 4) >> 1, kk = r < 4 ? r >> 1 : r & 1;
        gn[set][kk * 4 + jn] = *LDSP(const bf16x8, ln[(t >> 1) & 1][kk] + (t & 1) * G1_STAGE + TO(jn));
      }
    };
    auto mmg = [&](int ms, int ns, int h, int jn, int r, int f) __attribute__((always_inline)) {
      const int i = (r >> 1) ^ f, kk = r == 1 || r == 2;
      MFA(acc[h * 2 + i][jn], gm[ms][kk * 2 + i], gn[ns][kk * 4 + jn]);
    };
    f32x2 rsv, mrv;
    f32x4 cev[4];
    float tc[32];
    unsigned ep;
    auto rds = [&](int h, int r) __attribute__((always_inline)) {
      if (r == 0) mrv = *LDSP(const f32x2, al + h * 256); else if (r == 5) rsv = *LDSP(const f32x2, al + 512 + h * 256);
      else cev[r - 1] = *LDSP(const f32x4, ac + (r - 1) * 64);
    };
    auto prep = [&](int n) __attribute__((always_inline)) {
      tc[n] = llvm_fmul_legacy(mrv[n & 1], cev[n >> 3][(n >> 1) & 3]);
      asm volatile("" : "+v"(tc[n]));
    };
    auto epi = [&](int h, int n) __attribute__((always_inline)) {
      const int jn = n >> 4, r = (n >> 2) & 3, u = n & 3, e = (n >> 2) * 2;
      if (u == 0) {}
      else if (u == 1) asm volatile("v_fmac_f32 %0, %2, %4\n\tv_fmac_f32 %1, %3, %5\n\tv_cvt_pk_bf16_f32 %0, %0, %1" : "+v"(tc[e]), "+v"(tc[e + 1])
                                    : "v"(acc[h * 2][jn][r]), "v"(acc[h * 2 + 1][jn][r]), "v"(rsv[0]), "v"(rsv[1]));
      else if (u == 2) ep = __float_as_uint(tc[e]);
      else *LDSP(unsigned, (r ? ae[r - 1] + (jn & 1) * 128 + (jn >> 1) * lrw(4) * 128 : ae[3 + jn]) + m_off(h)) = ep;
      SB();
    };
    SB();
    half1(0);
    BAR_LGKM();
    SB();
    TL(14);
    SLOTS(16) {
      if (q < 8) quad(2, 3, 2, q); else quad(2, 0, 1, q - 8);
      if (q < 12) rdg(0, 14, 0, q); else rdg(1, 13, 0, q - 12);
      SB();
    }
    BAR();
    SB();
    TL(15);
    SLOTS(16) {
      mmg(0, 0, 0, q >> 2, q & 3, (q >> 2) & 1);
      if (q < 6) rds(0, q); else if (q < 14) rdg(1, 13, 0, q - 2);
      if (q >= 6) prep(q - 6);
      if (q >= 14) prep(q - 4);
      SB();
    }
    PAGE(9);
    BAR();
    SB();
    TL(16);
    {
      constexpr int ct[8] = {1, 1, 0, 0, 1, 0, 1, 0}, cj[8] = {0, 1, 0, 1, 2, 2, 3, 3}, cf[8] = {0, 1, 0, 1, 0, 1, 0, 1};
      constexpr auto en = [](int q) { return q < 15 ? 0 : q < 26 ? (q - 14) * 3 - (q == 25) : q < 27 ? 32 : q < 32 ? 32 + (q - 26) * 3 + (q == 31) : 48; };
      SLOTS(32) {
        mmg(ct[q >> 2], ct[q >> 2], 0, cj[q >> 2], q & 3, cf[q >> 2]);
        if (q < 12) rdg(0, 15, 0, q);
        else if (q < 15 || q == 17) { const int r = q < 15 ? q - 12 : 3; rdm1(2, 15, 1, r >> 1, r == 1 || r == 2); }
        else if (q == 20 || q == 23 || q == 25 || q == 26) { const int r = q == 20 ? 0 : q == 23 ? 1 : q - 23; rdm1(3, 13, 1, r >> 1, r == 1 || r == 2); }
        if (q < 15) prep(12 + q);
        if (q >= 10 && q < 15) prep(17 + q);
        SB();
#pragma unroll
        for (int n = q ? en(q - 1) : 0; n < en(q); ++n) epi(0, n);
      }
    }
    SLOTS(2) { mmg(2, 0, 1, 0, q, 0); SB(); }
    asm volatile("s_nop 1");
    SLOTS(16) { epi(0, 48 + q); if (q == 3 || q == 9) { mmg(2, 0, 1, 0, q == 3 ? 2 : 3, 0); SB(); } }
    BAR_LGKM();
    SB();
    TL(60);
    SLOTS(12) {
      mmg(2, 0, 1, 1 + (q >> 2), q & 3, ~(q >> 2) & 1);
      if (q < 6) rds(1, q); else rdg(0, 14, 1, q - 6);
      if (q >= 8) prep(q - 8);
      SB();
    }
    SLOTS(16) {
      mmg(3, 1, 1, q >> 2, q & 3, (q >> 2) & 1);
      if (q < 6) rdg(0, 14, 1, q + 6);
      if (q < 4) prep(4 + q); else { prep(2 * q); prep(2 * q + 1); }
      SB();
    }
    asm volatile("s_waitcnt lgkmcnt(0)" ::: "memory");
    if (lane == 0) asm volatile("ds_add_u32 %0, %1" :: "v"(lds_addr(smem + LDS_SEL + 12)), "v"(1) : "memory");
    SB();
    SLOTS(16) {
      mmg(0, 0, 1, q >> 2, q & 3, (q >> 2) & 1);
      if (q < 4) fn[0][q] = *(const bf16x8*)(smem + (wr * 128 + fl) * 128 + (q >> 1) * 2048 + ((q & 1) ? ko1 : ko0));
      else if (q < 8) fm[q & 1][(q - 4) >> 1] = *(const bf16x8*)(smem + G2_MRING + m_off(0) + (wc * 64 + fl) * 128 + TO((q - 4) >> 1) + ((q & 1) ? ko1 : ko0));
      SB();
      if (q >= 8) { epi(1, 3 * q - 24); epi(1, 3 * q - 23); epi(1, 3 * q - 22); }
    }
    TL(17);
    SLOTS(40) epi(1, 24 + q);
  }

  const char* pn = smem + (wr * 128 + fl) * 128;
  const char* pm = smem + G2_MRING + (wc * 64 + fl) * 128;
  f32x4 acc[8][4];
  auto mm = [&](bool imaj, int set, int q2, int s, int mode) __attribute__((always_inline)) {
    int i, jj, kk;
    if (imaj) { i = s >> 2; const int r = s & 3; jj = (r >> 1) ^ (i & 1); kk = (r == 1 || r == 2); }
    else { jj = s >> 3; const int r = s & 7; i = jj ? 3 - (r >> 1) : (r >> 1); kk = ((r + 1) >> 1) & 1; }
    if (mode == 1 && !(s & 1)) asm volatile("v_mfma_f32_16x16x32_bf16 %0, %2, %1, 0" : "=&v"(acc[q2 * 2 + jj][i]) : "v"(fm[kk][i]), "v"(fn[set][jj * 2 + kk]));
    else if (mode) asm volatile("v_mfma_f32_16x16x32_bf16 %0, %2, %1, %0" : "+v"(acc[q2 * 2 + jj][i]) : "v"(fm[kk][i]), "v"(fn[set][jj * 2 + kk]));
    else acc[q2 * 2 + jj][i] = __builtin_amdgcn_mfma_f32_16x16x32_bf16(fn[set][jj * 2 + kk], fm[kk][i], acc[q2 * 2 + jj][i], 0, 0, 0);
  };
  unsigned bn[2], bm[2], an[2], am[2];
#pragma unroll
  for (int kk = 0; kk < 2; ++kk) {
    bn[kk] = lds_addr(pn) + (kk ? ko1 : ko0); bm[kk] = lds_addr(pm) + (kk ? ko1 : ko0);
    asm volatile("" : "+v"(bn[kk]), "+v"(bm[kk]));
    an[kk] = bn[kk]; am[kk] = bm[kk] + m_off(0);
  }
#define RDN(jt, kk, o) (*LDSP(const bf16x8, an[kk] + (o) + (jt) * 2048))
#define RDM(i, kk, o) (*LDSP(const bf16x8, am[kk] + (o) + TO(i)))
  PAGE(10);
  TL(18);
  TL(19);
  tag = __builtin_amdgcn_readfirstlane(((const unsigned*)(smem + LDS_SEL))[5]);
  {
    int son = 0;
    auto tile = [&](int kt, int zero) __attribute__((always_inline)) {
      SLOTS(16) {
        mm(true, 0, 0, q, zero);
        if (!(q & 1)) {
          if (q < 8) fm[(q >> 1) & 1][2 + (q >> 2)] = RDM(2 + (q >> 2), (q >> 1) & 1, 0);
          else { const int r = (q - 8) >> 1; fn[1][r] = RDN(2 + (r >> 1), r & 1, 0); }
        }
        SB();
      }
      SLOTS(16) {
        mm(false, 1, 1, q, zero);
        if (!(q & 1) && q < 8) fn[0][q >> 1] = RDN(4 + (q >> 2), (q >> 1) & 1, 0);
        if (zero == 2 && q >= 9 && (q & 1) && q < 15) xq_load((q - 9) >> 1);
        SB();
      }
      const int som = son >> 1;
      son = son == 65536 ? 0 : son + 32768;
      SLOTS(16) {
        mm(false, 0, 2, q, zero);
        if (!(q & 1) && q < 8) fn[1][q >> 1] = RDN(6 + (q >> 2), (q >> 1) & 1, 0);
        if (q == 9 || q == 11) { an[q == 11] = bn[q == 11] + son; asm volatile("" : "+v"(an[q == 11])); }
        if (q == 13 || q == 15) { am[q == 15] = bm[q == 15] + som; asm volatile("" : "+v"(am[q == 15])); }
        SB();
      }

      BAR_LGKM();

      SB();
      SLOTS(16) {
        mm(true, 1, 3, q, zero);
        if (!(q & 1)) {
          if (q < 8) fn[0][q >> 1] = RDN(q >> 2, (q >> 1) & 1, 0);
          else fm[(q >> 1) & 1][(q - 8) >> 2] = RDM((q - 8) >> 2, (q >> 1) & 1, 0);
        }
        SB();
      }
    };
    SB();
    tile(0, 1);
#pragma unroll 1
    for (int kt = 1; kt < G2_NKT - 5; ++kt) tile(kt, 0);
    tile(G2_NKT - 5, 2);
  }
  unsigned fx;
  int fo = lane < 32 ? kq * 512 + lane * 4 : (int)0x80000000;
  {
    constexpr auto chp = [](int g) { return g == 0 || g_t[g] != g_t[g - 1]; };
    constexpr auto chn = [](int g) { return g + 1 < NG && g_t[g + 1] != g_t[g]; };
    unsigned an2[2], an3[2], am2[2];
#pragma unroll
    for (int kk = 0; kk < 2; ++kk) {
      an[kk] = bn[kk]; an2[kk] = bn[kk] + 65536; an3[kk] = bn[kk] + (G2_MRING - wr * 8192); am[kk] = bm[kk]; am2[kk] = bm[kk] - (G2_MRING - wc * 8192);
      asm volatile("" : "+v"(an[kk]), "+v"(am[kk]), "+v"(am2[kk]));
    }
    int vo;
    u32x4 pk;
    u32x2 kv;
    auto publish = [&](int d, int h) __attribute__((always_inline)) {
      if (h == 0) { vo = ml; PINV(vo); vo += (((d + kq + 1) & 3) * 4 + kq) * 4 * MSG; PINV(vo); }
      const f32x4 v = acc[2 * d + (h >> 2)][h & 3];
      pk[(h & 1) * 2] = pk2(v[0], v[1]); pk[(h & 1) * 2 + 1] = pk2(v[2], v[3]);
      if (h & 1) __builtin_amdgcn_raw_buffer_store_b128(pk, rs, vo + (h >> 1) * 1024, 0, 16);
    };
#define MSG_FLAG(d) __builtin_amdgcn_raw_buffer_store_b32(tag, rc, fo + (((d) + kq + 1) & 3) * 2048, 0, 16)
    auto rdn = [&](int g, int r) __attribute__((always_inline)) {
      fn[g & 1][r] = *LDSP(const bf16x8, (g_n[g] >= G2_MRING ? an3[r & 1] + (g_n[g] - G2_MRING) : g_n[g] >= 65536 ? an2[r & 1] + (g_n[g] - 65536) : an[r & 1] + g_n[g]) + (r >> 1) * 2048);
    };
    auto rdm = [&](int t, int i, int kk) __attribute__((always_inline)) { fm[kk][i] = *LDSP(const bf16x8, (t < 2 ? am[kk] + m_off(12 + t) : am2[kk] + n_off(10 + t) + 8192) + TO(i)); };
    auto group = [&](auto GI) __attribute__((always_inline)) {
      constexpr int gi = decltype(GI)::value;
      if (gi == 1) { BAR_LGKM();
        TL(47);
        SB(); }
      if (gi == 3 || gi == 7) {
        if (gi == 3) PAGE(11);
        TL(gi == 3 ? 53 : 55);
        BAR_LGKM();
        TL(gi == 3 ? 54 : 58);
        SB();
        if (gi == 3) kv = *(const u32x2*)(smem + LDS_SEL);
        SB();
      }
      if (gi == 13) {
        PAGE(13);
        BAR();
        SB(); }
#pragma unroll
      for (int q = 0; q < 16; ++q) {
        const int n = gi * 16 + q;
        if (n == 152) { WAIT_VM(4); MSG_FLAG(0); SB(); }
        if (n == 216) { WAIT_VM(5); MSG_FLAG(1); SB(); }
        if (n == 250) { WAIT_VM(1); MSG_FLAG(2); fx = bld4_sc1(rc, po); SB(); }
        if (n == 13 || n == 29) { PINV(an2[n == 29]); }
        if (n == 93 || n == 95) { PINV(an3[n == 95]); }
        mm(chp(gi) || chn(gi), gi & 1, gi >> 2, q, 2);
        constexpr bool ni = gi + 1 < NG && (chp(gi + 1 < NG ? gi + 1 : gi) || chn(gi + 1 < NG ? gi + 1 : gi));
        auto nr = [](int r) { return ni && r >= 2 ? 5 - r : r; };
        if constexpr (chp(gi) && chn(gi)) {
          if (q < 4) rdm(g_t[gi], 2 + (q >> 1), q & 1); else if (q < 8) rdn(gi + 1, nr(q - 4)); else if (q < 12) rdm(g_t[gi + 1], (q - 8) >> 1, q & 1);
        } else if constexpr (chp(gi)) {
          if (q < 4) rdm(g_t[gi], 2 + (q >> 1), q & 1);
          else if (gi + 1 < NG && q < 12 && !(q & 1)) rdn(gi + 1, nr((q - 4) >> 1));
        } else if constexpr (chn(gi)) {
          if (!(q & 1)) { if (q < 8) rdn(gi + 1, nr(q >> 1)); else rdm(g_t[gi + 1], (q - 8) >> 2, (q >> 1) & 1); }
        } else {
          if (!(q & 1) && q < 8) rdn(gi + 1, nr(q >> 1));
        }
        if (n >= 68 && n < 84 && !(n & 1)) publish(0, (n - 68) >> 1);
        if (gi == 8 && !(q & 1)) publish(1, q >> 1);
        if (gi == 12 && q < 8) publish(2, q);
        if (n == 60) { key[0] = __builtin_amdgcn_readfirstlane(kv.x); key[1] = __builtin_amdgcn_readfirstlane(kv.y); }
        SB();
      }
    };
#define GR4(i) group(IC<i>{}); group(IC<i + 1>{}); group(IC<i + 2>{}); group(IC<i + 3>{});
    GR4(0) GR4(4) GR4(8) GR4(12)
    asm volatile("s_nop 7");
    SB();
  }
  TL(50);
#pragma unroll
  for (int i = 0; i < 4; ++i) *(f32x4*)(smem + LDS_ACC + wr * 16384 + wc * 4096 + i * 1024 + lane * 16) = acc[7][i];
#pragma unroll
  for (int i = 0; i < 4; ++i) ya[i] = acc[6][i];
  asm volatile("s_waitcnt lgkmcnt(0)" ::: "memory");
  if (lane == 0) sig[0] = 2;
  SB();
  TL(63);
  wait_xq<3>(xq, sq);
  far_calc();
  cv_calc();
  asm volatile("s_waitcnt vmcnt(0)" : "+v"(fx) :: "memory");
  seen = (unsigned)__ballot(fx == tag);
  }

  PAGE(6);
  u32x4 pp[3][2];
  unsigned e_lo = 0x00003f80u, e_hi = 0x3f800000u;
  asm volatile("" : "+v"(e_lo), "+v"(e_hi));
  __amdgpu_buffer_rsrc_t rs_ = rs, rc_ = rc;
  int mo[3] = {MSG_IDX(0) * 4 * MSG, MSG_IDX(1) * 4 * MSG, MSG_IDX(2) * 4 * MSG}, mv = ml + ha * 2048, mv2 = mv + 1024;
  asm volatile("" : "+s"(rs_), "+s"(rc_), "+s"(mo[0]), "+s"(mo[1]), "+s"(mo[2]));
  int mt = lane < 16 * ha ? lane * 128 : (int)0x80000000;
  unsigned pf[3] = {0, 0, 0};
  asm volatile("" : "+v"(mv), "+v"(mv2), "+v"(mt));
#define MSG_POLL(m) do { \
    if (!((seen >> (m)) & 1)) { \
      unsigned pv_, it_ = SPIN_MAX; \
      asm volatile("1:\n\tbuffer_load_dword %0, %3, %4, 0 offen sc1\n\ts_waitcnt vmcnt(0)\n\tv_cmp_eq_u32 vcc, %5, %0\n\ts_mov_b32 %1, vcc_lo\n\t" \
                   "s_bitcmp1_b32 %1, %6\n\ts_cbranch_scc1 2f\n\ts_sub_u32 %2, %2, 1\n\ts_cbranch_scc1 2f\n\ts_sleep 1\n\ts_branch 1b\n2:" \
                   : "=&v"(pv_), "+s"(seen), "+s"(it_) : "v"(po), "s"(rc_), "s"(tag), "i"(m) : "vcc", "scc", "memory"); \
    } \
    pp[m][0] = __builtin_amdgcn_raw_buffer_load_b128(rs_, mv, mo[m], 16); \
    pp[m][1] = __builtin_amdgcn_raw_buffer_load_b128(rs_, mv2, mo[m], 16); \
    if (m) pf[m] = __builtin_amdgcn_raw_buffer_load_b32(rs_, mt, mo[m], 16); } while (0)
#define MSG_ADD(m) do { \
    _Pragma("unroll") for (int u = 0; u < 8; ++u) { \
      ya[u >> 1][(u & 1) * 2] = add_bf(pp[m][u >> 2][u & 3], e_lo, ya[u >> 1][(u & 1) * 2]); ya[u >> 1][(u & 1) * 2 + 1] = add_bf(pp[m][u >> 2][u & 3], e_hi, ya[u >> 1][(u & 1) * 2 + 1]); } } while (0)
  MSG_POLL(2);
  if (ha) {
    int it = 0;
    while (sig[0] != 2 && it < SPIN_MAX * 8) { __builtin_amdgcn_s_sleep(1); ++it; }
    asm volatile("" ::: "memory");
#pragma unroll
    for (int i = 0; i < 4; ++i) ya[i] = *(const f32x4*)(smem + LDS_ACC + (wid & 2) * 8192 + (wid & 1) * 4096 + i * 1024 + lane * 16);
  }
  MSG_POLL(1); MSG_POLL(0);
  SB();
  if (__builtin_expect(far0 == 0 || far1 == 0, 0)) {
    asm volatile("s_branch 2f\nk_pg12:\n\ts_setpc_b64 s[42:43]\n2:");
#pragma unroll 1
    for (int c = 0; c < 2; ++c) {
      if (c ? far1 : far0) continue;
      const int ic = (0xffff - (c ? key[1] : key[0])) & (S_ - 1);
      lds_vi cnt = (lds_vi)(smem + LDS_SEL + 24 + c * 4);
      int it = 0;
      while (cnt[0] != 4 && it < SPIN_MAX * 8) { __builtin_amdgcn_s_sleep(1); ++it; }
      asm volatile("" ::: "memory");
      const float* q = (const float*)(smem + LDS_FIN + c * 2048) + (wid & 1) * 64 + (fr >> 3) * 32 + col4(fr);
      const f32x4 v = (*(const f32x4*)q + *(const f32x4*)(q + 128)) + (*(const f32x4*)(q + 256) + *(const f32x4*)(q + 384));
#pragma unroll
      for (int r = 0; r < 4; ++r)
#pragma unroll
        for (int k = 0; k < 4; ++k) {
          cv[r * 4 + k] += (xo[r] >> 11) == ic ? v[k] : 0.f;
        }
    }
  }
#pragma unroll
  for (int e = 0; e < 16; ++e) ya[e & 3][e >> 2] += cv[e];
  asm volatile("" : "+v"(ya[0]), "+v"(ya[1]), "+v"(ya[2]), "+v"(ya[3]));
  SB();
  MSG_ADD(2); MSG_ADD(1);
  asm volatile("" : "+v"(ya[0]), "+v"(ya[1]), "+v"(ya[2]), "+v"(ya[3]));
  SB();
  MSG_ADD(0);
  asm volatile("" :: "v"(pf[0]), "v"(pf[1]), "v"(pf[2]));
  {
#pragma unroll
    for (int r = 0; r < 4; ++r) *(u32x2*)((char*)out + (unsigned)xo[r]) = (u32x2){pk2(ya[0][r], ya[1][r]), pk2(ya[2][r], ya[3][r])};
  }

  BAR();
  if (wid == 0) {
    const unsigned te = (unsigned)__builtin_amdgcn_s_memrealtime();
    const unsigned t0 = __builtin_amdgcn_readlane(tlv, 52);
    const __amdgpu_buffer_rsrc_t rg = mk_rsrc(wsp + (64u << 20));
    if (lane == 0) { __builtin_amdgcn_raw_buffer_store_b64((u32x2){tag, t0}, rg, g * 32, 0, 16); __builtin_amdgcn_raw_buffer_store_b64((u32x2){tag, (unsigned)__builtin_amdgcn_readlane(tlv, 1)}, rg, g * 32 + 8, 0, 16);
      __builtin_amdgcn_raw_buffer_store_b64((u32x2){tag, (unsigned)__builtin_amdgcn_readlane(tlv, 0)}, rg, g * 32 + 16, 0, 16); }
    if (g == 37) {
      int mn0, mx[2], sum[2];
      for (int it = 0; it < 100000; ++it) {
        unsigned bad = 0; mn0 = 0x7fffffff; mx[0] = mx[1] = -0x7fffffff; sum[0] = sum[1] = 0;
        for (int k = 0; k < 4; ++k) {
          const u32x4 q = __builtin_amdgcn_raw_buffer_load_b128(rg, (k * 64 + lane) * 32, 0, 16);
          const u32x2 q2 = __builtin_amdgcn_raw_buffer_load_b64(rg, (k * 64 + lane) * 32 + 16, 0, 16);
          bad |= (q.x ^ tag) | (q.z ^ tag) | (q2.x ^ tag);
          mn0 = min(mn0, (int)(q.y - t0)); mx[0] = max(mx[0], (int)(q.w - t0)); sum[0] += (int)(q.w - t0); mx[1] = max(mx[1], (int)(q2.y - t0)); sum[1] += (int)(q2.y - t0);
        }
        if (__all(bad == 0)) break;
        asm volatile("" ::: "memory");
      }
      for (int o = 32; o; o >>= 1) { mn0 = min(mn0, __shfl_xor(mn0, o, 64)); for (int k = 0; k < 2; ++k) { mx[k] = max(mx[k], __shfl_xor(mx[k], o, 64)); sum[k] += __shfl_xor(sum[k], o, 64); } }
      int code = 0;
      { int d = sum[0] / 256 - mn0; d = (d - 150) / 10; code = code * 16 + min(max(d, 0), 15); }
      { int d = sum[1] / 256 - mn0; d = (d - 0) / 10; code = code * 16 + min(max(d, 0), 15); }
      const unsigned tend = __builtin_amdgcn_readfirstlane(t0 + mn0 + 4000 + code * 400);
      for (int it = 0; it < (1 << 17) && (int)((unsigned)__builtin_amdgcn_s_memrealtime() - tend) < 0; ++it) __builtin_amdgcn_s_sleep(1);
    }
  }

}

extern "C" void kernel_launch(void* const* d_in, const int* in_sizes, int n_in, void* d_out, int out_size,
                              void* d_ws, size_t ws_size, hipStream_t stream) {
  const u16* x = (const u16*)d_in[0];
  const u16* ws_re = (const u16*)d_in[1];
  const u16* ws_im = (const u16*)d_in[2];
  const u16* wd_re = (const u16*)d_in[3];
  const u16* wd_im = (const u16*)d_in[4];
  const u16* w_r = (const u16*)d_in[6];
  const u16* w_gate = (const u16*)d_in[7];
  const u16* w_up = (const u16*)d_in[8];
  const u16* w_down = (const u16*)d_in[9];
  k_main<<<256, 512, 0, stream>>>(x, wd_re, wd_im, w_r, (char*)d_ws, ws_re, ws_im, w_gate, w_up, w_down, (u16*)d_out);
}
```

```cpp
#include <hip/hip_runtime.h>
#include <hip/hip_bf16.h>
#include <stdint.h>
#include <type_traits>

extern "C" __device__ unsigned long long llvm_dispatch_id(void) __asm("llvm.amdgcn.dispatch.id");
extern "C" __device__ float llvm_fmul_legacy(float, float) __asm("llvm.amdgcn.fmul.legacy");
typedef unsigned short u16;
typedef unsigned long long u64;
typedef __attribute__((ext_vector_type(8))) short bf16x8;
typedef __attribute__((ext_vector_type(4))) unsigned u32x4;
typedef __attribute__((ext_vector_type(2))) unsigned u32x2;
typedef __attribute__((ext_vector_type(4))) float f32x4;
typedef __attribute__((ext_vector_type(2))) float f32x2;
typedef __attribute__((ext_vector_type(2))) __bf16 bf16x2;
typedef __attribute__((address_space(3))) void* lds_vp;
typedef volatile __attribute__((address_space(3))) int* lds_vi;

constexpr int S_ = 2048, D_ = 1024, F_ = 3584;
template <int N> using IC = std::integral_constant<int, N>;

static __device__ __forceinline__ float bf2f(u16 u) { return __uint_as_float((unsigned)u << 16); }
static __device__ __forceinline__ u16 f2bf(float f) {
  __hip_bfloat16 b = __float2bfloat16(f);
  return *reinterpret_cast<u16*>(&b);
}
static __device__ __forceinline__ float rbf(float f) { return bf2f(f2bf(f)); }
static __device__ __forceinline__ float lo_bf(unsigned u) { return __uint_as_float(u << 16); }
static __device__ __forceinline__ float hi_bf(unsigned u) { return __uint_as_float(u & 0xffff0000u); }
static __device__ __forceinline__ unsigned pk2(float a, float b) {
  const bf16x2 v = __builtin_convertvector((f32x2){a, b}, bf16x2);
  return __builtin_bit_cast(unsigned, v);
}
static __device__ __forceinline__ float add_bf(unsigned u, unsigned e, float c) {
  return __builtin_amdgcn_fdot2_f32_bf16(__builtin_bit_cast(bf16x2, u), __builtin_bit_cast(bf16x2, e), c, false);
}
static __device__ __forceinline__ float ln_elem(float x, float mb, float sd, float ri) {
  const float d = rbf(x - mb);
  const float q0 = d * ri;
  return rbf(__builtin_fmaf(__builtin_fmaf(-q0, sd, d), ri, q0));
}
template <int CTRL> static __device__ __forceinline__ float dpp_f(float v) {
  return __int_as_float(__builtin_amdgcn_update_dpp(0, __float_as_int(v), CTRL, 0xf, 0xf, true));
}
template <int CTRL> static __device__ __forceinline__ unsigned dpp_u(unsigned v) {
  return (unsigned)__builtin_amdgcn_update_dpp(0, (int)v, CTRL, 0xf, 0xf, true);
}
static __device__ __forceinline__ float row_sum(float v) {
  v += dpp_f<0xB1>(v);
  v += dpp_f<0x4E>(v);
  v += dpp_f<0x141>(v);
  v += dpp_f<0x140>(v);
  return v;
}
static __device__ __forceinline__ float fold32(float a, float b) {
  const auto r = __builtin_amdgcn_permlane32_swap(__float_as_uint(a), __float_as_uint(b), false, false);
  return __uint_as_float(r[0]) + __uint_as_float(r[1]);
}
static __device__ __forceinline__ float fold16(float a, float b) {
  const auto r = __builtin_amdgcn_permlane16_swap(__float_as_uint(a), __float_as_uint(b), false, false);
  return __uint_as_float(r[0]) + __uint_as_float(r[1]);
}
static __device__ __forceinline__ float rdl(float v, int l) { return __int_as_float(__builtin_amdgcn_readlane(__float_as_int(v), l)); }

#define ST_JB 9
#define LNS 2
#define WSN 12
#define XJ 10
#define FSM 0xa2
#define GV_JB 7
#define DG 11
#define AB_STRIDE 32
#define SPIN_MAX (1 << 17)
#define VA0 5
#define VAN 4
#define SP0 3
#define SP1 7
#define SP1E 11
#define SPS 0x0000eeeeeeee00
#define DVS 0x00000400300000
#define GVS 0x00000022000000
constexpr int dig(unsigned long long t, int q) { return q < 0 ? 0 : (int)((t >> (4 * q)) & 15); }
constexpr int acc_dig(unsigned long long t, int q) { int a = 0; for (int i = 0; i < q; ++i) a += dig(t, i); return a; }

static __device__ __forceinline__ unsigned lds_addr(const void* p) { return (unsigned)(size_t)(lds_vp)p; }

constexpr int MSG = 4096;
constexpr size_t WS_ABT   = 0;
constexpr size_t WS_SLAB  = WS_ABT + (size_t)D_ * 4096 * 2;
constexpr size_t WS_GU    = WS_SLAB + (size_t)64 * 64 * MSG;
constexpr size_t WS_DN    = WS_GU + (size_t)8 * F_ * 16;
constexpr size_t WS_FLAGS = WS_DN + (size_t)2 * 8 * 32 * 64 * 8;
constexpr int FL_X = 0;
constexpr int FL_AB = 64 * 64 * 32;
constexpr int FL_ST = FL_AB + 2048 * AB_STRIDE;
constexpr int FL_CE = FL_ST + 4096;
constexpr int FL_KEY = FL_CE + 4096;
constexpr int FL_EPOCH = FL_KEY + 4096;
constexpr int WS_MB = (int)((WS_FLAGS + (FL_EPOCH + 1) * 4 + 1048575) >> 20);

#define WAIT_VM(n) asm volatile("s_waitcnt vmcnt(" #n ")" ::: "memory")
template <int N> static __device__ __forceinline__ void wait_vm() { asm volatile("s_waitcnt vmcnt(%0)" :: "i"(N) : "memory"); }
#define BAR_LGKM() asm volatile("s_waitcnt lgkmcnt(0)\n\ts_barrier" ::: "memory")
#define BAR() asm volatile("s_barrier" ::: "memory")
#define SB() __builtin_amdgcn_sched_barrier(0)
#define PAGE(n) asm volatile("s_branch 2f\n\t.p2align 12\nk_pg" #n ":\n\ts_setpc_b64 s[42:43]\n2:")
#define CALL_PG(n) asm volatile("s_getpc_b64 s[40:41]\n3:\n\ts_mov_b32 s42, k_pg" #n "-3b\n\ts_ashr_i32 s43, s42, 31\n\ts_add_u32 s40, s40, s42\n\ts_addc_u32 s41, s41, s43\n\t" \
                                "s_swappc_b64 s[42:43], s[40:41]" ::: "s40", "s41", "s42", "s43", "scc", "memory")

static __device__ __forceinline__ __amdgpu_buffer_rsrc_t mk_rsrc(const void* p) {
  return __builtin_amdgcn_make_buffer_rsrc((void*)p, 0, 0x7fffffff, 0x00020000);
}
#define DMA_(rsrc, voff, mbase, MI, sbase, SI, pol) do { unsigned _t; \
    asm volatile("s_add_i32 m0, %2, %3\n\ts_add_i32 %0, %5, %6\n\tbuffer_load_dwordx4 %1, %4, %0 offen" pol " lds" \
                 : "=&s"(_t) : "v"(voff), "s"(mbase), "i"(MI), "s"(rsrc), "s"(sbase), "i"(SI) : "memory", "scc"); } while (0)
#define DMA_I(rsrc, voff, mbase, MI, SI) \
    asm volatile("s_add_i32 m0, %1, %2\n\ts_nop 0\n\tbuffer_load_dwordx4 %0, %3, 0 offen offset:%4 lds" \
                 :: "v"(voff), "s"(mbase), "i"((MI) - (SI)), "s"(rsrc), "i"(SI) : "memory", "scc")
#define DMA_S(rsrc, voff, mbase, MI, soff) \
    asm volatile("s_add_i32 m0, %1, %2\n\ts_nop 0\n\tbuffer_load_dwordx4 %0, %3, %4 offen lds" \
                 :: "v"(voff), "s"(mbase), "i"(MI), "s"(rsrc), "s"(soff) : "memory", "scc")
#define TOUCH(rsrc, voff, mbase, MI, soff) \
    asm volatile("s_add_i32 m0, %1, %2\n\ts_nop 0\n\tbuffer_load_dword %0, %3, %4 offen lds" \
                 :: "v"(voff), "s"(mbase), "i"(MI), "s"(rsrc), "s"(soff) : "memory", "scc")
template <int OFF> static __device__ __forceinline__ u32x4 ld16_s(const void* p, int voff) {
  u32x4 v;
  asm volatile("global_load_dwordx4 %0, %1, %2 offset:%3" : "=v"(v) : "v"(voff), "s"(p), "i"(OFF) : "memory");
  return v;
}
template <int OFF> static __device__ __forceinline__ void sld(unsigned& sink, const void* p, int soff) {
  asm volatile("s_load_dword %0, %1, %2 offset:%3" : "+s"(sink) : "s"(p), "s"(soff), "i"(OFF) : "memory");
}
static __device__ __forceinline__ unsigned gld4_s(const void* p, unsigned voff) {
  unsigned v;
  asm volatile("global_load_dword %0, %1, %2" : "=v"(v) : "v"(voff), "s"(p) : "memory");
  return v;
}
static __device__ __forceinline__ u32x2 bld8_sc1(__amdgpu_buffer_rsrc_t r, unsigned voff) {
  u32x2 v;
  asm volatile("buffer_load_dwordx2 %0, %1, %2, 0 offen sc1" : "=v"(v) : "v"(voff), "s"(r) : "memory");
  return v;
}
template <int N> static __device__ __forceinline__ void wait_xs(unsigned& a, u32x2& b) { asm volatile("s_waitcnt vmcnt(%2)" : "+v"(a), "+v"(b) : "i"(N) : "memory"); }
template <int N> static __device__ __forceinline__ void wait_xq(u32x4 (&a)[2], u32x2& b) { asm volatile("s_waitcnt vmcnt(%3)" : "+v"(a[0]), "+v"(a[1]), "+v"(b) : "i"(N) : "memory"); }
static __device__ __forceinline__ u32x2 ld8_s_sc1(const void* p, int voff) {
  u32x2 v;
  asm volatile("global_load_dwordx2 %0, %1, %2 sc1" : "=v"(v) : "v"(voff), "s"(p) : "memory");
  return v;
}
static __device__ __forceinline__ void lgkm0(unsigned& sink) { asm volatile("s_waitcnt lgkmcnt(0)" : "+s"(sink) :: "memory"); }
template <typename T> static __device__ __forceinline__ void pin4(T& a, T& b, T& c, T& d) { asm volatile("" : "+s"(a), "+s"(b), "+s"(c), "+s"(d)); }
template <typename T, typename U> static __device__ __forceinline__ void pins(T& a, U& b) { asm volatile("" : "+s"(a), "+s"(b)); }
static __device__ __forceinline__ void launder(int& v, int& a, int& b) { asm volatile("" : "+v"(v)); asm volatile("" : "+s"(a), "+s"(b)); }
static __device__ __forceinline__ unsigned bld4_sc1(__amdgpu_buffer_rsrc_t r, int voff) {
  unsigned v;
  asm volatile("buffer_load_dword %0, %1, %2, 0 offen sc1" : "=v"(v) : "v"(voff), "s"(r) : "memory");
  return v;
}
static __device__ __forceinline__ u32x4 bld16_nt(__amdgpu_buffer_rsrc_t r, int voff, int soff) {
  u32x4 v;
  asm volatile("buffer_load_dwordx4 %0, %1, %2, %3 offen nt" : "=v"(v) : "v"(voff), "s"(r), "s"(soff) : "memory");
  return v;
}
static __device__ __forceinline__ void bld16_nt_io(u32x4& v, __amdgpu_buffer_rsrc_t r, int voff, int soff) {
  asm volatile("buffer_load_dwordx4 %0, %1, %2, %3 offen nt" : "+v"(v) : "v"(voff), "s"(r), "s"(soff) : "memory");
}
static __device__ __forceinline__ u32x4 bld16_sc1(__amdgpu_buffer_rsrc_t r, int voff, int soff) {
  u32x4 v;
  asm volatile("buffer_load_dwordx4 %0, %1, %2, %3 offen sc1" : "=v"(v) : "v"(voff), "s"(r), "s"(soff) : "memory");
  return v;
}
static __device__ __forceinline__ u32x2 granule_wait(const u64* p, unsigned tag) {
  u64 v = 0;
#pragma unroll 1
  for (int it = 0; it < SPIN_MAX; ++it) {
    v = __hip_atomic_load(p, __ATOMIC_RELAXED, __HIP_MEMORY_SCOPE_AGENT);
    if (__all((unsigned)(v >> 32) == tag)) break;
    __builtin_amdgcn_s_sleep(2);
  }
  return (u32x2){(unsigned)v, (unsigned)(v >> 32)};
}
static __device__ __forceinline__ float key_score(unsigned key) {
  const unsigned u = key >> 16;
  return bf2f((u16)((u & 0x8000u) ? (u & 0x7fffu) : (~u & 0xffffu)));
}
static __device__ __forceinline__ unsigned med3u(unsigned a, unsigned b, unsigned c) { return max(min(a, b), min(max(a, b), c)); }
static __device__ __forceinline__ void merge2(unsigned& a1, unsigned& a2, unsigned b1, unsigned b2) {
  const unsigned lo = min(a1, b1);
  a1 = max(a1, b1);
  a2 = max(lo, max(a2, b2));
}
static __device__ __forceinline__ float sqdev16(const u32x4 (&v)[2], float mb) {
  f32x2 q2 = {0.f, 0.f};
#pragma unroll
  for (int i = 0; i < 8; ++i) {
    const f32x2 e = (f32x2){lo_bf(v[i >> 2][i & 3]), hi_bf(v[i >> 2][i & 3])} - mb;
    q2 += e * e;
  }
  return q2.x + q2.y;
}

constexpr int lrw(int r) { return (r & ~6) | ((r & 2) << 1) | ((r & 4) >> 1); }
constexpr int swz(int r) { return ((r >> 1) & 4) | (r & 2); }
static __device__ __forceinline__ int sym_dn(int p) { return (p ^ (p >> 1)) & 1; }
static __device__ __forceinline__ int sym_hi(int n, int w, int st, int b, int z, int p) {
  return ((p >> 1) * (n / 2 * st) + b * (w * st)) ^ (-sym_dn(p) & (((n - 1) ^ (z * (n / 2))) * st));
}
static __device__ __forceinline__ int sym_lo(int w, int st, int p, int c) { return ((c - sym_dn(p)) * st) & ((w - 1) * st); }
static __device__ __forceinline__ int sym_row(int n, int w, int st, int b, int z, int p, int c) { return sym_lo(w, st, p, c) ^ sym_hi(n, w, st, b, z, p); }
constexpr int G1_NST = 4, G1_NKT = 16, G1_STAGE = 32768;
constexpr int G2_NKT = 16, G2_MRING = 98304;
constexpr int n_off(int k) { return (k % 3) * 32768; }
constexpr int m_off(int k) { return ((k + 2) % 3) * 16384; }
constexpr int k_ord(int k) { return k < 2 ? k : (k < 9 ? 2 * (k - 1) : 2 * (k - 8) + 1); }
constexpr int LDS_DUMMY = G2_MRING + 49152;
constexpr int LDS_GU = LDS_DUMMY + 4096;
constexpr int LDS_FIN = LDS_GU + 4096;
constexpr int LDS_MB = LDS_FIN + 4096;
constexpr int LDS_SEL = LDS_MB + 1536;
constexpr int LDS_SIG = LDS_SEL + 64;
constexpr int LDS_KEY = LDS_SIG + 32;
constexpr int LDS_BYTES = LDS_KEY + 128;
constexpr int LDS_ACC = 65536 + 8192;
constexpr int NG = 16;
constexpr int g_t[NG] = {0, 2, 1, 3, 3, 1, 2, 0, 0, 2, 1, 3, 3, 1, 2, 0};
constexpr int g_n[NG] = {0, 4096, 32768, 36864, 65536, 69632, 73728, 77824, G2_MRING + m_off(11), G2_MRING + m_off(11) + 4096, 0, 4096, 32768, 36864, 65536, 69632};
constexpr int g_h(int g) { return g == 8 || g == 9 ? 8192 : 16384; }
static_assert(n_off(12) == 0 && n_off(13) == 32768, "");

constexpr int sp_tile(int q) { return q < VA0 ? q : q + VAN < 16 ? q + VAN : -1; }
constexpr int dn_cnt(int q) { return dig(DVS, q); }
constexpr int dn_first(int q) { return acc_dig(DVS, q); }

static __device__ __forceinline__ void code_touch(unsigned& sink, unsigned off) {
  asm volatile("s_getpc_b64 s[40:41]\n\tk_main_rt:\n\ts_min_u32 %1, %1, k_main_end-k_main_rt-196\n\t"
               "s_load_dword %0, s[40:41], %1 offset:0\n\ts_load_dword %0, s[40:41], %1 offset:64\n\ts_load_dword %0, s[40:41], %1 offset:128\n\ts_load_dword %0, s[40:41], %1 offset:192"
               : "+s"(sink), "+s"(off) :: "s40", "s41", "scc", "memory");
}
static __device__ __forceinline__ void touch1(__amdgpu_buffer_rsrc_t r, int voff, unsigned mb, int soff) { TOUCH(r, voff, mb, LDS_DUMMY, soff); }
typedef const __attribute__((address_space(1))) u16* gptr;
typedef __attribute__((ext_vector_type(8))) u64 u64x8;
typedef __attribute__((ext_vector_type(2))) u64 u64x2;
extern "C" __global__ __launch_bounds__(512, 2) void k_main(const u16* x, const u16* wd_re, const u16* wd_im, const u16* w_r,
                                                 char* wsp, const u16* ws_re, const u16* ws_im,
                                                 const u16* w_gate, const u16* w_up, const u16* w_down, u16* out) {
  __shared__ __attribute__((aligned(16))) char smem[LDS_BYTES];
  u64x8 ka;
  u64x2 kb_;
  u64 kc;
  asm volatile("s_load_dwordx16 %0, %3, 0\n\ts_load_dwordx4 %1, %3, 64\n\ts_load_dwordx2 %2, %3, 80"
               : "=&s"(ka), "=&s"(kb_), "=&s"(kc) : "s"(__builtin_amdgcn_kernarg_segment_ptr()) : "memory");
  const int tid = threadIdx.x, lane = tid & 63;
  const int wid = __builtin_amdgcn_readfirstlane(tid >> 6);
  const int g = blockIdx.x, xcd = ((~g & 1) << 2) | ((g >> 1) & 3), j = g >> 3;
  if (wid < 4) {
    unsigned cw, co = (j * 4 + wid) * 448;
    asm volatile(".subsection 1\nk_main_end:\n.subsection 0\n\ts_min_u32 %1, %1, k_main_end-k_main-480\n\ts_getpc_b64 s[40:41]\n\ts_add_u32 s40, s40, %1\n\ts_addc_u32 s41, s41, 0\n\t"
                 "s_load_dword %0, s[40:41], 0\n\ts_load_dword %0, s[40:41], 64\n\ts_load_dword %0, s[40:41], 128\n\t"
                 "s_load_dword %0, s[40:41], 192\n\ts_load_dword %0, s[40:41], 256\n\ts_load_dword %0, s[40:41], 320\n\ts_load_dword %0, s[40:41], 384\n\ts_waitcnt lgkmcnt(0)"
                 : "=&s"(cw), "+s"(co) :: "s40", "s41", "scc", "memory");
  }
  const int pw = wid - 4;
  const int row0 = (wid & 3) * 8 + lrw(lane >> 3);
  const int cm = ((lane & 7) ^ swz(row0)) * 16;
  int vm = ((row0 & 15) * 2 + (row0 >> 4)) * (D_ * 2) + cm;
  asm volatile("" : "+v"(vm));
  const int kq = xcd >> 1;
  const int tt = (xcd & 1) * 4 + (j >> 3);
  const int et = j & 7;
  const int z = kq >> 1, st = (kq & 1) * 8 + tt;
  const int lrow = (kq & 1) * 1024 + (xcd & 1) * 512 + z * 256 + j * 8 + pw * 2;
  const int drow = ((kq & 1) * 2 + (xcd & 1)) * 256 + j * 8 + pw * 2;
  unsigned o_m = st * 128 * D_ * 2, o_l = lrow * D_ * 2, o_d = drow * D_ * 2, mb0 = lds_addr(smem) + pw * 1024;
  int l16 = lane * 16;
  int vn[4];
#pragma unroll
  for (int q = 0; q < 4; ++q) vn[q] = sym_lo(32, D_ * 2, (row0 >> 2) & 3, q * 8 + (row0 >> 4) * 4 + (row0 & 3)) ^ (sym_hi(D_, 32, D_ * 2, et, z, (row0 >> 2) & 3) | cm);
  asm volatile("" : "+s"(o_m), "+s"(o_l), "+s"(o_d), "+s"(mb0));
  asm volatile("" : "+v"(l16), "+v"(vn[0]), "+v"(vn[1]), "+v"(vn[2]), "+v"(vn[3]));
  asm volatile("s_waitcnt lgkmcnt(0)" : "+s"(ka), "+s"(kb_), "+s"(kc) :: "memory");
  x = (const u16*)(gptr)ka[0]; wd_re = (const u16*)(gptr)ka[1]; wd_im = (const u16*)(gptr)ka[2]; w_r = (const u16*)(gptr)ka[3];
  wsp = (char*)(gptr)ka[4]; ws_re = (const u16*)(gptr)ka[5]; ws_im = (const u16*)(gptr)ka[6]; w_gate = (const u16*)(gptr)ka[7];
  w_up = (const u16*)(gptr)kb_[0]; w_down = (const u16*)(gptr)kb_[1]; out = (u16*)(gptr)kc;
  int* flags = (int*)(wsp + WS_FLAGS);
  u64* stat = (u64*)(flags + FL_ST);
  const u16* wdz = z ? wd_im : wd_re;
  const __amdgpu_buffer_rsrc_t rm = mk_rsrc(x);
  const __amdgpu_buffer_rsrc_t rn = mk_rsrc(wdz);
#define G1_X(kt, so, q) DMA_(rm, vm, mb0, (so) + (q) * 4096, o_m, (kt) * 128 + (((q) & 1) * 64 + ((q) >> 1) * 32) * D_ * 2, "")
#define G1_W(kt, so, q) DMA_I(rn, vn[q], mb0, (so) + 16384 + (q) * 4096, (kt) * 128)
#define G1_STAGE_TILE(kt, so, W) do { SB(); G1_X(kt, so, 0); W(0); G1_X(kt, so, 1); W(1); G1_X(kt, so, 2); W(2); G1_X(kt, so, 3); W(3); \
    G1_W(kt, so, 0); W(4); G1_W(kt, so, 1); W(5); G1_W(kt, so, 2); W(6); G1_W(kt, so, 3); W(7); SB(); } while (0)
#define G1_STAGE_A(kt, so, W) do { SB(); G1_X(kt, so, 0); W(0); G1_X(kt, so, 2); W(1); \
    G1_W(kt, so, 0); W(2); G1_W(kt, so, 1); W(3); G1_W(kt, so, 2); W(4); G1_W(kt, so, 3); W(5); W(6); W(7); SB(); } while (0)
#define G1_STAGE_B(kt, so) do { SB(); G1_X(kt, so, 1); G1_X(kt, so, 3); SB(); } while (0)
#define NOW(i)
  const int fr = lane & 15, fq = lane >> 4;
  const int ha = wid >> 2;
  int trow, xo[4], xa[3], po, ml;
  const char *prs, *prc;
  auto col4 = [](int f) __attribute__((always_inline)) { return ((f & 2) << 3 | (f & 4) << 1 | (f & 1) << 2) ^ (-((f >> 3) & 1) & 28); };
  auto seg0 = [&](int hf, int dn) __attribute__((always_inline)) { return (hf * 512 + et * 32) ^ (-dn & (D_ - 32)); };
  auto epi_setup = [&](int part) __attribute__((always_inline)) {
    int t_ = tt, l_ = lane;
    asm volatile("" : "+s"(t_));
    asm volatile("" : "+v"(l_));
    const int ts = ((wid >> 1) & 1) * 8 + kq * 2 + ha;
    if (part == 0) { xo[3] = (seg0(wid & 1, (l_ >> 3) & 1) + col4(l_)) * 2; asm volatile("" : "+v"(xo[3])); }
    if (part == 1) {
      xo[3] |= sym_hi(S_, 64, D_ * 2, t_, z, l_ >> 4);
      ml = (l_ * 16) ^ (-sym_dn(l_ >> 4) & (z * 768));
      asm volatile("" : "+v"(xo[3]), "+v"(ml));
    }
    if (part == 2) {
      const int xh = xo[3];
#pragma unroll
      for (int r = 0; r < 4; ++r) xo[r] = sym_lo(64, D_ * 2, l_ >> 4, ts * 4 + r) ^ xh;
      const bool hi = xo[0] & 8;
      xa[0] = (hi ? xo[2] : xo[0]) & ~15; xa[1] = (hi ? xo[3] : xo[1]) & ~15;
      xa[2] = (sym_lo(64, D_ * 2, l_ >> 4, ts * 4 + (l_ & 3)) ^ xh) >> 11 << 3;
      asm volatile("" : "+v"(xo[0]), "+v"(xo[1]), "+v"(xo[2]), "+v"(xo[3]), "+v"(xa[0]), "+v"(xa[1]), "+v"(xa[2]));
    }
    if (part == 3) {
      trow = t_ * 16 + ts;
      prs = wsp + WS_SLAB + (size_t)((t_ * 8 + et) * 64 + (wid & 3)) * MSG;
      prc = (const char*)(flags + FL_X + ((t_ * 8 + et) * 64 + (wid & 3)) * 32);
      asm volatile("" : "+s"(trow), "+s"(prs), "+s"(prc));
      po = l_ < 3 ? (kq * 4 + ((kq + 1 + l_) & 3)) * 512 : (int)0x80000000;
      asm volatile("" : "+v"(po));
    }
  };
  auto tok_tile = [](unsigned key) __attribute__((always_inline)) {
    const unsigned h = (0xffff - key) & 1023, m = -(h >> 9);
    return (((h ^ m) & 0x1c0) | (((h ^ m) - m) & 63)) >> 2;
  };
#define rs mk_rsrc(prs)
#define rc mk_rsrc(prc)
  lds_vi sig = (lds_vi)(smem + LDS_SIG + (wid & 3) * 4);
  unsigned tag;
  u32x4 xq[2];
  u32x2 sq;
  f32x4 ya[4];
  unsigned key[2];
  float cv[16];
  unsigned far0, far1;
  auto xq_load = [&](int n) __attribute__((always_inline)) {
    if (n < 2) xq[n] = ld16_s<0>(x, xa[n]); else sq = ld8_s_sc1(stat, xa[2]);
  };
#define PINV(a) asm volatile("" : "+v"(a))
  auto far_calc = [&]() __attribute__((always_inline)) {
    far0 = tok_tile(key[0]) ^ trow; far1 = tok_tile(key[1]) ^ trow;
    asm volatile("" : "+s"(far0), "+s"(far1));
  };
  auto cv_calc = [&]() __attribute__((always_inline)) {
    if (__builtin_expect(!__all(sq.y == tag), 0)) sq = granule_wait(stat + (xa[2] >> 3), tag);
    const bool hi = xo[0] & 8;
    const float mb_ = lo_bf(sq.x), ri_ = __builtin_amdgcn_rcpf(hi_bf(sq.x));
#pragma unroll
    for (int r = 0; r < 4; ++r) {
      const float mb = r == 0 ? dpp_f<0x00>(mb_) : r == 1 ? dpp_f<0x55>(mb_) : r == 2 ? dpp_f<0xAA>(mb_) : dpp_f<0xFF>(mb_);
      const float ri = r == 0 ? dpp_f<0x00>(ri_) : r == 1 ? dpp_f<0x55>(ri_) : r == 2 ? dpp_f<0xAA>(ri_) : dpp_f<0xFF>(ri_);
#pragma unroll
      for (int u = 0; u < 2; ++u) {
        const unsigned own = xq[r & 1][(r & 2) + u], oth = dpp_u<0xB1>(xq[r & 1][2 - (r & 2) + u]);
        const unsigned w = hi == (r >= 2) ? own : oth;
        cv[r * 4 + 2 * u] = __builtin_fmaf(lo_bf(w) - mb, ri, lo_bf(w));
        cv[r * 4 + 2 * u + 1] = __builtin_fmaf(hi_bf(w) - mb, ri, hi_bf(w));
        PINV(cv[r * 4 + 2 * u]); PINV(cv[r * 4 + 2 * u + 1]);
      }
    }
  };
#define MSG_IDX(m) (kq * 4 + ((kq + 1 + (m)) & 3))
  unsigned seen = 0;

  if (__builtin_expect(wid >= 4, 1)) {
    __builtin_amdgcn_s_setprio(3);
    G1_STAGE_TILE(0, 0, NOW);
    constexpr int O_ST = (FL_ST - FL_AB) * 4, O_CE = (FL_CE - FL_AB) * 4, O_KEY = (FL_KEY - FL_AB) * 4, O_EP = (FL_EPOCH - FL_AB) * 4;
    const __amdgpu_buffer_rsrc_t rf = mk_rsrc(wsp + WS_FLAGS + FL_AB * 4);
    u32x4 lv[2], lv2[2], lw[2], ld[2], ld2[2];
    unsigned epv;
    {
      const char* xrow = (const char*)x + o_l;
      const char* wdrow = (const char*)wdz + o_d;
      lv[0] = ld16_s<0>(xrow, l16); lv[1] = ld16_s<1024>(xrow, l16);
      lv2[0] = ld16_s<2048>(xrow, l16); lv2[1] = ld16_s<3072>(xrow, l16);
      ld[0] = ld16_s<0>(wdrow, l16); ld[1] = ld16_s<1024>(wdrow, l16);
      ld2[0] = ld16_s<2048>(wdrow, l16); ld2[1] = ld16_s<3072>(wdrow, l16);
      lw[0] = ld16_s<0>(w_r, l16); lw[1] = ld16_s<1024>(w_r, l16);
      asm volatile("buffer_load_dword %0, off, %1, %2 sc1" : "=v"(epv) : "s"(rf), "s"(O_EP) : "memory");
    }
    SB();
    const unsigned long long did = llvm_dispatch_id();
    const unsigned dmix = (unsigned)did * 0x85EBCA6Bu + (unsigned)(did >> 32) * 0xC2B2AE35u + 0x27D4EB2Fu;
    const bool moe = et != 7;
    unsigned epoch;
    const int ci = lane & 15, rq = lane >> 4, tk = (lane >> 3) & 1;
    int mat, cg, dq, d0;
    u32x4 wv[16];
    const u16 *pgu, *pn2;
    const char *pm2, *spb;
#define rw __builtin_amdgcn_make_buffer_rsrc((void*)pgu, 0, moe ? 0x7fffffff : 0, 0x00020000)
#define rn2 mk_rsrc(pn2)
#define rm2 mk_rsrc(pm2)
    int vw, vn2[8], vm2;
    auto k_blk = [&](int k) __attribute__((always_inline)) { return tt ^ (k_ord(k) >> 1); };
#define KROT(kk_) (k_blk(kk_) * 256 + (k_ord(kk_) & 1) * 128)
    int tv2, tv7;
    unsigned sd = 0;
    auto spf = [&](auto N) __attribute__((always_inline)) {
      constexpr int n = decltype(N)::value;
      if constexpr (n >= 0 && sp_tile(n >> 4) >= 0) sld<((n >> 1) & 7) * 4096 + (n & 1) * 64>(sd, spb, KROT(sp_tile(n >> 4)));
    };
    const int cg8 = et ^ ((et & 1) << 1), rgb = (g & 7) * 4 + (j >> 3);
    const __amdgpu_buffer_rsrc_t rdn = mk_rsrc(w_down);
    const __amdgpu_buffer_rsrc_t rgu = mk_rsrc(wsp + WS_GU);
    int vdn, vgu;
    u32x4 dv[7], gv[4];
    int abo, abq[3];
    unsigned lma;
    unsigned sel[2][4];
    float g0, g1;
    u32x4 kk[4];
    unsigned k1 = 0, k2 = 0;
    const int ko_ = O_KEY + pw * 4096;
#define MM(a, b, c) __builtin_amdgcn_mfma_f32_16x16x32_bf16(__builtin_bit_cast(bf16x8, a), __builtin_bit_cast(bf16x8, b), c, 0, 0, 0)
#define PIN1(a) asm volatile("" : "+v"(a))
#define PIN2(a, b) asm volatile("" : "+v"(a), "+v"(b))
#define PIN3(a, b, c) asm volatile("" : "+v"(a), "+v"(b), "+v"(c))
    {
      int gro, go;
      u32x2 gr;
      u32x4 oh[5];
      f32x4 sa4, gw[2], gx[2];
      float su, gs, ta, tb, sx, mbv;
      unsigned dk[4], kl;
      const int hb = lane >> 5;
#pragma unroll
      for (int l = 0; l < WSN; ++l) asm volatile("" : "=v"(wv[l]));
      auto diag = [&](const f32x4& m) __attribute__((always_inline)) {
        return __uint_as_float((__float_as_uint(m[0]) & dk[0]) | (__float_as_uint(m[1]) & dk[1]) | (__float_as_uint(m[2]) & dk[2]) | (__float_as_uint(m[3]) & dk[3]));
      };
      auto setup = [&](auto JB, auto GP) __attribute__((always_inline)) {
        constexpr int jb = decltype(JB)::value, gp = decltype(GP)::value;
        SB();
        {
          int ln = lane, jj = j, p_ = pw;
          launder(ln, jj, p_);
          const int c_ = ln & 15, r_ = ln >> 4, e_ = jj & 7, t_ = (xcd & 1) * 4 + (jj >> 3);
          if constexpr (jb == 1 && gp == 0) {
            pn2 = ((kq >> 1) ? ws_im : ws_re) + (kq & 1) * 1024;
            spb = (const char*)(pn2 + (sym_row(S_, 64, 1, t_, 0, e_ >> 1, 0) + (e_ & 1) * 32 + p_ * 8) * S_);
            pins(pn2, spb);
          }
          if constexpr (jb == 1 && gp >= 1 && gp < 6) {
            const unsigned o = c_ == gp - 1 ? 0x3f803f80u : 0u;
            oh[gp - 1] = (u32x4){o, o, o, o};
            PIN1(oh[gp - 1]);
          }
          if constexpr (jb == 1 && gp == 6) {
            const int rk = moe ? e_ * 8 + (jj >> 3) * 2 + (xcd >> 2) : 0, r3 = ((rk - 14) * 43) >> 7, rr = rk - 14 - r3 * 3;
            const int c56 = rk < 14 ? rk * 4 + 1 : r3 * 4 + rr + ((rr + 1) >> 1);
            mat = (c56 + 36) >> 6; cg = c56 - mat * 28; dq = xcd & 3; d0 = dq * 256 + p_ * 64;
            pin4(mat, cg, dq, d0);
          }
          if constexpr (jb == 2 && gp == 0) {
            pgu = mat ? w_up : w_gate;
            pm2 = wsp + WS_ABT + (kq * 8 + e_) * 262144;
            pins(pgu, pm2);
            vw = ((d0 + r_ * 16) * F_ + cg * 128 + c_ * 8) * 2;
            PIN1(vw);
          }
          if constexpr (jb == 2 && (gp == 1 || gp == 2)) {
            dk[gp * 2 - 2] = c_ == r_ * 4 + gp * 2 - 2 ? ~0u : 0u; dk[gp * 2 - 1] = c_ == r_ * 4 + gp * 2 - 1 ? ~0u : 0u;
            PIN2(dk[gp * 2 - 2], dk[gp * 2 - 1]);
          }
          if constexpr (jb == 2 && gp == 3) { kl = lrow + (ln >> 5); go = (ln & 30) ? (int)0x80000000 : ((ln & 1) ? O_KEY : O_ST) + (int)kl * 8; PIN2(kl, go); }
          if constexpr (jb == 2 && gp == 4) { kl = 0xffff - kl; go = (ln & ~1) == 2 ? O_CE + (z * D_ + drow + (ln & 1)) * 8 : go; PIN2(kl, go); }
          if constexpr (jb == 2 && gp == 5) {
            gro = p_ < 2 ? O_ST + (st * 128 + p_ * 64 + ln) * 8 : O_CE + z * D_ * 8 + sym_row(D_, 32, 8, e_, z, (ln >> 2) & 3, (p_ - 2) * 16 + r_ * 4 + (ln & 3));
            lma = lds_addr(smem + LDS_MB) + (p_ < 2 ? p_ * 256 : 512 + p_ * 256) + ln * 4;
            PIN2(gro, lma);
          }
          if constexpr (jb == 7 && gp == 0) {
            vm2 = p_ * 1024 + ln * 16;
            PIN1(vm2);
          }
          if constexpr (jb == 2 && gp == 6) {
            const int r0 = p_ * 8 + lrw(ln >> 3);
            vn2[6] = sym_lo(64, S_ * 2, (r0 >> 2) & 3, (r0 >> 4) * 4 + (r0 & 3)); vn2[7] = sym_hi(S_, 64, S_ * 2, t_, z, (r0 >> 2) & 3) | (vm & 127);
            PIN2(vn2[6], vn2[7]);
          }
          if constexpr ((jb == 2 && gp == 7) || (jb == 7 && gp >= 6)) {
            constexpr int q0 = jb == 2 ? 0 : gp == 6 ? 3 : 6;
            const int lo = vn2[6], hi = vn2[7];
#pragma unroll
            for (int q = q0; q < q0 + 3 && q < 8; ++q) { vn2[q] = ((lo + ((q & 4) + ((q + kq + 1) & 3)) * 8 * S_ * 2) & (63 * S_ * 2)) ^ hi; PIN1(vn2[q]); }
          }
          if constexpr (jb == 7 && gp == 1) { vdn = rgb * 112 + p_ * 28 + r_ * 7; vgu = (((ln >> 3) & 1) * 4 * F_ + vdn + min(ln & 7, 6)) * 16; PIN2(vdn, vgu); }
          if constexpr (jb == 7 && gp == 2) { vdn = (vdn * D_ + cg8 * 128 + c_ * 8) * 2; PIN1(vdn); }
          if constexpr (jb == 7 && gp == 3) {
            abo = (((kq * 8 + e_) * 4 + p_) * 16 + c_) * AB_STRIDE * 4;
            abq[2] = ln >= 16 || (ln & 7) == t_ ? (int)0x80000000 : abo;
            abo = ln < 32 ? (((kq * 8 + e_) * 4 + p_) * 16 + t_) * AB_STRIDE * 4 + ln * 4 : (int)0x80000000;
            PIN2(abo, abq[2]);
          }
          if constexpr (jb == 7 && gp == 4) {
            abq[1] = ln >= 8 ? (int)0x80000000 : abq[2];
            abq[0] = ((ln ^ t_) & 4) ? (int)0x80000000 : abq[1];
            PIN2(abq[0], abq[1]);
          }
          if constexpr (jb == 8 && gp < 4) {
#pragma unroll
            for (int h = 0; h < 2; ++h) {
              const unsigned hv = h ? 0x0302u : 0x0100u;
              sel[h][gp] = ((ln & 7) >> 1) == gp ? ((ln & 1) ? (hv << 16) | 0x0c0cu : 0x0c0c0000u | hv) : 0x0c0c0c0cu;
            }
            PIN2(sel[0][gp], sel[1][gp]);
          }
        }
        SB();
      };
#define WS1(i) setup(IC<1>{}, IC<i>{})
#define WS2(i) setup(IC<2>{}, IC<i>{})
#define WS3(i) do { setup(IC<7>{}, IC<i>{}); if (i >= 4) setup(IC<8>{}, IC<(i) & 3>{}); spf(IC<(i < SP0 ? i : -1)>{}); } while (0)
      G1_STAGE_TILE(1, G1_STAGE, WS1);
      WAIT_VM(19); BAR();
      G1_STAGE_TILE(2, 2 * G1_STAGE, WS2);
      G1_STAGE_TILE(3, 3 * G1_STAGE, WS3);
      auto lnu = [&](auto U) __attribute__((always_inline)) {
        constexpr int jb = 1 + decltype(U)::value / 7, gp = decltype(U)::value % 7;
        SB();
        if constexpr (jb == 1) {
          const f32x4 z4 = {0.f, 0.f, 0.f, 0.f};
          if constexpr (gp == 0) { sa4 = MM(lv[0], oh[0], z4); gw[0] = MM(lv[0], lw[0], z4); gx[0] = MM(lv[0], lv[0], z4); PIN3(sa4, gw[0], gx[0]); }
          if constexpr (gp == 1) { sa4 = MM(lv2[0], oh[1], sa4); gw[1] = MM(lv2[0], lw[0], z4); gx[1] = MM(lv2[0], lv2[0], z4); PIN3(sa4, gw[1], gx[1]); }
          if constexpr (gp == 2) { sa4 = MM(lv[1], oh[0], sa4); gw[0] = MM(lv[1], lw[1], gw[0]); gx[0] = MM(lv[1], lv[1], gx[0]); PIN3(sa4, gw[0], gx[0]); }
          if constexpr (gp == 3) { sa4 = MM(lv2[1], oh[1], sa4); gw[1] = MM(lv2[1], lw[1], gw[1]); gx[1] = MM(lv2[1], lv2[1], gx[1]); PIN3(sa4, gw[1], gx[1]); }
          if constexpr (gp == 4) { sa4 = MM(ld[0], oh[2], sa4); sa4 = MM(ld[1], oh[2], sa4); PIN1(sa4); }
          if constexpr (gp == 5) { sa4 = MM(ld2[0], oh[3], sa4); sa4 = MM(ld2[1], oh[3], sa4); PIN1(sa4); }
          if constexpr (gp == 6) { sa4 = MM(lw[0], oh[4], sa4); sa4 = MM(lw[1], oh[4], sa4); PIN1(sa4);
            epoch = __builtin_amdgcn_readfirstlane(epv); tag = ((epoch + 1u) * 0x9E3779B1u ^ dmix) | 1u; }
        }
        if constexpr (jb == 2) {
          if constexpr (gp == 0) { su = (sa4[0] + sa4[1]) + (sa4[2] + sa4[3]); ta = diag(gw[0]); PIN2(su, ta); }
          if constexpr (gp == 1) { su = fold32(su, su); tb = diag(gw[1]); PIN2(su, tb); }
          if constexpr (gp == 2) { su = fold16(su, su); gs = fold32(ta, tb); PIN2(su, gs); }
          if constexpr (gp == 3) { ta = diag(gx[0]); tb = diag(gx[1]); PIN2(ta, tb); }
          if constexpr (gp == 4) { gs = fold16(gs, fold32(ta, tb)); PIN1(gs); }
          if constexpr (gp == 5) { gs += dpp_f<0xB1>(gs); gs += dpp_f<0x4E>(gs); PIN1(gs); }
          if constexpr (gp == 6) { gs += dpp_f<0x141>(gs); gs += dpp_f<0x140>(gs); PIN1(gs); }
        }
        if constexpr (jb == 3) {
          if constexpr (gp == 0) {
            const float s0 = rdl(su, 0), s1 = rdl(su, 1), q0 = rdl(gs, 16), q1 = rdl(gs, 48);
            sx = hb ? s1 : s0; ta = hb ? q1 : q0; PIN2(sx, ta);
          }
          if constexpr (gp == 1) {
            const float mean = sx * (1.0f / D_);
            mbv = rbf(mean);
            ta -= sx * mean;
            if (__builtin_expect(__any(!(sx * mean <= 16.f * ta)), 0)) {
              float q = fold32(sqdev16(lv, rdl(mbv, 0)), sqdev16(lv2, rdl(mbv, 32)));
              q = row_sum(fold16(q, q));
              const float dm = mean - mbv;
              ta = fmaxf(q - (float)D_ * dm * dm, 0.f);
            }
            PIN2(mbv, ta);
          }
          if constexpr (gp == 2) { ta = rbf(rbf(ta * (1.0f / D_)) + 1e-5f); gs -= mbv * rdl(su, 4); PIN2(ta, gs); }
          if constexpr (gp == 3) { ta = rbf(__builtin_amdgcn_sqrtf(ta)); tb = __builtin_amdgcn_rcpf(ta); PIN2(ta, tb); }
          if constexpr (gp == 4) {
            const unsigned rb = __float_as_uint(rbf(gs * tb));
            kl |= (rb ^ ((unsigned)((int)rb >> 31) | 0x80000000u)) & 0xffff0000u; PIN1(kl);
          }
          if constexpr (gp == 5) { kl = (lane & 2) ? __float_as_uint(su) : (lane & 1) ? kl : (__float_as_uint(mbv) >> 16) | __float_as_uint(ta); PIN1(kl); }
        }
        SB();
      };
      constexpr int LN_JB = 1 + (19 * LNS) / 8;
      auto work1 = [&](auto JB, auto GP) __attribute__((always_inline)) {
        constexpr int jb = decltype(JB)::value, gp = decltype(GP)::value, pos = (jb - 1) * 8 + gp;
        SB();
        if constexpr (pos % LNS == 0 && pos / LNS < 20) lnu(IC<pos / LNS>{});
        if constexpr (jb == ST_JB + 3) {
          if constexpr (gp == 0) { if (!__all(gr.y == tag)) gr = granule_wait((const u64*)((const char*)(flags + FL_AB) + gro), tag); PIN1(gr); }
          if constexpr (gp == 1) { g1 = __builtin_amdgcn_rcpf(__uint_as_float((gr.x & 0xffff0000u) ^ ((unsigned)z << 31))); PIN1(g1); }
          if constexpr (gp == 2) { g0 = pw < 2 ? -(lo_bf(gr.x) * g1) : __uint_as_float(gr.x); PIN1(g0); }
        }
        if constexpr (jb == 8 && (gp == 2 || gp == 5)) {
          static_assert(VAN == 4 && VA0 >= 2 && VA0 + VAN <= 9, "first halves");
          int ln = lane;
          PIN1(ln);
          const int i = (ln & 15) * 4 + pw, n = gp == 2 ? (moe ? 0 : 64) : (et < 2 ? 0 : 32);
          const int r0 = gp == 2 ? tt * 64 : sym_row(S_, 64, 1, tt, 0, et >> 1, 0) + (et & 1) * 32;
          (gp == 2 ? tv7 : tv2) = i < n ? (r0 + i) * (S_ * 2) + ((tt ^ (VA0 - 1 + (ln >> 4))) << 8) : (int)0x80000000;
          PIN1(gp == 2 ? tv7 : tv2);
        }
        if constexpr (gp < SP1 && jb <= SP1E) spf(IC<SP0 + (jb - 1) * SP1 + gp>{});
        SB();
      };
      auto step1 = [&](auto JB) __attribute__((always_inline)) {
        constexpr int jb = decltype(JB)::value;
        if constexpr (jb == 1) PAGE(1);
        if constexpr (jb == 7) PAGE(2);
        constexpr int n16 = jb == 12 ? 12 : jb == 11 ? 14 : 16;
        constexpr auto wl = [](int q) { return (q == LN_JB ? 1 : 0) + (q == ST_JB ? 1 : 0); };
        if constexpr (jb == 1) {
          asm volatile("s_waitcnt vmcnt(%11)" : "+v"(lv[0]), "+v"(lv[1]), "+v"(lw[0]), "+v"(lw[1]), "+v"(ld[0]), "+v"(ld[1]),
                       "+v"(lv2[0]), "+v"(lv2[1]), "+v"(ld2[0]), "+v"(ld2[1]), "+v"(epv) : "i"(16) : "memory");
        } else if constexpr (jb == ST_JB + 3) {
          asm volatile("s_waitcnt vmcnt(%1)" : "+v"(gr) : "i"(n16 + wl(jb - 2) + wl(jb - 1)) : "memory");
        } else wait_vm<n16 + wl(jb - 3) + wl(jb - 2) + wl(jb - 1)>();
        BAR();
        if constexpr (jb == 2) { if (pw == 0 && lane < 16) ((int*)(smem + LDS_SEL))[lane < 8 ? lane : lane + 8] = 0; }
        if constexpr (jb == 4) { if (pw == 0 && lane == 0) ((unsigned*)(smem + LDS_SEL))[5] = tag; }
#define W1(i) work1(JB, IC<i>{})
        if constexpr (jb < 10) G1_STAGE_TILE(jb - 1 + G1_NST, ((jb - 1) % G1_NST) * G1_STAGE, W1);
        else G1_STAGE_A(jb - 1 + G1_NST, ((jb - 1) % G1_NST) * G1_STAGE, W1);
        if constexpr (jb == 12) G1_STAGE_B(13, G1_STAGE);
        if constexpr (jb == LN_JB) __builtin_amdgcn_raw_buffer_store_b64((u32x2){kl, tag}, rf, go, 0, 16);
        if constexpr (jb == ST_JB) {
          asm volatile("buffer_load_dwordx2 %0, %1, %2, 0 offen sc1" : "=v"(gr) : "v"(gro), "s"(rf) : "memory");
        }
        if constexpr (jb <= 8) {
          if (et == jb - 1) {
#pragma unroll
            for (int l = 0; l < WSN; ++l) bld16_nt_io(wv[l], rw, vw, l * F_ * 2);
          }
        }
        SB();
      };
      static_assert(ST_JB + 3 <= 12 && ST_JB >= 6, "schedule");
      step1(IC<1>{}); step1(IC<2>{}); step1(IC<3>{}); step1(IC<4>{}); step1(IC<5>{}); step1(IC<6>{}); step1(IC<7>{}); step1(IC<8>{}); step1(IC<9>{}); step1(IC<10>{}); step1(IC<11>{}); step1(IC<12>{});
    }
#define G2_N(q, so) DMA_S(rn2, vn2[q], mb0, so, kb)
#define G2_STAGE_N(kk_, W) do { const int kb = KROT(kk_); constexpr int so = n_off(kk_); SB(); \
    G2_N(0, so); W(0); G2_N(1, so + 4096); W(1); G2_N(2, so + 8192); W(2); G2_N(3, so + 12288); W(3); \
    G2_N(4, so + 16384); W(4); G2_N(5, so + 20480); W(5); G2_N(6, so + 24576); W(6); G2_N(7, so + 28672); W(7); SB(); } while (0)
#define G2_STAGE_NH(kk_, h) do { const int kb = KROT(kk_); constexpr int so = n_off(kk_) + (h) * 16384; SB(); \
    G2_N((h) * 4, so); G2_N((h) * 4 + 1, so + 4096); G2_N((h) * 4 + 2, so + 8192); G2_N((h) * 4 + 3, so + 12288); SB(); } while (0)
#define G2_STAGE_Q(g, W, i) do { const int kb = KROT(12 + g_t[g]); SB(); G2_N((g) >> 2, g_n[g]); W(i); G2_N(4 + ((g) >> 2), g_n[g] + g_h(g)); W(i + 1); SB(); } while (0)
#define G2_STAGE_M2(kk_, W) do { const int kb = KROT(kk_) * 128; constexpr int so = n_off(kk_ - 2) + 8192; SB(); \
    DMA_(rm2, vm2, mb0, so, kb, 0, " sc1"); W(2); DMA_(rm2, vm2, mb0, so + 4096, kb, 4096, " sc1"); W(3); \
    DMA_(rm2, vm2, mb0, so + 16384, kb, 8192, " sc1"); W(4); DMA_(rm2, vm2, mb0, so + 20480, kb, 12288, " sc1"); W(5); SB(); } while (0)
#define G2_STAGE_M(kk_, W) do { const int kb = KROT(kk_) * 128; constexpr int so = G2_MRING + m_off(kk_); SB(); \
    DMA_(rm2, vm2, mb0, so, kb, 0, " sc1"); W(8); DMA_(rm2, vm2, mb0, so + 4096, kb, 4096, " sc1"); W(9); \
    DMA_(rm2, vm2, mb0, so + 8192, kb, 8192, " sc1"); W(10); DMA_(rm2, vm2, mb0, so + 12288, kb, 12288, " sc1"); W(11); SB(); } while (0)
    WAIT_VM(8); BAR();
    asm volatile("ds_write_b32 %0, %1" :: "v"(lma), "v"(g0) : "memory");
    if (pw < 2) asm volatile("ds_write_b32 %0, %1 offset:512" :: "v"(lma), "v"(g1) : "memory");
    u32x4 tv[4];
    unsigned ready = 0;
    auto need = [&](int k) __attribute__((always_inline)) {
      const int b = k_blk(k) + (k_ord(k) & 1) * 8;
#pragma unroll 1
      for (int it = 0; !((ready >> b) & 1) && it < SPIN_MAX; ++it) {
        ready |= (unsigned)__ballot(__builtin_amdgcn_raw_buffer_load_b32(rf, abq[k < 5 ? 0 : k < 9 ? 1 : 2], 0, 16) == tag);
        asm volatile("" ::: "memory");
      }
    };
    unsigned ksel, bad = 0;
    int isel;
    unsigned xw;
    auto bvec = [&](unsigned src, const unsigned (&sl)[4]) __attribute__((always_inline)) {
      return (u32x4){__builtin_amdgcn_perm(0, src, sl[0]), __builtin_amdgcn_perm(0, src, sl[1]),
                     __builtin_amdgcn_perm(0, src, sl[2]), __builtin_amdgcn_perm(0, src, sl[3])};
    };
#define BC8(v, p) (unsigned)__builtin_amdgcn_update_dpp(__builtin_amdgcn_update_dpp(0, (int)(v), 0x150 + (p), 0xf, 0x3, false), (int)(v), 0x158 + (p), 0xf, 0xc, false)
    float* lg = (float*)(smem + LDS_GU);
    f32x4 macc = {0.f, 0.f, 0.f, 0.f};
    u32x2 sv;
    unsigned hp, gbad;
    float s0, s1;
    auto work2 = [&](auto JB, auto GP) __attribute__((always_inline)) {
      constexpr int jb = decltype(JB)::value, gp = decltype(GP)::value;
      SB();
      if constexpr (jb == -1 && gp < 4) {
        constexpr int c = gp == 0 ? 0xB1 : gp == 1 ? 0x4E : gp == 2 ? 0x141 : 0x140;
        merge2(k1, k2, dpp_u<c>(k1), dpp_u<c>(k2));
        PIN2(k1, k2);
      }
      if constexpr (jb == -1 && gp == 4) {
        ksel = tk ? k2 : k1;
        isel = (0xffff - (int)(ksel & 0xffffu)) & (S_ - 1);
        k1 = (isel * D_ + d0 + rq * 16 + (lane & 7) * 2) * 2; k2 = O_ST + isel * 8;
        PIN1(ksel);
        xw = gld4_s(x, k1);
        sv = bld8_sc1(rf, k2);
      }
      if constexpr (jb == 0 && gp == 1) wait_xs<3 + 2 + 2>(xw, sv);
      if constexpr (jb == 0) {
        if constexpr (gp == 1) { if (!__all(sv.y == tag)) sv = granule_wait(stat + isel, tag); s0 = __builtin_amdgcn_rcpf(hi_bf(sv.x)); PIN1(s0); }
        if constexpr (gp == 2) { s1 = ln_elem(lo_bf(xw), lo_bf(sv.x), hi_bf(sv.x), s0); PIN1(s1); }
        if constexpr (gp == 3) { xw = pk2(s1, ln_elem(hi_bf(xw), lo_bf(sv.x), hi_bf(sv.x), s0)); PIN1(xw); }
      }
      if constexpr (jb == DG && gp < 7) { PIN1(dv[gp]); macc = MM(dv[gp], bvec(BC8(hp, gp), sel[0]), macc); PIN1(macc); }
      if constexpr ((jb == 0 && gp >= 4) || jb == 1) {
        constexpr int l = jb == 0 ? gp - 4 : gp + 4;
        if constexpr (!(l & 1)) hp = BC8(xw, l >> 1);
        PIN1(wv[l]);
        macc = MM(wv[l], bvec(hp, sel[l & 1]), macc);
        PIN2(macc, hp);
      }
      if constexpr (jb == GV_JB + 2) {
        if constexpr (gp == 0) { gbad = (gv[0].y ^ tag) | (gv[0].w ^ tag) | (gv[1].y ^ tag) | (gv[1].w ^ tag); PIN1(gbad); }
        if constexpr (gp == 1) { gbad |= (gv[2].y ^ tag) | (gv[2].w ^ tag) | (gv[3].y ^ tag) | (gv[3].w ^ tag); PIN1(gbad); }
        if constexpr (gp == 2) {
          s0 = ((__uint_as_float(gv[0].x) + __uint_as_float(gv[1].x)) + __uint_as_float(gv[2].x)) + __uint_as_float(gv[3].x);
          s1 = ((__uint_as_float(gv[0].z) + __uint_as_float(gv[1].z)) + __uint_as_float(gv[2].z)) + __uint_as_float(gv[3].z);
          PIN2(s0, s1);
        }
        if constexpr (gp == 3) {
#pragma unroll 1
          for (int it = 0; !__all(gbad == 0) && it < SPIN_MAX; ++it) {
            u32x4 q[4];
            gbad = 0;
#pragma unroll
            for (int i = 0; i < 4; ++i) { q[i] = __builtin_amdgcn_raw_buffer_load_b128(rgu, vgu, i * F_ * 16, 16); gbad |= (q[i].y ^ tag) | (q[i].w ^ tag); }
            s0 = ((__uint_as_float(q[0].x) + __uint_as_float(q[1].x)) + __uint_as_float(q[2].x)) + __uint_as_float(q[3].x);
            s1 = ((__uint_as_float(q[0].z) + __uint_as_float(q[1].z)) + __uint_as_float(q[2].z)) + __uint_as_float(q[3].z);
            asm volatile("" ::: "memory");
          }
          const unsigned gp2 = pk2(s0, s1);
          const unsigned up2 = dpp_u<0x128>(gp2);
          s0 = tk ? hi_bf(up2) : lo_bf(gp2); s1 = tk ? hi_bf(gp2) : lo_bf(up2);
          PIN2(s0, s1);
        }
        if constexpr (gp == 4) { s0 = rbf(s0 * __builtin_amdgcn_rcpf(1.f + __expf(-s0))); PIN1(s0); }
        if constexpr (gp == 5) { hp = pk2(s0 * s1, 0.f); PIN1(hp); }
      }
      if constexpr (jb == 10 && gp == 4) code_touch(sd, (j * 4 + pw) * 256);
      SB();
    };
#define W2M(i) work2(IC<-1>{}, IC<i>{})
#define W20(i) work2(IC<0>{}, IC<i>{})
#define W21(i) work2(IC<1>{}, IC<i>{})
    unsigned fs;
    {
      G1_STAGE_B(15, 3 * G1_STAGE);
      G1_STAGE_B(14, 2 * G1_STAGE);
      lgkm0(sd);
      BAR();
      G2_STAGE_NH(0, 0);
      WAIT_VM(6); BAR();
#pragma unroll
      for (int i = 0; i < 4; ++i) kk[i] = bld16_sc1(rf, l16, ko_ + i * 1024);
      G2_STAGE_NH(0, 1);
#pragma unroll
      for (int l = WSN; l < 16; ++l) wv[l] = bld16_nt(rw, vw, l * F_ * 2);
      touch1(rn2, tv7, mb0, 0);
      asm volatile("s_waitcnt vmcnt(%4)" : "+v"(kk[0]), "+v"(kk[1]), "+v"(kk[2]), "+v"(kk[3]) : "i"(16 - WSN + 5) : "memory");
#pragma unroll
      for (int i = 0; i < 4; ++i) {
        k2 = med3u(k1, k2, kk[i].x); k1 = max(k1, kk[i].x);
        k2 = med3u(k1, k2, kk[i].z); k1 = max(k1, kk[i].z);
        bad |= (kk[i].y ^ tag) | (kk[i].w ^ tag);
      }
#pragma unroll 1
      for (int it = 0; !__all(bad == 0) && it < SPIN_MAX; ++it) {
        k1 = 0; k2 = 0; bad = 0;
#pragma unroll
        for (int i = 0; i < 4; ++i) {
          const u32x4 q = __builtin_amdgcn_raw_buffer_load_b128(rf, l16, ko_ + i * 1024, 16);
          k2 = med3u(k1, k2, q.x); k1 = max(k1, q.x);
          k2 = med3u(k1, k2, q.z); k1 = max(k1, q.z);
          bad |= (q.y ^ tag) | (q.w ^ tag);
        }
        asm volatile("" ::: "memory");
      }
      merge2(k1, k2, dpp_u<0xB1>(k1), dpp_u<0xB1>(k2));
      merge2(k1, k2, dpp_u<0x4E>(k1), dpp_u<0x4E>(k2));
      merge2(k1, k2, dpp_u<0x141>(k1), dpp_u<0x141>(k2));
      merge2(k1, k2, dpp_u<0x140>(k1), dpp_u<0x140>(k2));
      if ((lane & 15) == 0) *(u32x2*)(smem + LDS_KEY + (pw * 4 + rq) * 8) = (u32x2){k1, k2};
      PAGE(3);
      wait_vm<17 - WSN>();
      BAR_LGKM();
#pragma unroll
      for (int jj = 0; jj < 4; ++jj) tv[jj] = *(const u32x4*)(smem + G2_MRING + m_off(0) + (jj * 4 + pw) * 1024 + lane * 16);
      { const u32x2 kp = *(const u32x2*)(smem + LDS_KEY + ci * 8); k1 = kp.x; k2 = kp.y; }
#pragma unroll
      for (int jj = 0; jj < 4; ++jj) __builtin_amdgcn_raw_buffer_store_b128(tv[jj], rm2, vm2 + jj * 4096 + 2 * tt * 16384, 0, 16);
      G2_STAGE_N(1, W2M);
      WAIT_VM(10);
      __builtin_amdgcn_raw_buffer_store_b32(tag, rf, abo, 0, 16);
      touch1(rn2, tv2, mb0, 0);
      {
        lds_vi rel = (lds_vi)(smem + LDS_SEL + 12);
        int it = 0;
        while (rel[0] != 4 && it < SPIN_MAX * 8) ++it;
        asm volatile("" ::: "memory");
      }
      G2_STAGE_N(2, W20);
      __builtin_amdgcn_s_sleep(2);
      WAIT_VM(10);
      fs = bld4_sc1(rf, abq[0]);
    }
    BAR();
#pragma unroll
    for (int jj = 0; jj < 4; ++jj) tv[jj] = *(const u32x4*)(smem + G2_MRING + m_off(1) + (jj * 4 + pw) * 1024 + lane * 16);
    constexpr int SPB = SP0 + SP1E * SP1;
    G2_STAGE_N(3, NOW);
    asm volatile("s_waitcnt vmcnt(8)" : "+v"(fs) :: "memory");
    ready = (unsigned)__ballot(fs == tag);
    need(2);
    G2_STAGE_M(2, NOW);
    need(3);
    G2_STAGE_M(3, NOW);
    fs = bld4_sc1(rf, abq[1]);
    W21(0); W21(1); W21(2); W21(3); W21(4); W21(5); W21(6); W21(7); W21(8); W21(9); W21(10); W21(11);
    asm volatile("" : "+v"(tv[0]), "+v"(tv[1]), "+v"(tv[2]), "+v"(tv[3]));
    auto step2 = [&](auto JB) __attribute__((always_inline)) {
      constexpr int jb = decltype(JB)::value;
      if constexpr (jb == 3) PAGE(4);
      if constexpr (jb == 9) PAGE(5);
      if constexpr (jb == 12) PAGE(7);
      constexpr auto must = [](int q) { return (q == 2 ? 4 : 0) + ((FSM >> q) & 1) + dig(GVS, q) + (q == XJ ? 3 : 0); };
      if constexpr (jb == 2) wait_vm<4 + must(1)>();
      else wait_vm<dn_cnt(jb - 2) + (jb == 5 ? 1 : 0) + 12 + must(jb - 1) + dn_cnt(jb - 1)>();
      if constexpr (jb == 3) { if (pw == 0 && (lane & ~8) == 0) ((unsigned*)(smem + LDS_SEL))[tk] = ksel; }
      if constexpr ((jb == 2 || jb == DG + 1)) {
        *(f32x4*)(lg + pw * 256 + lane * 4) = macc;
        macc = (f32x4){0.f, 0.f, 0.f, 0.f};
        lgkm0(sd);
      }
      BAR();
      float p0[4], p1[4];
      const int col = (pw & 1) * 64 + lane;
      if constexpr (jb == 3) {
        if (g == 0 && pw == 0 && lane == 0) __hip_atomic_store((unsigned*)flags + FL_EPOCH, epoch + 1u, __ATOMIC_RELAXED, __HIP_MEMORY_SCOPE_AGENT);
      }
      if constexpr (jb == DG + 1) {
        const float* p = lg + (pw & 1) * 32 + (lane >> 4) * 64 + ((2 * lane) & 7) * 4 + ((lane >> 2) & 3);
#pragma unroll
        for (int i = 0; i < 4; ++i) { p0[i] = p[i * 256]; p1[i] = p[i * 256 + 4]; }
      }
      if constexpr (jb == 2) {
        const float* p = lg + (col >> 5) * 64 + (col & 7) * 4 + ((col >> 3) & 3);
#pragma unroll
        for (int i = 0; i < 4; ++i) { p0[i] = p[i * 256]; p1[i] = p[i * 256 + 32]; }
      }
      if constexpr (jb == 4) {
        __builtin_amdgcn_raw_buffer_store_b32(tag, rf, abo + 8 * AB_STRIDE * 4, 0, 16);
      }
      if constexpr ((FSM >> (jb - 2)) & 1) {
        asm volatile("" : "+v"(fs));
        ready |= (unsigned)__ballot(fs == tag);
      }
      if constexpr (jb == GV_JB + 2) asm volatile("" : "+v"(gv[0]), "+v"(gv[1]), "+v"(gv[2]), "+v"(gv[3]));
      auto w2 = [&](auto GP) __attribute__((always_inline)) {
        constexpr int gp = decltype(GP)::value;
        if constexpr ((jb == 2 || jb == DG + 1) && gp == 0) {
          p0[0] = (p0[0] + p0[1]) + (p0[2] + p0[3]); p1[0] = (p1[0] + p1[1]) + (p1[2] + p1[3]);
          PIN2(p0[0], p1[0]);
        }
        if constexpr (jb == DG + 1 && gp == 1) {
          if (pw < 2) __builtin_amdgcn_raw_buffer_store_b64((u32x2){pk2(p0[0], p1[0]), tag}, mk_rsrc(wsp + WS_DN), (((pw * 8 + cg8) * 32 + rgb) * 64 + lane) * 8, 0, 16);
        }
        if constexpr (jb == XJ && gp % 4 == 1) epi_setup(gp / 4);
        work2(JB, GP);
        if constexpr (gp < dig(SPS, jb)) spf(IC<SPB + acc_dig(SPS, jb) + gp>{});
      };
#define W2(i) w2(IC<(i)>{})
      if (jb < 10) G2_STAGE_N(jb + 2, W2);
      if (jb == 10) { G2_STAGE_Q(0, W2, 0); need(14); G2_STAGE_M2(14, W2); }
      if (jb == 11) { G2_STAGE_Q(2, W2, 0); need(15); G2_STAGE_M2(15, W2); }
      if (jb < 12) {
        if (jb >= 10) { W2(6); W2(7); }
        need(jb + 2);
        G2_STAGE_M(jb + 2, W2);
      }
      if (jb == 10) G2_STAGE_Q(1, NOW, 0);
      if (jb == 11) G2_STAGE_Q(3, NOW, 0);
      if (jb == 12) { G2_STAGE_Q(4, W2, 0); G2_STAGE_Q(5, W2, 2); G2_STAGE_Q(6, W2, 4); G2_STAGE_Q(7, W2, 6); }
      if constexpr (jb == 2) {
#pragma unroll
        for (int jj = 0; jj < 4; ++jj) __builtin_amdgcn_raw_buffer_store_b128(tv[jj], rm2, vm2 + jj * 4096 + (2 * tt + 1) * 16384, 0, 16);
      }
      if constexpr ((FSM >> jb) & 1) fs = bld4_sc1(rf, abq[jb < 5 ? 1 : 2]);
#pragma unroll
      for (int i = acc_dig(GVS, jb); i < acc_dig(GVS, jb + 1); ++i) gv[i] = bld16_sc1(rgu, vgu, i * F_ * 16);
      if constexpr (jb == XJ) { xq_load(0); xq_load(1); xq_load(2); }
#pragma unroll
      for (int i = dn_first(jb); i < dn_first(jb) + dn_cnt(jb); ++i) dv[i] = bld16_nt(rdn, vdn, i * D_ * 2);
      if constexpr (jb == 2) {
        if (moe && pw < 2)
          __builtin_amdgcn_raw_buffer_store_b128((u32x4){__float_as_uint(p0[0]), tag, __float_as_uint(p1[0]), tag},
                                                 rgu, ((mat * 4 + dq) * F_ + cg * 128 + col) * 16, 0, 16);
      }
      SB();
      if constexpr (dig(SPS, jb) > 12) spf(IC<SPB + acc_dig(SPS, jb) + 12>{});
      if constexpr (dig(SPS, jb) > 13) spf(IC<SPB + acc_dig(SPS, jb) + 13>{});
      SB();
    };
    static_assert(DG + 1 <= 12 && DG >= GV_JB + 3 && dn_first(DG - 2) == 7 && acc_dig(GVS, GV_JB + 1) == 4, "schedule");
    static_assert(SPB + acc_dig(SPS, 14) == (16 - VAN) * 16, "all tiles are prefetched");
    step2(IC<2>{}); step2(IC<3>{}); step2(IC<4>{}); step2(IC<5>{}); step2(IC<6>{}); step2(IC<7>{}); step2(IC<8>{}); step2(IC<9>{}); step2(IC<10>{}); step2(IC<11>{}); step2(IC<12>{});
    lgkm0(sd);
    WAIT_VM(8); BAR();
    G2_STAGE_Q(8, NOW, 0); G2_STAGE_Q(9, NOW, 0); G2_STAGE_Q(10, NOW, 0); G2_STAGE_Q(11, NOW, 0);
    WAIT_VM(8); BAR();
    G2_STAGE_Q(12, NOW, 0); G2_STAGE_Q(13, NOW, 0);
    __builtin_amdgcn_s_setprio(0);
    asm volatile("" : "+v"(xq[0]), "+v"(xq[1]), "+v"(sq));
    cv_calc();
    __builtin_amdgcn_s_setprio(3);
    WAIT_VM(0); BAR();
    G2_STAGE_Q(14, NOW, 0); G2_STAGE_Q(15, NOW, 0);
    __builtin_amdgcn_s_setprio(0);
    epi_setup(3);
    key[0] = __builtin_amdgcn_readlane(ksel, 0); key[1] = __builtin_amdgcn_readlane(ksel, 8);
    far_calc();
    const int ec = seg0((lane >> 4) & 1, (lane >> 3) & 1);
    int dno = (ec >> 7) * 16384 + (lane >> 5) * 512 + (ec & 127) * 4 + (lane & 7) * 16;
    PIN1(dno);
    const __amdgpu_buffer_rsrc_t rd = mk_rsrc(wsp + WS_DN + pw * 4096);
    float sg0 = rbf(__builtin_amdgcn_rcpf(1.f + __expf(-key_score(key[0])))), sg1 = rbf(__builtin_amdgcn_rcpf(1.f + __expf(-key_score(key[1]))));
    PIN2(sg0, sg1);
    u32x4 dq_[4];
    asm volatile("s_waitcnt vmcnt(0)\n\ts_cmp_lg_u32 %7, 0\n\ts_cbranch_scc1 1f\n\ts_sleep 2\n\t"
                 "buffer_load_dwordx4 %0, %4, %5, %6 offen sc1\n\tbuffer_load_dwordx4 %1, %4, %5, %6 offen offset:1024 sc1\n\t"
                 "buffer_load_dwordx4 %2, %4, %5, %6 offen offset:2048 sc1\n\tbuffer_load_dwordx4 %3, %4, %5, %6 offen offset:3072 sc1\n"
                 "1:\n\ts_barrier\n\ts_waitcnt vmcnt(0)"
                 : "=&v"(dq_[0]), "=&v"(dq_[1]), "=&v"(dq_[2]), "=&v"(dq_[3])
                 : "v"(dno), "s"(rd), "s"((far0 & ~9) ? 131072 : 0), "s"(min(far0 & ~9, far1 & ~9)) : "scc", "memory");
    bool first = true;
#pragma unroll 1
    for (int c = 0; c < 2; ++c) {
      if (__builtin_expect(((c ? far1 : far0) & ~9) != 0, 1)) continue;
#pragma unroll 1
      for (int it = 0; it < SPIN_MAX; ++it) {
        unsigned bad = 0;
#pragma unroll
        for (int i = 0; i < 4; ++i) bad |= (dq_[i].y ^ tag) | (dq_[i].w ^ tag);
        if ((first || it) && __all(bad == 0)) break;
        if (it) __builtin_amdgcn_s_sleep(4);
#pragma unroll
        for (int i = 0; i < 4; ++i) dq_[i] = __builtin_amdgcn_raw_buffer_load_b128(rd, dno + i * 1024, c * 131072, 16);
        asm volatile("" ::: "memory");
      }
      first = false;
      const float sg = c ? sg1 : sg0;
      f32x4 s4 = {0.f, 0.f, 0.f, 0.f};
#pragma unroll
      for (int i = 0; i < 4; ++i) s4 += (f32x4){lo_bf(dq_[i].x), hi_bf(dq_[i].x), lo_bf(dq_[i].z), hi_bf(dq_[i].z)};
      *(f32x2*)(smem + LDS_FIN + (c * 4 + pw) * 512 + (lane & 31) * 16 + (lane >> 5) * 8) = (f32x2){fold32(s4[0], s4[2]) * sg, fold32(s4[1], s4[3]) * sg};
      asm volatile("s_waitcnt lgkmcnt(0)" ::: "memory");
      if (lane == 0) asm volatile("ds_add_u32 %0, %1" :: "v"(lds_addr(smem + LDS_SEL + 24 + c * 4)), "v"(1) : "memory");
    }
  } else {
  {
    const int q = j * 4 + wid;
    constexpr int Q1 = WS_MB, Q2 = Q1 + 8, Q3 = Q2 + 8, Q4 = Q3 + 7, Q5 = Q4 + 7, Q6 = Q5 + 7, Q7 = Q6 + 4, Q8 = Q7 + 4, QC = Q8 + 1;
    static_assert(QC <= 128, "warm-up slots");
    unsigned dmy = 0;
    if (q < QC) {
      const char* base; int b0;
      if (q < Q1) { base = wsp; b0 = 0; }
      else if (q < Q2) { base = (const char*)ws_re; b0 = Q1; }
      else if (q < Q3) { base = (const char*)ws_im; b0 = Q2; }
      else if (q < Q4) { base = (const char*)w_gate; b0 = Q3; }
      else if (q < Q5) { base = (const char*)w_up; b0 = Q4; }
      else if (q < Q6) { base = (const char*)w_down; b0 = Q5; }
      else if (q < Q7) { base = (const char*)out; b0 = Q6; }
      else if (q < Q8) { base = (const char*)x; b0 = Q7; }
      else { base = (const char*)w_r; b0 = Q8; }
      base += (size_t)(q - b0) << 20;
      asm volatile("s_load_dword %0, %1, 0" : "+s"(dmy) : "s"(base) : "memory");
    }
    if (wid == 0) { CALL_PG(1); CALL_PG(8); CALL_PG(5); CALL_PG(13); }
    else if (wid == 1) { CALL_PG(2); CALL_PG(9); CALL_PG(6); }
    else if (wid == 2) { CALL_PG(3); CALL_PG(10); CALL_PG(7); }
    else { CALL_PG(4); CALL_PG(11); CALL_PG(12); }
    BAR();
    lgkm0(dmy);
  }
  PAGE(8);
  epi_setup(0); epi_setup(1); epi_setup(2); epi_setup(3);
  const int wr = wid >> 1, wc = wid & 1;
  const int sw = swz(fr), fl = lrw(fr);
  const int ko0 = ((0 + fq) ^ sw) * 16, ko1 = ((4 + fq) ^ sw) * 16;
  bf16x8 fm[2][4], fn[2][4];
  {
    const char* pm = smem + (wr * 64 + fl) * 128;
    const char* pn = smem + 16384 + (wc * 64 + fl) * 128;
    f32x4 acc[4][4];
#pragma unroll
    for (int i = 0; i < 4; ++i)
#pragma unroll
      for (int jn = 0; jn < 4; ++jn)
#pragma unroll
        for (int q = 0; q < 4; ++q) acc[i][jn][q] = 0.f;
#define TO(i) ((i) * 2048)
#define LDSP(T, a) ((__attribute__((address_space(3))) T*)(size_t)(a))
#define SLOTS(n) _Pragma("unroll") for (int q = 0; q < (n); ++q)
#define MFA(c, m, n) asm volatile("v_mfma_f32_16x16x32_bf16 %0, %2, %1, %0" : "+v"(c) : "v"(m), "v"(n))
#define MFA2(c, m, n, m2, n2) asm volatile("v_mfma_f32_16x16x32_bf16 %0, %2, %1, %0\n\tv_mfma_f32_16x16x32_bf16 %0, %4, %3, %0" : "+v"(c) : "v"(m), "v"(n), "v"(m2), "v"(n2))
    unsigned lm[2][2], ln[2][2];
#pragma unroll
    for (int u = 0; u < 2; ++u)
#pragma unroll
      for (int kk = 0; kk < 2; ++kk) {
        lm[u][kk] = lds_addr(pm) + u * 2 * G1_STAGE + (kk ? ko1 : ko0);
        ln[u][kk] = lds_addr(pn) + u * 2 * G1_STAGE + (kk ? ko1 : ko0);
        asm volatile("" : "+v"(lm[u][kk]), "+v"(ln[u][kk]));
      }
    auto ldm = [&](int stg, int i, int kk) __attribute__((always_inline)) { fm[kk][i] = *LDSP(const bf16x8, lm[stg >> 1][kk] + (stg & 1) * G1_STAGE + TO(i)); };
    auto ldn = [&](int stg, int jn, int kk) __attribute__((always_inline)) { fn[kk][jn] = *LDSP(const bf16x8, ln[stg >> 1][kk] + (stg & 1) * G1_STAGE + TO(jn)); };
    auto quad = [&](int a, int c, int d, int s) __attribute__((always_inline)) {
      const int i = (s < 2 || s >= 6) ? a : a + 1, jn = s < 4 ? c : d, kk = (s == 1 || s == 2 || s == 5 || s == 6);
      MFA(acc[i][jn], fm[kk][i], fn[kk][jn]);
    };
    auto quad2 = [&](int a, int c, int d, int s) __attribute__((always_inline)) {
      const int i = (s < 2 || s >= 6) ? a : a + 1, jn = s < 4 ? c : d, kk = (s == 2 || s == 6);
      MFA2(acc[i][jn], fm[kk][i], fn[kk][jn], fm[1 - kk][i], fn[1 - kk][jn]);
    };
    auto half1 = [&](int stg) __attribute__((always_inline)) {
      const int ja = (stg & 1) * 2, jb = 2 - ja;
      SLOTS(8) {
        quad(0, ja, ja + 1, q);
        if (q < 4) ldn(stg, jb + (q >> 1), q & 1); else ldm(stg, 2 + ((q - 4) >> 1), q == 5 || q == 6);
        SB();
      }
      SLOTS(4) { quad2(0, jb, jb + 1, 2 * q); SB(); }
    };
    auto half2 = [&](int stg) __attribute__((always_inline)) {
      const int ja = (stg & 1) * 2, jb = 2 - ja, nx = (stg + 1) & 3;
      SLOTS(8) {
        quad(2, jb + 1, jb, q);
        if (!(q & 1)) ldm(nx, q >> 2, q == 2 || q == 4);
        SB();
      }
      SLOTS(8) {
        quad(2, ja, ja + 1, q);
        if (!(q & 1)) ldn(nx, jb + (q >> 2), (q >> 1) & 1);
        SB();
      }
    };
    unsigned ae[7];
    {
      int l_ = lane;
      asm volatile("" : "+v"(l_));
      const int p = l_ >> 4, dn = sym_dn(p), hf = (p >> 1) ^ (dn & z);
#pragma unroll
      for (int u = 0; u < 7; ++u) {
        const int T = u < 3 ? wc * 4 : (wc * 4 + u - 3 - dn) & 7, i = u < 3 ? (u + 1) ^ ((u & 1) ? 0 : dn * 2) : 0;
        const int f = dn * 8 + ((T & 2) << 1) + ((T & 4) >> 1) + (T & 1);
        ae[u] = lds_addr(smem + G2_MRING) + (hf * 64 + i * 16 + lrw(f)) * 128 + (((wr * 4 + ((l_ >> 2) & 3)) ^ swz(f)) * 16) + (l_ & 3) * 4;
        asm volatile("" : "+v"(ae[u]));
      }
    }
    unsigned al = lds_addr(smem + LDS_MB) + (wr * 32 + fr * 2) * 4, ac = lds_addr(smem + LDS_MB) + (256 + wc * 64 + fq * 4) * 4;
    asm volatile("" : "+v"(al), "+v"(ac));
    SLOTS(16) asm volatile("" : "+v"(acc[q >> 2][q & 3]));
    SB();
    SLOTS(4) { ldm(0, q >> 1, q & 1); ldn(0, q >> 1, q & 1); }
    SB();
#pragma unroll 1
    for (int k4 = 0; k4 < G1_NKT - 4; k4 += 4) {
      half1(0); BAR_LGKM();
      SB(); half2(0);
      half1(1); BAR_LGKM();
      SB(); half2(1);
      half1(2); BAR_LGKM();
      SB(); half2(2);
      half1(3); BAR_LGKM();
      SB(); half2(3);
    }
    bf16x8 gm[4][4], gn[2][8];
    auto rdm1 = [&](int set, int t, int h, int i, int kk) __attribute__((always_inline)) {
      gm[set][kk * 2 + i] = *LDSP(const bf16x8, lm[(t >> 1) & 1][kk] + (t & 1) * G1_STAGE + h * 4096 + i * 2048);
    };
    auto rdg = [&](int set, int t, int h, int r) __attribute__((always_inline)) {
      if (r == 0 || r == 2 || r == 4 || r == 5) rdm1(set, t, h, r >= 4, r == 2 || r == 4);
      else {
        const int jn = r < 4 ? 0 : (r - 4) >> 1, kk = r < 4 ? r >> 1 : r & 1;
        gn[set][kk * 4 + jn] = *LDSP(const bf16x8, ln[(t >> 1) & 1][kk] + (t & 1) * G1_STAGE + TO(jn));
      }
    };
    auto mmg = [&](int ms, int ns, int h, int jn, int r, int f) __attribute__((always_inline)) {
      const int i = (r >> 1) ^ f, kk = r == 1 || r == 2;
      MFA(acc[h * 2 + i][jn], gm[ms][kk * 2 + i], gn[ns][kk * 4 + jn]);
    };
    f32x2 rsv, mrv;
    f32x4 cev[4];
    float tc[32];
    unsigned ep;
    auto rds = [&](int h, int r) __attribute__((always_inline)) {
      if (r == 0) mrv = *LDSP(const f32x2, al + h * 256); else if (r == 5) rsv = *LDSP(const f32x2, al + 512 + h * 256);
      else cev[r - 1] = *LDSP(const f32x4, ac + (r - 1) * 64);
    };
    auto prep = [&](int n) __attribute__((always_inline)) {
      tc[n] = llvm_fmul_legacy(mrv[n & 1], cev[n >> 3][(n >> 1) & 3]);
      asm volatile("" : "+v"(tc[n]));
    };
    auto epi = [&](int h, int n) __attribute__((always_inline)) {
      const int jn = n >> 4, r = (n >> 2) & 3, u = n & 3, e = (n >> 2) * 2;
      if (u == 0) {}
      else if (u == 1) asm volatile("v_fmac_f32 %0, %2, %4\n\tv_fmac_f32 %1, %3, %5\n\tv_cvt_pk_bf16_f32 %0, %0, %1" : "+v"(tc[e]), "+v"(tc[e + 1])
                                    : "v"(acc[h * 2][jn][r]), "v"(acc[h * 2 + 1][jn][r]), "v"(rsv[0]), "v"(rsv[1]));
      else if (u == 2) ep = __float_as_uint(tc[e]);
      else *LDSP(unsigned, (r ? ae[r - 1] + (jn & 1) * 128 + (jn >> 1) * lrw(4) * 128 : ae[3 + jn]) + m_off(h)) = ep;
      SB();
    };
    SB();
    half1(0);
    BAR_LGKM();
    SB();
    SLOTS(16) {
      if (q < 8) quad(2, 3, 2, q); else quad(2, 0, 1, q - 8);
      if (q < 12) rdg(0, 14, 0, q); else rdg(1, 13, 0, q - 12);
      SB();
    }
    BAR();
    SB();
    SLOTS(16) {
      mmg(0, 0, 0, q >> 2, q & 3, (q >> 2) & 1);
      if (q < 6) rds(0, q); else if (q < 14) rdg(1, 13, 0, q - 2);
      if (q >= 6) prep(q - 6);
      if (q >= 14) prep(q - 4);
      SB();
    }
    PAGE(9);
    BAR();
    SB();
    {
      constexpr int ct[8] = {1, 1, 0, 0, 1, 0, 1, 0}, cj[8] = {0, 1, 0, 1, 2, 2, 3, 3}, cf[8] = {0, 1, 0, 1, 0, 1, 0, 1};
      constexpr auto en = [](int q) { return q < 15 ? 0 : q < 26 ? (q - 14) * 3 - (q == 25) : q < 27 ? 32 : q < 32 ? 32 + (q - 26) * 3 + (q == 31) : 48; };
      SLOTS(32) {
        mmg(ct[q >> 2], ct[q >> 2], 0, cj[q >> 2], q & 3, cf[q >> 2]);
        if (q < 12) rdg(0, 15, 0, q);
        else if (q < 15 || q == 17) { const int r = q < 15 ? q - 12 : 3; rdm1(2, 15, 1, r >> 1, r == 1 || r == 2); }
        else if (q == 20 || q == 23 || q == 25 || q == 26) { const int r = q == 20 ? 0 : q == 23 ? 1 : q - 23; rdm1(3, 13, 1, r >> 1, r == 1 || r == 2); }
        if (q < 15) prep(12 + q);
        if (q >= 10 && q < 15) prep(17 + q);
        SB();
#pragma unroll
        for (int n = q ? en(q - 1) : 0; n < en(q); ++n) epi(0, n);
      }
    }
    SLOTS(2) { mmg(2, 0, 1, 0, q, 0); SB(); }
    asm volatile("s_nop 1");
    SLOTS(16) { epi(0, 48 + q); if (q == 3 || q == 9) { mmg(2, 0, 1, 0, q == 3 ? 2 : 3, 0); SB(); } }
    BAR_LGKM();
    SB();
    SLOTS(12) {
      mmg(2, 0, 1, 1 + (q >> 2), q & 3, ~(q >> 2) & 1);
      if (q < 6) rds(1, q); else rdg(0, 14, 1, q - 6);
      if (q >= 8) prep(q - 8);
      SB();
    }
    SLOTS(16) {
      mmg(3, 1, 1, q >> 2, q & 3, (q >> 2) & 1);
      if (q < 6) rdg(0, 14, 1, q + 6);
      if (q < 4) prep(4 + q); else { prep(2 * q); prep(2 * q + 1); }
      SB();
    }
    asm volatile("s_waitcnt lgkmcnt(0)" ::: "memory");
    if (lane == 0) asm volatile("ds_add_u32 %0, %1" :: "v"(lds_addr(smem + LDS_SEL + 12)), "v"(1) : "memory");
    SB();
    SLOTS(16) {
      mmg(0, 0, 1, q >> 2, q & 3, (q >> 2) & 1);
      if (q < 4) fn[0][q] = *(const bf16x8*)(smem + (wr * 128 + fl) * 128 + (q >> 1) * 2048 + ((q & 1) ? ko1 : ko0));
      else if (q < 8) fm[q & 1][(q - 4) >> 1] = *(const bf16x8*)(smem + G2_MRING + m_off(0) + (wc * 64 + fl) * 128 + TO((q - 4) >> 1) + ((q & 1) ? ko1 : ko0));
      SB();
      if (q >= 8) { epi(1, 3 * q - 24); epi(1, 3 * q - 23); epi(1, 3 * q - 22); }
    }
    SLOTS(40) epi(1, 24 + q);
  }

  const char* pn = smem + (wr * 128 + fl) * 128;
  const char* pm = smem + G2_MRING + (wc * 64 + fl) * 128;
  f32x4 acc[8][4];
  auto mm = [&](bool imaj, int set, int q2, int s, int mode) __attribute__((always_inline)) {
    int i, jj, kk;
    if (imaj) { i = s >> 2; const int r = s & 3; jj = (r >> 1) ^ (i & 1); kk = (r == 1 || r == 2); }
    else { jj = s >> 3; const int r = s & 7; i = jj ? 3 - (r >> 1) : (r >> 1); kk = ((r + 1) >> 1) & 1; }
    if (mode == 1 && !(s & 1)) asm volatile("v_mfma_f32_16x16x32_bf16 %0, %2, %1, 0" : "=&v"(acc[q2 * 2 + jj][i]) : "v"(fm[kk][i]), "v"(fn[set][jj * 2 + kk]));
    else if (mode) asm volatile("v_mfma_f32_16x16x32_bf16 %0, %2, %1, %0" : "+v"(acc[q2 * 2 + jj][i]) : "v"(fm[kk][i]), "v"(fn[set][jj * 2 + kk]));
    else acc[q2 * 2 + jj][i] = __builtin_amdgcn_mfma_f32_16x16x32_bf16(fn[set][jj * 2 + kk], fm[kk][i], acc[q2 * 2 + jj][i], 0, 0, 0);
  };
  unsigned bn[2], bm[2], an[2], am[2];
#pragma unroll
  for (int kk = 0; kk < 2; ++kk) {
    bn[kk] = lds_addr(pn) + (kk ? ko1 : ko0); bm[kk] = lds_addr(pm) + (kk ? ko1 : ko0);
    asm volatile("" : "+v"(bn[kk]), "+v"(bm[kk]));
    an[kk] = bn[kk]; am[kk] = bm[kk] + m_off(0);
  }
#define RDN(jt, kk, o) (*LDSP(const bf16x8, an[kk] + (o) + (jt) * 2048))
#define RDM(i, kk, o) (*LDSP(const bf16x8, am[kk] + (o) + TO(i)))
  PAGE(10);
  tag = __builtin_amdgcn_readfirstlane(((const unsigned*)(smem + LDS_SEL))[5]);
  {
    int son = 0;
    auto tile = [&](int kt, int zero) __attribute__((always_inline)) {
      SLOTS(16) {
        mm(true, 0, 0, q, zero);
        if (!(q & 1)) {
          if (q < 8) fm[(q >> 1) & 1][2 + (q >> 2)] = RDM(2 + (q >> 2), (q >> 1) & 1, 0);
          else { const int r = (q - 8) >> 1; fn[1][r] = RDN(2 + (r >> 1), r & 1, 0); }
        }
        SB();
      }
      SLOTS(16) {
        mm(false, 1, 1, q, zero);
        if (!(q & 1) && q < 8) fn[0][q >> 1] = RDN(4 + (q >> 2), (q >> 1) & 1, 0);
        if (zero == 2 && q >= 9 && (q & 1) && q < 15) xq_load((q - 9) >> 1);
        SB();
      }
      const int som = son >> 1;
      son = son == 65536 ? 0 : son + 32768;
      SLOTS(16) {
        mm(false, 0, 2, q, zero);
        if (!(q & 1) && q < 8) fn[1][q >> 1] = RDN(6 + (q >> 2), (q >> 1) & 1, 0);
        if (q == 9 || q == 11) { an[q == 11] = bn[q == 11] + son; asm volatile("" : "+v"(an[q == 11])); }
        if (q == 13 || q == 15) { am[q == 15] = bm[q == 15] + som; asm volatile("" : "+v"(am[q == 15])); }
        SB();
      }
      BAR_LGKM();
      SB();
      SLOTS(16) {
        mm(true, 1, 3, q, zero);
        if (!(q & 1)) {
          if (q < 8) fn[0][q >> 1] = RDN(q >> 2, (q >> 1) & 1, 0);
          else fm[(q >> 1) & 1][(q - 8) >> 2] = RDM((q - 8) >> 2, (q >> 1) & 1, 0);
        }
        SB();
      }
    };
    SB();
    tile(0, 1);
#pragma unroll 1
    for (int kt = 1; kt < G2_NKT - 5; ++kt) tile(kt, 0);
    tile(G2_NKT - 5, 2);
  }
  unsigned fx;
  int fo = lane < 32 ? kq * 512 + lane * 4 : (int)0x80000000;
  {
    constexpr auto chp = [](int g) { return g == 0 || g_t[g] != g_t[g - 1]; };
    constexpr auto chn = [](int g) { return g + 1 < NG && g_t[g + 1] != g_t[g]; };
    unsigned an2[2], an3[2], am2[2];
#pragma unroll
    for (int kk = 0; kk < 2; ++kk) {
      an[kk] = bn[kk]; an2[kk] = bn[kk] + 65536; an3[kk] = bn[kk] + (G2_MRING - wr * 8192); am[kk] = bm[kk]; am2[kk] = bm[kk] - (G2_MRING - wc * 8192);
      asm volatile("" : "+v"(an[kk]), "+v"(am[kk]), "+v"(am2[kk]));
    }
    int vo;
    u32x4 pk;
    u32x2 kv;
    auto publish = [&](int d, int h) __attribute__((always_inline)) {
      if (h == 0) { vo = ml; PINV(vo); vo += (((d + kq + 1) & 3) * 4 + kq) * 4 * MSG; PINV(vo); }
      const f32x4 v = acc[2 * d + (h >> 2)][h & 3];
      pk[(h & 1) * 2] = pk2(v[0], v[1]); pk[(h & 1) * 2 + 1] = pk2(v[2], v[3]);
      if (h & 1) __builtin_amdgcn_raw_buffer_store_b128(pk, rs, vo + (h >> 1) * 1024, 0, 16);
    };
#define MSG_FLAG(d) __builtin_amdgcn_raw_buffer_store_b32(tag, rc, fo + (((d) + kq + 1) & 3) * 2048, 0, 16)
    auto rdn = [&](int g, int r) __attribute__((always_inline)) {
      fn[g & 1][r] = *LDSP(const bf16x8, (g_n[g] >= G2_MRING ? an3[r & 1] + (g_n[g] - G2_MRING) : g_n[g] >= 65536 ? an2[r & 1] + (g_n[g] - 65536) : an[r & 1] + g_n[g]) + (r >> 1) * 2048);
    };
    auto rdm = [&](int t, int i, int kk) __attribute__((always_inline)) { fm[kk][i] = *LDSP(const bf16x8, (t < 2 ? am[kk] + m_off(12 + t) : am2[kk] + n_off(10 + t) + 8192) + TO(i)); };
    auto group = [&](auto GI) __attribute__((always_inline)) {
      constexpr int gi = decltype(GI)::value;
      if (gi == 1) { BAR_LGKM();
        SB(); }
      if (gi == 3 || gi == 7) {
        if (gi == 3) PAGE(11);
        BAR_LGKM();
        SB();
        if (gi == 3) kv = *(const u32x2*)(smem + LDS_SEL);
        SB();
      }
      if (gi == 13) {
        PAGE(13);
        BAR();
        SB(); }
#pragma unroll
      for (int q = 0; q < 16; ++q) {
        const int n = gi * 16 + q;
        if (n == 152) { WAIT_VM(4); MSG_FLAG(0); SB(); }
        if (n == 216) { WAIT_VM(5); MSG_FLAG(1); SB(); }
        if (n == 250) { WAIT_VM(1); MSG_FLAG(2); fx = bld4_sc1(rc, po); SB(); }
        if (n == 13 || n == 29) { PINV(an2[n == 29]); }
        if (n == 93 || n == 95) { PINV(an3[n == 95]); }
        mm(chp(gi) || chn(gi), gi & 1, gi >> 2, q, 2);
        constexpr bool ni = gi + 1 < NG && (chp(gi + 1 < NG ? gi + 1 : gi) || chn(gi + 1 < NG ? gi + 1 : gi));
        auto nr = [](int r) { return ni && r >= 2 ? 5 - r : r; };
        if constexpr (chp(gi) && chn(gi)) {
          if (q < 4) rdm(g_t[gi], 2 + (q >> 1), q & 1); else if (q < 8) rdn(gi + 1, nr(q - 4)); else if (q < 12) rdm(g_t[gi + 1], (q - 8) >> 1, q & 1);
        } else if constexpr (chp(gi)) {
          if (q < 4) rdm(g_t[gi], 2 + (q >> 1), q & 1);
          else if (gi + 1 < NG && q < 12 && !(q & 1)) rdn(gi + 1, nr((q - 4) >> 1));
        } else if constexpr (chn(gi)) {
          if (!(q & 1)) { if (q < 8) rdn(gi + 1, nr(q >> 1)); else rdm(g_t[gi + 1], (q - 8) >> 2, (q >> 1) & 1); }
        } else {
          if (!(q & 1) && q < 8) rdn(gi + 1, nr(q >> 1));
        }
        if (n >= 68 && n < 84 && !(n & 1)) publish(0, (n - 68) >> 1);
        if (gi == 8 && !(q & 1)) publish(1, q >> 1);
        if (gi == 12 && q < 8) publish(2, q);
        if (n == 60) { key[0] = __builtin_amdgcn_readfirstlane(kv.x); key[1] = __builtin_amdgcn_readfirstlane(kv.y); }
        SB();
      }
    };
#define GR4(i) group(IC<i>{}); group(IC<i + 1>{}); group(IC<i + 2>{}); group(IC<i + 3>{});
    GR4(0) GR4(4) GR4(8) GR4(12)
    asm volatile("s_nop 7");
    SB();
  }
#pragma unroll
  for (int i = 0; i < 4; ++i) *(f32x4*)(smem + LDS_ACC + wr * 16384 + wc * 4096 + i * 1024 + lane * 16) = acc[7][i];
#pragma unroll
  for (int i = 0; i < 4; ++i) ya[i] = acc[6][i];
  asm volatile("s_waitcnt lgkmcnt(0)" ::: "memory");
  if (lane == 0) sig[0] = 2;
  SB();
  wait_xq<3>(xq, sq);
  far_calc();
  cv_calc();
  asm volatile("s_waitcnt vmcnt(0)" : "+v"(fx) :: "memory");
  seen = (unsigned)__ballot(fx == tag);
  }

  PAGE(6);
  u32x4 pp[3][2];
  unsigned e_lo = 0x00003f80u, e_hi = 0x3f800000u;
  asm volatile("" : "+v"(e_lo), "+v"(e_hi));
  __amdgpu_buffer_rsrc_t rs_ = rs, rc_ = rc;
  int mo[3] = {MSG_IDX(0) * 4 * MSG, MSG_IDX(1) * 4 * MSG, MSG_IDX(2) * 4 * MSG}, mv = ml + ha * 2048, mv2 = mv + 1024;
  asm volatile("" : "+s"(rs_), "+s"(rc_), "+s"(mo[0]), "+s"(mo[1]), "+s"(mo[2]));
  int mt = lane < 16 * ha ? lane * 128 : (int)0x80000000;
  unsigned pf[3] = {0, 0, 0};
  asm volatile("" : "+v"(mv), "+v"(mv2), "+v"(mt));
#define MSG_POLL(m) do { \
    if (!((seen >> (m)) & 1)) { \
      unsigned pv_, it_ = SPIN_MAX; \
      asm volatile("1:\n\tbuffer_load_dword %0, %3, %4, 0 offen sc1\n\ts_waitcnt vmcnt(0)\n\tv_cmp_eq_u32 vcc, %5, %0\n\ts_mov_b32 %1, vcc_lo\n\t" \
                   "s_bitcmp1_b32 %1, %6\n\ts_cbranch_scc1 2f\n\ts_sub_u32 %2, %2, 1\n\ts_cbranch_scc1 2f\n\ts_sleep 1\n\ts_branch 1b\n2:" \
                   : "=&v"(pv_), "+s"(seen), "+s"(it_) : "v"(po), "s"(rc_), "s"(tag), "i"(m) : "vcc", "scc", "memory"); \
    } \
    pp[m][0] = __builtin_amdgcn_raw_buffer_load_b128(rs_, mv, mo[m], 16); \
    pp[m][1] = __builtin_amdgcn_raw_buffer_load_b128(rs_, mv2, mo[m], 16); \
    if (m) pf[m] = __builtin_amdgcn_raw_buffer_load_b32(rs_, mt, mo[m], 16); } while (0)
#define MSG_ADD(m) do { \
    _Pragma("unroll") for (int u = 0; u < 8; ++u) { \
      ya[u >> 1][(u & 1) * 2] = add_bf(pp[m][u >> 2][u & 3], e_lo, ya[u >> 1][(u & 1) * 2]); ya[u >> 1][(u & 1) * 2 + 1] = add_bf(pp[m][u >> 2][u & 3], e_hi, ya[u >> 1][(u & 1) * 2 + 1]); } } while (0)
  MSG_POLL(2);
  if (ha) {
    int it = 0;
    while (sig[0] != 2 && it < SPIN_MAX * 8) { __builtin_amdgcn_s_sleep(1); ++it; }
    asm volatile("" ::: "memory");
#pragma unroll
    for (int i = 0; i < 4; ++i) ya[i] = *(const f32x4*)(smem + LDS_ACC + (wid & 2) * 8192 + (wid & 1) * 4096 + i * 1024 + lane * 16);
  }
  MSG_POLL(1); MSG_POLL(0);
  SB();
  if (__builtin_expect(far0 == 0 || far1 == 0, 0)) {
    asm volatile("s_branch 2f\nk_pg12:\n\ts_setpc_b64 s[42:43]\n2:");
#pragma unroll 1
    for (int c = 0; c < 2; ++c) {
      if (c ? far1 : far0) continue;
      const int ic = (0xffff - (c ? key[1] : key[0])) & (S_ - 1);
      lds_vi cnt = (lds_vi)(smem + LDS_SEL + 24 + c * 4);
      int it = 0;
      while (cnt[0] != 4 && it < SPIN_MAX * 8) { __builtin_amdgcn_s_sleep(1); ++it; }
      asm volatile("" ::: "memory");
      const float* q = (const float*)(smem + LDS_FIN + c * 2048) + (wid & 1) * 64 + (fr >> 3) * 32 + col4(fr);
      const f32x4 v = (*(const f32x4*)q + *(const f32x4*)(q + 128)) + (*(const f32x4*)(q + 256) + *(const f32x4*)(q + 384));
#pragma unroll
      for (int r = 0; r < 4; ++r)
#pragma unroll
        for (int k = 0; k < 4; ++k) {
          cv[r * 4 + k] += (xo[r] >> 11) == ic ? v[k] : 0.f;
        }
    }
  }
#pragma unroll
  for (int e = 0; e < 16; ++e) ya[e & 3][e >> 2] += cv[e];
  asm volatile("" : "+v"(ya[0]), "+v"(ya[1]), "+v"(ya[2]), "+v"(ya[3]));
  SB();
  MSG_ADD(2); MSG_ADD(1);
  asm volatile("" : "+v"(ya[0]), "+v"(ya[1]), "+v"(ya[2]), "+v"(ya[3]));
  SB();
  MSG_ADD(0);
  asm volatile("" :: "v"(pf[0]), "v"(pf[1]), "v"(pf[2]));
  {
#pragma unroll
    for (int r = 0; r < 4; ++r) *(u32x2*)((char*)out + (unsigned)xo[r]) = (u32x2){pk2(ya[0][r], ya[1][r]), pk2(ya[2][r], ya[3][r])};
  }
}

extern "C" void kernel_launch(void* const* d_in, const int* in_sizes, int n_in, void* d_out, int out_size,
                              void* d_ws, size_t ws_size, hipStream_t stream) {
  const u16* x = (const u16*)d_in[0];
  const u16* ws_re = (const u16*)d_in[1];
  const u16* ws_im = (const u16*)d_in[2];
  const u16* wd_re = (const u16*)d_in[3];
  const u16* wd_im = (const u16*)d_in[4];
  const u16* w_r = (const u16*)d_in[6];
  const u16* w_gate = (const u16*)d_in[7];
  const u16* w_up = (const u16*)d_in[8];
  const u16* w_down = (const u16*)d_in[9];
  k_main<<<256, 512, 0, stream>>>(x, wd_re, wd_im, w_r, (char*)d_ws, ws_re, ws_im, w_gate, w_up, w_down, (u16*)d_out);
}
```
